```python
import torch

def _softmax(x, axis=-1):
    return torch.softmax(x, dim=axis)
def _gelu(x):
    return torch.nn.functional.gelu(x, approximate="tanh")
def _silu(x):
    return torch.nn.functional.silu(x)
def _sigmoid(x):
    return torch.sigmoid(x)
def _layernorm(x, axis=-1, eps=1e-5):
    m = x.mean(dim=axis, keepdim=True)
    v = x.var(dim=axis, keepdim=True, unbiased=False)
    return (x - m) / torch.sqrt(v + eps)
def _scatter_add(tgt, idx, src, axis=0):
    return tgt.clone().index_add_(axis, idx, src)
def _segment_sum(src, seg, n):
    out = torch.zeros((n,) + src.shape[1:], dtype=src.dtype)
    return out.index_add_(0, seg, src)
def _pad(x, pw):
    flat = tuple(v for pair in reversed(pw) for v in pair)
    return torch.nn.functional.pad(x, flat)

kernel_name = 'concat_heads__mha_block_cached__gqa_block_cached__near_ridge'

def setup_inputs():

    _w = (lambda a: (lambda t: t.to(torch.bfloat16) if t.is_floating_point() else t)(torch.as_tensor(a)))
    def _A_setup():
        B, Sq, D = 8, 192, 640
        H, Sk, Dh = 32, 512, 128
        return {
            "x": _w((lambda *s: torch.randn(*s, dtype=torch.bfloat16) * 0.8)(B, Sq, D)),
            "k_cache": _w((lambda *s: torch.randn(*s, dtype=torch.bfloat16) * 0.8)(B, H, Sk, Dh)),
            "v_cache": _w((lambda *s: torch.randn(*s, dtype=torch.bfloat16) * 0.05)(B, H, Sk, Dh)),
            "wq": _w((lambda *s: torch.randn(*s, dtype=torch.bfloat16) * 0.8)(D, H * Dh) * (D ** -0.5)),
            "wo": _w((lambda *s: torch.randn(*s, dtype=torch.bfloat16) * 0.05)(H * Dh, D) * ((H * Dh) ** -0.5)),
        }
    _A_in = _A_setup()

    def _B_setup():
        B, Sq, D = 8, 192, 640
        Hq, Hkv, Sk, Dh = 32, 32, 512, 128
        assert Hq % Hkv == 0, (Hq, Hkv)
        return {
            "x": _w((lambda *s: torch.randn(*s, dtype=torch.bfloat16) * 0.8)(B, Sq, D)),
            "k_cache": _w((lambda *s: torch.randn(*s, dtype=torch.bfloat16) * 0.8)(B, Hkv, Sk, Dh)),
            "v_cache": _w((lambda *s: torch.randn(*s, dtype=torch.bfloat16) * 0.05)(B, Hkv, Sk, Dh)),
            "wq": _w((lambda *s: torch.randn(*s, dtype=torch.bfloat16) * 0.8)(D, Hq * Dh) * (D ** -0.5)),
            "wo": _w((lambda *s: torch.randn(*s, dtype=torch.bfloat16) * 0.05)(Hq * Dh, D) * ((Hq * Dh) ** -0.5)),
        }
    _B_in = _B_setup()

    return {
        "A_x": _A_in["x"],
        "A_k_cache": _A_in["k_cache"],
        "A_v_cache": _A_in["v_cache"],
        "A_wq": _A_in["wq"],
        "A_wo": _A_in["wo"],
        "B_x": _B_in["x"],
        "B_k_cache": _B_in["k_cache"],
        "B_v_cache": _B_in["v_cache"],
        "B_wq": _B_in["wq"],
        "B_wo": _B_in["wo"],
    }

def reference(A_x, A_k_cache, A_v_cache, A_wq, A_wo, B_x, B_k_cache, B_v_cache, B_wq, B_wo):
    def _A_ref(x, k_cache, v_cache, wq, wo):
        B, Sq, D = x.shape
        H, Dh = 32, 128
        q = (lambda x, *d: x.permute(*d))((x @ wq).reshape(B, Sq, H, Dh), 0, 2, 1, 3)
        scores = torch.einsum("bhqd,bhkd->bhqk", q, k_cache) * (Dh ** -0.5)
        p = _softmax(scores, axis=-1)
        o = torch.einsum("bhqk,bhkd->bhqd", p, v_cache)
        return (lambda x, *d: x.permute(*d))(o, 0, 2, 1, 3).reshape(B, Sq, H * Dh) @ wo

    def _B_ref(x, k_cache, v_cache, wq, wo):
        B, Sq, D = x.shape
        Hq, Dh = 32, 128
        Hkv = k_cache.shape[1]
        rep = Hq // Hkv
        q = (lambda x, *d: x.permute(*d))((x @ wq).reshape(B, Sq, Hq, Dh), 0, 2, 1, 3)
        k = (lambda x, n, axis: torch.repeat_interleave(x, n, dim=axis))(k_cache, rep, axis=1)
        v = (lambda x, n, axis: torch.repeat_interleave(x, n, dim=axis))(v_cache, rep, axis=1)
        scores = torch.einsum("bhqd,bhkd->bhqk", q, k) * (Dh ** -0.5)
        p = _softmax(scores, axis=-1)
        o = torch.einsum("bhqk,bhkd->bhqd", p, v)
        return (lambda x, *d: x.permute(*d))(o, 0, 2, 1, 3).reshape(B, Sq, Hq * Dh) @ wo

    a = _A_ref(A_x, A_k_cache, A_v_cache, A_wq, A_wo)
    b = _B_ref(B_x, B_k_cache, B_v_cache, B_wq, B_wo)
    return torch.concatenate([a, b], axis=-1)
```

```cpp
#include <hip/hip_runtime.h>
#include <hip/hip_bf16.h>
typedef unsigned short u16;
__device__ inline float b2f(u16 v){ return __uint_as_float(((unsigned)v)<<16); }
__device__ inline u16 f2b(float f){ unsigned u=__float_as_uint(f); u += 0x7fff + ((u>>16)&1); return (u16)(u>>16); }
__global__ __launch_bounds__(128) void k_attn(const u16* xA,const u16* kA,const u16* vA,const u16* wqA,
                       const u16* xB,const u16* kB,const u16* vB,const u16* wqB, u16* O){
  int bid=blockIdx.x; int rb=bid%24; int h=(bid/24)%32; int b=(bid/(24*32))%8; int br=bid/(24*32*8);
  const u16* x=br?xB:xA; const u16* k=br?kB:kA; const u16* v=br?vB:vA; const u16* wq=br?wqB:wqA;
  __shared__ float q[8][128]; __shared__ float s[8][512]; __shared__ float xs[8][640];
  int t=threadIdx.x;
  for(int i=t;i<8*640;i+=128) xs[i/640][i%640]=b2f(x[(long)(b*192+rb*8+i/640)*640+i%640]);
  __syncthreads();
  float acc[8]={0,0,0,0,0,0,0,0};
  for(int kk=0;kk<640;kk++){ float w=b2f(wq[(long)kk*4096+h*128+t]); for(int r=0;r<8;r++) acc[r]+=xs[r][kk]*w; }
  for(int r=0;r<8;r++) q[r][t]=b2f(f2b(acc[r]));
  __syncthreads();
  const u16* kh=k+(long)(b*32+h)*512*128; const u16* vh=v+(long)(b*32+h)*512*128;
  for(int j=t;j<512;j+=128){ float a[8]={0,0,0,0,0,0,0,0}; for(int d=0;d<128;d++){ float kv=b2f(kh[j*128+d]); for(int r=0;r<8;r++) a[r]+=q[r][d]*kv;} for(int r=0;r<8;r++) s[r][j]=a[r]*0.08838834764831845f; }
  __syncthreads();
  __shared__ float mx[8], sm[8];
  if(t<8){ float m=-1e30f; for(int j=0;j<512;j++) m=fmaxf(m,s[t][j]); float su=0; for(int j=0;j<512;j++){ float e=__expf(s[t][j]-m); s[t][j]=e; su+=e;} mx[t]=m; sm[t]=su; }
  __syncthreads();
  float o[8]={0,0,0,0,0,0,0,0};
  for(int j=0;j<512;j++){ float vv=b2f(vh[j*128+t]); for(int r=0;r<8;r++) o[r]+=s[r][j]*vv; }
  for(int r=0;r<8;r++) O[((long)br*1536+b*192+rb*8+r)*4096+h*128+t]=f2b(o[r]/sm[r]);
}
__global__ __launch_bounds__(128) void k_oproj(const u16* O,const u16* woA,const u16* woB,u16* out){
  int bid=blockIdx.x; int nb=bid%5; int rb=(bid/5)%192; int br=bid/(5*192);
  const u16* wo=br?woB:woA; int t=threadIdx.x;
  __shared__ float os[8][512];
  float acc[8]={0,0,0,0,0,0,0,0};
  for(int k0=0;k0<4096;k0+=512){
    __syncthreads();
    for(int i=t;i<8*512;i+=128) os[i/512][i%512]=b2f(O[((long)br*1536+rb*8+i/512)*4096+k0+i%512]);
    __syncthreads();
    for(int kk=0;kk<512;kk++){ float w=b2f(wo[(long)(k0+kk)*640+nb*128+t]); for(int r=0;r<8;r++) acc[r]+=os[r][kk]*w; }
  }
  for(int r=0;r<8;r++) out[(long)(rb*8+r)*1280+br*640+nb*128+t]=f2b(acc[r]);
}
extern "C" void kernel_launch(void* const* d_in, const int* in_sizes, int n_in, void* d_out, int out_size, void* d_ws, size_t ws_size, hipStream_t stream){
  u16* O=(u16*)d_ws;
  k_attn<<<2*8*32*24,128,0,stream>>>((const u16*)d_in[0],(const u16*)d_in[1],(const u16*)d_in[2],(const u16*)d_in[3],(const u16*)d_in[5],(const u16*)d_in[6],(const u16*)d_in[7],(const u16*)d_in[8],O);
  k_oproj<<<2*192*5,128,0,stream>>>(O,(const u16*)d_in[4],(const u16*)d_in[9],(u16*)d_out);
}
```

```cpp
#include <hip/hip_runtime.h>
#include <hip/hip_bf16.h>
typedef unsigned short u16;
typedef unsigned long long u64;

__device__ __forceinline__ float b2f(unsigned v) { return __uint_as_float(v << 16); }
__device__ __forceinline__ unsigned lane_id() { return __builtin_amdgcn_mbcnt_hi(~0u, __builtin_amdgcn_mbcnt_lo(~0u, 0u)); }
__device__ __forceinline__ unsigned pack_bf16(float lo, float hi) { unsigned r; asm("v_cvt_pk_bf16_f32 %0, %1, %2" : "=v"(r) : "v"(lo), "v"(hi)); return r; }

__device__ __noinline__ void attn_true_max(const u16* x, const u16* wq, const u16* k, const u16* v, char* o) {
  const unsigned l = lane_id();
  for (int r = 0; r < 96; ++r) {
    float q0 = 0.f, q1 = 0.f;
    for (int kk = 0; kk < 640; ++kk) {
      const float xv = b2f(x[r * 640 + kk]); const unsigned w = *(const unsigned*)(wq + (size_t)kk * 4096 + 2 * l);
      q0 = fmaf(xv, b2f(w & 0xffffu), q0); q1 = fmaf(xv, b2f(w >> 16), q1);
    }
    const unsigned qp = pack_bf16(q0, q1);
    float s0 = 0.f, s1 = 0.f, s2 = 0.f, s3 = 0.f, s4 = 0.f, s5 = 0.f, s6 = 0.f, s7 = 0.f;
    for (int d2 = 0; d2 < 64; ++d2) {
      const unsigned qq = __shfl(qp, d2); const float qa = b2f(qq & 0xffffu), qb = b2f(qq >> 16);
      const unsigned* kp = (const unsigned*)k + l * 64 + d2;
#define SC(n) { const unsigned kw = kp[(n) * 4096]; s##n = fmaf(qa, b2f(kw & 0xffffu), fmaf(qb, b2f(kw >> 16), s##n)); }
      SC(0) SC(1) SC(2) SC(3) SC(4) SC(5) SC(6) SC(7)
#undef SC
    }
    float m = fmaxf(fmaxf(fmaxf(s0, s1), fmaxf(s2, s3)), fmaxf(fmaxf(s4, s5), fmaxf(s6, s7)));
    for (int o2 = 32; o2; o2 >>= 1) m = fmaxf(m, __shfl_xor(m, o2));
    const float c = 0.12751743082f;
    s0 = exp2f((s0 - m) * c); s1 = exp2f((s1 - m) * c); s2 = exp2f((s2 - m) * c); s3 = exp2f((s3 - m) * c);
    s4 = exp2f((s4 - m) * c); s5 = exp2f((s5 - m) * c); s6 = exp2f((s6 - m) * c); s7 = exp2f((s7 - m) * c);
    float sum = ((s0 + s1) + (s2 + s3)) + ((s4 + s5) + (s6 + s7));
    for (int o2 = 32; o2; o2 >>= 1) sum += __shfl_xor(sum, o2);
    float a0 = 0.f, a1 = 0.f;
    for (int j = 0; j < 64; ++j) {
      const unsigned* vp = (const unsigned*)v + j * 64 + l;
#define PV(n) { const float p = __shfl(s##n, j); const unsigned vw = vp[(n) * 4096]; a0 = fmaf(p, b2f(vw & 0xffffu), a0); a1 = fmaf(p, b2f(vw >> 16), a1); }
      PV(0) PV(1) PV(2) PV(3) PV(4) PV(5) PV(6) PV(7)
#undef PV
    }
    const unsigned d = 2 * l, dd = d & 31;
    const unsigned off = (r >> 4) * 131072u + (d >> 5) * 1024u + (16u * ((dd >> 2) & 3) + (r & 15)) * 16u + 2u * (4u * (dd >> 4) + (dd & 3));
    __hip_atomic_store((__attribute__((address_space(1))) unsigned*)(o + off), pack_bf16(a0 / sum, a1 / sum), __ATOMIC_RELAXED, __HIP_MEMORY_SCOPE_AGENT);
  }
}

struct Args { const u16 *xA, *kA, *vA, *wqA, *woA, *xB, *kB, *vB, *wqB, *woB; char* ws; u16* out; };
typedef const __attribute__((address_space(4))) Args* ArgP;
#define FLAG_OFF (32u << 20)
#define PART_OFF (40u << 20)

__global__ __launch_bounds__(256, 1) void k_block(Args A) {
  __shared__ __attribute__((aligned(256))) char lds[163840];
  unsigned wg = blockIdx.x;
  unsigned wv = __builtin_amdgcn_readfirstlane(threadIdx.x >> 6);
  unsigned flag, mode = 0;
  ArgP a = (ArgP)__builtin_amdgcn_kernarg_segment_ptr();
  const u16 *x, *k, *v, *wq, *wo; char* ws = A.ws; u16* out = A.out;
  { const bool br = wg & 4; x = br ? A.xB : A.xA; k = br ? A.kB : A.kA; v = br ? A.vB : A.vA; wq = br ? A.wqB : A.wqA; wo = br ? A.woB : A.woA; }
  asm volatile("" : "+s"(mode));
#pragma clang loop unroll(disable)
  for (;;) {
    const unsigned xcd = wg & 7, idx = wg >> 3;
    const unsigned br = xcd >> 2, kq = xcd & 3, j = idx & 3, hp = 4 * kq + j, b = idx >> 2;
    const unsigned h = 2 * hp + (wv >> 1), rh = wv & 1, rhT = j >> 1, chT = j & 1, grp = (br * 8 + b) * 4 + j;
    const u16* px = x + (size_t)b * 192 * 640;
    const u16* pw = wq + hp * 256;
    const u16* pk = k + (size_t)(b * 32 + h) * 65536;
    const u16* pv = v + (size_t)(b * 32 + h) * 65536;
    const u16* pwo = wo + (size_t)kq * 1024 * 640 + chT * 320;
    const char* pwm = (const char*)wo + (kq * 32 + idx) * 40960;
    char* pws = ws;
    u16* pout = out + (size_t)(b * 192 + rhT * 96) * 1280 + br * 640 + chT * 320;
    unsigned o1 = (((br * 8 + b) * 12 + 6 * rh) * 128 + h * 4) * 1024, o2 = (((br * 8 + b) * 12 + 6 * rhT) * 128 + 32 * kq) * 1024;
    unsigned my1 = FLAG_OFF + ((br * 16 + 2 * b + rh) * 32 + h) * 128, fl1 = FLAG_OFF + ((br * 16 + 2 * b + rhT) * 32 + 8 * kq) * 128;
    unsigned my2 = FLAG_OFF + 131072 + grp * 8192 + (kq * 4 + wv) * 128, fl2 = FLAG_OFF + 131072 + (grp * 4 + kq) * 2048 + wv * 128;
    const unsigned pw1 = PART_OFF + grp * 262144 + kq * 16384 + wv * 4096, pr1 = PART_OFF + (grp * 4 + kq) * 65536 + wv * 4096;
    unsigned wsz = 640 * 8192 - hp * 512, wosz = 1024 * 1280 - chT * 640, wvi = wv, ldsi = (unsigned)(size_t)lds;
    const unsigned rot = 1 + 3 * ((b + idx) % 3), krot = 2 * (idx & 3) + (wv >> 1), hq0 = 2 * j;
    asm volatile(
"s_add_u32 s88, s56, 0x20c0000\n"
"s_addc_u32 s89, s57, 0\n"
"s_load_dwordx2 s[36:37], s[88:89], 0x0\n"
"s_cmp_lg_u32 %[mode], 0\n"
"s_cbranch_scc1 LREENT%=\n"
"s_and_b32 s71, s77, 1\n"
"v_mbcnt_lo_u32_b32 v244, -1, 0\n"
"v_mbcnt_hi_u32_b32 v244, -1, v244\n"
"s_and_b32 s41, s41, 0xffff\n"
"s_mov_b32 s42, 0x3c000\n"
"s_mov_b32 s43, 0x20000\n"
"s_mul_i32 s72, s77, 6144\n"
"s_add_u32 s60, s72, s76\n"
"s_mov_b32 s82, 0\n"
"v_mov_b32 v15, s77\n"
"v_mul_u32_u24 v15, 0xf000, v15\n"
"v_lshrrev_b32 v13, 4, v244\n"
"v_and_b32 v14, 7, v244\n"
"v_xor_b32 v14, v13, v14\n"
"v_lshlrev_b32 v14, 4, v14\n"
"v_lshrrev_b32 v192, 3, v244\n"
"v_mul_u32_u24 v192, 0x500, v192\n"
"v_add_u32 v192, v14, v192\n"
"v_add_u32 v192, v15, v192\n"
"v_xor_b32 v193, 64, v192\n"
"v_add_u32 v197, 0xc000, v193\n"
"v_add_u32 v196, 0x9c00, v192\n"
"v_add_u32 v195, 0x7800, v193\n"
"v_add_u32 v194, 0x4800, v192\n"
"v_add_u32 v193, 0x2400, v193\n"
"s_add_u32 m0, s60, 0x1c000\n"
"s_nop 0\n"
"buffer_load_dwordx4 v192, s[40:43], s82 offen lds\n"
"buffer_load_dwordx4 v193, s[40:43], s82 offen offset:1024 lds\n"
"buffer_load_dwordx4 v194, s[40:43], s82 offen offset:2048 lds\n"
"s_add_u32 m0, s60, 0x1cc00\n"
"s_nop 0\n"
"buffer_load_dwordx4 v195, s[40:43], s82 offen lds\n"
"buffer_load_dwordx4 v196, s[40:43], s82 offen offset:1024 lds\n"
"buffer_load_dwordx4 v197, s[40:43], s82 offen offset:2048 lds\n"
"s_and_b32 s45, s45, 0xffff\n"
"s_mov_b32 s47, 0x20000\n"
"s_lshl_b32 s72, s77, 12\n"
"s_add_u32 s61, s72, s76\n"
"s_mov_b32 s83, 0\n"
"v_mov_b32 v15, s77\n"
"v_lshlrev_b32 v15, 17, v15\n"
"v_and_b32 v14, 1, v244\n"
"v_lshlrev_b32 v14, 4, v14\n"
"v_lshrrev_b32 v11, 4, v244\n"
"v_and_b32 v13, 15, v244\n"
"v_lshrrev_b32 v13, 1, v13\n"
"v_xor_b32 v13, v11, v13\n"
"v_and_b32 v13, 3, v13\n"
"v_lshlrev_b32 v13, 5, v13\n"
"v_lshrrev_b32 v11, 4, v244\n"
"v_and_b32 v12, 15, v244\n"
"v_lshrrev_b32 v12, 1, v12\n"
"v_xor_b32 v12, v11, v12\n"
"v_lshrrev_b32 v12, 2, v12\n"
"v_lshlrev_b32 v12, 8, v12\n"
"v_lshrrev_b32 v204, 4, v244\n"
"v_lshlrev_b32 v204, 13, v204\n"
"v_add_u32 v204, v12, v204\n"
"v_add_u32 v204, v13, v204\n"
"v_add_u32 v204, v14, v204\n"
"v_add_u32 v204, v15, v204\n"
"v_xor_b32 v206, 0x100, v204\n"
"v_add_u32 v205, 0x7c00, v204\n"
"v_add_u32 v207, 0x17400, v206\n"
"v_add_u32 v206, 0xf800, v206\n"
"s_add_u32 m0, s61, 0x8000\n"
"s_nop 0\n"
"buffer_load_dwordx4 v204, s[44:47], s83 offen lds\n"
"buffer_load_dwordx4 v205, s[44:47], s83 offen offset:1024 lds\n"
"buffer_load_dwordx4 v206, s[44:47], s83 offen offset:2048 lds\n"
"buffer_load_dwordx4 v207, s[44:47], s83 offen offset:3072 lds\n"
"s_getpc_b64 s[78:79]\n"
"LPF%=:\n"
"s_lshl_b32 s72, %[idx], 2\n"
"s_add_u32 s72, s72, s77\n"
"s_lshl_b32 s72, s72, 10\n"
"v_and_b32 v0, 7, v244\n"
"v_lshlrev_b32 v0, 7, v0\n"
"v_add_u32 v0, s72, v0\n"
"v_min_u32 v0, LREENT%=-LPF%=, v0\n"
"global_load_dword v254, v0, s[78:79]\n"
"s_lshr_b32 s70, s77, 1\n"
"s_and_b32 s49, s49, 0xffff\n"
"s_mov_b32 s50, 0x20000\n"
"s_mov_b32 s51, 0x20000\n"
"s_and_b32 s53, s53, 0xffff\n"
"s_mov_b32 s54, 0x20000\n"
"s_mov_b32 s55, 0x20000\n"
"s_and_b32 s57, s57, 0xffff\n"
"s_mov_b32 s58, 0x4000000\n"
"s_mov_b32 s59, 0x20000\n"
"s_lshl_b32 s72, s70, 16\n"
"s_add_u32 s72, s72, s76\n"
"s_lshl_b32 s73, s71, 11\n"
"s_add_u32 s62, s72, s73\n"
"s_lshl_b32 s73, s71, 10\n"
"s_add_u32 s63, s72, s73\n"
"s_add_u32 s63, s63, 0x2000\n"
"s_lshl_b32 s84, %[krot], 13\n"
"s_sub_u32 s84, s84, 0x1000\n"
"s_mov_b32 s85, s84\n"
"v_mov_b32 v15, s71\n"
"v_lshlrev_b32 v15, 11, v15\n"
"v_mov_b32 v12, s71\n"
"v_lshlrev_b32 v12, 3, v12\n"
"v_lshrrev_b32 v13, 4, v244\n"
"v_add_u32 v13, v12, v13\n"
"v_and_b32 v14, 15, v244\n"
"v_xor_b32 v14, v13, v14\n"
"v_lshlrev_b32 v14, 4, v14\n"
"v_lshrrev_b32 v240, 4, v244\n"
"v_lshlrev_b32 v240, 8, v240\n"
"v_add_u32 v240, v14, v240\n"
"v_add_u32 v240, v15, v240\n"
"v_xor_b32 v241, 64, v240\n"
"v_mov_b32 v15, s71\n"
"v_lshlrev_b32 v15, 10, v15\n"
"v_mov_b32 v12, s71\n"
"v_lshlrev_b32 v12, 2, v12\n"
"v_lshrrev_b32 v13, 4, v244\n"
"v_add_u32 v13, v12, v13\n"
"v_lshlrev_b32 v13, 1, v13\n"
"v_and_b32 v14, 15, v244\n"
"v_xor_b32 v14, v13, v14\n"
"v_lshlrev_b32 v14, 4, v14\n"
"v_lshrrev_b32 v242, 4, v244\n"
"v_lshlrev_b32 v242, 8, v242\n"
"v_add_u32 v242, v14, v242\n"
"v_add_u32 v242, v15, v242\n"
"v_and_b32 v12, 15, v244\n"
"v_lshrrev_b32 v12, 1, v12\n"
"v_lshrrev_b32 v13, 4, v244\n"
"v_xor_b32 v13, v12, v13\n"
"v_lshlrev_b32 v13, 4, v13\n"
"v_and_b32 v12, 15, v244\n"
"v_lshlrev_b32 v12, 7, v12\n"
"v_mov_b32 v234, s71\n"
"v_mul_u32_u24 v234, 0x3000, v234\n"
"v_add_u32 v234, v12, v234\n"
"v_add_u32 v234, v13, v234\n"
"v_add_u32 v234, s76, v234\n"
"v_add_u32 v234, 0x1c000, v234\n"
"v_xor_b32 v235, 64, v234\n"
"v_and_b32 v13, 3, v244\n"
"v_lshlrev_b32 v13, 3, v13\n"
"v_mov_b32 v11, s70\n"
"v_lshlrev_b32 v11, 2, v11\n"
"v_lshrrev_b32 v10, 4, v244\n"
"v_and_b32 v10, 1, v10\n"
"v_lshlrev_b32 v10, 2, v10\n"
"v_lshrrev_b32 v12, 2, v244\n"
"v_and_b32 v12, 3, v12\n"
"v_or_b32 v12, v10, v12\n"
"v_xor_b32 v12, v11, v12\n"
"v_lshlrev_b32 v12, 5, v12\n"
"v_lshrrev_b32 v11, 2, v244\n"
"v_and_b32 v11, 3, v11\n"
"v_lshrrev_b32 v200, 4, v244\n"
"v_lshlrev_b32 v200, 3, v200\n"
"v_add_u32 v200, v11, v200\n"
"v_lshlrev_b32 v200, 8, v200\n"
"v_add_u32 v200, v12, v200\n"
"v_add_u32 v200, v13, v200\n"
"v_add_u32 v200, s76, v200\n"
"v_add_u32 v200, 0x8000, v200\n"
"v_xor_b32 v201, 32, v200\n"
"v_xor_b32 v202, 64, v200\n"
"v_xor_b32 v203, 0x60, v200\n"
"v_mov_b32 v13, s70\n"
"v_lshlrev_b32 v13, 16, v13\n"
"v_and_b32 v12, 3, v244\n"
"v_lshlrev_b32 v12, 3, v12\n"
"v_lshrrev_b32 v10, 4, v244\n"
"v_and_b32 v10, 1, v10\n"
"v_lshlrev_b32 v10, 2, v10\n"
"v_lshrrev_b32 v11, 2, v244\n"
"v_and_b32 v11, 3, v11\n"
"v_or_b32 v11, v10, v11\n"
"v_lshlrev_b32 v11, 5, v11\n"
"v_lshrrev_b32 v10, 2, v244\n"
"v_and_b32 v10, 3, v10\n"
"v_lshrrev_b32 v226, 4, v244\n"
"v_lshlrev_b32 v226, 2, v226\n"
"v_add_u32 v226, v10, v226\n"
"v_lshlrev_b32 v226, 8, v226\n"
"v_add_u32 v226, v11, v226\n"
"v_add_u32 v226, v12, v226\n"
"v_add_u32 v226, v13, v226\n"
"v_add_u32 v226, s76, v226\n"
"v_add_u32 v226, 0x2000, v226\n"
"v_xor_b32 v227, 32, v226\n"
"v_xor_b32 v228, 64, v226\n"
"v_xor_b32 v229, 0x60, v226\n"
"v_xor_b32 v230, 0x80, v226\n"
"v_xor_b32 v231, 0xa0, v226\n"
"v_xor_b32 v232, 0xc0, v226\n"
"v_xor_b32 v233, 0xe0, v226\n"
"v_mov_b32 v14, s70\n"
"v_lshlrev_b32 v14, 16, v14\n"
"v_lshrrev_b32 v13, 4, v244\n"
"v_and_b32 v13, 1, v13\n"
"v_lshlrev_b32 v13, 3, v13\n"
"v_and_b32 v11, 15, v244\n"
"v_lshrrev_b32 v12, 4, v244\n"
"v_lshrrev_b32 v12, 1, v12\n"
"v_xor_b32 v12, v11, v12\n"
"v_lshlrev_b32 v12, 4, v12\n"
"v_and_b32 v222, 15, v244\n"
"v_lshlrev_b32 v222, 8, v222\n"
"v_add_u32 v222, v12, v222\n"
"v_add_u32 v222, v13, v222\n"
"v_add_u32 v222, v14, v222\n"
"v_add_u32 v222, s76, v222\n"
"v_xor_b32 v223, 32, v222\n"
"v_xor_b32 v224, 64, v222\n"
"v_xor_b32 v225, 0x60, v222\n"
"v_xor_b32 v246, 0x80, v222\n"
"v_xor_b32 v247, 0xa0, v222\n"
"v_xor_b32 v248, 0xc0, v222\n"
"v_xor_b32 v249, 0xe0, v222\n"
"v_lshlrev_b32 v243, 4, v244\n"
"s_and_b32 s93, s93, 0xffff\n"
"s_mov_b32 s95, 0x20000\n"
"s_mul_i32 s87, %[hq0], 163840\n"
"s_lshl_b32 s72, s77, 12\n"
"s_add_u32 s91, s72, s76\n"
"v_mov_b32 v15, s77\n"
"v_mul_u32_u24 v15, 0xa000, v15\n"
"v_and_b32 v14, 1, v244\n"
"v_lshlrev_b32 v14, 4, v14\n"
"v_lshrrev_b32 v12, 4, v244\n"
"v_and_b32 v12, 3, v12\n"
"v_and_b32 v13, 7, v244\n"
"v_lshrrev_b32 v13, 1, v13\n"
"v_xor_b32 v13, v12, v13\n"
"v_lshlrev_b32 v13, 5, v13\n"
"v_lshrrev_b32 v245, 3, v244\n"
"v_mul_u32_u24 v245, 0x500, v245\n"
"v_add_u32 v245, v13, v245\n"
"v_add_u32 v245, v14, v245\n"
"v_add_u32 v245, v15, v245\n"
"v_add_u32 v251, 0x2400, v245\n"
"v_add_u32 v252, 0x4800, v245\n"
"v_add_u32 v253, 0x6c00, v245\n"
"v_min_u32 v250, 19, v244\n"
"v_lshlrev_b32 v250, 7, v250\n"
"s_mul_i32 s72, s77, 10240\n"
"s_add_u32 s72, s72, 0xa00\n"
"v_add_u32 v250, s72, v250\n"
"s_mov_b32 s69, 0x3e0293ee\n"
"v_mov_b32 v216, 0\n"
"v_mov_b32 v217, 0\n"
"v_mov_b32 v218, 0\n"
"v_mov_b32 v219, 0\n"
"v_mov_b32 v220, 0\n"
"v_mov_b32 v221, 0\n"
"s_add_u32 s65, s83, 128\n"
"s_add_u32 m0, s61, 0xc000\n"
"s_nop 0\n"
"buffer_load_dwordx4 v204, s[44:47], s65 offen lds\n"
"buffer_load_dwordx4 v205, s[44:47], s65 offen offset:1024 lds\n"
"buffer_load_dwordx4 v206, s[44:47], s65 offen offset:2048 lds\n"
"buffer_load_dwordx4 v207, s[44:47], s65 offen offset:3072 lds\n"
"s_waitcnt vmcnt(5)\n"
"s_barrier\n"
"ds_read_b64_tr_b16 a[96:97], v200 offset:0\n"
"ds_read_b64_tr_b16 a[98:99], v200 offset:1024\n"
"ds_read_b128 a[0:3], v234 offset:0\n"
"ds_read_b64_tr_b16 a[100:101], v200 offset:8192\n"
"ds_read_b64_tr_b16 a[102:103], v200 offset:9216\n"
"ds_read_b128 a[24:27], v235 offset:0\n"
"ds_read_b128 a[4:7], v234 offset:2048\n"
"ds_read_b128 a[28:31], v235 offset:2048\n"
"ds_read_b128 a[8:11], v234 offset:4096\n"
"ds_read_b128 a[32:35], v235 offset:4096\n"
"ds_read_b128 a[12:15], v234 offset:6144\n"
"ds_read_b128 a[36:39], v235 offset:6144\n"
"ds_read_b128 a[16:19], v234 offset:8192\n"
"ds_read_b128 a[40:43], v235 offset:8192\n"
"ds_read_b128 a[20:23], v234 offset:10240\n"
"ds_read_b128 a[44:47], v235 offset:10240\n"
"v_add_u32 v200, 0x4000, v200\n"
"s_waitcnt lgkmcnt(10)\n"
"v_mfma_f32_16x16x32_bf16 v[0:3], a[96:99], a[0:3], 0\n"
"s_lshl_b32 s83, %[rot], 19\n"
"s_add_u32 m0, s61, 0x18000\n"
"v_mfma_f32_16x16x32_bf16 v[0:3], a[100:103], a[24:27], v[0:3]\n"
"buffer_load_dwordx4 v204, s[44:47], s83 offen lds\n"
"s_waitcnt lgkmcnt(8)\n"
"ds_read_b64_tr_b16 a[104:105], v201 offset:0\n"
"v_mfma_f32_16x16x32_bf16 v[32:35], a[100:103], a[28:31], 0\n"
"v_mfma_f32_16x16x32_bf16 v[32:35], a[96:99], a[4:7], v[32:35]\n"
"s_waitcnt lgkmcnt(7)\n"
"ds_read_b64_tr_b16 a[106:107], v201 offset:1024\n"
"v_mfma_f32_16x16x32_bf16 v[64:67], a[96:99], a[8:11], 0\n"
"buffer_load_dwordx4 v205, s[44:47], s83 offen offset:1024 lds\n"
"v_mfma_f32_16x16x32_bf16 v[64:67], a[100:103], a[32:35], v[64:67]\n"
"s_waitcnt lgkmcnt(6)\n"
"ds_read_b64_tr_b16 a[108:109], v201 offset:8192\n"
"v_mfma_f32_16x16x32_bf16 v[96:99], a[100:103], a[36:39], 0\n"
"v_mfma_f32_16x16x32_bf16 v[96:99], a[96:99], a[12:15], v[96:99]\n"
"s_waitcnt lgkmcnt(5)\n"
"ds_read_b64_tr_b16 a[110:111], v201 offset:9216\n"
"v_add_u32 v201, 0x4000, v201\n"
"v_mfma_f32_16x16x32_bf16 v[128:131], a[96:99], a[16:19], 0\n"
"buffer_load_dwordx4 v206, s[44:47], s83 offen offset:2048 lds\n"
"v_mfma_f32_16x16x32_bf16 v[128:131], a[100:103], a[40:43], v[128:131]\n"
"s_waitcnt lgkmcnt(4)\n"
"ds_read_b64_tr_b16 a[112:113], v202 offset:0\n"
"v_mfma_f32_16x16x32_bf16 v[160:163], a[100:103], a[44:47], 0\n"
"s_waitcnt lgkmcnt(1)\n"
"v_mfma_f32_16x16x32_bf16 v[160:163], a[96:99], a[20:23], v[160:163]\n"
"ds_read_b64_tr_b16 a[114:115], v202 offset:1024\n"
"v_mfma_f32_16x16x32_bf16 v[4:7], a[104:107], a[0:3], 0\n"
"buffer_load_dwordx4 v207, s[44:47], s83 offen offset:3072 lds\n"
"v_mfma_f32_16x16x32_bf16 v[4:7], a[108:111], a[24:27], v[4:7]\n"
"ds_read_b64_tr_b16 a[116:117], v202 offset:8192\n"
"v_mfma_f32_16x16x32_bf16 v[36:39], a[108:111], a[28:31], 0\n"
"s_lshl_b32 s82, %[rot], 7\n"
"v_mfma_f32_16x16x32_bf16 v[36:39], a[104:107], a[4:7], v[36:39]\n"
"s_add_u32 m0, s60, 0x22000\n"
"ds_read_b64_tr_b16 a[118:119], v202 offset:9216\n"
"v_add_u32 v202, 0x4000, v202\n"
"v_mfma_f32_16x16x32_bf16 v[68:71], a[104:107], a[8:11], 0\n"
"buffer_load_dwordx4 v192, s[40:43], s82 offen lds\n"
"v_mfma_f32_16x16x32_bf16 v[68:71], a[108:111], a[32:35], v[68:71]\n"
"ds_read_b64_tr_b16 a[120:121], v203 offset:0\n"
"v_mfma_f32_16x16x32_bf16 v[100:103], a[108:111], a[36:39], 0\n"
"v_mfma_f32_16x16x32_bf16 v[100:103], a[104:107], a[12:15], v[100:103]\n"
"ds_read_b64_tr_b16 a[122:123], v203 offset:1024\n"
"v_mfma_f32_16x16x32_bf16 v[132:135], a[104:107], a[16:19], 0\n"
"v_mfma_f32_16x16x32_bf16 v[132:135], a[108:111], a[40:43], v[132:135]\n"
"ds_read_b64_tr_b16 a[124:125], v203 offset:8192\n"
"v_mfma_f32_16x16x32_bf16 v[164:167], a[108:111], a[44:47], 0\n"
"s_waitcnt lgkmcnt(3)\n"
"v_mfma_f32_16x16x32_bf16 v[164:167], a[104:107], a[20:23], v[164:167]\n"
"ds_read_b64_tr_b16 a[126:127], v203 offset:9216\n"
"v_add_u32 v203, 0x4000, v203\n"
"v_mfma_f32_16x16x32_bf16 v[8:11], a[112:115], a[0:3], 0\n"
"buffer_load_dwordx4 v193, s[40:43], s82 offen offset:1024 lds\n"
"v_mfma_f32_16x16x32_bf16 v[8:11], a[116:119], a[24:27], v[8:11]\n"
"v_mfma_f32_16x16x32_bf16 v[40:43], a[116:119], a[28:31], 0\n"
"v_mfma_f32_16x16x32_bf16 v[40:43], a[112:115], a[4:7], v[40:43]\n"
"v_mfma_f32_16x16x32_bf16 v[72:75], a[112:115], a[8:11], 0\n"
"buffer_load_dwordx4 v194, s[40:43], s82 offen offset:2048 lds\n"
"v_mfma_f32_16x16x32_bf16 v[72:75], a[116:119], a[32:35], v[72:75]\n"
"v_mfma_f32_16x16x32_bf16 v[104:107], a[116:119], a[36:39], 0\n"
"v_mfma_f32_16x16x32_bf16 v[104:107], a[112:115], a[12:15], v[104:107]\n"
"s_add_u32 m0, s60, 0x22c00\n"
"v_mfma_f32_16x16x32_bf16 v[136:139], a[112:115], a[16:19], 0\n"
"buffer_load_dwordx4 v195, s[40:43], s82 offen lds\n"
"v_mfma_f32_16x16x32_bf16 v[136:139], a[116:119], a[40:43], v[136:139]\n"
"v_mfma_f32_16x16x32_bf16 v[168:171], a[116:119], a[44:47], 0\n"
"s_waitcnt lgkmcnt(0)\n"
"v_mfma_f32_16x16x32_bf16 v[168:171], a[112:115], a[20:23], v[168:171]\n"
"v_mfma_f32_16x16x32_bf16 v[12:15], a[120:123], a[0:3], 0\n"
"buffer_load_dwordx4 v196, s[40:43], s82 offen offset:1024 lds\n"
"v_mfma_f32_16x16x32_bf16 v[12:15], a[124:127], a[24:27], v[12:15]\n"
"v_mfma_f32_16x16x32_bf16 v[44:47], a[124:127], a[28:31], 0\n"
"s_waitcnt vmcnt(9)\n"
"s_barrier\n"
"v_mfma_f32_16x16x32_bf16 v[44:47], a[120:123], a[4:7], v[44:47]\n"
"ds_read_b64_tr_b16 a[96:97], v200 offset:0\n"
"v_mfma_f32_16x16x32_bf16 v[76:79], a[120:123], a[8:11], 0\n"
"buffer_load_dwordx4 v197, s[40:43], s82 offen offset:2048 lds\n"
"v_mfma_f32_16x16x32_bf16 v[76:79], a[124:127], a[32:35], v[76:79]\n"
"ds_read_b64_tr_b16 a[98:99], v200 offset:1024\n"
"v_mfma_f32_16x16x32_bf16 v[108:111], a[124:127], a[36:39], 0\n"
"ds_read_b64_tr_b16 a[100:101], v200 offset:8192\n"
"v_mfma_f32_16x16x32_bf16 v[108:111], a[120:123], a[12:15], v[108:111]\n"
"ds_read_b64_tr_b16 a[102:103], v200 offset:9216\n"
"v_add_u32 v200, 0xc000, v200\n"
"v_mfma_f32_16x16x32_bf16 v[140:143], a[120:123], a[16:19], 0\n"
"v_mfma_f32_16x16x32_bf16 v[140:143], a[124:127], a[40:43], v[140:143]\n"
"v_mfma_f32_16x16x32_bf16 v[172:175], a[124:127], a[44:47], 0\n"
"v_mfma_f32_16x16x32_bf16 v[172:175], a[120:123], a[20:23], v[172:175]\n"
"s_waitcnt lgkmcnt(0)\n"
"v_mfma_f32_16x16x32_bf16 v[16:19], a[96:99], a[0:3], 0\n"
"s_add_u32 s65, s83, 128\n"
"s_add_u32 m0, s61, 0x8000\n"
"v_mfma_f32_16x16x32_bf16 v[16:19], a[100:103], a[24:27], v[16:19]\n"
"buffer_load_dwordx4 v204, s[44:47], s65 offen lds\n"
"ds_read_b64_tr_b16 a[104:105], v201 offset:0\n"
"v_mfma_f32_16x16x32_bf16 v[48:51], a[100:103], a[28:31], 0\n"
"v_mfma_f32_16x16x32_bf16 v[48:51], a[96:99], a[4:7], v[48:51]\n"
"ds_read_b64_tr_b16 a[106:107], v201 offset:1024\n"
"v_mfma_f32_16x16x32_bf16 v[80:83], a[96:99], a[8:11], 0\n"
"buffer_load_dwordx4 v205, s[44:47], s65 offen offset:1024 lds\n"
"v_mfma_f32_16x16x32_bf16 v[80:83], a[100:103], a[32:35], v[80:83]\n"
"ds_read_b64_tr_b16 a[108:109], v201 offset:8192\n"
"v_mfma_f32_16x16x32_bf16 v[112:115], a[100:103], a[36:39], 0\n"
"v_mfma_f32_16x16x32_bf16 v[112:115], a[96:99], a[12:15], v[112:115]\n"
"ds_read_b64_tr_b16 a[110:111], v201 offset:9216\n"
"v_add_u32 v201, 0xc000, v201\n"
"v_mfma_f32_16x16x32_bf16 v[144:147], a[96:99], a[16:19], 0\n"
"buffer_load_dwordx4 v206, s[44:47], s65 offen offset:2048 lds\n"
"v_mfma_f32_16x16x32_bf16 v[144:147], a[100:103], a[40:43], v[144:147]\n"
"ds_read_b64_tr_b16 a[112:113], v202 offset:0\n"
"v_mfma_f32_16x16x32_bf16 v[176:179], a[100:103], a[44:47], 0\n"
"s_waitcnt lgkmcnt(1)\n"
"v_mfma_f32_16x16x32_bf16 v[176:179], a[96:99], a[20:23], v[176:179]\n"
"ds_read_b64_tr_b16 a[114:115], v202 offset:1024\n"
"v_mfma_f32_16x16x32_bf16 v[20:23], a[104:107], a[0:3], 0\n"
"buffer_load_dwordx4 v207, s[44:47], s65 offen offset:3072 lds\n"
"v_mfma_f32_16x16x32_bf16 v[20:23], a[108:111], a[24:27], v[20:23]\n"
"ds_read_b64_tr_b16 a[116:117], v202 offset:8192\n"
"v_mfma_f32_16x16x32_bf16 v[52:55], a[108:111], a[28:31], 0\n"
"s_add_u32 s82, s82, 128\n"
"s_cmp_eq_u32 s82, 0x500\n"
"s_cselect_b32 s82, 128, s82\n"
"v_mfma_f32_16x16x32_bf16 v[52:55], a[104:107], a[4:7], v[52:55]\n"
"s_add_u32 m0, s60, 0x1c000\n"
"ds_read_b64_tr_b16 a[118:119], v202 offset:9216\n"
"v_add_u32 v202, 0xc000, v202\n"
"v_mfma_f32_16x16x32_bf16 v[84:87], a[104:107], a[8:11], 0\n"
"buffer_load_dwordx4 v192, s[40:43], s82 offen lds\n"
"v_mfma_f32_16x16x32_bf16 v[84:87], a[108:111], a[32:35], v[84:87]\n"
"ds_read_b64_tr_b16 a[120:121], v203 offset:0\n"
"v_mfma_f32_16x16x32_bf16 v[116:119], a[108:111], a[36:39], 0\n"
"v_mfma_f32_16x16x32_bf16 v[116:119], a[104:107], a[12:15], v[116:119]\n"
"ds_read_b64_tr_b16 a[122:123], v203 offset:1024\n"
"v_mfma_f32_16x16x32_bf16 v[148:151], a[104:107], a[16:19], 0\n"
"v_mfma_f32_16x16x32_bf16 v[148:151], a[108:111], a[40:43], v[148:151]\n"
"ds_read_b64_tr_b16 a[124:125], v203 offset:8192\n"
"v_mfma_f32_16x16x32_bf16 v[180:183], a[108:111], a[44:47], 0\n"
"s_waitcnt lgkmcnt(3)\n"
"v_mfma_f32_16x16x32_bf16 v[180:183], a[104:107], a[20:23], v[180:183]\n"
"ds_read_b64_tr_b16 a[126:127], v203 offset:9216\n"
"v_add_u32 v203, 0xc000, v203\n"
"v_mfma_f32_16x16x32_bf16 v[24:27], a[112:115], a[0:3], 0\n"
"buffer_load_dwordx4 v193, s[40:43], s82 offen offset:1024 lds\n"
"v_mfma_f32_16x16x32_bf16 v[24:27], a[116:119], a[24:27], v[24:27]\n"
"v_mfma_f32_16x16x32_bf16 v[56:59], a[116:119], a[28:31], 0\n"
"v_mfma_f32_16x16x32_bf16 v[56:59], a[112:115], a[4:7], v[56:59]\n"
"v_mfma_f32_16x16x32_bf16 v[88:91], a[112:115], a[8:11], 0\n"
"buffer_load_dwordx4 v194, s[40:43], s82 offen offset:2048 lds\n"
"v_mfma_f32_16x16x32_bf16 v[88:91], a[116:119], a[32:35], v[88:91]\n"
"v_mfma_f32_16x16x32_bf16 v[120:123], a[116:119], a[36:39], 0\n"
"v_mfma_f32_16x16x32_bf16 v[120:123], a[112:115], a[12:15], v[120:123]\n"
"s_add_u32 m0, s60, 0x1cc00\n"
"v_mfma_f32_16x16x32_bf16 v[152:155], a[112:115], a[16:19], 0\n"
"buffer_load_dwordx4 v195, s[40:43], s82 offen lds\n"
"v_mfma_f32_16x16x32_bf16 v[152:155], a[116:119], a[40:43], v[152:155]\n"
"v_mfma_f32_16x16x32_bf16 v[184:187], a[116:119], a[44:47], 0\n"
"s_waitcnt lgkmcnt(0)\n"
"s_waitcnt vmcnt(8)\n"
"s_barrier\n"
"v_mfma_f32_16x16x32_bf16 v[184:187], a[112:115], a[20:23], v[184:187]\n"
"ds_read_b64_tr_b16 a[96:97], v200 offset:0\n"
"v_mfma_f32_16x16x32_bf16 v[28:31], a[120:123], a[0:3], 0\n"
"buffer_load_dwordx4 v196, s[40:43], s82 offen offset:1024 lds\n"
"v_mfma_f32_16x16x32_bf16 v[28:31], a[124:127], a[24:27], v[28:31]\n"
"ds_read_b64_tr_b16 a[98:99], v200 offset:1024\n"
"v_mfma_f32_16x16x32_bf16 v[60:63], a[124:127], a[28:31], 0\n"
"ds_read_b64_tr_b16 a[100:101], v200 offset:8192\n"
"v_mfma_f32_16x16x32_bf16 v[60:63], a[120:123], a[4:7], v[60:63]\n"
"ds_read_b64_tr_b16 a[102:103], v200 offset:9216\n"
"v_add_u32 v200, 0xffff0000, v200\n"
"v_mfma_f32_16x16x32_bf16 v[92:95], a[120:123], a[8:11], 0\n"
"buffer_load_dwordx4 v197, s[40:43], s82 offen offset:2048 lds\n"
"ds_read_b128 a[48:51], v234 offset:24576\n"
"ds_read_b128 a[72:75], v235 offset:24576\n"
"v_mfma_f32_16x16x32_bf16 v[92:95], a[124:127], a[32:35], v[92:95]\n"
"ds_read_b128 a[52:55], v234 offset:26624\n"
"v_mfma_f32_16x16x32_bf16 v[124:127], a[124:127], a[36:39], 0\n"
"ds_read_b128 a[76:79], v235 offset:26624\n"
"ds_read_b128 a[56:59], v234 offset:28672\n"
"v_mfma_f32_16x16x32_bf16 v[124:127], a[120:123], a[12:15], v[124:127]\n"
"ds_read_b128 a[80:83], v235 offset:28672\n"
"v_mfma_f32_16x16x32_bf16 v[156:159], a[120:123], a[16:19], 0\n"
"ds_read_b128 a[60:63], v234 offset:30720\n"
"ds_read_b128 a[84:87], v235 offset:30720\n"
"v_mfma_f32_16x16x32_bf16 v[156:159], a[124:127], a[40:43], v[156:159]\n"
"ds_read_b128 a[64:67], v234 offset:32768\n"
"v_mfma_f32_16x16x32_bf16 v[188:191], a[124:127], a[44:47], 0\n"
"ds_read_b128 a[88:91], v235 offset:32768\n"
"ds_read_b128 a[68:71], v234 offset:34816\n"
"v_mfma_f32_16x16x32_bf16 v[188:191], a[120:123], a[20:23], v[188:191]\n"
"ds_read_b128 a[92:95], v235 offset:34816\n"
"s_waitcnt lgkmcnt(10)\n"
"v_mfma_f32_16x16x32_bf16 v[0:3], a[96:99], a[48:51], v[0:3]\n"
"s_add_u32 s83, s83, 0x80000\n"
"s_cmp_eq_u32 s83, 0x500000\n"
"s_cselect_b32 s83, 0x80000, s83\n"
"s_add_u32 m0, s61, 0xc000\n"
"s_nop 0\n"
"buffer_load_dwordx4 v204, s[44:47], s83 offen lds\n"
"v_mfma_f32_16x16x32_bf16 v[0:3], a[100:103], a[72:75], v[0:3]\n"
"s_waitcnt lgkmcnt(8)\n"
"ds_read_b64_tr_b16 a[104:105], v201 offset:0\n"
"v_mfma_f32_16x16x32_bf16 v[32:35], a[100:103], a[76:79], v[32:35]\n"
"v_mfma_f32_16x16x32_bf16 v[32:35], a[96:99], a[52:55], v[32:35]\n"
"s_waitcnt lgkmcnt(7)\n"
"ds_read_b64_tr_b16 a[106:107], v201 offset:1024\n"
"v_mfma_f32_16x16x32_bf16 v[64:67], a[96:99], a[56:59], v[64:67]\n"
"buffer_load_dwordx4 v205, s[44:47], s83 offen offset:1024 lds\n"
"v_mfma_f32_16x16x32_bf16 v[64:67], a[100:103], a[80:83], v[64:67]\n"
"s_waitcnt lgkmcnt(6)\n"
"ds_read_b64_tr_b16 a[108:109], v201 offset:8192\n"
"v_mfma_f32_16x16x32_bf16 v[96:99], a[100:103], a[84:87], v[96:99]\n"
"v_mfma_f32_16x16x32_bf16 v[96:99], a[96:99], a[60:63], v[96:99]\n"
"s_waitcnt lgkmcnt(5)\n"
"ds_read_b64_tr_b16 a[110:111], v201 offset:9216\n"
"v_add_u32 v201, 0xffff0000, v201\n"
"v_mfma_f32_16x16x32_bf16 v[128:131], a[96:99], a[64:67], v[128:131]\n"
"buffer_load_dwordx4 v206, s[44:47], s83 offen offset:2048 lds\n"
"v_mfma_f32_16x16x32_bf16 v[128:131], a[100:103], a[88:91], v[128:131]\n"
"s_waitcnt lgkmcnt(4)\n"
"ds_read_b64_tr_b16 a[112:113], v202 offset:0\n"
"v_mfma_f32_16x16x32_bf16 v[160:163], a[100:103], a[92:95], v[160:163]\n"
"s_waitcnt lgkmcnt(1)\n"
"v_mfma_f32_16x16x32_bf16 v[160:163], a[96:99], a[68:71], v[160:163]\n"
"ds_read_b64_tr_b16 a[114:115], v202 offset:1024\n"
"v_mfma_f32_16x16x32_bf16 v[4:7], a[104:107], a[48:51], v[4:7]\n"
"buffer_load_dwordx4 v207, s[44:47], s83 offen offset:3072 lds\n"
"v_mfma_f32_16x16x32_bf16 v[4:7], a[108:111], a[72:75], v[4:7]\n"
"ds_read_b64_tr_b16 a[116:117], v202 offset:8192\n"
"v_mfma_f32_16x16x32_bf16 v[36:39], a[108:111], a[76:79], v[36:39]\n"
"v_mfma_f32_16x16x32_bf16 v[36:39], a[104:107], a[52:55], v[36:39]\n"
"ds_read_b64_tr_b16 a[118:119], v202 offset:9216\n"
"v_add_u32 v202, 0xffff0000, v202\n"
"v_mfma_f32_16x16x32_bf16 v[68:71], a[104:107], a[56:59], v[68:71]\n"
"v_mfma_f32_16x16x32_bf16 v[68:71], a[108:111], a[80:83], v[68:71]\n"
"ds_read_b64_tr_b16 a[120:121], v203 offset:0\n"
"v_mfma_f32_16x16x32_bf16 v[100:103], a[108:111], a[84:87], v[100:103]\n"
"s_add_u32 m0, s62, 0x0\n"
"v_mfma_f32_16x16x32_bf16 v[100:103], a[104:107], a[60:63], v[100:103]\n"
"s_add_u32 s84, s84, 0x1000\n"
"s_and_b32 s84, s84, 0x1ffff\n"
"ds_read_b64_tr_b16 a[122:123], v203 offset:1024\n"
"v_mfma_f32_16x16x32_bf16 v[132:135], a[104:107], a[64:67], v[132:135]\n"
"buffer_load_dwordx4 v240, s[48:51], s84 offen nt lds\n"
"v_mfma_f32_16x16x32_bf16 v[132:135], a[108:111], a[88:91], v[132:135]\n"
"ds_read_b64_tr_b16 a[124:125], v203 offset:8192\n"
"v_mfma_f32_16x16x32_bf16 v[164:167], a[108:111], a[92:95], v[164:167]\n"
"s_waitcnt lgkmcnt(3)\n"
"v_mfma_f32_16x16x32_bf16 v[164:167], a[104:107], a[68:71], v[164:167]\n"
"ds_read_b64_tr_b16 a[126:127], v203 offset:9216\n"
"v_add_u32 v203, 0xffff0000, v203\n"
"v_mfma_f32_16x16x32_bf16 v[8:11], a[112:115], a[48:51], v[8:11]\n"
"buffer_load_dwordx4 v241, s[48:51], s84 offen offset:1024 nt lds\n"
"v_mfma_f32_16x16x32_bf16 v[8:11], a[116:119], a[72:75], v[8:11]\n"
"v_mfma_f32_16x16x32_bf16 v[40:43], a[116:119], a[76:79], v[40:43]\n"
"v_mfma_f32_16x16x32_bf16 v[40:43], a[112:115], a[52:55], v[40:43]\n"
"s_add_u32 m0, s62, 0x1000\n"
"v_mfma_f32_16x16x32_bf16 v[72:75], a[112:115], a[56:59], v[72:75]\n"
"s_add_u32 s84, s84, 0x1000\n"
"buffer_load_dwordx4 v240, s[48:51], s84 offen nt lds\n"
"v_mfma_f32_16x16x32_bf16 v[72:75], a[116:119], a[80:83], v[72:75]\n"
"v_mfma_f32_16x16x32_bf16 v[104:107], a[116:119], a[84:87], v[104:107]\n"
"v_mfma_f32_16x16x32_bf16 v[104:107], a[112:115], a[60:63], v[104:107]\n"
"v_mfma_f32_16x16x32_bf16 v[136:139], a[112:115], a[64:67], v[136:139]\n"
"buffer_load_dwordx4 v241, s[48:51], s84 offen offset:1024 nt lds\n"
"v_mfma_f32_16x16x32_bf16 v[136:139], a[116:119], a[88:91], v[136:139]\n"
"v_mfma_f32_16x16x32_bf16 v[168:171], a[116:119], a[92:95], v[168:171]\n"
"s_waitcnt lgkmcnt(0)\n"
"v_mfma_f32_16x16x32_bf16 v[168:171], a[112:115], a[68:71], v[168:171]\n"
"v_mfma_f32_16x16x32_bf16 v[12:15], a[120:123], a[48:51], v[12:15]\n"
"v_mfma_f32_16x16x32_bf16 v[12:15], a[124:127], a[72:75], v[12:15]\n"
"v_mfma_f32_16x16x32_bf16 v[44:47], a[124:127], a[76:79], v[44:47]\n"
"s_waitcnt vmcnt(8)\n"
"s_barrier\n"
"v_mfma_f32_16x16x32_bf16 v[44:47], a[120:123], a[52:55], v[44:47]\n"
"s_add_u32 s65, s83, 128\n"
"s_add_u32 m0, s61, 0x18000\n"
"ds_read_b64_tr_b16 a[96:97], v200 offset:0\n"
"v_mfma_f32_16x16x32_bf16 v[76:79], a[120:123], a[56:59], v[76:79]\n"
"buffer_load_dwordx4 v204, s[44:47], s65 offen lds\n"
"v_mfma_f32_16x16x32_bf16 v[76:79], a[124:127], a[80:83], v[76:79]\n"
"ds_read_b64_tr_b16 a[98:99], v200 offset:1024\n"
"v_mfma_f32_16x16x32_bf16 v[108:111], a[124:127], a[84:87], v[108:111]\n"
"ds_read_b64_tr_b16 a[100:101], v200 offset:8192\n"
"v_mfma_f32_16x16x32_bf16 v[108:111], a[120:123], a[60:63], v[108:111]\n"
"ds_read_b64_tr_b16 a[102:103], v200 offset:9216\n"
"v_add_u32 v200, 0x4000, v200\n"
"v_mfma_f32_16x16x32_bf16 v[140:143], a[120:123], a[64:67], v[140:143]\n"
"buffer_load_dwordx4 v205, s[44:47], s65 offen offset:1024 lds\n"
"v_mfma_f32_16x16x32_bf16 v[140:143], a[124:127], a[88:91], v[140:143]\n"
"v_mfma_f32_16x16x32_bf16 v[172:175], a[124:127], a[92:95], v[172:175]\n"
"v_mfma_f32_16x16x32_bf16 v[172:175], a[120:123], a[68:71], v[172:175]\n"
"s_waitcnt lgkmcnt(0)\n"
"v_mfma_f32_16x16x32_bf16 v[16:19], a[96:99], a[48:51], v[16:19]\n"
"buffer_load_dwordx4 v206, s[44:47], s65 offen offset:2048 lds\n"
"v_mfma_f32_16x16x32_bf16 v[16:19], a[100:103], a[72:75], v[16:19]\n"
"ds_read_b64_tr_b16 a[104:105], v201 offset:0\n"
"v_mfma_f32_16x16x32_bf16 v[48:51], a[100:103], a[76:79], v[48:51]\n"
"ds_read_b128 a[0:3], v234 offset:0\n"
"v_mfma_f32_16x16x32_bf16 v[48:51], a[96:99], a[52:55], v[48:51]\n"
"ds_read_b64_tr_b16 a[106:107], v201 offset:1024\n"
"v_mfma_f32_16x16x32_bf16 v[80:83], a[96:99], a[56:59], v[80:83]\n"
"buffer_load_dwordx4 v207, s[44:47], s65 offen offset:3072 lds\n"
"v_mfma_f32_16x16x32_bf16 v[80:83], a[100:103], a[80:83], v[80:83]\n"
"ds_read_b64_tr_b16 a[108:109], v201 offset:8192\n"
"v_mfma_f32_16x16x32_bf16 v[112:115], a[100:103], a[84:87], v[112:115]\n"
"s_add_u32 s82, s82, 128\n"
"s_cmp_eq_u32 s82, 0x500\n"
"s_cselect_b32 s82, 128, s82\n"
"ds_read_b128 a[24:27], v235 offset:0\n"
"v_mfma_f32_16x16x32_bf16 v[112:115], a[96:99], a[60:63], v[112:115]\n"
"s_add_u32 m0, s60, 0x22000\n"
"ds_read_b64_tr_b16 a[110:111], v201 offset:9216\n"
"v_add_u32 v201, 0x4000, v201\n"
"v_mfma_f32_16x16x32_bf16 v[144:147], a[96:99], a[64:67], v[144:147]\n"
"buffer_load_dwordx4 v192, s[40:43], s82 offen lds\n"
"v_mfma_f32_16x16x32_bf16 v[144:147], a[100:103], a[88:91], v[144:147]\n"
"ds_read_b64_tr_b16 a[112:113], v202 offset:0\n"
"v_mfma_f32_16x16x32_bf16 v[176:179], a[100:103], a[92:95], v[176:179]\n"
"s_waitcnt lgkmcnt(1)\n"
"ds_read_b128 a[4:7], v234 offset:2048\n"
"v_mfma_f32_16x16x32_bf16 v[176:179], a[96:99], a[68:71], v[176:179]\n"
"ds_read_b64_tr_b16 a[114:115], v202 offset:1024\n"
"v_mfma_f32_16x16x32_bf16 v[20:23], a[104:107], a[48:51], v[20:23]\n"
"buffer_load_dwordx4 v193, s[40:43], s82 offen offset:1024 lds\n"
"v_mfma_f32_16x16x32_bf16 v[20:23], a[108:111], a[72:75], v[20:23]\n"
"ds_read_b64_tr_b16 a[116:117], v202 offset:8192\n"
"v_mfma_f32_16x16x32_bf16 v[52:55], a[108:111], a[76:79], v[52:55]\n"
"ds_read_b128 a[28:31], v235 offset:2048\n"
"v_mfma_f32_16x16x32_bf16 v[52:55], a[104:107], a[52:55], v[52:55]\n"
"ds_read_b64_tr_b16 a[118:119], v202 offset:9216\n"
"v_add_u32 v202, 0x4000, v202\n"
"v_mfma_f32_16x16x32_bf16 v[84:87], a[104:107], a[56:59], v[84:87]\n"
"v_mfma_f32_16x16x32_bf16 v[84:87], a[108:111], a[80:83], v[84:87]\n"
"ds_read_b64_tr_b16 a[120:121], v203 offset:0\n"
"v_mfma_f32_16x16x32_bf16 v[116:119], a[108:111], a[84:87], v[116:119]\n"
"ds_read_b128 a[8:11], v234 offset:4096\n"
"v_mfma_f32_16x16x32_bf16 v[116:119], a[104:107], a[60:63], v[116:119]\n"
"ds_read_b64_tr_b16 a[122:123], v203 offset:1024\n"
"v_mfma_f32_16x16x32_bf16 v[148:151], a[104:107], a[64:67], v[148:151]\n"
"buffer_load_dwordx4 v194, s[40:43], s82 offen offset:2048 lds\n"
"v_mfma_f32_16x16x32_bf16 v[148:151], a[108:111], a[88:91], v[148:151]\n"
"ds_read_b64_tr_b16 a[124:125], v203 offset:8192\n"
"v_mfma_f32_16x16x32_bf16 v[180:183], a[108:111], a[92:95], v[180:183]\n"
"s_waitcnt lgkmcnt(4)\n"
"ds_read_b128 a[32:35], v235 offset:4096\n"
"v_mfma_f32_16x16x32_bf16 v[180:183], a[104:107], a[68:71], v[180:183]\n"
"s_add_u32 m0, s60, 0x22c00\n"
"ds_read_b64_tr_b16 a[126:127], v203 offset:9216\n"
"v_add_u32 v203, 0x4000, v203\n"
"v_mfma_f32_16x16x32_bf16 v[24:27], a[112:115], a[48:51], v[24:27]\n"
"buffer_load_dwordx4 v195, s[40:43], s82 offen lds\n"
"v_mfma_f32_16x16x32_bf16 v[24:27], a[116:119], a[72:75], v[24:27]\n"
"ds_read_b128 a[12:15], v234 offset:6144\n"
"v_mfma_f32_16x16x32_bf16 v[56:59], a[116:119], a[76:79], v[56:59]\n"
"ds_read_b128 a[36:39], v235 offset:6144\n"
"v_mfma_f32_16x16x32_bf16 v[56:59], a[112:115], a[52:55], v[56:59]\n"
"ds_read_b128 a[16:19], v234 offset:8192\n"
"v_mfma_f32_16x16x32_bf16 v[88:91], a[112:115], a[56:59], v[88:91]\n"
"buffer_load_dwordx4 v196, s[40:43], s82 offen offset:1024 lds\n"
"v_mfma_f32_16x16x32_bf16 v[88:91], a[116:119], a[80:83], v[88:91]\n"
"ds_read_b128 a[40:43], v235 offset:8192\n"
"v_mfma_f32_16x16x32_bf16 v[120:123], a[116:119], a[84:87], v[120:123]\n"
"ds_read_b128 a[20:23], v234 offset:10240\n"
"v_mfma_f32_16x16x32_bf16 v[120:123], a[112:115], a[60:63], v[120:123]\n"
"ds_read_b128 a[44:47], v235 offset:10240\n"
"v_mfma_f32_16x16x32_bf16 v[152:155], a[112:115], a[64:67], v[152:155]\n"
"buffer_load_dwordx4 v197, s[40:43], s82 offen offset:2048 lds\n"
"v_mfma_f32_16x16x32_bf16 v[152:155], a[116:119], a[88:91], v[152:155]\n"
"v_mfma_f32_16x16x32_bf16 v[184:187], a[116:119], a[92:95], v[184:187]\n"
"s_waitcnt lgkmcnt(6)\n"
"v_mfma_f32_16x16x32_bf16 v[184:187], a[112:115], a[68:71], v[184:187]\n"
"v_mfma_f32_16x16x32_bf16 v[28:31], a[120:123], a[48:51], v[28:31]\n"
"v_mfma_f32_16x16x32_bf16 v[28:31], a[124:127], a[72:75], v[28:31]\n"
"v_mfma_f32_16x16x32_bf16 v[60:63], a[124:127], a[76:79], v[60:63]\n"
"s_waitcnt vmcnt(14) lgkmcnt(0)\n"
"s_barrier\n"
"v_mfma_f32_16x16x32_bf16 v[60:63], a[120:123], a[52:55], v[60:63]\n"
"s_add_u32 s83, s83, 0x80000\n"
"s_cmp_eq_u32 s83, 0x500000\n"
"s_cselect_b32 s83, 0x80000, s83\n"
"s_add_u32 m0, s61, 0x8000\n"
"ds_read_b64_tr_b16 a[96:97], v200 offset:0\n"
"v_mfma_f32_16x16x32_bf16 v[92:95], a[120:123], a[56:59], v[92:95]\n"
"buffer_load_dwordx4 v204, s[44:47], s83 offen lds\n"
"v_mfma_f32_16x16x32_bf16 v[92:95], a[124:127], a[80:83], v[92:95]\n"
"ds_read_b64_tr_b16 a[98:99], v200 offset:1024\n"
"v_mfma_f32_16x16x32_bf16 v[124:127], a[124:127], a[84:87], v[124:127]\n"
"ds_read_b64_tr_b16 a[100:101], v200 offset:8192\n"
"v_mfma_f32_16x16x32_bf16 v[124:127], a[120:123], a[60:63], v[124:127]\n"
"ds_read_b64_tr_b16 a[102:103], v200 offset:9216\n"
"v_add_u32 v200, 0xc000, v200\n"
"v_mfma_f32_16x16x32_bf16 v[156:159], a[120:123], a[64:67], v[156:159]\n"
"buffer_load_dwordx4 v205, s[44:47], s83 offen offset:1024 lds\n"
"v_mfma_f32_16x16x32_bf16 v[156:159], a[124:127], a[88:91], v[156:159]\n"
"v_mfma_f32_16x16x32_bf16 v[188:191], a[124:127], a[92:95], v[188:191]\n"
"v_mfma_f32_16x16x32_bf16 v[188:191], a[120:123], a[68:71], v[188:191]\n"
"s_waitcnt lgkmcnt(0)\n"
"v_mfma_f32_16x16x32_bf16 v[0:3], a[96:99], a[0:3], v[0:3]\n"
"buffer_load_dwordx4 v206, s[44:47], s83 offen offset:2048 lds\n"
"v_mfma_f32_16x16x32_bf16 v[0:3], a[100:103], a[24:27], v[0:3]\n"
"ds_read_b64_tr_b16 a[104:105], v201 offset:0\n"
"v_mfma_f32_16x16x32_bf16 v[32:35], a[100:103], a[28:31], v[32:35]\n"
"v_mfma_f32_16x16x32_bf16 v[32:35], a[96:99], a[4:7], v[32:35]\n"
"ds_read_b64_tr_b16 a[106:107], v201 offset:1024\n"
"v_mfma_f32_16x16x32_bf16 v[64:67], a[96:99], a[8:11], v[64:67]\n"
"v_mfma_f32_16x16x32_bf16 v[64:67], a[100:103], a[32:35], v[64:67]\n"
"ds_read_b64_tr_b16 a[108:109], v201 offset:8192\n"
"v_mfma_f32_16x16x32_bf16 v[96:99], a[100:103], a[36:39], v[96:99]\n"
"v_mfma_f32_16x16x32_bf16 v[96:99], a[96:99], a[12:15], v[96:99]\n"
"ds_read_b64_tr_b16 a[110:111], v201 offset:9216\n"
"v_add_u32 v201, 0xc000, v201\n"
"v_mfma_f32_16x16x32_bf16 v[128:131], a[96:99], a[16:19], v[128:131]\n"
"buffer_load_dwordx4 v207, s[44:47], s83 offen offset:3072 lds\n"
"v_mfma_f32_16x16x32_bf16 v[128:131], a[100:103], a[40:43], v[128:131]\n"
"ds_read_b64_tr_b16 a[112:113], v202 offset:0\n"
"v_mfma_f32_16x16x32_bf16 v[160:163], a[100:103], a[44:47], v[160:163]\n"
"s_waitcnt lgkmcnt(1)\n"
"v_mfma_f32_16x16x32_bf16 v[160:163], a[96:99], a[20:23], v[160:163]\n"
"ds_read_b64_tr_b16 a[114:115], v202 offset:1024\n"
"v_mfma_f32_16x16x32_bf16 v[4:7], a[104:107], a[0:3], v[4:7]\n"
"v_mfma_f32_16x16x32_bf16 v[4:7], a[108:111], a[24:27], v[4:7]\n"
"ds_read_b64_tr_b16 a[116:117], v202 offset:8192\n"
"v_mfma_f32_16x16x32_bf16 v[36:39], a[108:111], a[28:31], v[36:39]\n"
"s_add_u32 s82, s82, 128\n"
"s_cmp_eq_u32 s82, 0x500\n"
"s_cselect_b32 s82, 128, s82\n"
"v_mfma_f32_16x16x32_bf16 v[36:39], a[104:107], a[4:7], v[36:39]\n"
"s_add_u32 m0, s60, 0x1c000\n"
"ds_read_b64_tr_b16 a[118:119], v202 offset:9216\n"
"v_add_u32 v202, 0xc000, v202\n"
"v_mfma_f32_16x16x32_bf16 v[68:71], a[104:107], a[8:11], v[68:71]\n"
"buffer_load_dwordx4 v192, s[40:43], s82 offen lds\n"
"v_mfma_f32_16x16x32_bf16 v[68:71], a[108:111], a[32:35], v[68:71]\n"
"ds_read_b64_tr_b16 a[120:121], v203 offset:0\n"
"v_mfma_f32_16x16x32_bf16 v[100:103], a[108:111], a[36:39], v[100:103]\n"
"v_mfma_f32_16x16x32_bf16 v[100:103], a[104:107], a[12:15], v[100:103]\n"
"ds_read_b64_tr_b16 a[122:123], v203 offset:1024\n"
"v_mfma_f32_16x16x32_bf16 v[132:135], a[104:107], a[16:19], v[132:135]\n"
"v_mfma_f32_16x16x32_bf16 v[132:135], a[108:111], a[40:43], v[132:135]\n"
"ds_read_b64_tr_b16 a[124:125], v203 offset:8192\n"
"v_mfma_f32_16x16x32_bf16 v[164:167], a[108:111], a[44:47], v[164:167]\n"
"s_waitcnt lgkmcnt(3)\n"
"v_mfma_f32_16x16x32_bf16 v[164:167], a[104:107], a[20:23], v[164:167]\n"
"ds_read_b64_tr_b16 a[126:127], v203 offset:9216\n"
"v_add_u32 v203, 0xc000, v203\n"
"v_mfma_f32_16x16x32_bf16 v[8:11], a[112:115], a[0:3], v[8:11]\n"
"buffer_load_dwordx4 v193, s[40:43], s82 offen offset:1024 lds\n"
"v_mfma_f32_16x16x32_bf16 v[8:11], a[116:119], a[24:27], v[8:11]\n"
"v_mfma_f32_16x16x32_bf16 v[40:43], a[116:119], a[28:31], v[40:43]\n"
"v_mfma_f32_16x16x32_bf16 v[40:43], a[112:115], a[4:7], v[40:43]\n"
"v_mfma_f32_16x16x32_bf16 v[72:75], a[112:115], a[8:11], v[72:75]\n"
"v_mfma_f32_16x16x32_bf16 v[72:75], a[116:119], a[32:35], v[72:75]\n"
"v_mfma_f32_16x16x32_bf16 v[104:107], a[116:119], a[36:39], v[104:107]\n"
"v_mfma_f32_16x16x32_bf16 v[104:107], a[112:115], a[12:15], v[104:107]\n"
"v_mfma_f32_16x16x32_bf16 v[136:139], a[112:115], a[16:19], v[136:139]\n"
"buffer_load_dwordx4 v194, s[40:43], s82 offen offset:2048 lds\n"
"v_mfma_f32_16x16x32_bf16 v[136:139], a[116:119], a[40:43], v[136:139]\n"
"v_mfma_f32_16x16x32_bf16 v[168:171], a[116:119], a[44:47], v[168:171]\n"
"s_waitcnt lgkmcnt(0)\n"
"v_mfma_f32_16x16x32_bf16 v[168:171], a[112:115], a[20:23], v[168:171]\n"
"v_mfma_f32_16x16x32_bf16 v[12:15], a[120:123], a[0:3], v[12:15]\n"
"v_mfma_f32_16x16x32_bf16 v[12:15], a[124:127], a[24:27], v[12:15]\n"
"v_mfma_f32_16x16x32_bf16 v[44:47], a[124:127], a[28:31], v[44:47]\n"
"s_waitcnt vmcnt(7)\n"
"s_barrier\n"
"v_mfma_f32_16x16x32_bf16 v[44:47], a[120:123], a[4:7], v[44:47]\n"
"s_add_u32 s65, s83, 128\n"
"s_add_u32 m0, s61, 0xc000\n"
"ds_read_b64_tr_b16 a[96:97], v200 offset:0\n"
"v_mfma_f32_16x16x32_bf16 v[76:79], a[120:123], a[8:11], v[76:79]\n"
"buffer_load_dwordx4 v204, s[44:47], s65 offen lds\n"
"v_mfma_f32_16x16x32_bf16 v[76:79], a[124:127], a[32:35], v[76:79]\n"
"ds_read_b64_tr_b16 a[98:99], v200 offset:1024\n"
"v_mfma_f32_16x16x32_bf16 v[108:111], a[124:127], a[36:39], v[108:111]\n"
"ds_read_b64_tr_b16 a[100:101], v200 offset:8192\n"
"v_mfma_f32_16x16x32_bf16 v[108:111], a[120:123], a[12:15], v[108:111]\n"
"ds_read_b64_tr_b16 a[102:103], v200 offset:9216\n"
"v_add_u32 v200, 0xffff0000, v200\n"
"v_mfma_f32_16x16x32_bf16 v[140:143], a[120:123], a[16:19], v[140:143]\n"
"buffer_load_dwordx4 v205, s[44:47], s65 offen offset:1024 lds\n"
"v_mfma_f32_16x16x32_bf16 v[140:143], a[124:127], a[40:43], v[140:143]\n"
"v_mfma_f32_16x16x32_bf16 v[172:175], a[124:127], a[44:47], v[172:175]\n"
"v_mfma_f32_16x16x32_bf16 v[172:175], a[120:123], a[20:23], v[172:175]\n"
"s_waitcnt lgkmcnt(0)\n"
"v_mfma_f32_16x16x32_bf16 v[16:19], a[96:99], a[0:3], v[16:19]\n"
"buffer_load_dwordx4 v206, s[44:47], s65 offen offset:2048 lds\n"
"v_mfma_f32_16x16x32_bf16 v[16:19], a[100:103], a[24:27], v[16:19]\n"
"ds_read_b64_tr_b16 a[104:105], v201 offset:0\n"
"v_mfma_f32_16x16x32_bf16 v[48:51], a[100:103], a[28:31], v[48:51]\n"
"ds_read_b128 a[48:51], v234 offset:24576\n"
"v_mfma_f32_16x16x32_bf16 v[48:51], a[96:99], a[4:7], v[48:51]\n"
"ds_read_b64_tr_b16 a[106:107], v201 offset:1024\n"
"v_mfma_f32_16x16x32_bf16 v[80:83], a[96:99], a[8:11], v[80:83]\n"
"buffer_load_dwordx4 v207, s[44:47], s65 offen offset:3072 lds\n"
"v_mfma_f32_16x16x32_bf16 v[80:83], a[100:103], a[32:35], v[80:83]\n"
"ds_read_b64_tr_b16 a[108:109], v201 offset:8192\n"
"v_mfma_f32_16x16x32_bf16 v[112:115], a[100:103], a[36:39], v[112:115]\n"
"ds_read_b128 a[72:75], v235 offset:24576\n"
"v_mfma_f32_16x16x32_bf16 v[112:115], a[96:99], a[12:15], v[112:115]\n"
"s_add_u32 m0, s60, 0x1cc00\n"
"ds_read_b64_tr_b16 a[110:111], v201 offset:9216\n"
"v_add_u32 v201, 0xffff0000, v201\n"
"v_mfma_f32_16x16x32_bf16 v[144:147], a[96:99], a[16:19], v[144:147]\n"
"buffer_load_dwordx4 v195, s[40:43], s82 offen lds\n"
"v_mfma_f32_16x16x32_bf16 v[144:147], a[100:103], a[40:43], v[144:147]\n"
"ds_read_b64_tr_b16 a[112:113], v202 offset:0\n"
"v_mfma_f32_16x16x32_bf16 v[176:179], a[100:103], a[44:47], v[176:179]\n"
"s_waitcnt lgkmcnt(1)\n"
"ds_read_b128 a[52:55], v234 offset:26624\n"
"v_mfma_f32_16x16x32_bf16 v[176:179], a[96:99], a[20:23], v[176:179]\n"
"ds_read_b64_tr_b16 a[114:115], v202 offset:1024\n"
"v_mfma_f32_16x16x32_bf16 v[20:23], a[104:107], a[0:3], v[20:23]\n"
"buffer_load_dwordx4 v196, s[40:43], s82 offen offset:1024 lds\n"
"v_mfma_f32_16x16x32_bf16 v[20:23], a[108:111], a[24:27], v[20:23]\n"
"ds_read_b64_tr_b16 a[116:117], v202 offset:8192\n"
"v_mfma_f32_16x16x32_bf16 v[52:55], a[108:111], a[28:31], v[52:55]\n"
"ds_read_b128 a[76:79], v235 offset:26624\n"
"v_mfma_f32_16x16x32_bf16 v[52:55], a[104:107], a[4:7], v[52:55]\n"
"ds_read_b64_tr_b16 a[118:119], v202 offset:9216\n"
"v_add_u32 v202, 0xffff0000, v202\n"
"v_mfma_f32_16x16x32_bf16 v[84:87], a[104:107], a[8:11], v[84:87]\n"
"buffer_load_dwordx4 v197, s[40:43], s82 offen offset:2048 lds\n"
"v_mfma_f32_16x16x32_bf16 v[84:87], a[108:111], a[32:35], v[84:87]\n"
"ds_read_b64_tr_b16 a[120:121], v203 offset:0\n"
"v_mfma_f32_16x16x32_bf16 v[116:119], a[108:111], a[36:39], v[116:119]\n"
"s_add_u32 m0, s63, 0x0\n"
"ds_read_b128 a[56:59], v234 offset:28672\n"
"v_mfma_f32_16x16x32_bf16 v[116:119], a[104:107], a[12:15], v[116:119]\n"
"s_add_u32 s85, s85, 0x1000\n"
"s_and_b32 s85, s85, 0x1ffff\n"
"ds_read_b64_tr_b16 a[122:123], v203 offset:1024\n"
"v_mfma_f32_16x16x32_bf16 v[148:151], a[104:107], a[16:19], v[148:151]\n"
"buffer_load_dwordx4 v242, s[52:55], s85 offen nt lds\n"
"v_mfma_f32_16x16x32_bf16 v[148:151], a[108:111], a[40:43], v[148:151]\n"
"ds_read_b64_tr_b16 a[124:125], v203 offset:8192\n"
"v_mfma_f32_16x16x32_bf16 v[180:183], a[108:111], a[44:47], v[180:183]\n"
"s_waitcnt lgkmcnt(4)\n"
"ds_read_b128 a[80:83], v235 offset:28672\n"
"v_mfma_f32_16x16x32_bf16 v[180:183], a[104:107], a[20:23], v[180:183]\n"
"ds_read_b64_tr_b16 a[126:127], v203 offset:9216\n"
"v_add_u32 v203, 0xffff0000, v203\n"
"v_mfma_f32_16x16x32_bf16 v[24:27], a[112:115], a[0:3], v[24:27]\n"
"buffer_load_dwordx4 v242, s[52:55], s85 offen offset:2048 nt lds\n"
"v_mfma_f32_16x16x32_bf16 v[24:27], a[116:119], a[24:27], v[24:27]\n"
"ds_read_b128 a[60:63], v234 offset:30720\n"
"v_mfma_f32_16x16x32_bf16 v[56:59], a[116:119], a[28:31], v[56:59]\n"
"ds_read_b128 a[84:87], v235 offset:30720\n"
"v_mfma_f32_16x16x32_bf16 v[56:59], a[112:115], a[4:7], v[56:59]\n"
"s_add_u32 m0, s63, 0x1000\n"
"ds_read_b128 a[64:67], v234 offset:32768\n"
"v_mfma_f32_16x16x32_bf16 v[88:91], a[112:115], a[8:11], v[88:91]\n"
"s_add_u32 s85, s85, 0x1000\n"
"buffer_load_dwordx4 v242, s[52:55], s85 offen nt lds\n"
"v_mfma_f32_16x16x32_bf16 v[88:91], a[116:119], a[32:35], v[88:91]\n"
"ds_read_b128 a[88:91], v235 offset:32768\n"
"v_mfma_f32_16x16x32_bf16 v[120:123], a[116:119], a[36:39], v[120:123]\n"
"ds_read_b128 a[68:71], v234 offset:34816\n"
"v_mfma_f32_16x16x32_bf16 v[120:123], a[112:115], a[12:15], v[120:123]\n"
"ds_read_b128 a[92:95], v235 offset:34816\n"
"v_mfma_f32_16x16x32_bf16 v[152:155], a[112:115], a[16:19], v[152:155]\n"
"buffer_load_dwordx4 v242, s[52:55], s85 offen offset:2048 nt lds\n"
"v_mfma_f32_16x16x32_bf16 v[152:155], a[116:119], a[40:43], v[152:155]\n"
"v_mfma_f32_16x16x32_bf16 v[184:187], a[116:119], a[44:47], v[184:187]\n"
"s_waitcnt lgkmcnt(6)\n"
"v_mfma_f32_16x16x32_bf16 v[184:187], a[112:115], a[20:23], v[184:187]\n"
"v_mfma_f32_16x16x32_bf16 v[28:31], a[120:123], a[0:3], v[28:31]\n"
"v_mfma_f32_16x16x32_bf16 v[28:31], a[124:127], a[24:27], v[28:31]\n"
"v_mfma_f32_16x16x32_bf16 v[60:63], a[124:127], a[28:31], v[60:63]\n"
"s_waitcnt vmcnt(14) lgkmcnt(0)\n"
"s_barrier\n"
"v_mfma_f32_16x16x32_bf16 v[60:63], a[120:123], a[4:7], v[60:63]\n"
"s_add_u32 s83, s83, 0x80000\n"
"s_cmp_eq_u32 s83, 0x500000\n"
"s_cselect_b32 s83, 0x80000, s83\n"
"s_add_u32 m0, s61, 0x18000\n"
"ds_read_b64_tr_b16 a[96:97], v200 offset:0\n"
"v_mfma_f32_16x16x32_bf16 v[92:95], a[120:123], a[8:11], v[92:95]\n"
"buffer_load_dwordx4 v204, s[44:47], s83 offen lds\n"
"v_mfma_f32_16x16x32_bf16 v[92:95], a[124:127], a[32:35], v[92:95]\n"
"ds_read_b64_tr_b16 a[98:99], v200 offset:1024\n"
"v_mfma_f32_16x16x32_bf16 v[124:127], a[124:127], a[36:39], v[124:127]\n"
"ds_read_b64_tr_b16 a[100:101], v200 offset:8192\n"
"v_mfma_f32_16x16x32_bf16 v[124:127], a[120:123], a[12:15], v[124:127]\n"
"ds_read_b64_tr_b16 a[102:103], v200 offset:9216\n"
"v_add_u32 v200, 0x4000, v200\n"
"v_mfma_f32_16x16x32_bf16 v[156:159], a[120:123], a[16:19], v[156:159]\n"
"buffer_load_dwordx4 v205, s[44:47], s83 offen offset:1024 lds\n"
"v_mfma_f32_16x16x32_bf16 v[156:159], a[124:127], a[40:43], v[156:159]\n"
"v_mfma_f32_16x16x32_bf16 v[188:191], a[124:127], a[44:47], v[188:191]\n"
"v_mfma_f32_16x16x32_bf16 v[188:191], a[120:123], a[20:23], v[188:191]\n"
"s_waitcnt lgkmcnt(0)\n"
"v_mfma_f32_16x16x32_bf16 v[0:3], a[96:99], a[48:51], v[0:3]\n"
"buffer_load_dwordx4 v206, s[44:47], s83 offen offset:2048 lds\n"
"v_mfma_f32_16x16x32_bf16 v[0:3], a[100:103], a[72:75], v[0:3]\n"
"ds_read_b64_tr_b16 a[104:105], v201 offset:0\n"
"v_mfma_f32_16x16x32_bf16 v[32:35], a[100:103], a[76:79], v[32:35]\n"
"ds_read_b64 a[192:193], v222 offset:0\n"
"v_mfma_f32_16x16x32_bf16 v[32:35], a[96:99], a[52:55], v[32:35]\n"
"ds_read_b64_tr_b16 a[106:107], v201 offset:1024\n"
"v_mfma_f32_16x16x32_bf16 v[64:67], a[96:99], a[56:59], v[64:67]\n"
"v_mfma_f32_16x16x32_bf16 v[64:67], a[100:103], a[80:83], v[64:67]\n"
"ds_read_b64_tr_b16 a[108:109], v201 offset:8192\n"
"v_mfma_f32_16x16x32_bf16 v[96:99], a[100:103], a[84:87], v[96:99]\n"
"ds_read_b64 a[194:195], v223 offset:0\n"
"v_mfma_f32_16x16x32_bf16 v[96:99], a[96:99], a[60:63], v[96:99]\n"
"ds_read_b64_tr_b16 a[110:111], v201 offset:9216\n"
"v_add_u32 v201, 0x4000, v201\n"
"v_mfma_f32_16x16x32_bf16 v[128:131], a[96:99], a[64:67], v[128:131]\n"
"buffer_load_dwordx4 v207, s[44:47], s83 offen offset:3072 lds\n"
"v_mfma_f32_16x16x32_bf16 v[128:131], a[100:103], a[88:91], v[128:131]\n"
"ds_read_b64_tr_b16 a[112:113], v202 offset:0\n"
"v_mfma_f32_16x16x32_bf16 v[160:163], a[100:103], a[92:95], v[160:163]\n"
"s_waitcnt lgkmcnt(1)\n"
"ds_read_b64 a[196:197], v224 offset:0\n"
"v_mfma_f32_16x16x32_bf16 v[160:163], a[96:99], a[68:71], v[160:163]\n"
"ds_read_b64_tr_b16 a[114:115], v202 offset:1024\n"
"v_mfma_f32_16x16x32_bf16 v[4:7], a[104:107], a[48:51], v[4:7]\n"
"s_add_u32 s82, s82, 128\n"
"s_cmp_eq_u32 s82, 0x500\n"
"s_cselect_b32 s82, 128, s82\n"
"v_mfma_f32_16x16x32_bf16 v[4:7], a[108:111], a[72:75], v[4:7]\n"
"ds_read_b64_tr_b16 a[116:117], v202 offset:8192\n"
"v_mfma_f32_16x16x32_bf16 v[36:39], a[108:111], a[76:79], v[36:39]\n"
"ds_read_b64 a[198:199], v225 offset:0\n"
"v_mfma_f32_16x16x32_bf16 v[36:39], a[104:107], a[52:55], v[36:39]\n"
"s_add_u32 m0, s60, 0x22000\n"
"ds_read_b64_tr_b16 a[118:119], v202 offset:9216\n"
"v_add_u32 v202, 0x4000, v202\n"
"v_mfma_f32_16x16x32_bf16 v[68:71], a[104:107], a[56:59], v[68:71]\n"
"buffer_load_dwordx4 v192, s[40:43], s82 offen lds\n"
"v_mfma_f32_16x16x32_bf16 v[68:71], a[108:111], a[80:83], v[68:71]\n"
"ds_read_b64_tr_b16 a[120:121], v203 offset:0\n"
"v_mfma_f32_16x16x32_bf16 v[100:103], a[108:111], a[84:87], v[100:103]\n"
"ds_read_b64 a[200:201], v246 offset:0\n"
"v_mfma_f32_16x16x32_bf16 v[100:103], a[104:107], a[60:63], v[100:103]\n"
"ds_read_b64_tr_b16 a[122:123], v203 offset:1024\n"
"v_mfma_f32_16x16x32_bf16 v[132:135], a[104:107], a[64:67], v[132:135]\n"
"v_mfma_f32_16x16x32_bf16 v[132:135], a[108:111], a[88:91], v[132:135]\n"
"ds_read_b64_tr_b16 a[124:125], v203 offset:8192\n"
"v_mfma_f32_16x16x32_bf16 v[164:167], a[108:111], a[92:95], v[164:167]\n"
"s_waitcnt lgkmcnt(4)\n"
"ds_read_b64 a[202:203], v247 offset:0\n"
"v_mfma_f32_16x16x32_bf16 v[164:167], a[104:107], a[68:71], v[164:167]\n"
"ds_read_b64_tr_b16 a[126:127], v203 offset:9216\n"
"v_add_u32 v203, 0x4000, v203\n"
"v_mfma_f32_16x16x32_bf16 v[8:11], a[112:115], a[48:51], v[8:11]\n"
"buffer_load_dwordx4 v193, s[40:43], s82 offen offset:1024 lds\n"
"v_mfma_f32_16x16x32_bf16 v[8:11], a[116:119], a[72:75], v[8:11]\n"
"ds_read_b64 a[204:205], v248 offset:0\n"
"v_mfma_f32_16x16x32_bf16 v[40:43], a[116:119], a[76:79], v[40:43]\n"
"ds_read_b64 a[206:207], v249 offset:0\n"
"v_mfma_f32_16x16x32_bf16 v[40:43], a[112:115], a[52:55], v[40:43]\n"
"ds_read_b64 a[208:209], v222 offset:4096\n"
"v_mfma_f32_16x16x32_bf16 v[72:75], a[112:115], a[56:59], v[72:75]\n"
"v_mfma_f32_16x16x32_bf16 v[72:75], a[116:119], a[80:83], v[72:75]\n"
"ds_read_b64 a[210:211], v223 offset:4096\n"
"v_mfma_f32_16x16x32_bf16 v[104:107], a[116:119], a[84:87], v[104:107]\n"
"ds_read_b64 a[212:213], v224 offset:4096\n"
"v_mfma_f32_16x16x32_bf16 v[104:107], a[112:115], a[60:63], v[104:107]\n"
"ds_read_b64 a[214:215], v225 offset:4096\n"
"v_mfma_f32_16x16x32_bf16 v[136:139], a[112:115], a[64:67], v[136:139]\n"
"buffer_load_dwordx4 v194, s[40:43], s82 offen offset:2048 lds\n"
"v_mfma_f32_16x16x32_bf16 v[136:139], a[116:119], a[88:91], v[136:139]\n"
"ds_read_b64 a[216:217], v246 offset:4096\n"
"v_mfma_f32_16x16x32_bf16 v[168:171], a[116:119], a[92:95], v[168:171]\n"
"s_waitcnt lgkmcnt(7)\n"
"ds_read_b64 a[218:219], v247 offset:4096\n"
"v_mfma_f32_16x16x32_bf16 v[168:171], a[112:115], a[68:71], v[168:171]\n"
"ds_read_b64 a[220:221], v248 offset:4096\n"
"v_mfma_f32_16x16x32_bf16 v[12:15], a[120:123], a[48:51], v[12:15]\n"
"v_mfma_f32_16x16x32_bf16 v[12:15], a[124:127], a[72:75], v[12:15]\n"
"ds_read_b64 a[222:223], v249 offset:4096\n"
"v_mfma_f32_16x16x32_bf16 v[44:47], a[124:127], a[76:79], v[44:47]\n"
"s_waitcnt vmcnt(11)\n"
"s_barrier\n"
"v_mfma_f32_16x16x32_bf16 v[44:47], a[120:123], a[52:55], v[44:47]\n"
"s_add_u32 s65, s83, 128\n"
"s_add_u32 m0, s61, 0x8000\n"
"ds_read_b64_tr_b16 a[96:97], v200 offset:0\n"
"v_mfma_f32_16x16x32_bf16 v[76:79], a[120:123], a[56:59], v[76:79]\n"
"buffer_load_dwordx4 v204, s[44:47], s65 offen lds\n"
"v_mfma_f32_16x16x32_bf16 v[76:79], a[124:127], a[80:83], v[76:79]\n"
"ds_read_b64_tr_b16 a[98:99], v200 offset:1024\n"
"v_mfma_f32_16x16x32_bf16 v[108:111], a[124:127], a[84:87], v[108:111]\n"
"ds_read_b64_tr_b16 a[100:101], v200 offset:8192\n"
"v_mfma_f32_16x16x32_bf16 v[108:111], a[120:123], a[60:63], v[108:111]\n"
"ds_read_b64_tr_b16 a[102:103], v200 offset:9216\n"
"v_add_u32 v200, 0xc000, v200\n"
"v_mfma_f32_16x16x32_bf16 v[140:143], a[120:123], a[64:67], v[140:143]\n"
"buffer_load_dwordx4 v205, s[44:47], s65 offen offset:1024 lds\n"
"v_mfma_f32_16x16x32_bf16 v[140:143], a[124:127], a[88:91], v[140:143]\n"
"v_mfma_f32_16x16x32_bf16 v[172:175], a[124:127], a[92:95], v[172:175]\n"
"v_mfma_f32_16x16x32_bf16 v[172:175], a[120:123], a[68:71], v[172:175]\n"
"s_waitcnt lgkmcnt(0)\n"
"v_mfma_f32_16x16x32_bf16 v[16:19], a[96:99], a[48:51], v[16:19]\n"
"buffer_load_dwordx4 v206, s[44:47], s65 offen offset:2048 lds\n"
"v_mfma_f32_16x16x32_bf16 v[16:19], a[100:103], a[72:75], v[16:19]\n"
"ds_read_b64_tr_b16 a[104:105], v201 offset:0\n"
"v_mfma_f32_16x16x32_bf16 v[48:51], a[100:103], a[76:79], v[48:51]\n"
"ds_read_b128 a[0:3], v234 offset:0\n"
"v_mfma_f32_16x16x32_bf16 v[48:51], a[96:99], a[52:55], v[48:51]\n"
"ds_read_b64_tr_b16 a[106:107], v201 offset:1024\n"
"v_mfma_f32_16x16x32_bf16 v[80:83], a[96:99], a[56:59], v[80:83]\n"
"v_mfma_f32_16x16x32_bf16 v[80:83], a[100:103], a[80:83], v[80:83]\n"
"ds_read_b64_tr_b16 a[108:109], v201 offset:8192\n"
"v_mfma_f32_16x16x32_bf16 v[112:115], a[100:103], a[84:87], v[112:115]\n"
"ds_read_b128 a[24:27], v235 offset:0\n"
"v_mfma_f32_16x16x32_bf16 v[112:115], a[96:99], a[60:63], v[112:115]\n"
"ds_read_b64_tr_b16 a[110:111], v201 offset:9216\n"
"v_add_u32 v201, 0xc000, v201\n"
"v_mfma_f32_16x16x32_bf16 v[144:147], a[96:99], a[64:67], v[144:147]\n"
"buffer_load_dwordx4 v207, s[44:47], s65 offen offset:3072 lds\n"
"v_mfma_f32_16x16x32_bf16 v[144:147], a[100:103], a[88:91], v[144:147]\n"
"ds_read_b64_tr_b16 a[112:113], v202 offset:0\n"
"v_mfma_f32_16x16x32_bf16 v[176:179], a[100:103], a[92:95], v[176:179]\n"
"s_waitcnt lgkmcnt(1)\n"
"ds_read_b128 a[4:7], v234 offset:2048\n"
"v_mfma_f32_16x16x32_bf16 v[176:179], a[96:99], a[68:71], v[176:179]\n"
"ds_read_b64_tr_b16 a[114:115], v202 offset:1024\n"
"v_mfma_f32_16x16x32_bf16 v[20:23], a[104:107], a[48:51], v[20:23]\n"
"v_mfma_f32_16x16x32_bf16 v[20:23], a[108:111], a[72:75], v[20:23]\n"
"ds_read_b64_tr_b16 a[116:117], v202 offset:8192\n"
"v_mfma_f32_16x16x32_bf16 v[52:55], a[108:111], a[76:79], v[52:55]\n"
"ds_read_b128 a[28:31], v235 offset:2048\n"
"v_mfma_f32_16x16x32_bf16 v[52:55], a[104:107], a[52:55], v[52:55]\n"
"s_add_u32 m0, s60, 0x22c00\n"
"ds_read_b64_tr_b16 a[118:119], v202 offset:9216\n"
"v_add_u32 v202, 0xc000, v202\n"
"v_mfma_f32_16x16x32_bf16 v[84:87], a[104:107], a[56:59], v[84:87]\n"
"buffer_load_dwordx4 v195, s[40:43], s82 offen lds\n"
"v_mfma_f32_16x16x32_bf16 v[84:87], a[108:111], a[80:83], v[84:87]\n"
"ds_read_b64_tr_b16 a[120:121], v203 offset:0\n"
"v_mfma_f32_16x16x32_bf16 v[116:119], a[108:111], a[84:87], v[116:119]\n"
"ds_read_b128 a[8:11], v234 offset:4096\n"
"v_mfma_f32_16x16x32_bf16 v[116:119], a[104:107], a[60:63], v[116:119]\n"
"ds_read_b64_tr_b16 a[122:123], v203 offset:1024\n"
"v_mfma_f32_16x16x32_bf16 v[148:151], a[104:107], a[64:67], v[148:151]\n"
"v_mfma_f32_16x16x32_bf16 v[148:151], a[108:111], a[88:91], v[148:151]\n"
"ds_read_b64_tr_b16 a[124:125], v203 offset:8192\n"
"v_mfma_f32_16x16x32_bf16 v[180:183], a[108:111], a[92:95], v[180:183]\n"
"s_waitcnt lgkmcnt(4)\n"
"ds_read_b128 a[32:35], v235 offset:4096\n"
"v_mfma_f32_16x16x32_bf16 v[180:183], a[104:107], a[68:71], v[180:183]\n"
"ds_read_b64_tr_b16 a[126:127], v203 offset:9216\n"
"v_add_u32 v203, 0xc000, v203\n"
"v_mfma_f32_16x16x32_bf16 v[24:27], a[112:115], a[48:51], v[24:27]\n"
"buffer_load_dwordx4 v196, s[40:43], s82 offen offset:1024 lds\n"
"v_mfma_f32_16x16x32_bf16 v[24:27], a[116:119], a[72:75], v[24:27]\n"
"ds_read_b128 a[12:15], v234 offset:6144\n"
"v_mfma_f32_16x16x32_bf16 v[56:59], a[116:119], a[76:79], v[56:59]\n"
"ds_read_b128 a[36:39], v235 offset:6144\n"
"v_mfma_f32_16x16x32_bf16 v[56:59], a[112:115], a[52:55], v[56:59]\n"
"ds_read_b128 a[16:19], v234 offset:8192\n"
"v_mfma_f32_16x16x32_bf16 v[88:91], a[112:115], a[56:59], v[88:91]\n"
"v_mfma_f32_16x16x32_bf16 v[88:91], a[116:119], a[80:83], v[88:91]\n"
"ds_read_b128 a[40:43], v235 offset:8192\n"
"v_mfma_f32_16x16x32_bf16 v[120:123], a[116:119], a[84:87], v[120:123]\n"
"ds_read_b128 a[20:23], v234 offset:10240\n"
"v_mfma_f32_16x16x32_bf16 v[120:123], a[112:115], a[60:63], v[120:123]\n"
"ds_read_b128 a[44:47], v235 offset:10240\n"
"v_mfma_f32_16x16x32_bf16 v[152:155], a[112:115], a[64:67], v[152:155]\n"
"buffer_load_dwordx4 v197, s[40:43], s82 offen offset:2048 lds\n"
"v_mfma_f32_16x16x32_bf16 v[152:155], a[116:119], a[88:91], v[152:155]\n"
"v_mfma_f32_16x16x32_bf16 v[184:187], a[116:119], a[92:95], v[184:187]\n"
"s_waitcnt lgkmcnt(6)\n"
"v_mfma_f32_16x16x32_bf16 v[184:187], a[112:115], a[68:71], v[184:187]\n"
"v_mfma_f32_16x16x32_bf16 v[28:31], a[120:123], a[48:51], v[28:31]\n"
"v_mfma_f32_16x16x32_bf16 v[28:31], a[124:127], a[72:75], v[28:31]\n"
"v_mfma_f32_16x16x32_bf16 v[60:63], a[124:127], a[76:79], v[60:63]\n"
"s_waitcnt vmcnt(10) lgkmcnt(0)\n"
"s_barrier\n"
"v_mfma_f32_16x16x32_bf16 v[60:63], a[120:123], a[52:55], v[60:63]\n"
"s_add_u32 s83, s83, 0x80000\n"
"s_cmp_eq_u32 s83, 0x500000\n"
"s_cselect_b32 s83, 0x80000, s83\n"
"s_add_u32 m0, s61, 0xc000\n"
"ds_read_b64_tr_b16 a[96:97], v200 offset:0\n"
"v_mfma_f32_16x16x32_bf16 v[92:95], a[120:123], a[56:59], v[92:95]\n"
"buffer_load_dwordx4 v204, s[44:47], s83 offen lds\n"
"v_mfma_f32_16x16x32_bf16 v[92:95], a[124:127], a[80:83], v[92:95]\n"
"ds_read_b64_tr_b16 a[98:99], v200 offset:1024\n"
"v_mfma_f32_16x16x32_bf16 v[124:127], a[124:127], a[84:87], v[124:127]\n"
"ds_read_b64_tr_b16 a[100:101], v200 offset:8192\n"
"v_mfma_f32_16x16x32_bf16 v[124:127], a[120:123], a[60:63], v[124:127]\n"
"ds_read_b64_tr_b16 a[102:103], v200 offset:9216\n"
"v_add_u32 v200, 0xffff0000, v200\n"
"v_mfma_f32_16x16x32_bf16 v[156:159], a[120:123], a[64:67], v[156:159]\n"
"buffer_load_dwordx4 v205, s[44:47], s83 offen offset:1024 lds\n"
"v_mfma_f32_16x16x32_bf16 v[156:159], a[124:127], a[88:91], v[156:159]\n"
"v_mfma_f32_16x16x32_bf16 v[188:191], a[124:127], a[92:95], v[188:191]\n"
"v_mfma_f32_16x16x32_bf16 v[188:191], a[120:123], a[68:71], v[188:191]\n"
"s_waitcnt lgkmcnt(0)\n"
"v_mfma_f32_16x16x32_bf16 v[0:3], a[96:99], a[0:3], v[0:3]\n"
"buffer_load_dwordx4 v206, s[44:47], s83 offen offset:2048 lds\n"
"v_mfma_f32_16x16x32_bf16 v[0:3], a[100:103], a[24:27], v[0:3]\n"
"ds_read_b64_tr_b16 a[104:105], v201 offset:0\n"
"v_mfma_f32_16x16x32_bf16 v[32:35], a[100:103], a[28:31], v[32:35]\n"
"ds_read_b64_tr_b16 a[224:225], v226 offset:0\n"
"v_mfma_f32_16x16x32_bf16 v[32:35], a[96:99], a[4:7], v[32:35]\n"
"ds_read_b64_tr_b16 a[106:107], v201 offset:1024\n"
"v_mfma_f32_16x16x32_bf16 v[64:67], a[96:99], a[8:11], v[64:67]\n"
"buffer_load_dwordx4 v207, s[44:47], s83 offen offset:3072 lds\n"
"v_mfma_f32_16x16x32_bf16 v[64:67], a[100:103], a[32:35], v[64:67]\n"
"ds_read_b64_tr_b16 a[108:109], v201 offset:8192\n"
"v_mfma_f32_16x16x32_bf16 v[96:99], a[100:103], a[36:39], v[96:99]\n"
"s_add_u32 s82, s82, 128\n"
"s_cmp_eq_u32 s82, 0x500\n"
"s_cselect_b32 s82, 128, s82\n"
"ds_read_b64_tr_b16 a[226:227], v226 offset:4096\n"
"v_mfma_f32_16x16x32_bf16 v[96:99], a[96:99], a[12:15], v[96:99]\n"
"s_add_u32 m0, s60, 0x1c000\n"
"ds_read_b64_tr_b16 a[110:111], v201 offset:9216\n"
"v_add_u32 v201, 0xffff0000, v201\n"
"v_mfma_f32_16x16x32_bf16 v[128:131], a[96:99], a[16:19], v[128:131]\n"
"buffer_load_dwordx4 v192, s[40:43], s82 offen lds\n"
"v_mfma_f32_16x16x32_bf16 v[128:131], a[100:103], a[40:43], v[128:131]\n"
"ds_read_b64_tr_b16 a[112:113], v202 offset:0\n"
"v_mfma_f32_16x16x32_bf16 v[160:163], a[100:103], a[44:47], v[160:163]\n"
"s_waitcnt lgkmcnt(1)\n"
"ds_read_b64_tr_b16 a[228:229], v227 offset:0\n"
"v_mfma_f32_16x16x32_bf16 v[160:163], a[96:99], a[20:23], v[160:163]\n"
"ds_read_b64_tr_b16 a[114:115], v202 offset:1024\n"
"v_mfma_f32_16x16x32_bf16 v[4:7], a[104:107], a[0:3], v[4:7]\n"
"buffer_load_dwordx4 v193, s[40:43], s82 offen offset:1024 lds\n"
"v_mfma_f32_16x16x32_bf16 v[4:7], a[108:111], a[24:27], v[4:7]\n"
"ds_read_b64_tr_b16 a[116:117], v202 offset:8192\n"
"v_mfma_f32_16x16x32_bf16 v[36:39], a[108:111], a[28:31], v[36:39]\n"
"ds_read_b64_tr_b16 a[230:231], v227 offset:4096\n"
"v_mfma_f32_16x16x32_bf16 v[36:39], a[104:107], a[4:7], v[36:39]\n"
"ds_read_b64_tr_b16 a[118:119], v202 offset:9216\n"
"v_add_u32 v202, 0xffff0000, v202\n"
"v_mfma_f32_16x16x32_bf16 v[68:71], a[104:107], a[8:11], v[68:71]\n"
"buffer_load_dwordx4 v194, s[40:43], s82 offen offset:2048 lds\n"
"v_mfma_f32_16x16x32_bf16 v[68:71], a[108:111], a[32:35], v[68:71]\n"
"ds_read_b64_tr_b16 a[120:121], v203 offset:0\n"
"v_mfma_f32_16x16x32_bf16 v[100:103], a[108:111], a[36:39], v[100:103]\n"
"s_add_u32 m0, s62, 0x4000\n"
"ds_read_b64_tr_b16 a[232:233], v228 offset:0\n"
"v_mfma_f32_16x16x32_bf16 v[100:103], a[104:107], a[12:15], v[100:103]\n"
"s_add_u32 s84, s84, 0x1000\n"
"s_and_b32 s84, s84, 0x1ffff\n"
"ds_read_b64_tr_b16 a[122:123], v203 offset:1024\n"
"v_mfma_f32_16x16x32_bf16 v[132:135], a[104:107], a[16:19], v[132:135]\n"
"buffer_load_dwordx4 v240, s[48:51], s84 offen nt lds\n"
"v_mfma_f32_16x16x32_bf16 v[132:135], a[108:111], a[40:43], v[132:135]\n"
"ds_read_b64_tr_b16 a[124:125], v203 offset:8192\n"
"v_mfma_f32_16x16x32_bf16 v[164:167], a[108:111], a[44:47], v[164:167]\n"
"s_waitcnt lgkmcnt(4)\n"
"ds_read_b64_tr_b16 a[234:235], v228 offset:4096\n"
"v_mfma_f32_16x16x32_bf16 v[164:167], a[104:107], a[20:23], v[164:167]\n"
"ds_read_b64_tr_b16 a[126:127], v203 offset:9216\n"
"v_add_u32 v203, 0xffff0000, v203\n"
"v_mfma_f32_16x16x32_bf16 v[8:11], a[112:115], a[0:3], v[8:11]\n"
"buffer_load_dwordx4 v241, s[48:51], s84 offen offset:1024 nt lds\n"
"v_mfma_f32_16x16x32_bf16 v[8:11], a[116:119], a[24:27], v[8:11]\n"
"ds_read_b64_tr_b16 a[236:237], v229 offset:0\n"
"v_mfma_f32_16x16x32_bf16 v[40:43], a[116:119], a[28:31], v[40:43]\n"
"ds_read_b64_tr_b16 a[238:239], v229 offset:4096\n"
"v_mfma_f32_16x16x32_bf16 v[40:43], a[112:115], a[4:7], v[40:43]\n"
"s_add_u32 m0, s62, 0x5000\n"
"ds_read_b64_tr_b16 a[240:241], v230 offset:0\n"
"v_mfma_f32_16x16x32_bf16 v[72:75], a[112:115], a[8:11], v[72:75]\n"
"s_add_u32 s84, s84, 0x1000\n"
"buffer_load_dwordx4 v240, s[48:51], s84 offen nt lds\n"
"v_mfma_f32_16x16x32_bf16 v[72:75], a[116:119], a[32:35], v[72:75]\n"
"ds_read_b64_tr_b16 a[242:243], v230 offset:4096\n"
"v_mfma_f32_16x16x32_bf16 v[104:107], a[116:119], a[36:39], v[104:107]\n"
"ds_read_b64_tr_b16 a[244:245], v231 offset:0\n"
"v_mfma_f32_16x16x32_bf16 v[104:107], a[112:115], a[12:15], v[104:107]\n"
"ds_read_b64_tr_b16 a[246:247], v231 offset:4096\n"
"v_mfma_f32_16x16x32_bf16 v[136:139], a[112:115], a[16:19], v[136:139]\n"
"buffer_load_dwordx4 v241, s[48:51], s84 offen offset:1024 nt lds\n"
"v_mfma_f32_16x16x32_bf16 v[136:139], a[116:119], a[40:43], v[136:139]\n"
"ds_read_b64_tr_b16 a[248:249], v232 offset:0\n"
"v_mfma_f32_16x16x32_bf16 v[168:171], a[116:119], a[44:47], v[168:171]\n"
"s_waitcnt lgkmcnt(7)\n"
"ds_read_b64_tr_b16 a[250:251], v232 offset:4096\n"
"v_mfma_f32_16x16x32_bf16 v[168:171], a[112:115], a[20:23], v[168:171]\n"
"ds_read_b64_tr_b16 a[252:253], v233 offset:0\n"
"v_mfma_f32_16x16x32_bf16 v[12:15], a[120:123], a[0:3], v[12:15]\n"
"v_mfma_f32_16x16x32_bf16 v[12:15], a[124:127], a[24:27], v[12:15]\n"
"ds_read_b64_tr_b16 a[254:255], v233 offset:4096\n"
"v_mfma_f32_16x16x32_bf16 v[44:47], a[124:127], a[28:31], v[44:47]\n"
"s_waitcnt vmcnt(11)\n"
"s_barrier\n"
"v_mfma_f32_16x16x32_bf16 v[44:47], a[120:123], a[4:7], v[44:47]\n"
"s_add_u32 s65, s83, 128\n"
"s_add_u32 m0, s61, 0x18000\n"
"ds_read_b64_tr_b16 a[96:97], v200 offset:0\n"
"v_mfma_f32_16x16x32_bf16 v[76:79], a[120:123], a[8:11], v[76:79]\n"
"buffer_load_dwordx4 v204, s[44:47], s65 offen lds\n"
"v_mfma_f32_16x16x32_bf16 v[76:79], a[124:127], a[32:35], v[76:79]\n"
"ds_read_b64_tr_b16 a[98:99], v200 offset:1024\n"
"v_mfma_f32_16x16x32_bf16 v[108:111], a[124:127], a[36:39], v[108:111]\n"
"ds_read_b64_tr_b16 a[100:101], v200 offset:8192\n"
"v_mfma_f32_16x16x32_bf16 v[108:111], a[120:123], a[12:15], v[108:111]\n"
"ds_read_b64_tr_b16 a[102:103], v200 offset:9216\n"
"v_add_u32 v200, 0x4000, v200\n"
"v_mfma_f32_16x16x32_bf16 v[140:143], a[120:123], a[16:19], v[140:143]\n"
"buffer_load_dwordx4 v205, s[44:47], s65 offen offset:1024 lds\n"
"v_mfma_f32_16x16x32_bf16 v[140:143], a[124:127], a[40:43], v[140:143]\n"
"v_mfma_f32_16x16x32_bf16 v[172:175], a[124:127], a[44:47], v[172:175]\n"
"v_mfma_f32_16x16x32_bf16 v[172:175], a[120:123], a[20:23], v[172:175]\n"
"s_waitcnt lgkmcnt(0)\n"
"v_mfma_f32_16x16x32_bf16 v[16:19], a[96:99], a[0:3], v[16:19]\n"
"buffer_load_dwordx4 v206, s[44:47], s65 offen offset:2048 lds\n"
"v_mfma_f32_16x16x32_bf16 v[16:19], a[100:103], a[24:27], v[16:19]\n"
"ds_read_b64_tr_b16 a[104:105], v201 offset:0\n"
"v_mfma_f32_16x16x32_bf16 v[48:51], a[100:103], a[28:31], v[48:51]\n"
"ds_read_b128 a[48:51], v234 offset:24576\n"
"v_mfma_f32_16x16x32_bf16 v[48:51], a[96:99], a[4:7], v[48:51]\n"
"ds_read_b64_tr_b16 a[106:107], v201 offset:1024\n"
"v_mfma_f32_16x16x32_bf16 v[80:83], a[96:99], a[8:11], v[80:83]\n"
"v_mfma_f32_16x16x32_bf16 v[80:83], a[100:103], a[32:35], v[80:83]\n"
"ds_read_b64_tr_b16 a[108:109], v201 offset:8192\n"
"v_mfma_f32_16x16x32_bf16 v[112:115], a[100:103], a[36:39], v[112:115]\n"
"ds_read_b128 a[72:75], v235 offset:24576\n"
"v_mfma_f32_16x16x32_bf16 v[112:115], a[96:99], a[12:15], v[112:115]\n"
"ds_read_b64_tr_b16 a[110:111], v201 offset:9216\n"
"v_add_u32 v201, 0x4000, v201\n"
"v_mfma_f32_16x16x32_bf16 v[144:147], a[96:99], a[16:19], v[144:147]\n"
"buffer_load_dwordx4 v207, s[44:47], s65 offen offset:3072 lds\n"
"v_mfma_f32_16x16x32_bf16 v[144:147], a[100:103], a[40:43], v[144:147]\n"
"ds_read_b64_tr_b16 a[112:113], v202 offset:0\n"
"v_mfma_f32_16x16x32_bf16 v[176:179], a[100:103], a[44:47], v[176:179]\n"
"s_waitcnt lgkmcnt(1)\n"
"ds_read_b128 a[52:55], v234 offset:26624\n"
"v_mfma_f32_16x16x32_bf16 v[176:179], a[96:99], a[20:23], v[176:179]\n"
"ds_read_b64_tr_b16 a[114:115], v202 offset:1024\n"
"v_mfma_f32_16x16x32_bf16 v[20:23], a[104:107], a[0:3], v[20:23]\n"
"v_mfma_f32_16x16x32_bf16 v[20:23], a[108:111], a[24:27], v[20:23]\n"
"ds_read_b64_tr_b16 a[116:117], v202 offset:8192\n"
"v_mfma_f32_16x16x32_bf16 v[52:55], a[108:111], a[28:31], v[52:55]\n"
"ds_read_b128 a[76:79], v235 offset:26624\n"
"v_mfma_f32_16x16x32_bf16 v[52:55], a[104:107], a[4:7], v[52:55]\n"
"s_add_u32 m0, s60, 0x1cc00\n"
"ds_read_b64_tr_b16 a[118:119], v202 offset:9216\n"
"v_add_u32 v202, 0x4000, v202\n"
"v_mfma_f32_16x16x32_bf16 v[84:87], a[104:107], a[8:11], v[84:87]\n"
"buffer_load_dwordx4 v195, s[40:43], s82 offen lds\n"
"v_mfma_f32_16x16x32_bf16 v[84:87], a[108:111], a[32:35], v[84:87]\n"
"ds_read_b64_tr_b16 a[120:121], v203 offset:0\n"
"v_mfma_f32_16x16x32_bf16 v[116:119], a[108:111], a[36:39], v[116:119]\n"
"ds_read_b128 a[56:59], v234 offset:28672\n"
"v_mfma_f32_16x16x32_bf16 v[116:119], a[104:107], a[12:15], v[116:119]\n"
"ds_read_b64_tr_b16 a[122:123], v203 offset:1024\n"
"v_mfma_f32_16x16x32_bf16 v[148:151], a[104:107], a[16:19], v[148:151]\n"
"v_mfma_f32_16x16x32_bf16 v[148:151], a[108:111], a[40:43], v[148:151]\n"
"ds_read_b64_tr_b16 a[124:125], v203 offset:8192\n"
"v_mfma_f32_16x16x32_bf16 v[180:183], a[108:111], a[44:47], v[180:183]\n"
"s_waitcnt lgkmcnt(4)\n"
"ds_read_b128 a[80:83], v235 offset:28672\n"
"v_mfma_f32_16x16x32_bf16 v[180:183], a[104:107], a[20:23], v[180:183]\n"
"ds_read_b64_tr_b16 a[126:127], v203 offset:9216\n"
"v_add_u32 v203, 0x4000, v203\n"
"v_mfma_f32_16x16x32_bf16 v[24:27], a[112:115], a[0:3], v[24:27]\n"
"buffer_load_dwordx4 v196, s[40:43], s82 offen offset:1024 lds\n"
"v_mfma_f32_16x16x32_bf16 v[24:27], a[116:119], a[24:27], v[24:27]\n"
"ds_read_b128 a[60:63], v234 offset:30720\n"
"v_mfma_f32_16x16x32_bf16 v[56:59], a[116:119], a[28:31], v[56:59]\n"
"ds_read_b128 a[84:87], v235 offset:30720\n"
"v_mfma_f32_16x16x32_bf16 v[56:59], a[112:115], a[4:7], v[56:59]\n"
"ds_read_b128 a[64:67], v234 offset:32768\n"
"v_mfma_f32_16x16x32_bf16 v[88:91], a[112:115], a[8:11], v[88:91]\n"
"v_mfma_f32_16x16x32_bf16 v[88:91], a[116:119], a[32:35], v[88:91]\n"
"ds_read_b128 a[88:91], v235 offset:32768\n"
"v_mfma_f32_16x16x32_bf16 v[120:123], a[116:119], a[36:39], v[120:123]\n"
"ds_read_b128 a[68:71], v234 offset:34816\n"
"v_mfma_f32_16x16x32_bf16 v[120:123], a[112:115], a[12:15], v[120:123]\n"
"ds_read_b128 a[92:95], v235 offset:34816\n"
"v_mfma_f32_16x16x32_bf16 v[152:155], a[112:115], a[16:19], v[152:155]\n"
"buffer_load_dwordx4 v197, s[40:43], s82 offen offset:2048 lds\n"
"v_mfma_f32_16x16x32_bf16 v[152:155], a[116:119], a[40:43], v[152:155]\n"
"v_mfma_f32_16x16x32_bf16 v[184:187], a[116:119], a[44:47], v[184:187]\n"
"s_waitcnt lgkmcnt(6)\n"
"v_mfma_f32_16x16x32_bf16 v[184:187], a[112:115], a[20:23], v[184:187]\n"
"v_mfma_f32_16x16x32_bf16 v[28:31], a[120:123], a[0:3], v[28:31]\n"
"v_mfma_f32_16x16x32_bf16 v[28:31], a[124:127], a[24:27], v[28:31]\n"
"v_mfma_f32_16x16x32_bf16 v[60:63], a[124:127], a[28:31], v[60:63]\n"
"s_waitcnt vmcnt(14) lgkmcnt(0)\n"
"s_barrier\n"
"v_mfma_f32_16x16x32_bf16 v[60:63], a[120:123], a[4:7], v[60:63]\n"
"s_add_u32 s83, s83, 0x80000\n"
"s_cmp_eq_u32 s83, 0x500000\n"
"s_cselect_b32 s83, 0x80000, s83\n"
"s_add_u32 m0, s61, 0x8000\n"
"ds_read_b64_tr_b16 a[96:97], v200 offset:0\n"
"v_mfma_f32_16x16x32_bf16 v[92:95], a[120:123], a[8:11], v[92:95]\n"
"buffer_load_dwordx4 v204, s[44:47], s83 offen lds\n"
"v_mfma_f32_16x16x32_bf16 v[92:95], a[124:127], a[32:35], v[92:95]\n"
"ds_read_b64_tr_b16 a[98:99], v200 offset:1024\n"
"v_mfma_f32_16x16x32_bf16 v[124:127], a[124:127], a[36:39], v[124:127]\n"
"ds_read_b64_tr_b16 a[100:101], v200 offset:8192\n"
"v_mfma_f32_16x16x32_bf16 v[124:127], a[120:123], a[12:15], v[124:127]\n"
"ds_read_b64_tr_b16 a[102:103], v200 offset:9216\n"
"v_add_u32 v200, 0xc000, v200\n"
"v_mfma_f32_16x16x32_bf16 v[156:159], a[120:123], a[16:19], v[156:159]\n"
"buffer_load_dwordx4 v205, s[44:47], s83 offen offset:1024 lds\n"
"v_mfma_f32_16x16x32_bf16 v[156:159], a[124:127], a[40:43], v[156:159]\n"
"v_mfma_f32_16x16x32_bf16 v[188:191], a[124:127], a[44:47], v[188:191]\n"
"v_mfma_f32_16x16x32_bf16 v[188:191], a[120:123], a[20:23], v[188:191]\n"
"s_waitcnt lgkmcnt(0)\n"
"v_mfma_f32_16x16x32_bf16 v[0:3], a[96:99], a[48:51], v[0:3]\n"
"buffer_load_dwordx4 v206, s[44:47], s83 offen offset:2048 lds\n"
"v_mfma_f32_16x16x32_bf16 v[0:3], a[100:103], a[72:75], v[0:3]\n"
"ds_read_b64_tr_b16 a[104:105], v201 offset:0\n"
"v_mfma_f32_16x16x32_bf16 v[32:35], a[100:103], a[76:79], v[32:35]\n"
"v_mfma_f32_16x16x32_bf16 v[32:35], a[96:99], a[52:55], v[32:35]\n"
"ds_read_b64_tr_b16 a[106:107], v201 offset:1024\n"
"v_mfma_f32_16x16x32_bf16 v[64:67], a[96:99], a[56:59], v[64:67]\n"
"buffer_load_dwordx4 v207, s[44:47], s83 offen offset:3072 lds\n"
"v_mfma_f32_16x16x32_bf16 v[64:67], a[100:103], a[80:83], v[64:67]\n"
"ds_read_b64_tr_b16 a[108:109], v201 offset:8192\n"
"v_mfma_f32_16x16x32_bf16 v[96:99], a[100:103], a[84:87], v[96:99]\n"
"s_add_u32 s82, s82, 128\n"
"s_cmp_eq_u32 s82, 0x500\n"
"s_cselect_b32 s82, 128, s82\n"
"v_mfma_f32_16x16x32_bf16 v[96:99], a[96:99], a[60:63], v[96:99]\n"
"s_add_u32 m0, s60, 0x22000\n"
"ds_read_b64_tr_b16 a[110:111], v201 offset:9216\n"
"v_add_u32 v201, 0xc000, v201\n"
"v_mfma_f32_16x16x32_bf16 v[128:131], a[96:99], a[64:67], v[128:131]\n"
"buffer_load_dwordx4 v192, s[40:43], s82 offen lds\n"
"v_mfma_f32_16x16x32_bf16 v[128:131], a[100:103], a[88:91], v[128:131]\n"
"ds_read_b64_tr_b16 a[112:113], v202 offset:0\n"
"v_mfma_f32_16x16x32_bf16 v[160:163], a[100:103], a[92:95], v[160:163]\n"
"s_waitcnt lgkmcnt(1)\n"
"v_mfma_f32_16x16x32_bf16 v[160:163], a[96:99], a[68:71], v[160:163]\n"
"ds_read_b64_tr_b16 a[114:115], v202 offset:1024\n"
"v_mfma_f32_16x16x32_bf16 v[4:7], a[104:107], a[48:51], v[4:7]\n"
"buffer_load_dwordx4 v193, s[40:43], s82 offen offset:1024 lds\n"
"v_mfma_f32_16x16x32_bf16 v[4:7], a[108:111], a[72:75], v[4:7]\n"
"ds_read_b64_tr_b16 a[116:117], v202 offset:8192\n"
"v_mfma_f32_16x16x32_bf16 v[36:39], a[108:111], a[76:79], v[36:39]\n"
"v_mfma_f32_16x16x32_bf16 v[36:39], a[104:107], a[52:55], v[36:39]\n"
"ds_read_b64_tr_b16 a[118:119], v202 offset:9216\n"
"v_add_u32 v202, 0xc000, v202\n"
"v_mfma_f32_16x16x32_bf16 v[68:71], a[104:107], a[56:59], v[68:71]\n"
"buffer_load_dwordx4 v194, s[40:43], s82 offen offset:2048 lds\n"
"v_mfma_f32_16x16x32_bf16 v[68:71], a[108:111], a[80:83], v[68:71]\n"
"ds_read_b64_tr_b16 a[120:121], v203 offset:0\n"
"v_mfma_f32_16x16x32_bf16 v[100:103], a[108:111], a[84:87], v[100:103]\n"
"s_add_u32 m0, s63, 0x4000\n"
"v_mfma_f32_16x16x32_bf16 v[100:103], a[104:107], a[60:63], v[100:103]\n"
"s_add_u32 s85, s85, 0x1000\n"
"s_and_b32 s85, s85, 0x1ffff\n"
"ds_read_b64_tr_b16 a[122:123], v203 offset:1024\n"
"v_mfma_f32_16x16x32_bf16 v[132:135], a[104:107], a[64:67], v[132:135]\n"
"buffer_load_dwordx4 v242, s[52:55], s85 offen nt lds\n"
"v_mfma_f32_16x16x32_bf16 v[132:135], a[108:111], a[88:91], v[132:135]\n"
"ds_read_b64_tr_b16 a[124:125], v203 offset:8192\n"
"v_mfma_f32_16x16x32_bf16 v[164:167], a[108:111], a[92:95], v[164:167]\n"
"s_waitcnt lgkmcnt(3)\n"
"v_mfma_f32_16x16x32_bf16 v[164:167], a[104:107], a[68:71], v[164:167]\n"
"ds_read_b64_tr_b16 a[126:127], v203 offset:9216\n"
"v_add_u32 v203, 0xc000, v203\n"
"v_mfma_f32_16x16x32_bf16 v[8:11], a[112:115], a[48:51], v[8:11]\n"
"buffer_load_dwordx4 v242, s[52:55], s85 offen offset:2048 nt lds\n"
"v_mfma_f32_16x16x32_bf16 v[8:11], a[116:119], a[72:75], v[8:11]\n"
"v_mfma_f32_16x16x32_bf16 v[40:43], a[116:119], a[76:79], v[40:43]\n"
"v_mfma_f32_16x16x32_bf16 v[40:43], a[112:115], a[52:55], v[40:43]\n"
"s_add_u32 m0, s63, 0x5000\n"
"v_mfma_f32_16x16x32_bf16 v[72:75], a[112:115], a[56:59], v[72:75]\n"
"s_add_u32 s85, s85, 0x1000\n"
"buffer_load_dwordx4 v242, s[52:55], s85 offen nt lds\n"
"v_mfma_f32_16x16x32_bf16 v[72:75], a[116:119], a[80:83], v[72:75]\n"
"v_mfma_f32_16x16x32_bf16 v[104:107], a[116:119], a[84:87], v[104:107]\n"
"v_mfma_f32_16x16x32_bf16 v[104:107], a[112:115], a[60:63], v[104:107]\n"
"v_mfma_f32_16x16x32_bf16 v[136:139], a[112:115], a[64:67], v[136:139]\n"
"buffer_load_dwordx4 v242, s[52:55], s85 offen offset:2048 nt lds\n"
"v_mfma_f32_16x16x32_bf16 v[136:139], a[116:119], a[88:91], v[136:139]\n"
"v_mfma_f32_16x16x32_bf16 v[168:171], a[116:119], a[92:95], v[168:171]\n"
"s_waitcnt lgkmcnt(0)\n"
"v_mfma_f32_16x16x32_bf16 v[168:171], a[112:115], a[68:71], v[168:171]\n"
"v_mfma_f32_16x16x32_bf16 v[12:15], a[120:123], a[48:51], v[12:15]\n"
"v_mfma_f32_16x16x32_bf16 v[12:15], a[124:127], a[72:75], v[12:15]\n"
"v_mfma_f32_16x16x32_bf16 v[44:47], a[124:127], a[76:79], v[44:47]\n"
"s_waitcnt vmcnt(11)\n"
"s_barrier\n"
"v_mfma_f32_16x16x32_bf16 v[44:47], a[120:123], a[52:55], v[44:47]\n"
"s_add_u32 s65, s83, 128\n"
"s_add_u32 m0, s61, 0xc000\n"
"ds_read_b64_tr_b16 a[96:97], v200 offset:0\n"
"v_mfma_f32_16x16x32_bf16 v[76:79], a[120:123], a[56:59], v[76:79]\n"
"buffer_load_dwordx4 v204, s[44:47], s65 offen lds\n"
"v_mfma_f32_16x16x32_bf16 v[76:79], a[124:127], a[80:83], v[76:79]\n"
"ds_read_b64_tr_b16 a[98:99], v200 offset:1024\n"
"v_mfma_f32_16x16x32_bf16 v[108:111], a[124:127], a[84:87], v[108:111]\n"
"ds_read_b64_tr_b16 a[100:101], v200 offset:8192\n"
"v_mfma_f32_16x16x32_bf16 v[108:111], a[120:123], a[60:63], v[108:111]\n"
"ds_read_b64_tr_b16 a[102:103], v200 offset:9216\n"
"v_add_u32 v200, 0xffff0000, v200\n"
"v_mfma_f32_16x16x32_bf16 v[140:143], a[120:123], a[64:67], v[140:143]\n"
"buffer_load_dwordx4 v205, s[44:47], s65 offen offset:1024 lds\n"
"v_mfma_f32_16x16x32_bf16 v[140:143], a[124:127], a[88:91], v[140:143]\n"
"v_mfma_f32_16x16x32_bf16 v[172:175], a[124:127], a[92:95], v[172:175]\n"
"v_mfma_f32_16x16x32_bf16 v[172:175], a[120:123], a[68:71], v[172:175]\n"
"s_waitcnt lgkmcnt(0)\n"
"v_mfma_f32_16x16x32_bf16 v[16:19], a[96:99], a[48:51], v[16:19]\n"
"buffer_load_dwordx4 v206, s[44:47], s65 offen offset:2048 lds\n"
"v_mfma_f32_16x16x32_bf16 v[16:19], a[100:103], a[72:75], v[16:19]\n"
"ds_read_b64_tr_b16 a[104:105], v201 offset:0\n"
"v_mfma_f32_16x16x32_bf16 v[48:51], a[100:103], a[76:79], v[48:51]\n"
"ds_read_b128 a[0:3], v234 offset:0\n"
"v_mfma_f32_16x16x32_bf16 v[48:51], a[96:99], a[52:55], v[48:51]\n"
"ds_read_b64_tr_b16 a[106:107], v201 offset:1024\n"
"v_mfma_f32_16x16x32_bf16 v[80:83], a[96:99], a[56:59], v[80:83]\n"
"v_mfma_f32_16x16x32_bf16 v[80:83], a[100:103], a[80:83], v[80:83]\n"
"ds_read_b64_tr_b16 a[108:109], v201 offset:8192\n"
"v_mfma_f32_16x16x32_bf16 v[112:115], a[100:103], a[84:87], v[112:115]\n"
"ds_read_b128 a[24:27], v235 offset:0\n"
"v_mfma_f32_16x16x32_bf16 v[112:115], a[96:99], a[60:63], v[112:115]\n"
"ds_read_b64_tr_b16 a[110:111], v201 offset:9216\n"
"v_add_u32 v201, 0xffff0000, v201\n"
"v_mfma_f32_16x16x32_bf16 v[144:147], a[96:99], a[64:67], v[144:147]\n"
"buffer_load_dwordx4 v207, s[44:47], s65 offen offset:3072 lds\n"
"v_mfma_f32_16x16x32_bf16 v[144:147], a[100:103], a[88:91], v[144:147]\n"
"ds_read_b64_tr_b16 a[112:113], v202 offset:0\n"
"v_mfma_f32_16x16x32_bf16 v[176:179], a[100:103], a[92:95], v[176:179]\n"
"s_waitcnt lgkmcnt(1)\n"
"ds_read_b128 a[4:7], v234 offset:2048\n"
"v_mfma_f32_16x16x32_bf16 v[176:179], a[96:99], a[68:71], v[176:179]\n"
"ds_read_b64_tr_b16 a[114:115], v202 offset:1024\n"
"v_mfma_f32_16x16x32_bf16 v[20:23], a[104:107], a[48:51], v[20:23]\n"
"v_mfma_f32_16x16x32_bf16 v[20:23], a[108:111], a[72:75], v[20:23]\n"
"ds_read_b64_tr_b16 a[116:117], v202 offset:8192\n"
"v_mfma_f32_16x16x32_bf16 v[52:55], a[108:111], a[76:79], v[52:55]\n"
"ds_read_b128 a[28:31], v235 offset:2048\n"
"v_mfma_f32_16x16x32_bf16 v[52:55], a[104:107], a[52:55], v[52:55]\n"
"s_add_u32 m0, s60, 0x22c00\n"
"ds_read_b64_tr_b16 a[118:119], v202 offset:9216\n"
"v_add_u32 v202, 0xffff0000, v202\n"
"v_mfma_f32_16x16x32_bf16 v[84:87], a[104:107], a[56:59], v[84:87]\n"
"buffer_load_dwordx4 v195, s[40:43], s82 offen lds\n"
"v_mfma_f32_16x16x32_bf16 v[84:87], a[108:111], a[80:83], v[84:87]\n"
"ds_read_b64_tr_b16 a[120:121], v203 offset:0\n"
"v_mfma_f32_16x16x32_bf16 v[116:119], a[108:111], a[84:87], v[116:119]\n"
"ds_read_b128 a[8:11], v234 offset:4096\n"
"v_mfma_f32_16x16x32_bf16 v[116:119], a[104:107], a[60:63], v[116:119]\n"
"ds_read_b64_tr_b16 a[122:123], v203 offset:1024\n"
"v_mfma_f32_16x16x32_bf16 v[148:151], a[104:107], a[64:67], v[148:151]\n"
"v_mfma_f32_16x16x32_bf16 v[148:151], a[108:111], a[88:91], v[148:151]\n"
"ds_read_b64_tr_b16 a[124:125], v203 offset:8192\n"
"v_mfma_f32_16x16x32_bf16 v[180:183], a[108:111], a[92:95], v[180:183]\n"
"s_waitcnt lgkmcnt(4)\n"
"ds_read_b128 a[32:35], v235 offset:4096\n"
"v_mfma_f32_16x16x32_bf16 v[180:183], a[104:107], a[68:71], v[180:183]\n"
"ds_read_b64_tr_b16 a[126:127], v203 offset:9216\n"
"v_add_u32 v203, 0xffff0000, v203\n"
"v_mfma_f32_16x16x32_bf16 v[24:27], a[112:115], a[48:51], v[24:27]\n"
"buffer_load_dwordx4 v196, s[40:43], s82 offen offset:1024 lds\n"
"v_mfma_f32_16x16x32_bf16 v[24:27], a[116:119], a[72:75], v[24:27]\n"
"ds_read_b128 a[12:15], v234 offset:6144\n"
"v_mfma_f32_16x16x32_bf16 v[56:59], a[116:119], a[76:79], v[56:59]\n"
"ds_read_b128 a[36:39], v235 offset:6144\n"
"v_mfma_f32_16x16x32_bf16 v[56:59], a[112:115], a[52:55], v[56:59]\n"
"ds_read_b128 a[16:19], v234 offset:8192\n"
"v_mfma_f32_16x16x32_bf16 v[88:91], a[112:115], a[56:59], v[88:91]\n"
"v_mfma_f32_16x16x32_bf16 v[88:91], a[116:119], a[80:83], v[88:91]\n"
"ds_read_b128 a[40:43], v235 offset:8192\n"
"v_mfma_f32_16x16x32_bf16 v[120:123], a[116:119], a[84:87], v[120:123]\n"
"ds_read_b128 a[20:23], v234 offset:10240\n"
"v_mfma_f32_16x16x32_bf16 v[120:123], a[112:115], a[60:63], v[120:123]\n"
"ds_read_b128 a[44:47], v235 offset:10240\n"
"v_mfma_f32_16x16x32_bf16 v[152:155], a[112:115], a[64:67], v[152:155]\n"
"buffer_load_dwordx4 v197, s[40:43], s82 offen offset:2048 lds\n"
"v_mfma_f32_16x16x32_bf16 v[152:155], a[116:119], a[88:91], v[152:155]\n"
"v_mfma_f32_16x16x32_bf16 v[184:187], a[116:119], a[92:95], v[184:187]\n"
"s_waitcnt lgkmcnt(6)\n"
"v_mfma_f32_16x16x32_bf16 v[184:187], a[112:115], a[68:71], v[184:187]\n"
"v_mfma_f32_16x16x32_bf16 v[28:31], a[120:123], a[48:51], v[28:31]\n"
"v_mfma_f32_16x16x32_bf16 v[28:31], a[124:127], a[72:75], v[28:31]\n"
"v_mfma_f32_16x16x32_bf16 v[60:63], a[124:127], a[76:79], v[60:63]\n"
"s_waitcnt vmcnt(14) lgkmcnt(0)\n"
"s_barrier\n"
"v_mfma_f32_16x16x32_bf16 v[60:63], a[120:123], a[52:55], v[60:63]\n"
"s_add_u32 s83, s83, 0x80000\n"
"s_cmp_eq_u32 s83, 0x500000\n"
"s_cselect_b32 s83, 0x80000, s83\n"
"s_add_u32 m0, s61, 0x18000\n"
"ds_read_b64_tr_b16 a[96:97], v200 offset:0\n"
"v_mfma_f32_16x16x32_bf16 v[92:95], a[120:123], a[56:59], v[92:95]\n"
"buffer_load_dwordx4 v204, s[44:47], s83 offen lds\n"
"v_mfma_f32_16x16x32_bf16 v[92:95], a[124:127], a[80:83], v[92:95]\n"
"ds_read_b64_tr_b16 a[98:99], v200 offset:1024\n"
"v_mfma_f32_16x16x32_bf16 v[124:127], a[124:127], a[84:87], v[124:127]\n"
"ds_read_b64_tr_b16 a[100:101], v200 offset:8192\n"
"v_mfma_f32_16x16x32_bf16 v[124:127], a[120:123], a[60:63], v[124:127]\n"
"ds_read_b64_tr_b16 a[102:103], v200 offset:9216\n"
"v_add_u32 v200, 0x4000, v200\n"
"v_mfma_f32_16x16x32_bf16 v[156:159], a[120:123], a[64:67], v[156:159]\n"
"buffer_load_dwordx4 v205, s[44:47], s83 offen offset:1024 lds\n"
"v_mfma_f32_16x16x32_bf16 v[156:159], a[124:127], a[88:91], v[156:159]\n"
"v_mfma_f32_16x16x32_bf16 v[188:191], a[124:127], a[92:95], v[188:191]\n"
"v_mfma_f32_16x16x32_bf16 v[188:191], a[120:123], a[68:71], v[188:191]\n"
"s_waitcnt lgkmcnt(0)\n"
"v_mfma_f32_16x16x32_bf16 v[0:3], a[96:99], a[0:3], v[0:3]\n"
"buffer_load_dwordx4 v206, s[44:47], s83 offen offset:2048 lds\n"
"v_mfma_f32_16x16x32_bf16 v[0:3], a[100:103], a[24:27], v[0:3]\n"
"ds_read_b64_tr_b16 a[104:105], v201 offset:0\n"
"v_mfma_f32_16x16x32_bf16 v[32:35], a[100:103], a[28:31], v[32:35]\n"
"ds_read_b64 a[128:129], v222 offset:16384\n"
"v_mfma_f32_16x16x32_bf16 v[32:35], a[96:99], a[4:7], v[32:35]\n"
"ds_read_b64_tr_b16 a[106:107], v201 offset:1024\n"
"v_mfma_f32_16x16x32_bf16 v[64:67], a[96:99], a[8:11], v[64:67]\n"
"v_mfma_f32_16x16x32_bf16 v[64:67], a[100:103], a[32:35], v[64:67]\n"
"ds_read_b64_tr_b16 a[108:109], v201 offset:8192\n"
"v_mfma_f32_16x16x32_bf16 v[96:99], a[100:103], a[36:39], v[96:99]\n"
"ds_read_b64 a[130:131], v223 offset:16384\n"
"v_mfma_f32_16x16x32_bf16 v[96:99], a[96:99], a[12:15], v[96:99]\n"
"ds_read_b64_tr_b16 a[110:111], v201 offset:9216\n"
"v_add_u32 v201, 0x4000, v201\n"
"v_mfma_f32_16x16x32_bf16 v[128:131], a[96:99], a[16:19], v[128:131]\n"
"buffer_load_dwordx4 v207, s[44:47], s83 offen offset:3072 lds\n"
"v_mfma_f32_16x16x32_bf16 v[128:131], a[100:103], a[40:43], v[128:131]\n"
"ds_read_b64_tr_b16 a[112:113], v202 offset:0\n"
"v_mfma_f32_16x16x32_bf16 v[160:163], a[100:103], a[44:47], v[160:163]\n"
"s_waitcnt lgkmcnt(1)\n"
"ds_read_b64 a[132:133], v224 offset:16384\n"
"v_mfma_f32_16x16x32_bf16 v[160:163], a[96:99], a[20:23], v[160:163]\n"
"ds_read_b64_tr_b16 a[114:115], v202 offset:1024\n"
"v_mfma_f32_16x16x32_bf16 v[4:7], a[104:107], a[0:3], v[4:7]\n"
"s_add_u32 s82, s82, 128\n"
"s_cmp_eq_u32 s82, 0x500\n"
"s_cselect_b32 s82, 128, s82\n"
"v_mfma_f32_16x16x32_bf16 v[4:7], a[108:111], a[24:27], v[4:7]\n"
"ds_read_b64_tr_b16 a[116:117], v202 offset:8192\n"
"v_mfma_f32_16x16x32_bf16 v[36:39], a[108:111], a[28:31], v[36:39]\n"
"ds_read_b64 a[134:135], v225 offset:16384\n"
"v_mfma_f32_16x16x32_bf16 v[36:39], a[104:107], a[4:7], v[36:39]\n"
"s_add_u32 m0, s60, 0x1c000\n"
"ds_read_b64_tr_b16 a[118:119], v202 offset:9216\n"
"v_add_u32 v202, 0x4000, v202\n"
"v_mfma_f32_16x16x32_bf16 v[68:71], a[104:107], a[8:11], v[68:71]\n"
"buffer_load_dwordx4 v192, s[40:43], s82 offen lds\n"
"v_mfma_f32_16x16x32_bf16 v[68:71], a[108:111], a[32:35], v[68:71]\n"
"ds_read_b64_tr_b16 a[120:121], v203 offset:0\n"
"v_mfma_f32_16x16x32_bf16 v[100:103], a[108:111], a[36:39], v[100:103]\n"
"ds_read_b64 a[136:137], v246 offset:16384\n"
"v_mfma_f32_16x16x32_bf16 v[100:103], a[104:107], a[12:15], v[100:103]\n"
"ds_read_b64_tr_b16 a[122:123], v203 offset:1024\n"
"v_mfma_f32_16x16x32_bf16 v[132:135], a[104:107], a[16:19], v[132:135]\n"
"v_mfma_f32_16x16x32_bf16 v[132:135], a[108:111], a[40:43], v[132:135]\n"
"ds_read_b64_tr_b16 a[124:125], v203 offset:8192\n"
"v_mfma_f32_16x16x32_bf16 v[164:167], a[108:111], a[44:47], v[164:167]\n"
"s_waitcnt lgkmcnt(4)\n"
"ds_read_b64 a[138:139], v247 offset:16384\n"
"v_mfma_f32_16x16x32_bf16 v[164:167], a[104:107], a[20:23], v[164:167]\n"
"ds_read_b64_tr_b16 a[126:127], v203 offset:9216\n"
"v_add_u32 v203, 0x4000, v203\n"
"v_mfma_f32_16x16x32_bf16 v[8:11], a[112:115], a[0:3], v[8:11]\n"
"buffer_load_dwordx4 v193, s[40:43], s82 offen offset:1024 lds\n"
"v_mfma_f32_16x16x32_bf16 v[8:11], a[116:119], a[24:27], v[8:11]\n"
"ds_read_b64 a[140:141], v248 offset:16384\n"
"v_mfma_f32_16x16x32_bf16 v[40:43], a[116:119], a[28:31], v[40:43]\n"
"ds_read_b64 a[142:143], v249 offset:16384\n"
"v_mfma_f32_16x16x32_bf16 v[40:43], a[112:115], a[4:7], v[40:43]\n"
"ds_read_b64 a[144:145], v222 offset:20480\n"
"v_mfma_f32_16x16x32_bf16 v[72:75], a[112:115], a[8:11], v[72:75]\n"
"v_mfma_f32_16x16x32_bf16 v[72:75], a[116:119], a[32:35], v[72:75]\n"
"ds_read_b64 a[146:147], v223 offset:20480\n"
"v_mfma_f32_16x16x32_bf16 v[104:107], a[116:119], a[36:39], v[104:107]\n"
"ds_read_b64 a[148:149], v224 offset:20480\n"
"v_mfma_f32_16x16x32_bf16 v[104:107], a[112:115], a[12:15], v[104:107]\n"
"ds_read_b64 a[150:151], v225 offset:20480\n"
"v_mfma_f32_16x16x32_bf16 v[136:139], a[112:115], a[16:19], v[136:139]\n"
"buffer_load_dwordx4 v194, s[40:43], s82 offen offset:2048 lds\n"
"v_mfma_f32_16x16x32_bf16 v[136:139], a[116:119], a[40:43], v[136:139]\n"
"ds_read_b64 a[152:153], v246 offset:20480\n"
"v_mfma_f32_16x16x32_bf16 v[168:171], a[116:119], a[44:47], v[168:171]\n"
"s_waitcnt lgkmcnt(7)\n"
"ds_read_b64 a[154:155], v247 offset:20480\n"
"v_mfma_f32_16x16x32_bf16 v[168:171], a[112:115], a[20:23], v[168:171]\n"
"ds_read_b64 a[156:157], v248 offset:20480\n"
"v_mfma_f32_16x16x32_bf16 v[12:15], a[120:123], a[0:3], v[12:15]\n"
"v_mfma_f32_16x16x32_bf16 v[12:15], a[124:127], a[24:27], v[12:15]\n"
"ds_read_b64 a[158:159], v249 offset:20480\n"
"v_mfma_f32_16x16x32_bf16 v[44:47], a[124:127], a[28:31], v[44:47]\n"
"s_waitcnt vmcnt(7)\n"
"s_barrier\n"
"v_mfma_f32_16x16x32_bf16 v[44:47], a[120:123], a[4:7], v[44:47]\n"
"s_add_u32 s65, s83, 128\n"
"s_add_u32 m0, s61, 0x8000\n"
"ds_read_b64_tr_b16 a[96:97], v200 offset:0\n"
"v_mfma_f32_16x16x32_bf16 v[76:79], a[120:123], a[8:11], v[76:79]\n"
"buffer_load_dwordx4 v204, s[44:47], s65 offen lds\n"
"v_mfma_f32_16x16x32_bf16 v[76:79], a[124:127], a[32:35], v[76:79]\n"
"ds_read_b64_tr_b16 a[98:99], v200 offset:1024\n"
"v_mfma_f32_16x16x32_bf16 v[108:111], a[124:127], a[36:39], v[108:111]\n"
"ds_read_b64_tr_b16 a[100:101], v200 offset:8192\n"
"v_mfma_f32_16x16x32_bf16 v[108:111], a[120:123], a[12:15], v[108:111]\n"
"ds_read_b64_tr_b16 a[102:103], v200 offset:9216\n"
"v_add_u32 v200, 0xc000, v200\n"
"v_mfma_f32_16x16x32_bf16 v[140:143], a[120:123], a[16:19], v[140:143]\n"
"buffer_load_dwordx4 v205, s[44:47], s65 offen offset:1024 lds\n"
"v_mfma_f32_16x16x32_bf16 v[140:143], a[124:127], a[40:43], v[140:143]\n"
"v_mfma_f32_16x16x32_bf16 v[172:175], a[124:127], a[44:47], v[172:175]\n"
"v_mfma_f32_16x16x32_bf16 v[172:175], a[120:123], a[20:23], v[172:175]\n"
"s_waitcnt lgkmcnt(0)\n"
"v_mfma_f32_16x16x32_bf16 v[16:19], a[96:99], a[0:3], v[16:19]\n"
"buffer_load_dwordx4 v206, s[44:47], s65 offen offset:2048 lds\n"
"v_mfma_f32_16x16x32_bf16 v[16:19], a[100:103], a[24:27], v[16:19]\n"
"ds_read_b64_tr_b16 a[104:105], v201 offset:0\n"
"v_mfma_f32_16x16x32_bf16 v[48:51], a[100:103], a[28:31], v[48:51]\n"
"ds_read_b128 a[48:51], v234 offset:24576\n"
"v_mfma_f32_16x16x32_bf16 v[48:51], a[96:99], a[4:7], v[48:51]\n"
"ds_read_b64_tr_b16 a[106:107], v201 offset:1024\n"
"v_mfma_f32_16x16x32_bf16 v[80:83], a[96:99], a[8:11], v[80:83]\n"
"buffer_load_dwordx4 v207, s[44:47], s65 offen offset:3072 lds\n"
"v_mfma_f32_16x16x32_bf16 v[80:83], a[100:103], a[32:35], v[80:83]\n"
"ds_read_b64_tr_b16 a[108:109], v201 offset:8192\n"
"v_mfma_f32_16x16x32_bf16 v[112:115], a[100:103], a[36:39], v[112:115]\n"
"ds_read_b128 a[72:75], v235 offset:24576\n"
"v_mfma_f32_16x16x32_bf16 v[112:115], a[96:99], a[12:15], v[112:115]\n"
"s_add_u32 m0, s60, 0x1cc00\n"
"ds_read_b64_tr_b16 a[110:111], v201 offset:9216\n"
"v_add_u32 v201, 0xc000, v201\n"
"v_mfma_f32_16x16x32_bf16 v[144:147], a[96:99], a[16:19], v[144:147]\n"
"buffer_load_dwordx4 v195, s[40:43], s82 offen lds\n"
"v_mfma_f32_16x16x32_bf16 v[144:147], a[100:103], a[40:43], v[144:147]\n"
"ds_read_b64_tr_b16 a[112:113], v202 offset:0\n"
"v_mfma_f32_16x16x32_bf16 v[176:179], a[100:103], a[44:47], v[176:179]\n"
"s_waitcnt lgkmcnt(1)\n"
"ds_read_b128 a[52:55], v234 offset:26624\n"
"v_mfma_f32_16x16x32_bf16 v[176:179], a[96:99], a[20:23], v[176:179]\n"
"ds_read_b64_tr_b16 a[114:115], v202 offset:1024\n"
"v_mfma_f32_16x16x32_bf16 v[20:23], a[104:107], a[0:3], v[20:23]\n"
"buffer_load_dwordx4 v196, s[40:43], s82 offen offset:1024 lds\n"
"v_mfma_f32_16x16x32_bf16 v[20:23], a[108:111], a[24:27], v[20:23]\n"
"ds_read_b64_tr_b16 a[116:117], v202 offset:8192\n"
"v_mfma_f32_16x16x32_bf16 v[52:55], a[108:111], a[28:31], v[52:55]\n"
"ds_read_b128 a[76:79], v235 offset:26624\n"
"v_mfma_f32_16x16x32_bf16 v[52:55], a[104:107], a[4:7], v[52:55]\n"
"ds_read_b64_tr_b16 a[118:119], v202 offset:9216\n"
"v_add_u32 v202, 0xc000, v202\n"
"v_mfma_f32_16x16x32_bf16 v[84:87], a[104:107], a[8:11], v[84:87]\n"
"buffer_load_dwordx4 v197, s[40:43], s82 offen offset:2048 lds\n"
"v_mfma_f32_16x16x32_bf16 v[84:87], a[108:111], a[32:35], v[84:87]\n"
"ds_read_b64_tr_b16 a[120:121], v203 offset:0\n"
"v_mfma_f32_16x16x32_bf16 v[116:119], a[108:111], a[36:39], v[116:119]\n"
"s_add_u32 m0, s62, 0x0\n"
"ds_read_b128 a[56:59], v234 offset:28672\n"
"v_mfma_f32_16x16x32_bf16 v[116:119], a[104:107], a[12:15], v[116:119]\n"
"s_add_u32 s84, s84, 0x5000\n"
"s_and_b32 s84, s84, 0x1ffff\n"
"ds_read_b64_tr_b16 a[122:123], v203 offset:1024\n"
"v_mfma_f32_16x16x32_bf16 v[148:151], a[104:107], a[16:19], v[148:151]\n"
"buffer_load_dwordx4 v240, s[48:51], s84 offen nt lds\n"
"v_mfma_f32_16x16x32_bf16 v[148:151], a[108:111], a[40:43], v[148:151]\n"
"ds_read_b64_tr_b16 a[124:125], v203 offset:8192\n"
"v_mfma_f32_16x16x32_bf16 v[180:183], a[108:111], a[44:47], v[180:183]\n"
"s_waitcnt lgkmcnt(4)\n"
"ds_read_b128 a[80:83], v235 offset:28672\n"
"v_mfma_f32_16x16x32_bf16 v[180:183], a[104:107], a[20:23], v[180:183]\n"
"ds_read_b64_tr_b16 a[126:127], v203 offset:9216\n"
"v_add_u32 v203, 0xc000, v203\n"
"v_mfma_f32_16x16x32_bf16 v[24:27], a[112:115], a[0:3], v[24:27]\n"
"buffer_load_dwordx4 v241, s[48:51], s84 offen offset:1024 nt lds\n"
"v_mfma_f32_16x16x32_bf16 v[24:27], a[116:119], a[24:27], v[24:27]\n"
"ds_read_b128 a[60:63], v234 offset:30720\n"
"v_mfma_f32_16x16x32_bf16 v[56:59], a[116:119], a[28:31], v[56:59]\n"
"ds_read_b128 a[84:87], v235 offset:30720\n"
"v_mfma_f32_16x16x32_bf16 v[56:59], a[112:115], a[4:7], v[56:59]\n"
"s_add_u32 m0, s62, 0x1000\n"
"ds_read_b128 a[64:67], v234 offset:32768\n"
"v_mfma_f32_16x16x32_bf16 v[88:91], a[112:115], a[8:11], v[88:91]\n"
"s_add_u32 s84, s84, 0x1000\n"
"buffer_load_dwordx4 v240, s[48:51], s84 offen nt lds\n"
"v_mfma_f32_16x16x32_bf16 v[88:91], a[116:119], a[32:35], v[88:91]\n"
"ds_read_b128 a[88:91], v235 offset:32768\n"
"v_mfma_f32_16x16x32_bf16 v[120:123], a[116:119], a[36:39], v[120:123]\n"
"ds_read_b128 a[68:71], v234 offset:34816\n"
"v_mfma_f32_16x16x32_bf16 v[120:123], a[112:115], a[12:15], v[120:123]\n"
"ds_read_b128 a[92:95], v235 offset:34816\n"
"v_mfma_f32_16x16x32_bf16 v[152:155], a[112:115], a[16:19], v[152:155]\n"
"buffer_load_dwordx4 v241, s[48:51], s84 offen offset:1024 nt lds\n"
"v_mfma_f32_16x16x32_bf16 v[152:155], a[116:119], a[40:43], v[152:155]\n"
"v_mfma_f32_16x16x32_bf16 v[184:187], a[116:119], a[44:47], v[184:187]\n"
"s_waitcnt lgkmcnt(6)\n"
"v_mfma_f32_16x16x32_bf16 v[184:187], a[112:115], a[20:23], v[184:187]\n"
"v_mfma_f32_16x16x32_bf16 v[28:31], a[120:123], a[0:3], v[28:31]\n"
"v_mfma_f32_16x16x32_bf16 v[28:31], a[124:127], a[24:27], v[28:31]\n"
"v_mfma_f32_16x16x32_bf16 v[60:63], a[124:127], a[28:31], v[60:63]\n"
"s_waitcnt vmcnt(14) lgkmcnt(0)\n"
"s_barrier\n"
"v_mfma_f32_16x16x32_bf16 v[60:63], a[120:123], a[4:7], v[60:63]\n"
"s_add_u32 s83, s83, 0x80000\n"
"s_cmp_eq_u32 s83, 0x500000\n"
"s_cselect_b32 s83, 0x80000, s83\n"
"s_add_u32 m0, s61, 0xc000\n"
"ds_read_b64_tr_b16 a[96:97], v200 offset:0\n"
"v_mfma_f32_16x16x32_bf16 v[92:95], a[120:123], a[8:11], v[92:95]\n"
"buffer_load_dwordx4 v204, s[44:47], s83 offen lds\n"
"v_mfma_f32_16x16x32_bf16 v[92:95], a[124:127], a[32:35], v[92:95]\n"
"ds_read_b64_tr_b16 a[98:99], v200 offset:1024\n"
"v_mfma_f32_16x16x32_bf16 v[124:127], a[124:127], a[36:39], v[124:127]\n"
"ds_read_b64_tr_b16 a[100:101], v200 offset:8192\n"
"v_mfma_f32_16x16x32_bf16 v[124:127], a[120:123], a[12:15], v[124:127]\n"
"ds_read_b64_tr_b16 a[102:103], v200 offset:9216\n"
"v_add_u32 v200, 0xffff0000, v200\n"
"v_mfma_f32_16x16x32_bf16 v[156:159], a[120:123], a[16:19], v[156:159]\n"
"buffer_load_dwordx4 v205, s[44:47], s83 offen offset:1024 lds\n"
"v_mfma_f32_16x16x32_bf16 v[156:159], a[124:127], a[40:43], v[156:159]\n"
"v_mfma_f32_16x16x32_bf16 v[188:191], a[124:127], a[44:47], v[188:191]\n"
"v_mfma_f32_16x16x32_bf16 v[188:191], a[120:123], a[20:23], v[188:191]\n"
"s_waitcnt lgkmcnt(0)\n"
"v_mfma_f32_16x16x32_bf16 v[0:3], a[96:99], a[48:51], v[0:3]\n"
"buffer_load_dwordx4 v206, s[44:47], s83 offen offset:2048 lds\n"
"v_mfma_f32_16x16x32_bf16 v[0:3], a[100:103], a[72:75], v[0:3]\n"
"ds_read_b64_tr_b16 a[104:105], v201 offset:0\n"
"v_mfma_f32_16x16x32_bf16 v[32:35], a[100:103], a[76:79], v[32:35]\n"
"v_mfma_f32_16x16x32_bf16 v[32:35], a[96:99], a[52:55], v[32:35]\n"
"ds_read_b64_tr_b16 a[106:107], v201 offset:1024\n"
"v_mfma_f32_16x16x32_bf16 v[64:67], a[96:99], a[56:59], v[64:67]\n"
"v_mfma_f32_16x16x32_bf16 v[64:67], a[100:103], a[80:83], v[64:67]\n"
"ds_read_b64_tr_b16 a[108:109], v201 offset:8192\n"
"v_mfma_f32_16x16x32_bf16 v[96:99], a[100:103], a[84:87], v[96:99]\n"
"v_mfma_f32_16x16x32_bf16 v[96:99], a[96:99], a[60:63], v[96:99]\n"
"ds_read_b64_tr_b16 a[110:111], v201 offset:9216\n"
"v_add_u32 v201, 0xffff0000, v201\n"
"v_mfma_f32_16x16x32_bf16 v[128:131], a[96:99], a[64:67], v[128:131]\n"
"buffer_load_dwordx4 v207, s[44:47], s83 offen offset:3072 lds\n"
"v_mfma_f32_16x16x32_bf16 v[128:131], a[100:103], a[88:91], v[128:131]\n"
"ds_read_b64_tr_b16 a[112:113], v202 offset:0\n"
"v_mfma_f32_16x16x32_bf16 v[160:163], a[100:103], a[92:95], v[160:163]\n"
"s_waitcnt lgkmcnt(1)\n"
"v_mfma_f32_16x16x32_bf16 v[160:163], a[96:99], a[68:71], v[160:163]\n"
"ds_read_b64_tr_b16 a[114:115], v202 offset:1024\n"
"v_mfma_f32_16x16x32_bf16 v[4:7], a[104:107], a[48:51], v[4:7]\n"
"v_mfma_f32_16x16x32_bf16 v[4:7], a[108:111], a[72:75], v[4:7]\n"
"ds_read_b64_tr_b16 a[116:117], v202 offset:8192\n"
"v_mfma_f32_16x16x32_bf16 v[36:39], a[108:111], a[76:79], v[36:39]\n"
"s_add_u32 s82, s82, 128\n"
"s_cmp_eq_u32 s82, 0x500\n"
"s_cselect_b32 s82, 128, s82\n"
"v_mfma_f32_16x16x32_bf16 v[36:39], a[104:107], a[52:55], v[36:39]\n"
"s_add_u32 m0, s60, 0x22000\n"
"ds_read_b64_tr_b16 a[118:119], v202 offset:9216\n"
"v_add_u32 v202, 0xffff0000, v202\n"
"v_mfma_f32_16x16x32_bf16 v[68:71], a[104:107], a[56:59], v[68:71]\n"
"buffer_load_dwordx4 v192, s[40:43], s82 offen lds\n"
"v_mfma_f32_16x16x32_bf16 v[68:71], a[108:111], a[80:83], v[68:71]\n"
"ds_read_b64_tr_b16 a[120:121], v203 offset:0\n"
"v_mfma_f32_16x16x32_bf16 v[100:103], a[108:111], a[84:87], v[100:103]\n"
"v_mfma_f32_16x16x32_bf16 v[100:103], a[104:107], a[60:63], v[100:103]\n"
"ds_read_b64_tr_b16 a[122:123], v203 offset:1024\n"
"v_mfma_f32_16x16x32_bf16 v[132:135], a[104:107], a[64:67], v[132:135]\n"
"v_mfma_f32_16x16x32_bf16 v[132:135], a[108:111], a[88:91], v[132:135]\n"
"ds_read_b64_tr_b16 a[124:125], v203 offset:8192\n"
"v_mfma_f32_16x16x32_bf16 v[164:167], a[108:111], a[92:95], v[164:167]\n"
"s_waitcnt lgkmcnt(3)\n"
"v_mfma_f32_16x16x32_bf16 v[164:167], a[104:107], a[68:71], v[164:167]\n"
"ds_read_b64_tr_b16 a[126:127], v203 offset:9216\n"
"v_add_u32 v203, 0xffff0000, v203\n"
"v_mfma_f32_16x16x32_bf16 v[8:11], a[112:115], a[48:51], v[8:11]\n"
"buffer_load_dwordx4 v193, s[40:43], s82 offen offset:1024 lds\n"
"v_mfma_f32_16x16x32_bf16 v[8:11], a[116:119], a[72:75], v[8:11]\n"
"v_mfma_f32_16x16x32_bf16 v[40:43], a[116:119], a[76:79], v[40:43]\n"
"v_mfma_f32_16x16x32_bf16 v[40:43], a[112:115], a[52:55], v[40:43]\n"
"v_mfma_f32_16x16x32_bf16 v[72:75], a[112:115], a[56:59], v[72:75]\n"
"v_mfma_f32_16x16x32_bf16 v[72:75], a[116:119], a[80:83], v[72:75]\n"
"v_mfma_f32_16x16x32_bf16 v[104:107], a[116:119], a[84:87], v[104:107]\n"
"v_mfma_f32_16x16x32_bf16 v[104:107], a[112:115], a[60:63], v[104:107]\n"
"v_mfma_f32_16x16x32_bf16 v[136:139], a[112:115], a[64:67], v[136:139]\n"
"buffer_load_dwordx4 v194, s[40:43], s82 offen offset:2048 lds\n"
"v_mfma_f32_16x16x32_bf16 v[136:139], a[116:119], a[88:91], v[136:139]\n"
"v_mfma_f32_16x16x32_bf16 v[168:171], a[116:119], a[92:95], v[168:171]\n"
"s_waitcnt lgkmcnt(0)\n"
"v_mfma_f32_16x16x32_bf16 v[168:171], a[112:115], a[68:71], v[168:171]\n"
"v_mfma_f32_16x16x32_bf16 v[12:15], a[120:123], a[48:51], v[12:15]\n"
"v_mfma_f32_16x16x32_bf16 v[12:15], a[124:127], a[72:75], v[12:15]\n"
"v_mfma_f32_16x16x32_bf16 v[44:47], a[124:127], a[76:79], v[44:47]\n"
"s_waitcnt vmcnt(11)\n"
"s_barrier\n"
"v_mfma_f32_16x16x32_bf16 v[44:47], a[120:123], a[52:55], v[44:47]\n"
"s_add_u32 s65, s83, 128\n"
"s_add_u32 m0, s61, 0x18000\n"
"ds_read_b64_tr_b16 a[96:97], v200 offset:0\n"
"v_mfma_f32_16x16x32_bf16 v[76:79], a[120:123], a[56:59], v[76:79]\n"
"buffer_load_dwordx4 v204, s[44:47], s65 offen lds\n"
"v_mfma_f32_16x16x32_bf16 v[76:79], a[124:127], a[80:83], v[76:79]\n"
"ds_read_b64_tr_b16 a[98:99], v200 offset:1024\n"
"v_mfma_f32_16x16x32_bf16 v[108:111], a[124:127], a[84:87], v[108:111]\n"
"ds_read_b64_tr_b16 a[100:101], v200 offset:8192\n"
"v_mfma_f32_16x16x32_bf16 v[108:111], a[120:123], a[60:63], v[108:111]\n"
"ds_read_b64_tr_b16 a[102:103], v200 offset:9216\n"
"v_add_u32 v200, 0x4000, v200\n"
"v_mfma_f32_16x16x32_bf16 v[140:143], a[120:123], a[64:67], v[140:143]\n"
"buffer_load_dwordx4 v205, s[44:47], s65 offen offset:1024 lds\n"
"v_mfma_f32_16x16x32_bf16 v[140:143], a[124:127], a[88:91], v[140:143]\n"
"v_mfma_f32_16x16x32_bf16 v[172:175], a[124:127], a[92:95], v[172:175]\n"
"v_mfma_f32_16x16x32_bf16 v[172:175], a[120:123], a[68:71], v[172:175]\n"
"s_waitcnt lgkmcnt(0)\n"
"v_mfma_f32_16x16x32_bf16 v[16:19], a[96:99], a[48:51], v[16:19]\n"
"buffer_load_dwordx4 v206, s[44:47], s65 offen offset:2048 lds\n"
"v_mfma_f32_16x16x32_bf16 v[16:19], a[100:103], a[72:75], v[16:19]\n"
"ds_read_b64_tr_b16 a[104:105], v201 offset:0\n"
"v_mfma_f32_16x16x32_bf16 v[48:51], a[100:103], a[76:79], v[48:51]\n"
"ds_read_b128 a[0:3], v234 offset:0\n"
"v_mfma_f32_16x16x32_bf16 v[48:51], a[96:99], a[52:55], v[48:51]\n"
"ds_read_b64_tr_b16 a[106:107], v201 offset:1024\n"
"v_mfma_f32_16x16x32_bf16 v[80:83], a[96:99], a[56:59], v[80:83]\n"
"buffer_load_dwordx4 v207, s[44:47], s65 offen offset:3072 lds\n"
"v_mfma_f32_16x16x32_bf16 v[80:83], a[100:103], a[80:83], v[80:83]\n"
"ds_read_b64_tr_b16 a[108:109], v201 offset:8192\n"
"v_mfma_f32_16x16x32_bf16 v[112:115], a[100:103], a[84:87], v[112:115]\n"
"ds_read_b128 a[24:27], v235 offset:0\n"
"v_mfma_f32_16x16x32_bf16 v[112:115], a[96:99], a[60:63], v[112:115]\n"
"s_add_u32 m0, s60, 0x22c00\n"
"ds_read_b64_tr_b16 a[110:111], v201 offset:9216\n"
"v_add_u32 v201, 0x4000, v201\n"
"v_mfma_f32_16x16x32_bf16 v[144:147], a[96:99], a[64:67], v[144:147]\n"
"buffer_load_dwordx4 v195, s[40:43], s82 offen lds\n"
"v_mfma_f32_16x16x32_bf16 v[144:147], a[100:103], a[88:91], v[144:147]\n"
"ds_read_b64_tr_b16 a[112:113], v202 offset:0\n"
"v_mfma_f32_16x16x32_bf16 v[176:179], a[100:103], a[92:95], v[176:179]\n"
"s_waitcnt lgkmcnt(1)\n"
"ds_read_b128 a[4:7], v234 offset:2048\n"
"v_mfma_f32_16x16x32_bf16 v[176:179], a[96:99], a[68:71], v[176:179]\n"
"ds_read_b64_tr_b16 a[114:115], v202 offset:1024\n"
"v_mfma_f32_16x16x32_bf16 v[20:23], a[104:107], a[48:51], v[20:23]\n"
"buffer_load_dwordx4 v196, s[40:43], s82 offen offset:1024 lds\n"
"v_mfma_f32_16x16x32_bf16 v[20:23], a[108:111], a[72:75], v[20:23]\n"
"ds_read_b64_tr_b16 a[116:117], v202 offset:8192\n"
"v_mfma_f32_16x16x32_bf16 v[52:55], a[108:111], a[76:79], v[52:55]\n"
"ds_read_b128 a[28:31], v235 offset:2048\n"
"v_mfma_f32_16x16x32_bf16 v[52:55], a[104:107], a[52:55], v[52:55]\n"
"ds_read_b64_tr_b16 a[118:119], v202 offset:9216\n"
"v_add_u32 v202, 0x4000, v202\n"
"v_mfma_f32_16x16x32_bf16 v[84:87], a[104:107], a[56:59], v[84:87]\n"
"buffer_load_dwordx4 v197, s[40:43], s82 offen offset:2048 lds\n"
"v_mfma_f32_16x16x32_bf16 v[84:87], a[108:111], a[80:83], v[84:87]\n"
"ds_read_b64_tr_b16 a[120:121], v203 offset:0\n"
"v_mfma_f32_16x16x32_bf16 v[116:119], a[108:111], a[84:87], v[116:119]\n"
"s_add_u32 m0, s63, 0x0\n"
"ds_read_b128 a[8:11], v234 offset:4096\n"
"v_mfma_f32_16x16x32_bf16 v[116:119], a[104:107], a[60:63], v[116:119]\n"
"s_add_u32 s85, s85, 0x5000\n"
"s_and_b32 s85, s85, 0x1ffff\n"
"ds_read_b64_tr_b16 a[122:123], v203 offset:1024\n"
"v_mfma_f32_16x16x32_bf16 v[148:151], a[104:107], a[64:67], v[148:151]\n"
"buffer_load_dwordx4 v242, s[52:55], s85 offen nt lds\n"
"v_mfma_f32_16x16x32_bf16 v[148:151], a[108:111], a[88:91], v[148:151]\n"
"ds_read_b64_tr_b16 a[124:125], v203 offset:8192\n"
"v_mfma_f32_16x16x32_bf16 v[180:183], a[108:111], a[92:95], v[180:183]\n"
"s_waitcnt lgkmcnt(4)\n"
"ds_read_b128 a[32:35], v235 offset:4096\n"
"v_mfma_f32_16x16x32_bf16 v[180:183], a[104:107], a[68:71], v[180:183]\n"
"ds_read_b64_tr_b16 a[126:127], v203 offset:9216\n"
"v_add_u32 v203, 0x4000, v203\n"
"v_mfma_f32_16x16x32_bf16 v[24:27], a[112:115], a[48:51], v[24:27]\n"
"buffer_load_dwordx4 v242, s[52:55], s85 offen offset:2048 nt lds\n"
"v_mfma_f32_16x16x32_bf16 v[24:27], a[116:119], a[72:75], v[24:27]\n"
"ds_read_b128 a[12:15], v234 offset:6144\n"
"v_mfma_f32_16x16x32_bf16 v[56:59], a[116:119], a[76:79], v[56:59]\n"
"ds_read_b128 a[36:39], v235 offset:6144\n"
"v_mfma_f32_16x16x32_bf16 v[56:59], a[112:115], a[52:55], v[56:59]\n"
"s_add_u32 m0, s63, 0x1000\n"
"ds_read_b128 a[16:19], v234 offset:8192\n"
"v_mfma_f32_16x16x32_bf16 v[88:91], a[112:115], a[56:59], v[88:91]\n"
"s_add_u32 s85, s85, 0x1000\n"
"buffer_load_dwordx4 v242, s[52:55], s85 offen nt lds\n"
"v_mfma_f32_16x16x32_bf16 v[88:91], a[116:119], a[80:83], v[88:91]\n"
"ds_read_b128 a[40:43], v235 offset:8192\n"
"v_mfma_f32_16x16x32_bf16 v[120:123], a[116:119], a[84:87], v[120:123]\n"
"ds_read_b128 a[20:23], v234 offset:10240\n"
"v_mfma_f32_16x16x32_bf16 v[120:123], a[112:115], a[60:63], v[120:123]\n"
"ds_read_b128 a[44:47], v235 offset:10240\n"
"v_mfma_f32_16x16x32_bf16 v[152:155], a[112:115], a[64:67], v[152:155]\n"
"buffer_load_dwordx4 v242, s[52:55], s85 offen offset:2048 nt lds\n"
"v_mfma_f32_16x16x32_bf16 v[152:155], a[116:119], a[88:91], v[152:155]\n"
"v_mfma_f32_16x16x32_bf16 v[184:187], a[116:119], a[92:95], v[184:187]\n"
"s_waitcnt lgkmcnt(6)\n"
"v_mfma_f32_16x16x32_bf16 v[184:187], a[112:115], a[68:71], v[184:187]\n"
"v_mfma_f32_16x16x32_bf16 v[28:31], a[120:123], a[48:51], v[28:31]\n"
"v_mfma_f32_16x16x32_bf16 v[28:31], a[124:127], a[72:75], v[28:31]\n"
"v_mfma_f32_16x16x32_bf16 v[60:63], a[124:127], a[76:79], v[60:63]\n"
"s_waitcnt vmcnt(14) lgkmcnt(0)\n"
"s_barrier\n"
"v_mfma_f32_16x16x32_bf16 v[60:63], a[120:123], a[52:55], v[60:63]\n"
"s_add_u32 s83, s83, 0x80000\n"
"s_cmp_eq_u32 s83, 0x500000\n"
"s_cselect_b32 s83, 0x80000, s83\n"
"s_add_u32 m0, s61, 0x8000\n"
"ds_read_b64_tr_b16 a[96:97], v200 offset:0\n"
"v_mfma_f32_16x16x32_bf16 v[92:95], a[120:123], a[56:59], v[92:95]\n"
"buffer_load_dwordx4 v204, s[44:47], s83 offen lds\n"
"v_mfma_f32_16x16x32_bf16 v[92:95], a[124:127], a[80:83], v[92:95]\n"
"ds_read_b64_tr_b16 a[98:99], v200 offset:1024\n"
"v_mfma_f32_16x16x32_bf16 v[124:127], a[124:127], a[84:87], v[124:127]\n"
"ds_read_b64_tr_b16 a[100:101], v200 offset:8192\n"
"v_mfma_f32_16x16x32_bf16 v[124:127], a[120:123], a[60:63], v[124:127]\n"
"ds_read_b64_tr_b16 a[102:103], v200 offset:9216\n"
"v_add_u32 v200, 0xc000, v200\n"
"v_mfma_f32_16x16x32_bf16 v[156:159], a[120:123], a[64:67], v[156:159]\n"
"buffer_load_dwordx4 v205, s[44:47], s83 offen offset:1024 lds\n"
"v_mfma_f32_16x16x32_bf16 v[156:159], a[124:127], a[88:91], v[156:159]\n"
"v_mfma_f32_16x16x32_bf16 v[188:191], a[124:127], a[92:95], v[188:191]\n"
"v_mfma_f32_16x16x32_bf16 v[188:191], a[120:123], a[68:71], v[188:191]\n"
"s_waitcnt lgkmcnt(0)\n"
"v_mfma_f32_16x16x32_bf16 v[0:3], a[96:99], a[0:3], v[0:3]\n"
"buffer_load_dwordx4 v206, s[44:47], s83 offen offset:2048 lds\n"
"v_mfma_f32_16x16x32_bf16 v[0:3], a[100:103], a[24:27], v[0:3]\n"
"ds_read_b64_tr_b16 a[104:105], v201 offset:0\n"
"v_mfma_f32_16x16x32_bf16 v[32:35], a[100:103], a[28:31], v[32:35]\n"
"v_mfma_f32_16x16x32_bf16 v[32:35], a[96:99], a[4:7], v[32:35]\n"
"ds_read_b64_tr_b16 a[106:107], v201 offset:1024\n"
"v_mfma_f32_16x16x32_bf16 v[64:67], a[96:99], a[8:11], v[64:67]\n"
"v_mfma_f32_16x16x32_bf16 v[64:67], a[100:103], a[32:35], v[64:67]\n"
"ds_read_b64_tr_b16 a[108:109], v201 offset:8192\n"
"v_mfma_f32_16x16x32_bf16 v[96:99], a[100:103], a[36:39], v[96:99]\n"
"v_mfma_f32_16x16x32_bf16 v[96:99], a[96:99], a[12:15], v[96:99]\n"
"ds_read_b64_tr_b16 a[110:111], v201 offset:9216\n"
"v_add_u32 v201, 0xc000, v201\n"
"v_mfma_f32_16x16x32_bf16 v[128:131], a[96:99], a[16:19], v[128:131]\n"
"v_mfma_f32_16x16x32_bf16 v[128:131], a[100:103], a[40:43], v[128:131]\n"
"ds_read_b64_tr_b16 a[112:113], v202 offset:0\n"
"v_mfma_f32_16x16x32_bf16 v[160:163], a[100:103], a[44:47], v[160:163]\n"
"s_waitcnt lgkmcnt(1)\n"
"v_mfma_f32_16x16x32_bf16 v[160:163], a[96:99], a[20:23], v[160:163]\n"
"ds_read_b64_tr_b16 a[114:115], v202 offset:1024\n"
"v_mfma_f32_16x16x32_bf16 v[4:7], a[104:107], a[0:3], v[4:7]\n"
"buffer_load_dwordx4 v207, s[44:47], s83 offen offset:3072 lds\n"
"v_mfma_f32_16x16x32_bf16 v[4:7], a[108:111], a[24:27], v[4:7]\n"
"ds_read_b64_tr_b16 a[116:117], v202 offset:8192\n"
"v_mfma_f32_16x16x32_bf16 v[36:39], a[108:111], a[28:31], v[36:39]\n"
"v_mfma_f32_16x16x32_bf16 v[36:39], a[104:107], a[4:7], v[36:39]\n"
"ds_read_b64_tr_b16 a[118:119], v202 offset:9216\n"
"v_add_u32 v202, 0xc000, v202\n"
"v_mfma_f32_16x16x32_bf16 v[68:71], a[104:107], a[8:11], v[68:71]\n"
"v_mfma_f32_16x16x32_bf16 v[68:71], a[108:111], a[32:35], v[68:71]\n"
"ds_read_b64_tr_b16 a[120:121], v203 offset:0\n"
"v_mfma_f32_16x16x32_bf16 v[100:103], a[108:111], a[36:39], v[100:103]\n"
"v_mfma_f32_16x16x32_bf16 v[100:103], a[104:107], a[12:15], v[100:103]\n"
"ds_read_b64_tr_b16 a[122:123], v203 offset:1024\n"
"v_mfma_f32_16x16x32_bf16 v[132:135], a[104:107], a[16:19], v[132:135]\n"
"v_mfma_f32_16x16x32_bf16 v[132:135], a[108:111], a[40:43], v[132:135]\n"
"ds_read_b64_tr_b16 a[124:125], v203 offset:8192\n"
"v_mfma_f32_16x16x32_bf16 v[164:167], a[108:111], a[44:47], v[164:167]\n"
"s_waitcnt lgkmcnt(3)\n"
"v_mfma_f32_16x16x32_bf16 v[164:167], a[104:107], a[20:23], v[164:167]\n"
"ds_read_b64_tr_b16 a[126:127], v203 offset:9216\n"
"v_add_u32 v203, 0xc000, v203\n"
"v_mfma_f32_16x16x32_bf16 v[8:11], a[112:115], a[0:3], v[8:11]\n"
"global_load_dword v254, v250, %[pwm] offset:-2560\n"
"v_mfma_f32_16x16x32_bf16 v[8:11], a[116:119], a[24:27], v[8:11]\n"
"v_mfma_f32_16x16x32_bf16 v[40:43], a[116:119], a[28:31], v[40:43]\n"
"v_mfma_f32_16x16x32_bf16 v[40:43], a[112:115], a[4:7], v[40:43]\n"
"v_mfma_f32_16x16x32_bf16 v[72:75], a[112:115], a[8:11], v[72:75]\n"
"v_mfma_f32_16x16x32_bf16 v[72:75], a[116:119], a[32:35], v[72:75]\n"
"v_mfma_f32_16x16x32_bf16 v[104:107], a[116:119], a[36:39], v[104:107]\n"
"v_mfma_f32_16x16x32_bf16 v[104:107], a[112:115], a[12:15], v[104:107]\n"
"v_mfma_f32_16x16x32_bf16 v[136:139], a[112:115], a[16:19], v[136:139]\n"
"v_mfma_f32_16x16x32_bf16 v[136:139], a[116:119], a[40:43], v[136:139]\n"
"v_mfma_f32_16x16x32_bf16 v[168:171], a[116:119], a[44:47], v[168:171]\n"
"s_waitcnt lgkmcnt(0)\n"
"v_mfma_f32_16x16x32_bf16 v[168:171], a[112:115], a[20:23], v[168:171]\n"
"v_mfma_f32_16x16x32_bf16 v[12:15], a[120:123], a[0:3], v[12:15]\n"
"v_mfma_f32_16x16x32_bf16 v[12:15], a[124:127], a[24:27], v[12:15]\n"
"v_mfma_f32_16x16x32_bf16 v[44:47], a[124:127], a[28:31], v[44:47]\n"
"s_waitcnt vmcnt(9)\n"
"s_barrier\n"
"v_mfma_f32_16x16x32_bf16 v[44:47], a[120:123], a[4:7], v[44:47]\n"
"s_add_u32 s65, s83, 128\n"
"s_add_u32 m0, s61, 0xc000\n"
"ds_read_b64_tr_b16 a[96:97], v200 offset:0\n"
"v_mfma_f32_16x16x32_bf16 v[76:79], a[120:123], a[8:11], v[76:79]\n"
"buffer_load_dwordx4 v204, s[44:47], s65 offen lds\n"
"v_mfma_f32_16x16x32_bf16 v[76:79], a[124:127], a[32:35], v[76:79]\n"
"ds_read_b64_tr_b16 a[98:99], v200 offset:1024\n"
"v_mfma_f32_16x16x32_bf16 v[108:111], a[124:127], a[36:39], v[108:111]\n"
"ds_read_b64_tr_b16 a[100:101], v200 offset:8192\n"
"v_mfma_f32_16x16x32_bf16 v[108:111], a[120:123], a[12:15], v[108:111]\n"
"ds_read_b64_tr_b16 a[102:103], v200 offset:9216\n"
"v_add_u32 v200, 0xffff0000, v200\n"
"v_mfma_f32_16x16x32_bf16 v[140:143], a[120:123], a[16:19], v[140:143]\n"
"buffer_load_dwordx4 v205, s[44:47], s65 offen offset:1024 lds\n"
"v_mfma_f32_16x16x32_bf16 v[140:143], a[124:127], a[40:43], v[140:143]\n"
"v_mfma_f32_16x16x32_bf16 v[172:175], a[124:127], a[44:47], v[172:175]\n"
"v_mfma_f32_16x16x32_bf16 v[172:175], a[120:123], a[20:23], v[172:175]\n"
"s_waitcnt lgkmcnt(0)\n"
"v_mfma_f32_16x16x32_bf16 v[16:19], a[96:99], a[0:3], v[16:19]\n"
"buffer_load_dwordx4 v206, s[44:47], s65 offen offset:2048 lds\n"
"v_mfma_f32_16x16x32_bf16 v[16:19], a[100:103], a[24:27], v[16:19]\n"
"ds_read_b64_tr_b16 a[104:105], v201 offset:0\n"
"v_mfma_f32_16x16x32_bf16 v[48:51], a[100:103], a[28:31], v[48:51]\n"
"ds_read_b128 a[48:51], v234 offset:24576\n"
"v_mfma_f32_16x16x32_bf16 v[48:51], a[96:99], a[4:7], v[48:51]\n"
"ds_read_b64_tr_b16 a[106:107], v201 offset:1024\n"
"v_mfma_f32_16x16x32_bf16 v[80:83], a[96:99], a[8:11], v[80:83]\n"
"buffer_load_dwordx4 v207, s[44:47], s65 offen offset:3072 lds\n"
"v_mfma_f32_16x16x32_bf16 v[80:83], a[100:103], a[32:35], v[80:83]\n"
"s_add_u32 m0, s62, 0x4000\n"
"ds_read_b64_tr_b16 a[108:109], v201 offset:8192\n"
"v_mfma_f32_16x16x32_bf16 v[112:115], a[100:103], a[36:39], v[112:115]\n"
"s_add_u32 s84, s84, 0x1000\n"
"s_and_b32 s84, s84, 0x1ffff\n"
"ds_read_b128 a[72:75], v235 offset:24576\n"
"v_mfma_f32_16x16x32_bf16 v[112:115], a[96:99], a[12:15], v[112:115]\n"
"ds_read_b64_tr_b16 a[110:111], v201 offset:9216\n"
"v_add_u32 v201, 0xffff0000, v201\n"
"v_mfma_f32_16x16x32_bf16 v[144:147], a[96:99], a[16:19], v[144:147]\n"
"buffer_load_dwordx4 v240, s[48:51], s84 offen nt lds\n"
"v_mfma_f32_16x16x32_bf16 v[144:147], a[100:103], a[40:43], v[144:147]\n"
"ds_read_b64_tr_b16 a[112:113], v202 offset:0\n"
"v_mfma_f32_16x16x32_bf16 v[176:179], a[100:103], a[44:47], v[176:179]\n"
"s_waitcnt lgkmcnt(1)\n"
"ds_read_b128 a[52:55], v234 offset:26624\n"
"v_mfma_f32_16x16x32_bf16 v[176:179], a[96:99], a[20:23], v[176:179]\n"
"ds_read_b64_tr_b16 a[114:115], v202 offset:1024\n"
"v_mfma_f32_16x16x32_bf16 v[20:23], a[104:107], a[0:3], v[20:23]\n"
"v_mfma_f32_16x16x32_bf16 v[20:23], a[108:111], a[24:27], v[20:23]\n"
"ds_read_b64_tr_b16 a[116:117], v202 offset:8192\n"
"v_mfma_f32_16x16x32_bf16 v[52:55], a[108:111], a[28:31], v[52:55]\n"
"ds_read_b128 a[76:79], v235 offset:26624\n"
"v_mfma_f32_16x16x32_bf16 v[52:55], a[104:107], a[4:7], v[52:55]\n"
"ds_read_b64_tr_b16 a[118:119], v202 offset:9216\n"
"v_add_u32 v202, 0xffff0000, v202\n"
"v_mfma_f32_16x16x32_bf16 v[84:87], a[104:107], a[8:11], v[84:87]\n"
"buffer_load_dwordx4 v241, s[48:51], s84 offen offset:1024 nt lds\n"
"v_mfma_f32_16x16x32_bf16 v[84:87], a[108:111], a[32:35], v[84:87]\n"
"ds_read_b64_tr_b16 a[120:121], v203 offset:0\n"
"v_mfma_f32_16x16x32_bf16 v[116:119], a[108:111], a[36:39], v[116:119]\n"
"ds_read_b128 a[56:59], v234 offset:28672\n"
"v_mfma_f32_16x16x32_bf16 v[116:119], a[104:107], a[12:15], v[116:119]\n"
"s_add_u32 m0, s62, 0x5000\n"
"ds_read_b64_tr_b16 a[122:123], v203 offset:1024\n"
"v_mfma_f32_16x16x32_bf16 v[148:151], a[104:107], a[16:19], v[148:151]\n"
"s_add_u32 s84, s84, 0x1000\n"
"buffer_load_dwordx4 v240, s[48:51], s84 offen nt lds\n"
"v_mfma_f32_16x16x32_bf16 v[148:151], a[108:111], a[40:43], v[148:151]\n"
"ds_read_b64_tr_b16 a[124:125], v203 offset:8192\n"
"v_mfma_f32_16x16x32_bf16 v[180:183], a[108:111], a[44:47], v[180:183]\n"
"s_waitcnt lgkmcnt(4)\n"
"ds_read_b128 a[80:83], v235 offset:28672\n"
"v_mfma_f32_16x16x32_bf16 v[180:183], a[104:107], a[20:23], v[180:183]\n"
"ds_read_b64_tr_b16 a[126:127], v203 offset:9216\n"
"v_add_u32 v203, 0xffff0000, v203\n"
"v_mfma_f32_16x16x32_bf16 v[24:27], a[112:115], a[0:3], v[24:27]\n"
"v_mfma_f32_16x16x32_bf16 v[24:27], a[116:119], a[24:27], v[24:27]\n"
"ds_read_b128 a[60:63], v234 offset:30720\n"
"v_mfma_f32_16x16x32_bf16 v[56:59], a[116:119], a[28:31], v[56:59]\n"
"ds_read_b128 a[84:87], v235 offset:30720\n"
"v_mfma_f32_16x16x32_bf16 v[56:59], a[112:115], a[4:7], v[56:59]\n"
"ds_read_b128 a[64:67], v234 offset:32768\n"
"v_mfma_f32_16x16x32_bf16 v[88:91], a[112:115], a[8:11], v[88:91]\n"
"buffer_load_dwordx4 v241, s[48:51], s84 offen offset:1024 nt lds\n"
"v_mfma_f32_16x16x32_bf16 v[88:91], a[116:119], a[32:35], v[88:91]\n"
"ds_read_b128 a[88:91], v235 offset:32768\n"
"v_mfma_f32_16x16x32_bf16 v[120:123], a[116:119], a[36:39], v[120:123]\n"
"ds_read_b128 a[68:71], v234 offset:34816\n"
"v_mfma_f32_16x16x32_bf16 v[120:123], a[112:115], a[12:15], v[120:123]\n"
"ds_read_b128 a[92:95], v235 offset:34816\n"
"v_mfma_f32_16x16x32_bf16 v[152:155], a[112:115], a[16:19], v[152:155]\n"
"global_load_dword v254, v250, %[pwm] offset:0\n"
"v_mfma_f32_16x16x32_bf16 v[152:155], a[116:119], a[40:43], v[152:155]\n"
"v_mfma_f32_16x16x32_bf16 v[184:187], a[116:119], a[44:47], v[184:187]\n"
"s_waitcnt lgkmcnt(6)\n"
"v_mfma_f32_16x16x32_bf16 v[184:187], a[112:115], a[20:23], v[184:187]\n"
"v_mfma_f32_16x16x32_bf16 v[28:31], a[120:123], a[0:3], v[28:31]\n"
"v_mfma_f32_16x16x32_bf16 v[28:31], a[124:127], a[24:27], v[28:31]\n"
"v_mfma_f32_16x16x32_bf16 v[60:63], a[124:127], a[28:31], v[60:63]\n"
"s_waitcnt vmcnt(10) lgkmcnt(0)\n"
"s_barrier\n"
"v_mfma_f32_16x16x32_bf16 v[60:63], a[120:123], a[4:7], v[60:63]\n"
"ds_read_b64_tr_b16 a[96:97], v200 offset:0\n"
"v_mfma_f32_16x16x32_bf16 v[92:95], a[120:123], a[8:11], v[92:95]\n"
"v_mfma_f32_16x16x32_bf16 v[92:95], a[124:127], a[32:35], v[92:95]\n"
"ds_read_b64_tr_b16 a[98:99], v200 offset:1024\n"
"v_mfma_f32_16x16x32_bf16 v[124:127], a[124:127], a[36:39], v[124:127]\n"
"ds_read_b64_tr_b16 a[100:101], v200 offset:8192\n"
"v_mfma_f32_16x16x32_bf16 v[124:127], a[120:123], a[12:15], v[124:127]\n"
"ds_read_b64_tr_b16 a[102:103], v200 offset:9216\n"
"v_add_u32 v200, 0x4000, v200\n"
"v_mfma_f32_16x16x32_bf16 v[156:159], a[120:123], a[16:19], v[156:159]\n"
"v_mfma_f32_16x16x32_bf16 v[156:159], a[124:127], a[40:43], v[156:159]\n"
"v_mfma_f32_16x16x32_bf16 v[188:191], a[124:127], a[44:47], v[188:191]\n"
"v_mfma_f32_16x16x32_bf16 v[188:191], a[120:123], a[20:23], v[188:191]\n"
"s_waitcnt lgkmcnt(0)\n"
"v_mfma_f32_16x16x32_bf16 v[0:3], a[96:99], a[48:51], v[0:3]\n"
"global_load_dword v254, v250, %[pwm] offset:2560\n"
"v_mfma_f32_16x16x32_bf16 v[0:3], a[100:103], a[72:75], v[0:3]\n"
"ds_read_b64_tr_b16 a[104:105], v201 offset:0\n"
"v_mfma_f32_16x16x32_bf16 v[32:35], a[100:103], a[76:79], v[32:35]\n"
"v_mfma_f32_16x16x32_bf16 v[32:35], a[96:99], a[52:55], v[32:35]\n"
"ds_read_b64_tr_b16 a[106:107], v201 offset:1024\n"
"v_mfma_f32_16x16x32_bf16 v[64:67], a[96:99], a[56:59], v[64:67]\n"
"v_mfma_f32_16x16x32_bf16 v[64:67], a[100:103], a[80:83], v[64:67]\n"
"ds_read_b64_tr_b16 a[108:109], v201 offset:8192\n"
"v_mfma_f32_16x16x32_bf16 v[96:99], a[100:103], a[84:87], v[96:99]\n"
"v_mfma_f32_16x16x32_bf16 v[96:99], a[96:99], a[60:63], v[96:99]\n"
"ds_read_b64_tr_b16 a[110:111], v201 offset:9216\n"
"v_add_u32 v201, 0x4000, v201\n"
"v_mfma_f32_16x16x32_bf16 v[128:131], a[96:99], a[64:67], v[128:131]\n"
"v_mfma_f32_16x16x32_bf16 v[128:131], a[100:103], a[88:91], v[128:131]\n"
"ds_read_b64_tr_b16 a[112:113], v202 offset:0\n"
"v_mfma_f32_16x16x32_bf16 v[160:163], a[100:103], a[92:95], v[160:163]\n"
"s_waitcnt lgkmcnt(1)\n"
"v_mfma_f32_16x16x32_bf16 v[160:163], a[96:99], a[68:71], v[160:163]\n"
"ds_read_b64_tr_b16 a[114:115], v202 offset:1024\n"
"v_mfma_f32_16x16x32_bf16 v[4:7], a[104:107], a[48:51], v[4:7]\n"
"v_mfma_f32_16x16x32_bf16 v[4:7], a[108:111], a[72:75], v[4:7]\n"
"ds_read_b64_tr_b16 a[116:117], v202 offset:8192\n"
"v_mfma_f32_16x16x32_bf16 v[36:39], a[108:111], a[76:79], v[36:39]\n"
"v_mfma_f32_16x16x32_bf16 v[36:39], a[104:107], a[52:55], v[36:39]\n"
"ds_read_b64_tr_b16 a[118:119], v202 offset:9216\n"
"v_add_u32 v202, 0x4000, v202\n"
"v_mfma_f32_16x16x32_bf16 v[68:71], a[104:107], a[56:59], v[68:71]\n"
"v_mfma_f32_16x16x32_bf16 v[68:71], a[108:111], a[80:83], v[68:71]\n"
"ds_read_b64_tr_b16 a[120:121], v203 offset:0\n"
"v_mul_f32 v0, s69, v0\n"
"v_mfma_f32_16x16x32_bf16 v[100:103], a[108:111], a[84:87], v[100:103]\n"
"v_mul_f32 v1, s69, v1\n"
"v_mul_f32 v2, s69, v2\n"
"v_mfma_f32_16x16x32_bf16 v[100:103], a[104:107], a[60:63], v[100:103]\n"
"ds_read_b64_tr_b16 a[122:123], v203 offset:1024\n"
"v_mul_f32 v3, s69, v3\n"
"v_mfma_f32_16x16x32_bf16 v[132:135], a[104:107], a[64:67], v[132:135]\n"
"v_mul_f32 v4, s69, v4\n"
"v_mul_f32 v5, s69, v5\n"
"v_mfma_f32_16x16x32_bf16 v[132:135], a[108:111], a[88:91], v[132:135]\n"
"ds_read_b64_tr_b16 a[124:125], v203 offset:8192\n"
"v_mul_f32 v6, s69, v6\n"
"v_mfma_f32_16x16x32_bf16 v[164:167], a[108:111], a[92:95], v[164:167]\n"
"s_waitcnt lgkmcnt(3)\n"
"v_mul_f32 v7, s69, v7\n"
"v_cvt_pk_bf16_f32 v0, v0, v1\n"
"v_mfma_f32_16x16x32_bf16 v[164:167], a[104:107], a[68:71], v[164:167]\n"
"ds_read_b64_tr_b16 a[126:127], v203 offset:9216\n"
"v_add_u32 v203, 0x4000, v203\n"
"v_mfma_f32_16x16x32_bf16 v[8:11], a[112:115], a[48:51], v[8:11]\n"
"v_cvt_pk_bf16_f32 v1, v2, v3\n"
"v_cvt_pk_bf16_f32 v2, v4, v5\n"
"v_mfma_f32_16x16x32_bf16 v[8:11], a[116:119], a[72:75], v[8:11]\n"
"v_cvt_pk_bf16_f32 v3, v6, v7\n"
"v_mul_f32 v32, s69, v32\n"
"v_mfma_f32_16x16x32_bf16 v[40:43], a[116:119], a[76:79], v[40:43]\n"
"v_mul_f32 v33, s69, v33\n"
"v_mul_f32 v34, s69, v34\n"
"v_mfma_f32_16x16x32_bf16 v[40:43], a[112:115], a[52:55], v[40:43]\n"
"v_mul_f32 v35, s69, v35\n"
"v_mul_f32 v36, s69, v36\n"
"v_mfma_f32_16x16x32_bf16 v[72:75], a[112:115], a[56:59], v[72:75]\n"
"v_mul_f32 v37, s69, v37\n"
"v_mul_f32 v38, s69, v38\n"
"v_mfma_f32_16x16x32_bf16 v[72:75], a[116:119], a[80:83], v[72:75]\n"
"v_mul_f32 v39, s69, v39\n"
"v_cvt_pk_bf16_f32 v32, v32, v33\n"
"v_mfma_f32_16x16x32_bf16 v[104:107], a[116:119], a[84:87], v[104:107]\n"
"v_cvt_pk_bf16_f32 v33, v34, v35\n"
"v_cvt_pk_bf16_f32 v34, v36, v37\n"
"v_mfma_f32_16x16x32_bf16 v[104:107], a[112:115], a[60:63], v[104:107]\n"
"v_cvt_pk_bf16_f32 v35, v38, v39\n"
"v_mul_f32 v64, s69, v64\n"
"v_mfma_f32_16x16x32_bf16 v[136:139], a[112:115], a[64:67], v[136:139]\n"
"v_mul_f32 v65, s69, v65\n"
"v_mul_f32 v66, s69, v66\n"
"v_mfma_f32_16x16x32_bf16 v[136:139], a[116:119], a[88:91], v[136:139]\n"
"v_mul_f32 v67, s69, v67\n"
"v_mul_f32 v68, s69, v68\n"
"v_mfma_f32_16x16x32_bf16 v[168:171], a[116:119], a[92:95], v[168:171]\n"
"s_waitcnt lgkmcnt(0)\n"
"v_mul_f32 v69, s69, v69\n"
"v_mul_f32 v70, s69, v70\n"
"v_mfma_f32_16x16x32_bf16 v[168:171], a[112:115], a[68:71], v[168:171]\n"
"v_mul_f32 v71, s69, v71\n"
"v_cvt_pk_bf16_f32 v64, v64, v65\n"
"v_mfma_f32_16x16x32_bf16 v[12:15], a[120:123], a[48:51], v[12:15]\n"
"v_cvt_pk_bf16_f32 v65, v66, v67\n"
"v_cvt_pk_bf16_f32 v66, v68, v69\n"
"v_mfma_f32_16x16x32_bf16 v[12:15], a[124:127], a[72:75], v[12:15]\n"
"v_cvt_pk_bf16_f32 v67, v70, v71\n"
"v_mul_f32 v96, s69, v96\n"
"v_mfma_f32_16x16x32_bf16 v[44:47], a[124:127], a[76:79], v[44:47]\n"
"s_waitcnt vmcnt(6)\n"
"s_barrier\n"
"v_mul_f32 v97, s69, v97\n"
"v_mul_f32 v98, s69, v98\n"
"v_mfma_f32_16x16x32_bf16 v[44:47], a[120:123], a[52:55], v[44:47]\n"
"ds_read_b64_tr_b16 a[96:97], v200 offset:0\n"
"v_mul_f32 v99, s69, v99\n"
"v_mfma_f32_16x16x32_bf16 v[76:79], a[120:123], a[56:59], v[76:79]\n"
"v_mul_f32 v100, s69, v100\n"
"v_mul_f32 v101, s69, v101\n"
"v_mfma_f32_16x16x32_bf16 v[76:79], a[124:127], a[80:83], v[76:79]\n"
"ds_read_b64_tr_b16 a[98:99], v200 offset:1024\n"
"v_mul_f32 v102, s69, v102\n"
"v_mfma_f32_16x16x32_bf16 v[108:111], a[124:127], a[84:87], v[108:111]\n"
"ds_read_b64_tr_b16 a[100:101], v200 offset:8192\n"
"v_mul_f32 v103, s69, v103\n"
"v_mfma_f32_16x16x32_bf16 v[108:111], a[120:123], a[60:63], v[108:111]\n"
"ds_read_b64_tr_b16 a[102:103], v200 offset:9216\n"
"v_cvt_pk_bf16_f32 v96, v96, v97\n"
"v_mfma_f32_16x16x32_bf16 v[140:143], a[120:123], a[64:67], v[140:143]\n"
"v_cvt_pk_bf16_f32 v97, v98, v99\n"
"v_cvt_pk_bf16_f32 v98, v100, v101\n"
"v_mfma_f32_16x16x32_bf16 v[140:143], a[124:127], a[88:91], v[140:143]\n"
"v_cvt_pk_bf16_f32 v99, v102, v103\n"
"v_mul_f32 v128, s69, v128\n"
"v_mfma_f32_16x16x32_bf16 v[172:175], a[124:127], a[92:95], v[172:175]\n"
"v_mul_f32 v129, s69, v129\n"
"v_mul_f32 v130, s69, v130\n"
"v_mfma_f32_16x16x32_bf16 v[172:175], a[120:123], a[68:71], v[172:175]\n"
"s_waitcnt lgkmcnt(0)\n"
"v_add_u32 v250, 0x1400, v250\n"
"v_mul_f32 v131, s69, v131\n"
"v_mfma_f32_16x16x32_bf16 v[16:19], a[96:99], a[48:51], v[16:19]\n"
"global_load_dword v254, v250, %[pwm] offset:0\n"
"v_mul_f32 v132, s69, v132\n"
"v_mul_f32 v133, s69, v133\n"
"v_mfma_f32_16x16x32_bf16 v[16:19], a[100:103], a[72:75], v[16:19]\n"
"ds_read_b64_tr_b16 a[104:105], v201 offset:0\n"
"v_mul_f32 v134, s69, v134\n"
"v_mfma_f32_16x16x32_bf16 v[48:51], a[100:103], a[76:79], v[48:51]\n"
"v_mul_f32 v135, s69, v135\n"
"v_cvt_pk_bf16_f32 v128, v128, v129\n"
"v_mfma_f32_16x16x32_bf16 v[48:51], a[96:99], a[52:55], v[48:51]\n"
"ds_read_b64_tr_b16 a[106:107], v201 offset:1024\n"
"v_cvt_pk_bf16_f32 v129, v130, v131\n"
"v_mfma_f32_16x16x32_bf16 v[80:83], a[96:99], a[56:59], v[80:83]\n"
"v_cvt_pk_bf16_f32 v130, v132, v133\n"
"v_cvt_pk_bf16_f32 v131, v134, v135\n"
"v_mfma_f32_16x16x32_bf16 v[80:83], a[100:103], a[80:83], v[80:83]\n"
"ds_read_b64_tr_b16 a[108:109], v201 offset:8192\n"
"v_mul_f32 v160, s69, v160\n"
"v_mfma_f32_16x16x32_bf16 v[112:115], a[100:103], a[84:87], v[112:115]\n"
"v_mul_f32 v161, s69, v161\n"
"v_mul_f32 v162, s69, v162\n"
"v_mfma_f32_16x16x32_bf16 v[112:115], a[96:99], a[60:63], v[112:115]\n"
"ds_read_b64_tr_b16 a[110:111], v201 offset:9216\n"
"v_mul_f32 v163, s69, v163\n"
"v_mfma_f32_16x16x32_bf16 v[144:147], a[96:99], a[64:67], v[144:147]\n"
"v_mul_f32 v164, s69, v164\n"
"v_mul_f32 v165, s69, v165\n"
"v_mfma_f32_16x16x32_bf16 v[144:147], a[100:103], a[88:91], v[144:147]\n"
"ds_read_b64_tr_b16 a[112:113], v202 offset:0\n"
"v_mul_f32 v166, s69, v166\n"
"v_mfma_f32_16x16x32_bf16 v[176:179], a[100:103], a[92:95], v[176:179]\n"
"s_waitcnt lgkmcnt(1)\n"
"v_mul_f32 v167, s69, v167\n"
"v_cvt_pk_bf16_f32 v160, v160, v161\n"
"v_mfma_f32_16x16x32_bf16 v[176:179], a[96:99], a[68:71], v[176:179]\n"
"ds_read_b64_tr_b16 a[114:115], v202 offset:1024\n"
"v_cvt_pk_bf16_f32 v161, v162, v163\n"
"v_mfma_f32_16x16x32_bf16 v[20:23], a[104:107], a[48:51], v[20:23]\n"
"v_cvt_pk_bf16_f32 v162, v164, v165\n"
"v_cvt_pk_bf16_f32 v163, v166, v167\n"
"v_mfma_f32_16x16x32_bf16 v[20:23], a[108:111], a[72:75], v[20:23]\n"
"ds_read_b64_tr_b16 a[116:117], v202 offset:8192\n"
"v_mul_f32 v8, s69, v8\n"
"v_mfma_f32_16x16x32_bf16 v[52:55], a[108:111], a[76:79], v[52:55]\n"
"v_mul_f32 v9, s69, v9\n"
"v_mul_f32 v10, s69, v10\n"
"v_mfma_f32_16x16x32_bf16 v[52:55], a[104:107], a[52:55], v[52:55]\n"
"ds_read_b64_tr_b16 a[118:119], v202 offset:9216\n"
"v_mul_f32 v11, s69, v11\n"
"v_mfma_f32_16x16x32_bf16 v[84:87], a[104:107], a[56:59], v[84:87]\n"
"v_mul_f32 v12, s69, v12\n"
"v_mul_f32 v13, s69, v13\n"
"v_mfma_f32_16x16x32_bf16 v[84:87], a[108:111], a[80:83], v[84:87]\n"
"ds_read_b64_tr_b16 a[120:121], v203 offset:0\n"
"v_mul_f32 v14, s69, v14\n"
"v_mfma_f32_16x16x32_bf16 v[116:119], a[108:111], a[84:87], v[116:119]\n"
"v_mul_f32 v15, s69, v15\n"
"v_cvt_pk_bf16_f32 v8, v8, v9\n"
"v_mfma_f32_16x16x32_bf16 v[116:119], a[104:107], a[60:63], v[116:119]\n"
"ds_read_b64_tr_b16 a[122:123], v203 offset:1024\n"
"v_cvt_pk_bf16_f32 v9, v10, v11\n"
"v_mfma_f32_16x16x32_bf16 v[148:151], a[104:107], a[64:67], v[148:151]\n"
"v_cvt_pk_bf16_f32 v10, v12, v13\n"
"v_cvt_pk_bf16_f32 v11, v14, v15\n"
"v_mfma_f32_16x16x32_bf16 v[148:151], a[108:111], a[88:91], v[148:151]\n"
"ds_read_b64_tr_b16 a[124:125], v203 offset:8192\n"
"v_mul_f32 v40, s69, v40\n"
"v_mfma_f32_16x16x32_bf16 v[180:183], a[108:111], a[92:95], v[180:183]\n"
"s_waitcnt lgkmcnt(3)\n"
"v_mul_f32 v41, s69, v41\n"
"v_mul_f32 v42, s69, v42\n"
"v_mfma_f32_16x16x32_bf16 v[180:183], a[104:107], a[68:71], v[180:183]\n"
"ds_read_b64_tr_b16 a[126:127], v203 offset:9216\n"
"v_mul_f32 v43, s69, v43\n"
"v_mfma_f32_16x16x32_bf16 v[24:27], a[112:115], a[48:51], v[24:27]\n"
"v_mul_f32 v44, s69, v44\n"
"v_mul_f32 v45, s69, v45\n"
"v_mfma_f32_16x16x32_bf16 v[24:27], a[116:119], a[72:75], v[24:27]\n"
"v_mul_f32 v46, s69, v46\n"
"v_mul_f32 v47, s69, v47\n"
"v_mfma_f32_16x16x32_bf16 v[56:59], a[116:119], a[76:79], v[56:59]\n"
"v_cvt_pk_bf16_f32 v40, v40, v41\n"
"v_cvt_pk_bf16_f32 v41, v42, v43\n"
"v_mfma_f32_16x16x32_bf16 v[56:59], a[112:115], a[52:55], v[56:59]\n"
"v_cvt_pk_bf16_f32 v42, v44, v45\n"
"v_cvt_pk_bf16_f32 v43, v46, v47\n"
"v_mfma_f32_16x16x32_bf16 v[88:91], a[112:115], a[56:59], v[88:91]\n"
"v_mul_f32 v72, s69, v72\n"
"v_mul_f32 v73, s69, v73\n"
"v_mfma_f32_16x16x32_bf16 v[88:91], a[116:119], a[80:83], v[88:91]\n"
"v_mul_f32 v74, s69, v74\n"
"v_mul_f32 v75, s69, v75\n"
"v_mfma_f32_16x16x32_bf16 v[120:123], a[116:119], a[84:87], v[120:123]\n"
"s_waitcnt lgkmcnt(0)\n"
"s_barrier\n"
"v_mul_f32 v76, s69, v76\n"
"v_mul_f32 v77, s69, v77\n"
"v_mfma_f32_16x16x32_bf16 v[120:123], a[112:115], a[60:63], v[120:123]\n"
"v_mul_f32 v78, s69, v78\n"
"v_mul_f32 v79, s69, v79\n"
"v_mfma_f32_16x16x32_bf16 v[152:155], a[112:115], a[64:67], v[152:155]\n"
"v_cvt_pk_bf16_f32 v72, v72, v73\n"
"v_cvt_pk_bf16_f32 v73, v74, v75\n"
"v_mfma_f32_16x16x32_bf16 v[152:155], a[116:119], a[88:91], v[152:155]\n"
"v_cvt_pk_bf16_f32 v74, v76, v77\n"
"v_cvt_pk_bf16_f32 v75, v78, v79\n"
"v_mfma_f32_16x16x32_bf16 v[184:187], a[116:119], a[92:95], v[184:187]\n"
"v_mul_f32 v104, s69, v104\n"
"v_mul_f32 v105, s69, v105\n"
"v_mfma_f32_16x16x32_bf16 v[184:187], a[112:115], a[68:71], v[184:187]\n"
"v_mul_f32 v106, s69, v106\n"
"v_mul_f32 v107, s69, v107\n"
"v_mfma_f32_16x16x32_bf16 v[28:31], a[120:123], a[48:51], v[28:31]\n"
"v_mul_f32 v108, s69, v108\n"
"v_mul_f32 v109, s69, v109\n"
"v_mfma_f32_16x16x32_bf16 v[28:31], a[124:127], a[72:75], v[28:31]\n"
"v_mul_f32 v110, s69, v110\n"
"v_mul_f32 v111, s69, v111\n"
"v_mfma_f32_16x16x32_bf16 v[60:63], a[124:127], a[76:79], v[60:63]\n"
"v_cvt_pk_bf16_f32 v104, v104, v105\n"
"v_cvt_pk_bf16_f32 v105, v106, v107\n"
"v_mfma_f32_16x16x32_bf16 v[60:63], a[120:123], a[52:55], v[60:63]\n"
"v_cvt_pk_bf16_f32 v106, v108, v109\n"
"v_cvt_pk_bf16_f32 v107, v110, v111\n"
"v_mfma_f32_16x16x32_bf16 v[92:95], a[120:123], a[56:59], v[92:95]\n"
"v_mul_f32 v136, s69, v136\n"
"v_mul_f32 v137, s69, v137\n"
"v_mfma_f32_16x16x32_bf16 v[92:95], a[124:127], a[80:83], v[92:95]\n"
"v_mul_f32 v138, s69, v138\n"
"v_mul_f32 v139, s69, v139\n"
"v_mfma_f32_16x16x32_bf16 v[124:127], a[124:127], a[84:87], v[124:127]\n"
"v_mul_f32 v140, s69, v140\n"
"v_mul_f32 v141, s69, v141\n"
"v_mfma_f32_16x16x32_bf16 v[124:127], a[120:123], a[60:63], v[124:127]\n"
"v_mul_f32 v142, s69, v142\n"
"v_mul_f32 v143, s69, v143\n"
"v_mfma_f32_16x16x32_bf16 v[156:159], a[120:123], a[64:67], v[156:159]\n"
"v_cvt_pk_bf16_f32 v136, v136, v137\n"
"v_cvt_pk_bf16_f32 v137, v138, v139\n"
"v_mfma_f32_16x16x32_bf16 v[156:159], a[124:127], a[88:91], v[156:159]\n"
"v_cvt_pk_bf16_f32 v138, v140, v141\n"
"v_cvt_pk_bf16_f32 v139, v142, v143\n"
"v_mfma_f32_16x16x32_bf16 v[188:191], a[124:127], a[92:95], v[188:191]\n"
"v_mul_f32 v168, s69, v168\n"
"v_mul_f32 v169, s69, v169\n"
"v_mfma_f32_16x16x32_bf16 v[188:191], a[120:123], a[68:71], v[188:191]\n"
"v_mul_f32 v170, s69, v170\n"
"v_mul_f32 v171, s69, v171\n"
"v_mfma_f32_16x16x32_bf16 v[4:7], a[192:195], v[0:3], 0\n"
"v_mul_f32 v172, s69, v172\n"
"v_mul_f32 v173, s69, v173\n"
"v_mfma_f32_16x16x32_bf16 v[36:39], a[192:195], v[32:35], 0\n"
"v_mul_f32 v174, s69, v174\n"
"v_mul_f32 v175, s69, v175\n"
"v_mfma_f32_16x16x32_bf16 v[68:71], a[192:195], v[64:67], 0\n"
"v_cvt_pk_bf16_f32 v168, v168, v169\n"
"v_cvt_pk_bf16_f32 v169, v170, v171\n"
"v_mfma_f32_16x16x32_bf16 v[100:103], a[192:195], v[96:99], 0\n"
"s_add_u32 m0, s62, 0x8000\n"
"v_cvt_pk_bf16_f32 v170, v172, v173\n"
"v_cvt_pk_bf16_f32 v171, v174, v175\n"
"v_mfma_f32_16x16x32_bf16 v[132:135], a[192:195], v[128:131], 0\n"
"s_add_u32 s84, s84, 0x19000\n"
"s_and_b32 s84, s84, 0x1ffff\n"
"v_mul_f32 v16, s69, v16\n"
"v_mul_f32 v17, s69, v17\n"
"v_mfma_f32_16x16x32_bf16 v[164:167], a[192:195], v[160:163], 0\n"
"buffer_load_dwordx4 v240, s[48:51], s84 offen nt lds\n"
"v_mfma_f32_16x16x32_bf16 v[12:15], a[208:211], v[0:3], 0\n"
"v_mul_f32 v18, s69, v18\n"
"v_mul_f32 v19, s69, v19\n"
"v_mfma_f32_16x16x32_bf16 v[44:47], a[208:211], v[32:35], 0\n"
"v_mul_f32 v20, s69, v20\n"
"v_mul_f32 v21, s69, v21\n"
"v_mfma_f32_16x16x32_bf16 v[76:79], a[208:211], v[64:67], 0\n"
"v_mul_f32 v22, s69, v22\n"
"v_mul_f32 v23, s69, v23\n"
"v_mfma_f32_16x16x32_bf16 v[108:111], a[208:211], v[96:99], 0\n"
"v_cvt_pk_bf16_f32 v16, v16, v17\n"
"v_cvt_pk_bf16_f32 v17, v18, v19\n"
"v_mfma_f32_16x16x32_bf16 v[140:143], a[208:211], v[128:131], 0\n"
"v_cvt_pk_bf16_f32 v18, v20, v21\n"
"v_cvt_pk_bf16_f32 v19, v22, v23\n"
"v_mfma_f32_16x16x32_bf16 v[172:175], a[208:211], v[160:163], 0\n"
"v_mul_f32 v48, s69, v48\n"
"v_mul_f32 v49, s69, v49\n"
"v_mfma_f32_16x16x32_bf16 v[4:7], a[196:199], v[8:11], v[4:7]\n"
"v_mul_f32 v50, s69, v50\n"
"v_mul_f32 v51, s69, v51\n"
"v_mfma_f32_16x16x32_bf16 v[36:39], a[196:199], v[40:43], v[36:39]\n"
"v_mul_f32 v52, s69, v52\n"
"v_mul_f32 v53, s69, v53\n"
"v_mfma_f32_16x16x32_bf16 v[68:71], a[196:199], v[72:75], v[68:71]\n"
"v_mul_f32 v54, s69, v54\n"
"v_mul_f32 v55, s69, v55\n"
"v_mfma_f32_16x16x32_bf16 v[100:103], a[196:199], v[104:107], v[100:103]\n"
"buffer_load_dwordx4 v241, s[48:51], s84 offen offset:1024 nt lds\n"
"v_mfma_f32_16x16x32_bf16 v[132:135], a[196:199], v[136:139], v[132:135]\n"
"v_cvt_pk_bf16_f32 v48, v48, v49\n"
"v_cvt_pk_bf16_f32 v49, v50, v51\n"
"v_mfma_f32_16x16x32_bf16 v[164:167], a[196:199], v[168:171], v[164:167]\n"
"v_cvt_pk_bf16_f32 v50, v52, v53\n"
"v_cvt_pk_bf16_f32 v51, v54, v55\n"
"v_mfma_f32_16x16x32_bf16 v[12:15], a[212:215], v[8:11], v[12:15]\n"
"v_mul_f32 v80, s69, v80\n"
"v_mul_f32 v81, s69, v81\n"
"v_mfma_f32_16x16x32_bf16 v[44:47], a[212:215], v[40:43], v[44:47]\n"
"v_mul_f32 v82, s69, v82\n"
"v_mul_f32 v83, s69, v83\n"
"v_mfma_f32_16x16x32_bf16 v[76:79], a[212:215], v[72:75], v[76:79]\n"
"v_mul_f32 v84, s69, v84\n"
"v_mul_f32 v85, s69, v85\n"
"v_mfma_f32_16x16x32_bf16 v[108:111], a[212:215], v[104:107], v[108:111]\n"
"v_mul_f32 v86, s69, v86\n"
"v_mul_f32 v87, s69, v87\n"
"v_mfma_f32_16x16x32_bf16 v[140:143], a[212:215], v[136:139], v[140:143]\n"
"v_cvt_pk_bf16_f32 v80, v80, v81\n"
"v_cvt_pk_bf16_f32 v81, v82, v83\n"
"v_mfma_f32_16x16x32_bf16 v[172:175], a[212:215], v[168:171], v[172:175]\n"
"v_cvt_pk_bf16_f32 v82, v84, v85\n"
"v_cvt_pk_bf16_f32 v83, v86, v87\n"
"v_mfma_f32_16x16x32_bf16 v[4:7], a[200:203], v[16:19], v[4:7]\n"
"s_add_u32 m0, s62, 0x9000\n"
"v_mul_f32 v112, s69, v112\n"
"v_mul_f32 v113, s69, v113\n"
"v_mfma_f32_16x16x32_bf16 v[36:39], a[200:203], v[48:51], v[36:39]\n"
"s_add_u32 s84, s84, 0x1000\n"
"buffer_load_dwordx4 v240, s[48:51], s84 offen nt lds\n"
"v_mfma_f32_16x16x32_bf16 v[68:71], a[200:203], v[80:83], v[68:71]\n"
"v_mul_f32 v114, s69, v114\n"
"v_mul_f32 v115, s69, v115\n"
"v_mul_f32 v116, s69, v116\n"
"v_mul_f32 v117, s69, v117\n"
"v_mul_f32 v118, s69, v118\n"
"v_mul_f32 v119, s69, v119\n"
"v_cvt_pk_bf16_f32 v112, v112, v113\n"
"v_cvt_pk_bf16_f32 v113, v114, v115\n"
"v_cvt_pk_bf16_f32 v114, v116, v117\n"
"v_cvt_pk_bf16_f32 v115, v118, v119\n"
"s_nop 1\n"
"v_mfma_f32_16x16x32_bf16 v[100:103], a[200:203], v[112:115], v[100:103]\n"
"v_mul_f32 v144, s69, v144\n"
"v_mul_f32 v145, s69, v145\n"
"v_mul_f32 v146, s69, v146\n"
"v_mul_f32 v147, s69, v147\n"
"v_mul_f32 v148, s69, v148\n"
"v_mul_f32 v149, s69, v149\n"
"v_mul_f32 v150, s69, v150\n"
"v_mul_f32 v151, s69, v151\n"
"v_cvt_pk_bf16_f32 v144, v144, v145\n"
"v_cvt_pk_bf16_f32 v145, v146, v147\n"
"v_cvt_pk_bf16_f32 v146, v148, v149\n"
"v_cvt_pk_bf16_f32 v147, v150, v151\n"
"s_nop 1\n"
"v_mfma_f32_16x16x32_bf16 v[132:135], a[200:203], v[144:147], v[132:135]\n"
"v_mul_f32 v176, s69, v176\n"
"v_mul_f32 v177, s69, v177\n"
"v_mul_f32 v178, s69, v178\n"
"v_mul_f32 v179, s69, v179\n"
"v_mul_f32 v180, s69, v180\n"
"v_mul_f32 v181, s69, v181\n"
"v_mul_f32 v182, s69, v182\n"
"v_mul_f32 v183, s69, v183\n"
"v_cvt_pk_bf16_f32 v176, v176, v177\n"
"v_cvt_pk_bf16_f32 v177, v178, v179\n"
"v_cvt_pk_bf16_f32 v178, v180, v181\n"
"v_cvt_pk_bf16_f32 v179, v182, v183\n"
"s_nop 1\n"
"v_mfma_f32_16x16x32_bf16 v[164:167], a[200:203], v[176:179], v[164:167]\n"
"v_mul_f32 v24, s69, v24\n"
"v_mul_f32 v25, s69, v25\n"
"v_mfma_f32_16x16x32_bf16 v[12:15], a[216:219], v[16:19], v[12:15]\n"
"v_mul_f32 v26, s69, v26\n"
"v_mul_f32 v27, s69, v27\n"
"v_mfma_f32_16x16x32_bf16 v[44:47], a[216:219], v[48:51], v[44:47]\n"
"v_mul_f32 v28, s69, v28\n"
"v_mul_f32 v29, s69, v29\n"
"v_mfma_f32_16x16x32_bf16 v[76:79], a[216:219], v[80:83], v[76:79]\n"
"v_mul_f32 v30, s69, v30\n"
"v_mul_f32 v31, s69, v31\n"
"v_mfma_f32_16x16x32_bf16 v[108:111], a[216:219], v[112:115], v[108:111]\n"
"v_cvt_pk_bf16_f32 v24, v24, v25\n"
"v_cvt_pk_bf16_f32 v25, v26, v27\n"
"v_mfma_f32_16x16x32_bf16 v[140:143], a[216:219], v[144:147], v[140:143]\n"
"v_cvt_pk_bf16_f32 v26, v28, v29\n"
"v_cvt_pk_bf16_f32 v27, v30, v31\n"
"v_mfma_f32_16x16x32_bf16 v[172:175], a[216:219], v[176:179], v[172:175]\n"
"buffer_load_dwordx4 v241, s[48:51], s84 offen offset:1024 nt lds\n"
"v_mfma_f32_16x16x32_bf16 v[4:7], a[204:207], v[24:27], v[4:7]\n"
"v_mul_f32 v56, s69, v56\n"
"v_mul_f32 v57, s69, v57\n"
"v_mul_f32 v58, s69, v58\n"
"v_mul_f32 v59, s69, v59\n"
"v_mul_f32 v60, s69, v60\n"
"v_mul_f32 v61, s69, v61\n"
"v_mul_f32 v62, s69, v62\n"
"v_mul_f32 v63, s69, v63\n"
"v_cvt_pk_bf16_f32 v56, v56, v57\n"
"v_cvt_pk_bf16_f32 v57, v58, v59\n"
"v_cvt_pk_bf16_f32 v58, v60, v61\n"
"v_cvt_pk_bf16_f32 v59, v62, v63\n"
"s_nop 1\n"
"v_mfma_f32_16x16x32_bf16 v[36:39], a[204:207], v[56:59], v[36:39]\n"
"v_mul_f32 v88, s69, v88\n"
"v_mul_f32 v89, s69, v89\n"
"v_mul_f32 v90, s69, v90\n"
"v_mul_f32 v91, s69, v91\n"
"v_mul_f32 v92, s69, v92\n"
"v_mul_f32 v93, s69, v93\n"
"v_mul_f32 v94, s69, v94\n"
"v_mul_f32 v95, s69, v95\n"
"v_cvt_pk_bf16_f32 v88, v88, v89\n"
"v_cvt_pk_bf16_f32 v89, v90, v91\n"
"v_cvt_pk_bf16_f32 v90, v92, v93\n"
"v_cvt_pk_bf16_f32 v91, v94, v95\n"
"s_nop 1\n"
"v_mfma_f32_16x16x32_bf16 v[68:71], a[204:207], v[88:91], v[68:71]\n"
"v_mul_f32 v120, s69, v120\n"
"v_mul_f32 v121, s69, v121\n"
"v_mul_f32 v122, s69, v122\n"
"v_mul_f32 v123, s69, v123\n"
"v_mul_f32 v124, s69, v124\n"
"v_mul_f32 v125, s69, v125\n"
"v_mul_f32 v126, s69, v126\n"
"v_mul_f32 v127, s69, v127\n"
"v_cvt_pk_bf16_f32 v120, v120, v121\n"
"v_cvt_pk_bf16_f32 v121, v122, v123\n"
"v_cvt_pk_bf16_f32 v122, v124, v125\n"
"v_cvt_pk_bf16_f32 v123, v126, v127\n"
"s_nop 1\n"
"v_mfma_f32_16x16x32_bf16 v[100:103], a[204:207], v[120:123], v[100:103]\n"
"v_mul_f32 v152, s69, v152\n"
"v_mul_f32 v153, s69, v153\n"
"v_mul_f32 v154, s69, v154\n"
"v_mul_f32 v155, s69, v155\n"
"v_mul_f32 v156, s69, v156\n"
"v_mul_f32 v157, s69, v157\n"
"v_mul_f32 v158, s69, v158\n"
"v_mul_f32 v159, s69, v159\n"
"v_cvt_pk_bf16_f32 v152, v152, v153\n"
"v_cvt_pk_bf16_f32 v153, v154, v155\n"
"v_cvt_pk_bf16_f32 v154, v156, v157\n"
"v_cvt_pk_bf16_f32 v155, v158, v159\n"
"s_nop 1\n"
"v_mfma_f32_16x16x32_bf16 v[132:135], a[204:207], v[152:155], v[132:135]\n"
"v_mul_f32 v184, s69, v184\n"
"v_mul_f32 v185, s69, v185\n"
"v_mul_f32 v186, s69, v186\n"
"v_mul_f32 v187, s69, v187\n"
"v_mul_f32 v188, s69, v188\n"
"v_mul_f32 v189, s69, v189\n"
"v_mul_f32 v190, s69, v190\n"
"v_mul_f32 v191, s69, v191\n"
"v_cvt_pk_bf16_f32 v184, v184, v185\n"
"v_cvt_pk_bf16_f32 v185, v186, v187\n"
"v_cvt_pk_bf16_f32 v186, v188, v189\n"
"v_cvt_pk_bf16_f32 v187, v190, v191\n"
"s_nop 1\n"
"v_mfma_f32_16x16x32_bf16 v[164:167], a[204:207], v[184:187], v[164:167]\n"
"v_mfma_f32_16x16x32_bf16 v[12:15], a[220:223], v[24:27], v[12:15]\n"
"v_mfma_f32_16x16x32_bf16 v[44:47], a[220:223], v[56:59], v[44:47]\n"
"v_mfma_f32_16x16x32_bf16 v[76:79], a[220:223], v[88:91], v[76:79]\n"
"v_mfma_f32_16x16x32_bf16 v[108:111], a[220:223], v[120:123], v[108:111]\n"
"v_mfma_f32_16x16x32_bf16 v[140:143], a[220:223], v[152:155], v[140:143]\n"
"v_mfma_f32_16x16x32_bf16 v[172:175], a[220:223], v[184:187], v[172:175]\n"
"s_nop 3\n"
"s_add_u32 s41, %[kq], 1\n"
"s_and_b32 s41, s41, 3\n"
"s_lshl_b32 s41, s41, 7\n"
"s_add_u32 s42, %[kq], 2\n"
"s_and_b32 s42, s42, 3\n"
"s_lshl_b32 s42, s42, 7\n"
"s_add_u32 s43, %[kq], 3\n"
"s_and_b32 s43, s43, 3\n"
"s_lshl_b32 s43, s43, 7\n"
"s_add_u32 s65, %[kq], 4\n"
"s_and_b32 s65, s65, 3\n"
"s_lshl_b32 s65, s65, 7\n"
"s_add_u32 m0, s63, 0x8000\n"
"s_add_u32 s85, s85, 0x1b000\n"
"s_and_b32 s85, s85, 0x1ffff\n"
"buffer_load_dwordx4 v242, s[52:55], s85 offen nt lds\n"
"v_max3_f32 v192, v4, v5, v6\n"
"v_max3_f32 v193, v7, v12, v13\n"
"v_max3_f32 v192, v192, v14, v15\n"
"v_max3_f32 v196, v36, v37, v38\n"
"v_max3_f32 v197, v39, v44, v45\n"
"v_max3_f32 v196, v196, v46, v47\n"
"v_max3_f32 v200, v68, v69, v70\n"
"v_max3_f32 v201, v71, v76, v77\n"
"v_max3_f32 v200, v200, v78, v79\n"
"v_max3_f32 v204, v100, v101, v102\n"
"v_max3_f32 v205, v103, v108, v109\n"
"v_max3_f32 v204, v204, v110, v111\n"
"v_max3_f32 v208, v132, v133, v134\n"
"v_max3_f32 v209, v135, v140, v141\n"
"v_max3_f32 v208, v208, v142, v143\n"
"v_max3_f32 v212, v164, v165, v166\n"
"v_max3_f32 v213, v167, v172, v173\n"
"v_max3_f32 v212, v212, v174, v175\n"
"v_max_f32 v192, v192, v193\n"
"v_max_f32 v196, v196, v197\n"
"v_max_f32 v200, v200, v201\n"
"v_max_f32 v204, v204, v205\n"
"v_max_f32 v208, v208, v209\n"
"v_max_f32 v212, v212, v213\n"
"v_mov_b32 v193, v192\n"
"v_mov_b32 v197, v196\n"
"v_mov_b32 v201, v200\n"
"v_mov_b32 v205, v204\n"
"v_mov_b32 v209, v208\n"
"v_mov_b32 v213, v212\n"
"v_permlane32_swap_b32 v192, v193\n"
"v_permlane32_swap_b32 v196, v197\n"
"v_permlane32_swap_b32 v200, v201\n"
"v_permlane32_swap_b32 v204, v205\n"
"v_permlane32_swap_b32 v208, v209\n"
"v_permlane32_swap_b32 v212, v213\n"
"v_max_f32 v192, v192, v193\n"
"v_max_f32 v196, v196, v197\n"
"v_max_f32 v200, v200, v201\n"
"v_max_f32 v204, v204, v205\n"
"buffer_load_dwordx4 v242, s[52:55], s85 offen offset:2048 nt lds\n"
"v_max_f32 v208, v208, v209\n"
"v_max_f32 v212, v212, v213\n"
"v_mov_b32 v193, v192\n"
"v_mov_b32 v197, v196\n"
"v_mov_b32 v201, v200\n"
"v_mov_b32 v205, v204\n"
"v_mov_b32 v209, v208\n"
"v_mov_b32 v213, v212\n"
"v_permlane16_swap_b32 v192, v193\n"
"v_permlane16_swap_b32 v196, v197\n"
"v_permlane16_swap_b32 v200, v201\n"
"v_permlane16_swap_b32 v204, v205\n"
"v_permlane16_swap_b32 v208, v209\n"
"v_permlane16_swap_b32 v212, v213\n"
"v_max_f32 v192, v192, v193\n"
"v_max_f32 v196, v196, v197\n"
"v_max_f32 v200, v200, v201\n"
"v_max_f32 v204, v204, v205\n"
"v_max_f32 v208, v208, v209\n"
"v_max_f32 v212, v212, v213\n"
"v_sub_f32 v193, 0, v192\n"
"v_sub_f32 v194, 0, v192\n"
"v_sub_f32 v195, 0, v192\n"
"v_sub_f32 v192, 0, v192\n"
"v_sub_f32 v197, 0, v196\n"
"v_sub_f32 v198, 0, v196\n"
"v_sub_f32 v199, 0, v196\n"
"v_sub_f32 v196, 0, v196\n"
"v_sub_f32 v201, 0, v200\n"
"v_sub_f32 v202, 0, v200\n"
"v_sub_f32 v203, 0, v200\n"
"v_sub_f32 v200, 0, v200\n"
"v_sub_f32 v205, 0, v204\n"
"v_sub_f32 v206, 0, v204\n"
"v_sub_f32 v207, 0, v204\n"
"v_sub_f32 v204, 0, v204\n"
"v_sub_f32 v209, 0, v208\n"
"v_sub_f32 v210, 0, v208\n"
"v_sub_f32 v211, 0, v208\n"
"v_sub_f32 v208, 0, v208\n"
"s_add_u32 m0, s63, 0x9000\n"
"s_add_u32 s85, s85, 0x1000\n"
"buffer_load_dwordx4 v242, s[52:55], s85 offen nt lds\n"
"v_sub_f32 v213, 0, v212\n"
"v_sub_f32 v214, 0, v212\n"
"v_sub_f32 v215, 0, v212\n"
"v_sub_f32 v212, 0, v212\n"
"v_add_f32 v4, v4, v193\n"
"v_add_f32 v5, v5, v193\n"
"v_add_f32 v6, v6, v193\n"
"v_add_f32 v7, v7, v193\n"
"v_add_f32 v36, v36, v197\n"
"v_add_f32 v37, v37, v197\n"
"v_add_f32 v38, v38, v197\n"
"v_add_f32 v39, v39, v197\n"
"v_add_f32 v68, v68, v201\n"
"v_add_f32 v69, v69, v201\n"
"v_add_f32 v70, v70, v201\n"
"v_add_f32 v71, v71, v201\n"
"v_add_f32 v100, v100, v205\n"
"v_add_f32 v101, v101, v205\n"
"v_add_f32 v102, v102, v205\n"
"v_add_f32 v103, v103, v205\n"
"v_add_f32 v132, v132, v209\n"
"v_add_f32 v133, v133, v209\n"
"v_add_f32 v134, v134, v209\n"
"v_add_f32 v135, v135, v209\n"
"v_add_f32 v164, v164, v213\n"
"v_add_f32 v165, v165, v213\n"
"v_add_f32 v166, v166, v213\n"
"v_add_f32 v167, v167, v213\n"
"v_add_f32 v12, v12, v193\n"
"v_add_f32 v13, v13, v193\n"
"v_add_f32 v14, v14, v193\n"
"v_add_f32 v15, v15, v193\n"
"v_add_f32 v44, v44, v197\n"
"v_add_f32 v45, v45, v197\n"
"v_add_f32 v46, v46, v197\n"
"v_add_f32 v47, v47, v197\n"
"v_add_f32 v76, v76, v201\n"
"v_add_f32 v77, v77, v201\n"
"v_add_f32 v78, v78, v201\n"
"v_add_f32 v79, v79, v201\n"
"buffer_load_dwordx4 v242, s[52:55], s85 offen offset:2048 nt lds\n"
"v_add_f32 v108, v108, v205\n"
"v_add_f32 v109, v109, v205\n"
"v_add_f32 v110, v110, v205\n"
"v_add_f32 v111, v111, v205\n"
"v_add_f32 v140, v140, v209\n"
"v_add_f32 v141, v141, v209\n"
"v_add_f32 v142, v142, v209\n"
"v_add_f32 v143, v143, v209\n"
"v_add_f32 v172, v172, v213\n"
"v_add_f32 v173, v173, v213\n"
"v_add_f32 v174, v174, v213\n"
"v_add_f32 v175, v175, v213\n"
"v_exp_f32 v4, v4\n"
"v_exp_f32 v5, v5\n"
"v_add_f32 v216, v216, v4\n"
"v_exp_f32 v6, v6\n"
"v_add_f32 v216, v216, v5\n"
"v_cvt_pk_bf16_f32 v20, v4, v5\n"
"v_exp_f32 v7, v7\n"
"v_add_f32 v216, v216, v6\n"
"v_exp_f32 v36, v36\n"
"v_add_f32 v216, v216, v7\n"
"v_cvt_pk_bf16_f32 v21, v6, v7\n"
"v_exp_f32 v37, v37\n"
"v_add_f32 v217, v217, v36\n"
"v_exp_f32 v38, v38\n"
"v_add_f32 v217, v217, v37\n"
"v_cvt_pk_bf16_f32 v52, v36, v37\n"
"v_exp_f32 v39, v39\n"
"v_add_f32 v217, v217, v38\n"
"v_exp_f32 v68, v68\n"
"v_add_f32 v217, v217, v39\n"
"v_cvt_pk_bf16_f32 v53, v38, v39\n"
"v_exp_f32 v69, v69\n"
"v_add_f32 v218, v218, v68\n"
"v_exp_f32 v70, v70\n"
"v_add_f32 v218, v218, v69\n"
"v_cvt_pk_bf16_f32 v84, v68, v69\n"
"v_exp_f32 v71, v71\n"
"v_add_f32 v218, v218, v70\n"
"v_exp_f32 v100, v100\n"
"v_add_f32 v218, v218, v71\n"
"v_cvt_pk_bf16_f32 v85, v70, v71\n"
"v_exp_f32 v101, v101\n"
"v_add_f32 v219, v219, v100\n"
"v_exp_f32 v102, v102\n"
"v_add_f32 v219, v219, v101\n"
"v_cvt_pk_bf16_f32 v116, v100, v101\n"
"v_exp_f32 v103, v103\n"
"v_add_f32 v219, v219, v102\n"
"v_exp_f32 v132, v132\n"
"v_add_f32 v219, v219, v103\n"
"v_cvt_pk_bf16_f32 v117, v102, v103\n"
"v_exp_f32 v133, v133\n"
"v_add_f32 v220, v220, v132\n"
"v_exp_f32 v134, v134\n"
"v_add_f32 v220, v220, v133\n"
"v_cvt_pk_bf16_f32 v148, v132, v133\n"
"v_exp_f32 v135, v135\n"
"v_add_f32 v220, v220, v134\n"
"v_exp_f32 v164, v164\n"
"v_add_f32 v220, v220, v135\n"
"v_cvt_pk_bf16_f32 v149, v134, v135\n"
"v_exp_f32 v165, v165\n"
"v_add_f32 v221, v221, v164\n"
"v_exp_f32 v166, v166\n"
"v_add_f32 v221, v221, v165\n"
"v_cvt_pk_bf16_f32 v180, v164, v165\n"
"v_exp_f32 v167, v167\n"
"v_add_f32 v221, v221, v166\n"
"v_exp_f32 v12, v12\n"
"v_add_f32 v221, v221, v167\n"
"v_cvt_pk_bf16_f32 v181, v166, v167\n"
"v_exp_f32 v13, v13\n"
"v_add_f32 v216, v216, v12\n"
"v_exp_f32 v14, v14\n"
"v_add_f32 v216, v216, v13\n"
"v_cvt_pk_bf16_f32 v22, v12, v13\n"
"v_exp_f32 v15, v15\n"
"v_add_f32 v216, v216, v14\n"
"v_exp_f32 v44, v44\n"
"v_add_f32 v216, v216, v15\n"
"v_cvt_pk_bf16_f32 v23, v14, v15\n"
"v_exp_f32 v45, v45\n"
"v_add_f32 v217, v217, v44\n"
"v_exp_f32 v46, v46\n"
"v_add_f32 v217, v217, v45\n"
"v_cvt_pk_bf16_f32 v54, v44, v45\n"
"v_exp_f32 v47, v47\n"
"v_add_f32 v217, v217, v46\n"
"v_exp_f32 v76, v76\n"
"v_add_f32 v217, v217, v47\n"
"v_cvt_pk_bf16_f32 v55, v46, v47\n"
"v_exp_f32 v77, v77\n"
"v_add_f32 v218, v218, v76\n"
"v_exp_f32 v78, v78\n"
"v_add_f32 v218, v218, v77\n"
"v_cvt_pk_bf16_f32 v86, v76, v77\n"
"v_exp_f32 v79, v79\n"
"v_add_f32 v218, v218, v78\n"
"s_memrealtime s[88:89]\n"
"s_waitcnt lgkmcnt(0)\n"
"s_add_u32 s86, s88, 69\n"
"s_nop 0\n"
"v_mov_b32 v234, -1\n"
"v_mov_b32 v235, -1\n"
"v_mov_b32 v236, -1\n"
"v_mov_b32 v237, -1\n"
"s_cmp_eq_u32 %[kq], 3\n"
"s_cselect_b32 s73, 0, -1\n"
"s_add_u32 s72, %[kq], 1\n"
"s_and_b32 s72, s72, 3\n"
"s_lshl_b32 s72, s72, 14\n"
"s_add_u32 s68, s72, %[pr1]\n"
"v_mfma_f32_16x16x32_bf16 v[4:7], a[128:131], v[0:3], v[192:195]\n"
"v_exp_f32 v108, v108\n"
"v_mfma_f32_16x16x32_bf16 v[4:7], a[132:135], v[8:11], v[4:7]\n"
"s_add_u32 m0, s62, 0xc000\n"
"v_add_f32 v218, v218, v79\n"
"v_cvt_pk_bf16_f32 v87, v78, v79\n"
"v_mfma_f32_16x16x32_bf16 v[4:7], a[136:139], v[16:19], v[4:7]\n"
"s_add_u32 s84, s84, 0x1000\n"
"s_and_b32 s84, s84, 0x1ffff\n"
"buffer_load_dwordx4 v240, s[48:51], s84 offen nt lds\n"
"v_exp_f32 v109, v109\n"
"v_mfma_f32_16x16x32_bf16 v[4:7], a[140:143], v[24:27], v[4:7]\n"
"v_add_f32 v219, v219, v108\n"
"v_add_f32 v219, v219, v109\n"
"v_mfma_f32_16x16x32_bf16 v[36:39], a[140:143], v[56:59], v[196:199]\n"
"v_exp_f32 v110, v110\n"
"v_mfma_f32_16x16x32_bf16 v[36:39], a[136:139], v[48:51], v[36:39]\n"
"v_cvt_pk_bf16_f32 v118, v108, v109\n"
"v_add_f32 v219, v219, v110\n"
"v_mfma_f32_16x16x32_bf16 v[36:39], a[132:135], v[40:43], v[36:39]\n"
"v_exp_f32 v111, v111\n"
"v_mfma_f32_16x16x32_bf16 v[36:39], a[128:131], v[32:35], v[36:39]\n"
"v_add_f32 v219, v219, v111\n"
"v_cvt_pk_bf16_f32 v119, v110, v111\n"
"v_mfma_f32_16x16x32_bf16 v[68:71], a[128:131], v[64:67], v[200:203]\n"
"v_exp_f32 v140, v140\n"
"v_mfma_f32_16x16x32_bf16 v[68:71], a[132:135], v[72:75], v[68:71]\n"
"v_add_f32 v220, v220, v140\n"
"v_mfma_f32_16x16x32_bf16 v[68:71], a[136:139], v[80:83], v[68:71]\n"
"v_exp_f32 v141, v141\n"
"v_mfma_f32_16x16x32_bf16 v[68:71], a[140:143], v[88:91], v[68:71]\n"
"v_add_f32 v220, v220, v141\n"
"v_cvt_pk_bf16_f32 v150, v140, v141\n"
"v_mfma_f32_16x16x32_bf16 v[100:103], a[140:143], v[120:123], v[204:207]\n"
"v_exp_f32 v142, v142\n"
"v_mfma_f32_16x16x32_bf16 v[100:103], a[136:139], v[112:115], v[100:103]\n"
"v_add_f32 v220, v220, v142\n"
"v_mfma_f32_16x16x32_bf16 v[100:103], a[132:135], v[104:107], v[100:103]\n"
"buffer_load_dwordx4 v241, s[48:51], s84 offen offset:1024 nt lds\n"
"v_exp_f32 v143, v143\n"
"v_mfma_f32_16x16x32_bf16 v[100:103], a[128:131], v[96:99], v[100:103]\n"
"v_add_f32 v220, v220, v143\n"
"v_cvt_pk_bf16_f32 v151, v142, v143\n"
"v_mfma_f32_16x16x32_bf16 v[132:135], a[128:131], v[128:131], v[208:211]\n"
"v_exp_f32 v172, v172\n"
"v_mfma_f32_16x16x32_bf16 v[132:135], a[132:135], v[136:139], v[132:135]\n"
"v_add_f32 v221, v221, v172\n"
"v_mfma_f32_16x16x32_bf16 v[132:135], a[136:139], v[144:147], v[132:135]\n"
"v_exp_f32 v173, v173\n"
"v_mfma_f32_16x16x32_bf16 v[132:135], a[140:143], v[152:155], v[132:135]\n"
"v_add_f32 v221, v221, v173\n"
"v_cvt_pk_bf16_f32 v182, v172, v173\n"
"v_mfma_f32_16x16x32_bf16 v[164:167], a[140:143], v[184:187], v[212:215]\n"
"v_exp_f32 v174, v174\n"
"v_mfma_f32_16x16x32_bf16 v[164:167], a[136:139], v[176:179], v[164:167]\n"
"v_add_f32 v221, v221, v174\n"
"v_mfma_f32_16x16x32_bf16 v[164:167], a[132:135], v[168:171], v[164:167]\n"
"v_exp_f32 v175, v175\n"
"v_mfma_f32_16x16x32_bf16 v[164:167], a[128:131], v[160:163], v[164:167]\n"
"v_add_f32 v221, v221, v175\n"
"v_cvt_pk_bf16_f32 v183, v174, v175\n"
"v_mfma_f32_16x16x32_bf16 v[12:15], a[144:147], v[0:3], v[192:195]\n"
"v_exp_f32 v4, v4\n"
"v_mfma_f32_16x16x32_bf16 v[12:15], a[148:151], v[8:11], v[12:15]\n"
"s_add_u32 m0, s62, 0xd000\n"
"v_add_f32 v216, v216, v4\n"
"v_mfma_f32_16x16x32_bf16 v[12:15], a[152:155], v[16:19], v[12:15]\n"
"s_add_u32 s84, s84, 0x1000\n"
"buffer_load_dwordx4 v240, s[48:51], s84 offen nt lds\n"
"v_exp_f32 v5, v5\n"
"v_mfma_f32_16x16x32_bf16 v[12:15], a[156:159], v[24:27], v[12:15]\n"
"v_add_f32 v216, v216, v5\n"
"v_cvt_pk_bf16_f32 v28, v4, v5\n"
"v_mfma_f32_16x16x32_bf16 v[44:47], a[156:159], v[56:59], v[196:199]\n"
"v_exp_f32 v6, v6\n"
"v_mfma_f32_16x16x32_bf16 v[44:47], a[152:155], v[48:51], v[44:47]\n"
"v_add_f32 v216, v216, v6\n"
"v_mfma_f32_16x16x32_bf16 v[44:47], a[148:151], v[40:43], v[44:47]\n"
"v_exp_f32 v7, v7\n"
"v_mfma_f32_16x16x32_bf16 v[44:47], a[144:147], v[32:35], v[44:47]\n"
"v_add_f32 v216, v216, v7\n"
"v_cvt_pk_bf16_f32 v29, v6, v7\n"
"v_mfma_f32_16x16x32_bf16 v[76:79], a[144:147], v[64:67], v[200:203]\n"
"v_exp_f32 v36, v36\n"
"v_mfma_f32_16x16x32_bf16 v[76:79], a[148:151], v[72:75], v[76:79]\n"
"v_add_f32 v217, v217, v36\n"
"v_mfma_f32_16x16x32_bf16 v[76:79], a[152:155], v[80:83], v[76:79]\n"
"v_exp_f32 v37, v37\n"
"v_mfma_f32_16x16x32_bf16 v[76:79], a[156:159], v[88:91], v[76:79]\n"
"v_add_f32 v217, v217, v37\n"
"v_cvt_pk_bf16_f32 v60, v36, v37\n"
"v_mfma_f32_16x16x32_bf16 v[108:111], a[156:159], v[120:123], v[204:207]\n"
"v_exp_f32 v38, v38\n"
"v_mfma_f32_16x16x32_bf16 v[108:111], a[152:155], v[112:115], v[108:111]\n"
"v_add_f32 v217, v217, v38\n"
"v_mfma_f32_16x16x32_bf16 v[108:111], a[148:151], v[104:107], v[108:111]\n"
"buffer_load_dwordx4 v241, s[48:51], s84 offen offset:1024 nt lds\n"
"v_exp_f32 v39, v39\n"
"v_mfma_f32_16x16x32_bf16 v[108:111], a[144:147], v[96:99], v[108:111]\n"
"v_add_f32 v217, v217, v39\n"
"v_cvt_pk_bf16_f32 v61, v38, v39\n"
"v_mfma_f32_16x16x32_bf16 v[140:143], a[144:147], v[128:131], v[208:211]\n"
"v_exp_f32 v68, v68\n"
"v_mfma_f32_16x16x32_bf16 v[140:143], a[148:151], v[136:139], v[140:143]\n"
"v_add_f32 v218, v218, v68\n"
"v_mfma_f32_16x16x32_bf16 v[140:143], a[152:155], v[144:147], v[140:143]\n"
"v_exp_f32 v69, v69\n"
"v_mfma_f32_16x16x32_bf16 v[140:143], a[156:159], v[152:155], v[140:143]\n"
"v_add_f32 v218, v218, v69\n"
"v_cvt_pk_bf16_f32 v92, v68, v69\n"
"v_mfma_f32_16x16x32_bf16 v[172:175], a[156:159], v[184:187], v[212:215]\n"
"v_exp_f32 v70, v70\n"
"v_mfma_f32_16x16x32_bf16 v[172:175], a[152:155], v[176:179], v[172:175]\n"
"v_add_f32 v218, v218, v70\n"
"v_mfma_f32_16x16x32_bf16 v[172:175], a[148:151], v[168:171], v[172:175]\n"
"v_exp_f32 v71, v71\n"
"v_mfma_f32_16x16x32_bf16 v[172:175], a[144:147], v[160:163], v[172:175]\n"
"v_add_f32 v218, v218, v71\n"
"v_cvt_pk_bf16_f32 v93, v70, v71\n"
"v_mfma_f32_16x16x32_bf16 a[0:3], a[224:227], v[20:23], 0\n"
"s_add_u32 m0, s63, 0xc000\n"
"v_exp_f32 v100, v100\n"
"v_mfma_f32_16x16x32_bf16 a[4:7], a[224:227], v[52:55], 0\n"
"s_add_u32 s85, s85, 0x1000\n"
"s_and_b32 s85, s85, 0x1ffff\n"
"v_add_f32 v219, v219, v100\n"
"v_mfma_f32_16x16x32_bf16 a[8:11], a[224:227], v[84:87], 0\n"
"buffer_load_dwordx4 v242, s[52:55], s85 offen nt lds\n"
"v_exp_f32 v101, v101\n"
"v_mfma_f32_16x16x32_bf16 a[12:15], a[224:227], v[116:119], 0\n"
"v_add_f32 v219, v219, v101\n"
"v_cvt_pk_bf16_f32 v124, v100, v101\n"
"v_mfma_f32_16x16x32_bf16 a[16:19], a[224:227], v[148:151], 0\n"
"s_memrealtime s[88:89]\n"
"v_exp_f32 v102, v102\n"
"v_mfma_f32_16x16x32_bf16 a[20:23], a[224:227], v[180:183], 0\n"
"v_add_f32 v219, v219, v102\n"
"v_mfma_f32_16x16x32_bf16 a[44:47], a[228:231], v[180:183], 0\n"
"v_exp_f32 v103, v103\n"
"v_mfma_f32_16x16x32_bf16 a[40:43], a[228:231], v[148:151], 0\n"
"v_add_f32 v219, v219, v103\n"
"v_cvt_pk_bf16_f32 v125, v102, v103\n"
"v_mfma_f32_16x16x32_bf16 a[36:39], a[228:231], v[116:119], 0\n"
"LP0_%=:\n"
"s_waitcnt lgkmcnt(0)\n"
"s_sub_u32 s89, s88, s86\n"
"s_cmp_ge_i32 s89, 0\n"
"s_cbranch_scc1 LQ0_%=\n"
"s_memrealtime s[88:89]\n"
"s_branch LP0_%=\n"
"LQ0_%=:\n"
"s_waitcnt vmcnt(9)\n"
"s_barrier\n"
"v_exp_f32 v132, v132\n"
"v_mfma_f32_16x16x32_bf16 a[32:35], a[228:231], v[84:87], 0\n"
"ds_read_b64 a[192:193], v222 offset:32768\n"
"v_add_f32 v220, v220, v132\n"
"v_mfma_f32_16x16x32_bf16 a[28:31], a[228:231], v[52:55], 0\n"
"s_add_u32 s86, s86, 124\n"
"v_exp_f32 v133, v133\n"
"v_mfma_f32_16x16x32_bf16 a[24:27], a[228:231], v[20:23], 0\n"
"v_add_f32 v220, v220, v133\n"
"v_cvt_pk_bf16_f32 v156, v132, v133\n"
"v_mfma_f32_16x16x32_bf16 a[48:51], a[232:235], v[20:23], 0\n"
"v_exp_f32 v134, v134\n"
"v_mfma_f32_16x16x32_bf16 a[52:55], a[232:235], v[52:55], 0\n"
"ds_read_b64 a[194:195], v223 offset:32768\n"
"v_add_f32 v220, v220, v134\n"
"v_mfma_f32_16x16x32_bf16 a[56:59], a[232:235], v[84:87], 0\n"
"buffer_load_dwordx4 v242, s[52:55], s85 offen offset:2048 nt lds\n"
"v_exp_f32 v135, v135\n"
"v_mfma_f32_16x16x32_bf16 a[60:63], a[232:235], v[116:119], 0\n"
"v_add_f32 v220, v220, v135\n"
"v_cvt_pk_bf16_f32 v157, v134, v135\n"
"v_mfma_f32_16x16x32_bf16 a[64:67], a[232:235], v[148:151], 0\n"
"v_exp_f32 v164, v164\n"
"v_mfma_f32_16x16x32_bf16 a[68:71], a[232:235], v[180:183], 0\n"
"ds_read_b64 a[196:197], v224 offset:32768\n"
"v_add_f32 v221, v221, v164\n"
"v_mfma_f32_16x16x32_bf16 a[92:95], a[236:239], v[180:183], 0\n"
"v_exp_f32 v165, v165\n"
"v_mfma_f32_16x16x32_bf16 a[88:91], a[236:239], v[148:151], 0\n"
"v_add_f32 v221, v221, v165\n"
"v_cvt_pk_bf16_f32 v188, v164, v165\n"
"v_mfma_f32_16x16x32_bf16 a[84:87], a[236:239], v[116:119], 0\n"
"v_exp_f32 v166, v166\n"
"v_mfma_f32_16x16x32_bf16 a[80:83], a[236:239], v[84:87], 0\n"
"ds_read_b64 a[198:199], v225 offset:32768\n"
"v_add_f32 v221, v221, v166\n"
"v_mfma_f32_16x16x32_bf16 a[76:79], a[236:239], v[52:55], 0\n"
"v_exp_f32 v167, v167\n"
"v_mfma_f32_16x16x32_bf16 a[72:75], a[236:239], v[20:23], 0\n"
"v_add_f32 v221, v221, v167\n"
"v_cvt_pk_bf16_f32 v189, v166, v167\n"
"v_mfma_f32_16x16x32_bf16 a[96:99], a[240:243], v[20:23], 0\n"
"v_exp_f32 v12, v12\n"
"v_mfma_f32_16x16x32_bf16 a[100:103], a[240:243], v[52:55], 0\n"
"s_add_u32 m0, s63, 0xd000\n"
"ds_read_b64 a[200:201], v246 offset:32768\n"
"v_add_f32 v216, v216, v12\n"
"v_mfma_f32_16x16x32_bf16 a[104:107], a[240:243], v[84:87], 0\n"
"s_add_u32 s85, s85, 0x1000\n"
"buffer_load_dwordx4 v242, s[52:55], s85 offen nt lds\n"
"v_exp_f32 v13, v13\n"
"v_mfma_f32_16x16x32_bf16 a[108:111], a[240:243], v[116:119], 0\n"
"v_add_f32 v216, v216, v13\n"
"v_cvt_pk_bf16_f32 v30, v12, v13\n"
"v_mfma_f32_16x16x32_bf16 a[112:115], a[240:243], v[148:151], 0\n"
"v_exp_f32 v14, v14\n"
"v_mfma_f32_16x16x32_bf16 a[116:119], a[240:243], v[180:183], 0\n"
"ds_read_b64 a[202:203], v247 offset:32768\n"
"v_add_f32 v216, v216, v14\n"
"v_mfma_f32_16x16x32_bf16 a[140:143], a[244:247], v[180:183], 0\n"
"v_exp_f32 v15, v15\n"
"v_mfma_f32_16x16x32_bf16 a[136:139], a[244:247], v[148:151], 0\n"
"v_add_f32 v216, v216, v15\n"
"v_cvt_pk_bf16_f32 v31, v14, v15\n"
"v_mfma_f32_16x16x32_bf16 a[132:135], a[244:247], v[116:119], 0\n"
"v_exp_f32 v44, v44\n"
"v_mfma_f32_16x16x32_bf16 a[128:131], a[244:247], v[84:87], 0\n"
"ds_read_b64 a[204:205], v248 offset:32768\n"
"v_add_f32 v217, v217, v44\n"
"v_mfma_f32_16x16x32_bf16 a[124:127], a[244:247], v[52:55], 0\n"
"v_exp_f32 v45, v45\n"
"v_mfma_f32_16x16x32_bf16 a[120:123], a[244:247], v[20:23], 0\n"
"v_add_f32 v217, v217, v45\n"
"v_cvt_pk_bf16_f32 v62, v44, v45\n"
"v_mfma_f32_16x16x32_bf16 a[144:147], a[248:251], v[20:23], 0\n"
"v_exp_f32 v46, v46\n"
"v_mfma_f32_16x16x32_bf16 a[148:151], a[248:251], v[52:55], 0\n"
"ds_read_b64 a[206:207], v249 offset:32768\n"
"v_add_f32 v217, v217, v46\n"
"v_mfma_f32_16x16x32_bf16 a[152:155], a[248:251], v[84:87], 0\n"
"buffer_load_dwordx4 v242, s[52:55], s85 offen offset:2048 nt lds\n"
"v_exp_f32 v47, v47\n"
"v_mfma_f32_16x16x32_bf16 a[156:159], a[248:251], v[116:119], 0\n"
"v_add_f32 v217, v217, v47\n"
"v_cvt_pk_bf16_f32 v63, v46, v47\n"
"v_mfma_f32_16x16x32_bf16 a[160:163], a[248:251], v[148:151], 0\n"
"v_exp_f32 v76, v76\n"
"v_mfma_f32_16x16x32_bf16 a[164:167], a[248:251], v[180:183], 0\n"
"v_add_f32 v218, v218, v76\n"
"v_mfma_f32_16x16x32_bf16 a[188:191], a[252:255], v[180:183], 0\n"
"v_exp_f32 v77, v77\n"
"v_mfma_f32_16x16x32_bf16 a[184:187], a[252:255], v[148:151], 0\n"
"v_add_f32 v218, v218, v77\n"
"v_cvt_pk_bf16_f32 v94, v76, v77\n"
"v_mfma_f32_16x16x32_bf16 a[180:183], a[252:255], v[116:119], 0\n"
"v_exp_f32 v78, v78\n"
"v_mfma_f32_16x16x32_bf16 a[176:179], a[252:255], v[84:87], 0\n"
"v_add_f32 v218, v218, v78\n"
"v_mfma_f32_16x16x32_bf16 a[172:175], a[252:255], v[52:55], 0\n"
"s_waitcnt lgkmcnt(0)\n"
"v_exp_f32 v79, v79\n"
"v_mfma_f32_16x16x32_bf16 a[168:171], a[252:255], v[20:23], 0\n"
"v_add_f32 v218, v218, v79\n"
"v_cvt_pk_bf16_f32 v95, v78, v79\n"
"v_mfma_f32_16x16x32_bf16 v[4:7], a[192:195], v[0:3], v[192:195]\n"
"v_exp_f32 v108, v108\n"
"v_mfma_f32_16x16x32_bf16 v[4:7], a[196:199], v[8:11], v[4:7]\n"
"ds_read_b64 a[208:209], v222 offset:36864\n"
"v_add_f32 v219, v219, v108\n"
"v_mfma_f32_16x16x32_bf16 v[4:7], a[200:203], v[16:19], v[4:7]\n"
"v_exp_f32 v109, v109\n"
"v_mfma_f32_16x16x32_bf16 v[4:7], a[204:207], v[24:27], v[4:7]\n"
"ds_read_b64 a[210:211], v223 offset:36864\n"
"v_add_f32 v219, v219, v109\n"
"v_mfma_f32_16x16x32_bf16 v[36:39], a[204:207], v[56:59], v[196:199]\n"
"buffer_store_dwordx4 v[234:237], v243, s[56:59], s68 offen sc1\n"
"v_exp_f32 v110, v110\n"
"v_mfma_f32_16x16x32_bf16 v[36:39], a[200:203], v[48:51], v[36:39]\n"
"ds_read_b64 a[212:213], v224 offset:36864\n"
"v_cvt_pk_bf16_f32 v126, v108, v109\n"
"v_mfma_f32_16x16x32_bf16 v[36:39], a[196:199], v[40:43], v[36:39]\n"
"v_exp_f32 v111, v111\n"
"v_mfma_f32_16x16x32_bf16 v[36:39], a[192:195], v[32:35], v[36:39]\n"
"ds_read_b64 a[214:215], v225 offset:36864\n"
"v_add_f32 v219, v219, v110\n"
"v_mfma_f32_16x16x32_bf16 v[68:71], a[192:195], v[64:67], v[200:203]\n"
"v_exp_f32 v140, v140\n"
"v_mfma_f32_16x16x32_bf16 v[68:71], a[196:199], v[72:75], v[68:71]\n"
"ds_read_b64 a[216:217], v246 offset:36864\n"
"v_add_f32 v219, v219, v111\n"
"v_mfma_f32_16x16x32_bf16 v[68:71], a[200:203], v[80:83], v[68:71]\n"
"v_exp_f32 v141, v141\n"
"v_mfma_f32_16x16x32_bf16 v[68:71], a[204:207], v[88:91], v[68:71]\n"
"ds_read_b64 a[218:219], v247 offset:36864\n"
"v_cvt_pk_bf16_f32 v127, v110, v111\n"
"v_mfma_f32_16x16x32_bf16 v[100:103], a[204:207], v[120:123], v[204:207]\n"
"buffer_store_dwordx4 v[234:237], v243, s[56:59], s68 offen offset:1024 sc1\n"
"v_exp_f32 v142, v142\n"
"v_mfma_f32_16x16x32_bf16 v[100:103], a[200:203], v[112:115], v[100:103]\n"
"ds_read_b64 a[220:221], v248 offset:36864\n"
"v_add_f32 v220, v220, v140\n"
"v_mfma_f32_16x16x32_bf16 v[100:103], a[196:199], v[104:107], v[100:103]\n"
"v_exp_f32 v143, v143\n"
"v_mfma_f32_16x16x32_bf16 v[100:103], a[192:195], v[96:99], v[100:103]\n"
"ds_read_b64 a[222:223], v249 offset:36864\n"
"v_add_f32 v220, v220, v141\n"
"v_mfma_f32_16x16x32_bf16 v[132:135], a[192:195], v[128:131], v[208:211]\n"
"v_exp_f32 v172, v172\n"
"v_mfma_f32_16x16x32_bf16 v[132:135], a[196:199], v[136:139], v[132:135]\n"
"ds_read_b64_tr_b16 a[224:225], v226 offset:16384\n"
"v_cvt_pk_bf16_f32 v158, v140, v141\n"
"v_mfma_f32_16x16x32_bf16 v[132:135], a[200:203], v[144:147], v[132:135]\n"
"v_exp_f32 v173, v173\n"
"v_mfma_f32_16x16x32_bf16 v[132:135], a[204:207], v[152:155], v[132:135]\n"
"ds_read_b64_tr_b16 a[226:227], v226 offset:20480\n"
"v_add_f32 v220, v220, v142\n"
"v_mfma_f32_16x16x32_bf16 v[164:167], a[204:207], v[184:187], v[212:215]\n"
"buffer_store_dwordx4 v[234:237], v243, s[56:59], s68 offen offset:2048 sc1\n"
"v_exp_f32 v174, v174\n"
"v_mfma_f32_16x16x32_bf16 v[164:167], a[200:203], v[176:179], v[164:167]\n"
"ds_read_b64_tr_b16 a[228:229], v227 offset:16384\n"
"v_add_f32 v220, v220, v143\n"
"v_mfma_f32_16x16x32_bf16 v[164:167], a[196:199], v[168:171], v[164:167]\n"
"s_waitcnt lgkmcnt(3)\n"
"v_exp_f32 v175, v175\n"
"v_mfma_f32_16x16x32_bf16 v[164:167], a[192:195], v[160:163], v[164:167]\n"
"ds_read_b64_tr_b16 a[230:231], v227 offset:20480\n"
"v_cvt_pk_bf16_f32 v159, v142, v143\n"
"v_mfma_f32_16x16x32_bf16 v[12:15], a[208:211], v[0:3], v[192:195]\n"
"v_exp_f32 v4, v4\n"
"v_mfma_f32_16x16x32_bf16 v[12:15], a[212:215], v[8:11], v[12:15]\n"
"ds_read_b64_tr_b16 a[232:233], v228 offset:16384\n"
"v_add_f32 v221, v221, v172\n"
"v_mfma_f32_16x16x32_bf16 v[12:15], a[216:219], v[16:19], v[12:15]\n"
"v_exp_f32 v5, v5\n"
"v_mfma_f32_16x16x32_bf16 v[12:15], a[220:223], v[24:27], v[12:15]\n"
"ds_read_b64_tr_b16 a[234:235], v228 offset:20480\n"
"v_add_f32 v221, v221, v173\n"
"v_cvt_pk_bf16_f32 v190, v172, v173\n"
"v_mfma_f32_16x16x32_bf16 v[44:47], a[220:223], v[56:59], v[196:199]\n"
"v_exp_f32 v6, v6\n"
"v_mfma_f32_16x16x32_bf16 v[44:47], a[216:219], v[48:51], v[44:47]\n"
"ds_read_b64_tr_b16 a[236:237], v229 offset:16384\n"
"v_add_f32 v221, v221, v174\n"
"v_mfma_f32_16x16x32_bf16 v[44:47], a[212:215], v[40:43], v[44:47]\n"
"v_exp_f32 v7, v7\n"
"v_mfma_f32_16x16x32_bf16 v[44:47], a[208:211], v[32:35], v[44:47]\n"
"ds_read_b64_tr_b16 a[238:239], v229 offset:20480\n"
"v_add_f32 v221, v221, v175\n"
"v_cvt_pk_bf16_f32 v191, v174, v175\n"
"v_mfma_f32_16x16x32_bf16 v[76:79], a[208:211], v[64:67], v[200:203]\n"
"v_exp_f32 v36, v36\n"
"v_mfma_f32_16x16x32_bf16 v[76:79], a[212:215], v[72:75], v[76:79]\n"
"ds_read_b64_tr_b16 a[240:241], v230 offset:16384\n"
"v_add_f32 v216, v216, v4\n"
"v_mov_b32 v234, s73\n"
"v_mov_b32 v235, s73\n"
"v_mfma_f32_16x16x32_bf16 v[76:79], a[216:219], v[80:83], v[76:79]\n"
"v_exp_f32 v37, v37\n"
"v_mfma_f32_16x16x32_bf16 v[76:79], a[220:223], v[88:91], v[76:79]\n"
"ds_read_b64_tr_b16 a[242:243], v230 offset:20480\n"
"v_add_f32 v216, v216, v5\n"
"v_cvt_pk_bf16_f32 v20, v4, v5\n"
"v_mov_b32 v236, s73\n"
"v_mov_b32 v237, s73\n"
"v_mfma_f32_16x16x32_bf16 v[108:111], a[220:223], v[120:123], v[204:207]\n"
"v_exp_f32 v38, v38\n"
"v_mfma_f32_16x16x32_bf16 v[108:111], a[216:219], v[112:115], v[108:111]\n"
"ds_read_b64_tr_b16 a[244:245], v231 offset:16384\n"
"v_add_f32 v216, v216, v6\n"
"v_mfma_f32_16x16x32_bf16 v[108:111], a[212:215], v[104:107], v[108:111]\n"
"v_exp_f32 v39, v39\n"
"v_mfma_f32_16x16x32_bf16 v[108:111], a[208:211], v[96:99], v[108:111]\n"
"ds_read_b64_tr_b16 a[246:247], v231 offset:20480\n"
"v_add_f32 v216, v216, v7\n"
"v_cvt_pk_bf16_f32 v21, v6, v7\n"
"v_mfma_f32_16x16x32_bf16 v[140:143], a[208:211], v[128:131], v[208:211]\n"
"buffer_store_dwordx4 v[234:237], v243, s[56:59], s68 offen offset:3072 sc1\n"
"v_exp_f32 v68, v68\n"
"v_mfma_f32_16x16x32_bf16 v[140:143], a[212:215], v[136:139], v[140:143]\n"
"ds_read_b64_tr_b16 a[248:249], v232 offset:16384\n"
"v_add_f32 v217, v217, v36\n"
"v_mfma_f32_16x16x32_bf16 v[140:143], a[216:219], v[144:147], v[140:143]\n"
"v_exp_f32 v69, v69\n"
"v_mfma_f32_16x16x32_bf16 v[140:143], a[220:223], v[152:155], v[140:143]\n"
"ds_read_b64_tr_b16 a[250:251], v232 offset:20480\n"
"v_add_f32 v217, v217, v37\n"
"v_cvt_pk_bf16_f32 v52, v36, v37\n"
"v_mfma_f32_16x16x32_bf16 v[172:175], a[220:223], v[184:187], v[212:215]\n"
"v_exp_f32 v70, v70\n"
"v_mfma_f32_16x16x32_bf16 v[172:175], a[216:219], v[176:179], v[172:175]\n"
"ds_read_b64_tr_b16 a[252:253], v233 offset:16384\n"
"v_add_f32 v217, v217, v38\n"
"v_mfma_f32_16x16x32_bf16 v[172:175], a[212:215], v[168:171], v[172:175]\n"
"s_waitcnt lgkmcnt(3)\n"
"v_exp_f32 v71, v71\n"
"v_mfma_f32_16x16x32_bf16 v[172:175], a[208:211], v[160:163], v[172:175]\n"
"ds_read_b64_tr_b16 a[254:255], v233 offset:20480\n"
"v_add_f32 v217, v217, v39\n"
"v_cvt_pk_bf16_f32 v53, v38, v39\n"
"v_mfma_f32_16x16x32_bf16 a[0:3], a[224:227], v[28:31], a[0:3]\n"
"v_exp_f32 v100, v100\n"
"v_mfma_f32_16x16x32_bf16 a[4:7], a[224:227], v[60:63], a[4:7]\n"
"v_add_f32 v218, v218, v68\n"
"v_add_f32 v218, v218, v69\n"
"v_mfma_f32_16x16x32_bf16 a[8:11], a[224:227], v[92:95], a[8:11]\n"
"v_exp_f32 v101, v101\n"
"v_mfma_f32_16x16x32_bf16 a[12:15], a[224:227], v[124:127], a[12:15]\n"
"v_cvt_pk_bf16_f32 v84, v68, v69\n"
"v_add_f32 v218, v218, v70\n"
"v_mfma_f32_16x16x32_bf16 a[16:19], a[224:227], v[156:159], a[16:19]\n"
"s_memrealtime s[88:89]\n"
"v_exp_f32 v102, v102\n"
"v_mfma_f32_16x16x32_bf16 a[20:23], a[224:227], v[188:191], a[20:23]\n"
"v_add_f32 v218, v218, v71\n"
"v_cvt_pk_bf16_f32 v85, v70, v71\n"
"v_mfma_f32_16x16x32_bf16 a[44:47], a[228:231], v[188:191], a[44:47]\n"
"v_exp_f32 v103, v103\n"
"v_mfma_f32_16x16x32_bf16 a[40:43], a[228:231], v[156:159], a[40:43]\n"
"v_add_f32 v219, v219, v100\n"
"v_add_f32 v219, v219, v101\n"
"v_mfma_f32_16x16x32_bf16 a[36:39], a[228:231], v[124:127], a[36:39]\n"
"LP1_%=:\n"
"s_waitcnt lgkmcnt(0)\n"
"s_sub_u32 s89, s88, s86\n"
"s_cmp_ge_i32 s89, 0\n"
"s_cbranch_scc1 LQ1_%=\n"
"s_memrealtime s[88:89]\n"
"s_branch LP1_%=\n"
"LQ1_%=:\n"
"s_waitcnt vmcnt(8) lgkmcnt(0)\n"
"s_barrier\n"
"v_exp_f32 v132, v132\n"
"v_mfma_f32_16x16x32_bf16 a[32:35], a[228:231], v[92:95], a[32:35]\n"
"ds_read_b64 a[192:193], v222 offset:49152\n"
"v_cvt_pk_bf16_f32 v116, v100, v101\n"
"v_mfma_f32_16x16x32_bf16 a[28:31], a[228:231], v[60:63], a[28:31]\n"
"s_add_u32 s86, s86, 124\n"
"v_exp_f32 v133, v133\n"
"v_mfma_f32_16x16x32_bf16 a[24:27], a[228:231], v[28:31], a[24:27]\n"
"v_add_f32 v219, v219, v102\n"
"v_add_f32 v219, v219, v103\n"
"v_mfma_f32_16x16x32_bf16 a[48:51], a[232:235], v[28:31], a[48:51]\n"
"v_exp_f32 v134, v134\n"
"v_mfma_f32_16x16x32_bf16 a[52:55], a[232:235], v[60:63], a[52:55]\n"
"s_add_u32 m0, s62, 0x8000\n"
"ds_read_b64 a[194:195], v223 offset:49152\n"
"v_cvt_pk_bf16_f32 v117, v102, v103\n"
"v_mfma_f32_16x16x32_bf16 a[56:59], a[232:235], v[92:95], a[56:59]\n"
"s_add_u32 s84, s84, 0x5000\n"
"s_and_b32 s84, s84, 0x1ffff\n"
"buffer_load_dwordx4 v240, s[48:51], s84 offen nt lds\n"
"v_exp_f32 v135, v135\n"
"v_mfma_f32_16x16x32_bf16 a[60:63], a[232:235], v[124:127], a[60:63]\n"
"v_add_f32 v220, v220, v132\n"
"v_add_f32 v220, v220, v133\n"
"v_mfma_f32_16x16x32_bf16 a[64:67], a[232:235], v[156:159], a[64:67]\n"
"v_exp_f32 v164, v164\n"
"v_mfma_f32_16x16x32_bf16 a[68:71], a[232:235], v[188:191], a[68:71]\n"
"ds_read_b64 a[196:197], v224 offset:49152\n"
"v_cvt_pk_bf16_f32 v148, v132, v133\n"
"v_mfma_f32_16x16x32_bf16 a[92:95], a[236:239], v[188:191], a[92:95]\n"
"v_exp_f32 v165, v165\n"
"v_mfma_f32_16x16x32_bf16 a[88:91], a[236:239], v[156:159], a[88:91]\n"
"v_add_f32 v220, v220, v134\n"
"v_add_f32 v220, v220, v135\n"
"v_mfma_f32_16x16x32_bf16 a[84:87], a[236:239], v[124:127], a[84:87]\n"
"v_exp_f32 v166, v166\n"
"v_mfma_f32_16x16x32_bf16 a[80:83], a[236:239], v[92:95], a[80:83]\n"
"ds_read_b64 a[198:199], v225 offset:49152\n"
"v_cvt_pk_bf16_f32 v149, v134, v135\n"
"v_mfma_f32_16x16x32_bf16 a[76:79], a[236:239], v[60:63], a[76:79]\n"
"v_exp_f32 v167, v167\n"
"v_mfma_f32_16x16x32_bf16 a[72:75], a[236:239], v[28:31], a[72:75]\n"
"v_add_f32 v221, v221, v164\n"
"v_add_f32 v221, v221, v165\n"
"v_mfma_f32_16x16x32_bf16 a[96:99], a[240:243], v[28:31], a[96:99]\n"
"v_exp_f32 v12, v12\n"
"v_mfma_f32_16x16x32_bf16 a[100:103], a[240:243], v[60:63], a[100:103]\n"
"ds_read_b64 a[200:201], v246 offset:49152\n"
"v_cvt_pk_bf16_f32 v180, v164, v165\n"
"v_mfma_f32_16x16x32_bf16 a[104:107], a[240:243], v[92:95], a[104:107]\n"
"v_exp_f32 v13, v13\n"
"v_mfma_f32_16x16x32_bf16 a[108:111], a[240:243], v[124:127], a[108:111]\n"
"v_add_f32 v221, v221, v166\n"
"v_add_f32 v221, v221, v167\n"
"v_mfma_f32_16x16x32_bf16 a[112:115], a[240:243], v[156:159], a[112:115]\n"
"buffer_load_dwordx4 v241, s[48:51], s84 offen offset:1024 nt lds\n"
"v_exp_f32 v14, v14\n"
"v_mfma_f32_16x16x32_bf16 a[116:119], a[240:243], v[188:191], a[116:119]\n"
"ds_read_b64 a[202:203], v247 offset:49152\n"
"v_cvt_pk_bf16_f32 v181, v166, v167\n"
"v_mfma_f32_16x16x32_bf16 a[140:143], a[244:247], v[188:191], a[140:143]\n"
"v_exp_f32 v15, v15\n"
"v_mfma_f32_16x16x32_bf16 a[136:139], a[244:247], v[156:159], a[136:139]\n"
"v_add_f32 v216, v216, v12\n"
"v_add_f32 v216, v216, v13\n"
"v_mfma_f32_16x16x32_bf16 a[132:135], a[244:247], v[124:127], a[132:135]\n"
"v_exp_f32 v44, v44\n"
"v_mfma_f32_16x16x32_bf16 a[128:131], a[244:247], v[92:95], a[128:131]\n"
"ds_read_b64 a[204:205], v248 offset:49152\n"
"v_cvt_pk_bf16_f32 v22, v12, v13\n"
"v_mfma_f32_16x16x32_bf16 a[124:127], a[244:247], v[60:63], a[124:127]\n"
"v_exp_f32 v45, v45\n"
"v_mfma_f32_16x16x32_bf16 a[120:123], a[244:247], v[28:31], a[120:123]\n"
"v_add_f32 v216, v216, v14\n"
"v_add_f32 v216, v216, v15\n"
"v_mfma_f32_16x16x32_bf16 a[144:147], a[248:251], v[28:31], a[144:147]\n"
"v_exp_f32 v46, v46\n"
"v_mfma_f32_16x16x32_bf16 a[148:151], a[248:251], v[60:63], a[148:151]\n"
"ds_read_b64 a[206:207], v249 offset:49152\n"
"v_cvt_pk_bf16_f32 v23, v14, v15\n"
"v_mfma_f32_16x16x32_bf16 a[152:155], a[248:251], v[92:95], a[152:155]\n"
"v_exp_f32 v47, v47\n"
"v_mfma_f32_16x16x32_bf16 a[156:159], a[248:251], v[124:127], a[156:159]\n"
"s_add_u32 m0, s62, 0x9000\n"
"v_add_f32 v217, v217, v44\n"
"v_add_f32 v217, v217, v45\n"
"v_mfma_f32_16x16x32_bf16 a[160:163], a[248:251], v[156:159], a[160:163]\n"
"s_add_u32 s84, s84, 0x1000\n"
"buffer_load_dwordx4 v240, s[48:51], s84 offen nt lds\n"
"v_exp_f32 v76, v76\n"
"v_mfma_f32_16x16x32_bf16 a[164:167], a[248:251], v[188:191], a[164:167]\n"
"v_cvt_pk_bf16_f32 v54, v44, v45\n"
"v_add_f32 v217, v217, v46\n"
"v_mfma_f32_16x16x32_bf16 a[188:191], a[252:255], v[188:191], a[188:191]\n"
"v_exp_f32 v77, v77\n"
"v_mfma_f32_16x16x32_bf16 a[184:187], a[252:255], v[156:159], a[184:187]\n"
"v_add_f32 v217, v217, v47\n"
"v_cvt_pk_bf16_f32 v55, v46, v47\n"
"v_mfma_f32_16x16x32_bf16 a[180:183], a[252:255], v[124:127], a[180:183]\n"
"v_exp_f32 v78, v78\n"
"v_mfma_f32_16x16x32_bf16 a[176:179], a[252:255], v[92:95], a[176:179]\n"
"v_add_f32 v218, v218, v76\n"
"v_add_f32 v218, v218, v77\n"
"v_mfma_f32_16x16x32_bf16 a[172:175], a[252:255], v[60:63], a[172:175]\n"
"s_waitcnt lgkmcnt(0)\n"
"v_exp_f32 v79, v79\n"
"v_mfma_f32_16x16x32_bf16 a[168:171], a[252:255], v[28:31], a[168:171]\n"
"v_cvt_pk_bf16_f32 v86, v76, v77\n"
"v_add_f32 v218, v218, v78\n"
"v_mfma_f32_16x16x32_bf16 v[4:7], a[192:195], v[0:3], v[192:195]\n"
"v_exp_f32 v108, v108\n"
"v_mfma_f32_16x16x32_bf16 v[4:7], a[196:199], v[8:11], v[4:7]\n"
"ds_read_b64 a[208:209], v222 offset:53248\n"
"v_add_f32 v218, v218, v79\n"
"v_mfma_f32_16x16x32_bf16 v[4:7], a[200:203], v[16:19], v[4:7]\n"
"v_exp_f32 v109, v109\n"
"v_mfma_f32_16x16x32_bf16 v[4:7], a[204:207], v[24:27], v[4:7]\n"
"ds_read_b64 a[210:211], v223 offset:53248\n"
"v_cvt_pk_bf16_f32 v87, v78, v79\n"
"v_mfma_f32_16x16x32_bf16 v[36:39], a[204:207], v[56:59], v[196:199]\n"
"v_exp_f32 v110, v110\n"
"v_mfma_f32_16x16x32_bf16 v[36:39], a[200:203], v[48:51], v[36:39]\n"
"ds_read_b64 a[212:213], v224 offset:53248\n"
"v_add_f32 v219, v219, v108\n"
"v_mfma_f32_16x16x32_bf16 v[36:39], a[196:199], v[40:43], v[36:39]\n"
"buffer_load_dwordx4 v241, s[48:51], s84 offen offset:1024 nt lds\n"
"v_exp_f32 v111, v111\n"
"v_mfma_f32_16x16x32_bf16 v[36:39], a[192:195], v[32:35], v[36:39]\n"
"ds_read_b64 a[214:215], v225 offset:53248\n"
"v_add_f32 v219, v219, v109\n"
"v_mfma_f32_16x16x32_bf16 v[68:71], a[192:195], v[64:67], v[200:203]\n"
"v_exp_f32 v140, v140\n"
"v_mfma_f32_16x16x32_bf16 v[68:71], a[196:199], v[72:75], v[68:71]\n"
"ds_read_b64 a[216:217], v246 offset:53248\n"
"v_cvt_pk_bf16_f32 v118, v108, v109\n"
"v_mfma_f32_16x16x32_bf16 v[68:71], a[200:203], v[80:83], v[68:71]\n"
"v_exp_f32 v141, v141\n"
"v_mfma_f32_16x16x32_bf16 v[68:71], a[204:207], v[88:91], v[68:71]\n"
"ds_read_b64 a[218:219], v247 offset:53248\n"
"v_add_f32 v219, v219, v110\n"
"v_mfma_f32_16x16x32_bf16 v[100:103], a[204:207], v[120:123], v[204:207]\n"
"v_exp_f32 v142, v142\n"
"v_mfma_f32_16x16x32_bf16 v[100:103], a[200:203], v[112:115], v[100:103]\n"
"ds_read_b64 a[220:221], v248 offset:53248\n"
"v_add_f32 v219, v219, v111\n"
"v_mfma_f32_16x16x32_bf16 v[100:103], a[196:199], v[104:107], v[100:103]\n"
"v_exp_f32 v143, v143\n"
"v_mfma_f32_16x16x32_bf16 v[100:103], a[192:195], v[96:99], v[100:103]\n"
"ds_read_b64 a[222:223], v249 offset:53248\n"
"v_cvt_pk_bf16_f32 v119, v110, v111\n"
"v_mfma_f32_16x16x32_bf16 v[132:135], a[192:195], v[128:131], v[208:211]\n"
"v_exp_f32 v172, v172\n"
"v_mfma_f32_16x16x32_bf16 v[132:135], a[196:199], v[136:139], v[132:135]\n"
"ds_read_b64_tr_b16 a[224:225], v226 offset:32768\n"
"v_add_f32 v220, v220, v140\n"
"v_mfma_f32_16x16x32_bf16 v[132:135], a[200:203], v[144:147], v[132:135]\n"
"v_exp_f32 v173, v173\n"
"v_mfma_f32_16x16x32_bf16 v[132:135], a[204:207], v[152:155], v[132:135]\n"
"s_add_u32 m0, s63, 0x4000\n"
"ds_read_b64_tr_b16 a[226:227], v226 offset:36864\n"
"v_add_f32 v220, v220, v141\n"
"v_cvt_pk_bf16_f32 v150, v140, v141\n"
"v_mfma_f32_16x16x32_bf16 v[164:167], a[204:207], v[184:187], v[212:215]\n"
"s_add_u32 s85, s85, 0x3000\n"
"s_and_b32 s85, s85, 0x1ffff\n"
"buffer_load_dwordx4 v242, s[52:55], s85 offen nt lds\n"
"v_exp_f32 v174, v174\n"
"v_mfma_f32_16x16x32_bf16 v[164:167], a[200:203], v[176:179], v[164:167]\n"
"ds_read_b64_tr_b16 a[228:229], v227 offset:32768\n"
"v_add_f32 v220, v220, v142\n"
"v_mfma_f32_16x16x32_bf16 v[164:167], a[196:199], v[168:171], v[164:167]\n"
"s_waitcnt lgkmcnt(3)\n"
"v_exp_f32 v175, v175\n"
"v_mfma_f32_16x16x32_bf16 v[164:167], a[192:195], v[160:163], v[164:167]\n"
"ds_read_b64_tr_b16 a[230:231], v227 offset:36864\n"
"v_add_f32 v220, v220, v143\n"
"v_cvt_pk_bf16_f32 v151, v142, v143\n"
"v_mfma_f32_16x16x32_bf16 v[12:15], a[208:211], v[0:3], v[192:195]\n"
"v_exp_f32 v4, v4\n"
"v_mfma_f32_16x16x32_bf16 v[12:15], a[212:215], v[8:11], v[12:15]\n"
"ds_read_b64_tr_b16 a[232:233], v228 offset:32768\n"
"v_add_f32 v221, v221, v172\n"
"v_mfma_f32_16x16x32_bf16 v[12:15], a[216:219], v[16:19], v[12:15]\n"
"v_exp_f32 v5, v5\n"
"v_mfma_f32_16x16x32_bf16 v[12:15], a[220:223], v[24:27], v[12:15]\n"
"ds_read_b64_tr_b16 a[234:235], v228 offset:36864\n"
"v_add_f32 v221, v221, v173\n"
"v_cvt_pk_bf16_f32 v182, v172, v173\n"
"v_mfma_f32_16x16x32_bf16 v[44:47], a[220:223], v[56:59], v[196:199]\n"
"v_exp_f32 v6, v6\n"
"v_mfma_f32_16x16x32_bf16 v[44:47], a[216:219], v[48:51], v[44:47]\n"
"ds_read_b64_tr_b16 a[236:237], v229 offset:32768\n"
"v_add_f32 v221, v221, v174\n"
"v_mfma_f32_16x16x32_bf16 v[44:47], a[212:215], v[40:43], v[44:47]\n"
"v_exp_f32 v7, v7\n"
"v_mfma_f32_16x16x32_bf16 v[44:47], a[208:211], v[32:35], v[44:47]\n"
"ds_read_b64_tr_b16 a[238:239], v229 offset:36864\n"
"v_add_f32 v221, v221, v175\n"
"v_cvt_pk_bf16_f32 v183, v174, v175\n"
"v_mfma_f32_16x16x32_bf16 v[76:79], a[208:211], v[64:67], v[200:203]\n"
"v_exp_f32 v36, v36\n"
"v_mfma_f32_16x16x32_bf16 v[76:79], a[212:215], v[72:75], v[76:79]\n"
"ds_read_b64_tr_b16 a[240:241], v230 offset:32768\n"
"v_add_f32 v216, v216, v4\n"
"v_mfma_f32_16x16x32_bf16 v[76:79], a[216:219], v[80:83], v[76:79]\n"
"buffer_load_dwordx4 v242, s[52:55], s85 offen offset:2048 nt lds\n"
"v_exp_f32 v37, v37\n"
"v_mfma_f32_16x16x32_bf16 v[76:79], a[220:223], v[88:91], v[76:79]\n"
"ds_read_b64_tr_b16 a[242:243], v230 offset:36864\n"
"v_add_f32 v216, v216, v5\n"
"v_cvt_pk_bf16_f32 v28, v4, v5\n"
"v_mfma_f32_16x16x32_bf16 v[108:111], a[220:223], v[120:123], v[204:207]\n"
"v_exp_f32 v38, v38\n"
"v_mfma_f32_16x16x32_bf16 v[108:111], a[216:219], v[112:115], v[108:111]\n"
"ds_read_b64_tr_b16 a[244:245], v231 offset:32768\n"
"v_add_f32 v216, v216, v6\n"
"v_mfma_f32_16x16x32_bf16 v[108:111], a[212:215], v[104:107], v[108:111]\n"
"v_exp_f32 v39, v39\n"
"v_mfma_f32_16x16x32_bf16 v[108:111], a[208:211], v[96:99], v[108:111]\n"
"ds_read_b64_tr_b16 a[246:247], v231 offset:36864\n"
"v_add_f32 v216, v216, v7\n"
"v_cvt_pk_bf16_f32 v29, v6, v7\n"
"v_mfma_f32_16x16x32_bf16 v[140:143], a[208:211], v[128:131], v[208:211]\n"
"v_exp_f32 v68, v68\n"
"v_mfma_f32_16x16x32_bf16 v[140:143], a[212:215], v[136:139], v[140:143]\n"
"ds_read_b64_tr_b16 a[248:249], v232 offset:32768\n"
"v_add_f32 v217, v217, v36\n"
"v_mfma_f32_16x16x32_bf16 v[140:143], a[216:219], v[144:147], v[140:143]\n"
"v_exp_f32 v69, v69\n"
"v_mfma_f32_16x16x32_bf16 v[140:143], a[220:223], v[152:155], v[140:143]\n"
"ds_read_b64_tr_b16 a[250:251], v232 offset:36864\n"
"v_add_f32 v217, v217, v37\n"
"v_cvt_pk_bf16_f32 v60, v36, v37\n"
"v_mfma_f32_16x16x32_bf16 v[172:175], a[220:223], v[184:187], v[212:215]\n"
"v_exp_f32 v70, v70\n"
"v_mfma_f32_16x16x32_bf16 v[172:175], a[216:219], v[176:179], v[172:175]\n"
"ds_read_b64_tr_b16 a[252:253], v233 offset:32768\n"
"v_add_f32 v217, v217, v38\n"
"v_mfma_f32_16x16x32_bf16 v[172:175], a[212:215], v[168:171], v[172:175]\n"
"s_waitcnt lgkmcnt(3)\n"
"v_exp_f32 v71, v71\n"
"v_mfma_f32_16x16x32_bf16 v[172:175], a[208:211], v[160:163], v[172:175]\n"
"s_add_u32 m0, s63, 0x5000\n"
"ds_read_b64_tr_b16 a[254:255], v233 offset:36864\n"
"v_add_f32 v217, v217, v39\n"
"v_cvt_pk_bf16_f32 v61, v38, v39\n"
"v_mfma_f32_16x16x32_bf16 a[0:3], a[224:227], v[20:23], a[0:3]\n"
"s_add_u32 s85, s85, 0x1000\n"
"buffer_load_dwordx4 v242, s[52:55], s85 offen nt lds\n"
"v_exp_f32 v100, v100\n"
"v_mfma_f32_16x16x32_bf16 a[4:7], a[224:227], v[52:55], a[4:7]\n"
"v_add_f32 v218, v218, v68\n"
"v_add_f32 v218, v218, v69\n"
"v_mfma_f32_16x16x32_bf16 a[8:11], a[224:227], v[84:87], a[8:11]\n"
"v_exp_f32 v101, v101\n"
"v_mfma_f32_16x16x32_bf16 a[12:15], a[224:227], v[116:119], a[12:15]\n"
"v_cvt_pk_bf16_f32 v92, v68, v69\n"
"v_add_f32 v218, v218, v70\n"
"v_mfma_f32_16x16x32_bf16 a[16:19], a[224:227], v[148:151], a[16:19]\n"
"s_memrealtime s[88:89]\n"
"v_exp_f32 v102, v102\n"
"v_mfma_f32_16x16x32_bf16 a[20:23], a[224:227], v[180:183], a[20:23]\n"
"v_add_f32 v218, v218, v71\n"
"v_cvt_pk_bf16_f32 v93, v70, v71\n"
"v_mfma_f32_16x16x32_bf16 a[44:47], a[228:231], v[180:183], a[44:47]\n"
"v_exp_f32 v103, v103\n"
"v_mfma_f32_16x16x32_bf16 a[40:43], a[228:231], v[148:151], a[40:43]\n"
"v_add_f32 v219, v219, v100\n"
"v_add_f32 v219, v219, v101\n"
"v_mfma_f32_16x16x32_bf16 a[36:39], a[228:231], v[116:119], a[36:39]\n"
"LP2_%=:\n"
"s_waitcnt lgkmcnt(0)\n"
"s_sub_u32 s89, s88, s86\n"
"s_cmp_ge_i32 s89, 0\n"
"s_cbranch_scc1 LQ2_%=\n"
"s_memrealtime s[88:89]\n"
"s_branch LP2_%=\n"
"LQ2_%=:\n"
"s_waitcnt vmcnt(11) lgkmcnt(0)\n"
"s_barrier\n"
"v_exp_f32 v132, v132\n"
"v_mfma_f32_16x16x32_bf16 a[32:35], a[228:231], v[84:87], a[32:35]\n"
"ds_read_b64 a[192:193], v222 offset:0\n"
"v_cvt_pk_bf16_f32 v124, v100, v101\n"
"v_mfma_f32_16x16x32_bf16 a[28:31], a[228:231], v[52:55], a[28:31]\n"
"s_add_u32 s86, s86, 124\n"
"v_exp_f32 v133, v133\n"
"v_mfma_f32_16x16x32_bf16 a[24:27], a[228:231], v[20:23], a[24:27]\n"
"v_add_f32 v219, v219, v102\n"
"v_add_f32 v219, v219, v103\n"
"v_mfma_f32_16x16x32_bf16 a[48:51], a[232:235], v[20:23], a[48:51]\n"
"buffer_load_dwordx4 v242, s[52:55], s85 offen offset:2048 nt lds\n"
"v_exp_f32 v134, v134\n"
"v_mfma_f32_16x16x32_bf16 a[52:55], a[232:235], v[52:55], a[52:55]\n"
"ds_read_b64 a[194:195], v223 offset:0\n"
"v_cvt_pk_bf16_f32 v125, v102, v103\n"
"v_mfma_f32_16x16x32_bf16 a[56:59], a[232:235], v[84:87], a[56:59]\n"
"v_exp_f32 v135, v135\n"
"v_mfma_f32_16x16x32_bf16 a[60:63], a[232:235], v[116:119], a[60:63]\n"
"v_add_f32 v220, v220, v132\n"
"v_add_f32 v220, v220, v133\n"
"v_mfma_f32_16x16x32_bf16 a[64:67], a[232:235], v[148:151], a[64:67]\n"
"v_exp_f32 v164, v164\n"
"v_mfma_f32_16x16x32_bf16 a[68:71], a[232:235], v[180:183], a[68:71]\n"
"ds_read_b64 a[196:197], v224 offset:0\n"
"v_cvt_pk_bf16_f32 v156, v132, v133\n"
"v_mfma_f32_16x16x32_bf16 a[92:95], a[236:239], v[180:183], a[92:95]\n"
"v_exp_f32 v165, v165\n"
"v_mfma_f32_16x16x32_bf16 a[88:91], a[236:239], v[148:151], a[88:91]\n"
"v_add_f32 v220, v220, v134\n"
"v_add_f32 v220, v220, v135\n"
"v_mfma_f32_16x16x32_bf16 a[84:87], a[236:239], v[116:119], a[84:87]\n"
"v_exp_f32 v166, v166\n"
"v_mfma_f32_16x16x32_bf16 a[80:83], a[236:239], v[84:87], a[80:83]\n"
"ds_read_b64 a[198:199], v225 offset:0\n"
"v_cvt_pk_bf16_f32 v157, v134, v135\n"
"v_mfma_f32_16x16x32_bf16 a[76:79], a[236:239], v[52:55], a[76:79]\n"
"v_exp_f32 v167, v167\n"
"v_mfma_f32_16x16x32_bf16 a[72:75], a[236:239], v[20:23], a[72:75]\n"
"v_add_f32 v221, v221, v164\n"
"v_add_f32 v221, v221, v165\n"
"v_mfma_f32_16x16x32_bf16 a[96:99], a[240:243], v[20:23], a[96:99]\n"
"v_exp_f32 v12, v12\n"
"v_mfma_f32_16x16x32_bf16 a[100:103], a[240:243], v[52:55], a[100:103]\n"
"s_add_u32 m0, s62, 0xc000\n"
"ds_read_b64 a[200:201], v246 offset:0\n"
"v_cvt_pk_bf16_f32 v188, v164, v165\n"
"v_mfma_f32_16x16x32_bf16 a[104:107], a[240:243], v[84:87], a[104:107]\n"
"s_add_u32 s84, s84, 0x1000\n"
"s_and_b32 s84, s84, 0x1ffff\n"
"buffer_load_dwordx4 v240, s[48:51], s84 offen nt lds\n"
"v_exp_f32 v13, v13\n"
"v_mfma_f32_16x16x32_bf16 a[108:111], a[240:243], v[116:119], a[108:111]\n"
"v_add_f32 v221, v221, v166\n"
"v_add_f32 v221, v221, v167\n"
"v_mfma_f32_16x16x32_bf16 a[112:115], a[240:243], v[148:151], a[112:115]\n"
"v_exp_f32 v14, v14\n"
"v_mfma_f32_16x16x32_bf16 a[116:119], a[240:243], v[180:183], a[116:119]\n"
"ds_read_b64 a[202:203], v247 offset:0\n"
"v_cvt_pk_bf16_f32 v189, v166, v167\n"
"v_mfma_f32_16x16x32_bf16 a[140:143], a[244:247], v[180:183], a[140:143]\n"
"v_exp_f32 v15, v15\n"
"v_mfma_f32_16x16x32_bf16 a[136:139], a[244:247], v[148:151], a[136:139]\n"
"v_add_f32 v216, v216, v12\n"
"v_add_f32 v216, v216, v13\n"
"v_mfma_f32_16x16x32_bf16 a[132:135], a[244:247], v[116:119], a[132:135]\n"
"v_exp_f32 v44, v44\n"
"v_mfma_f32_16x16x32_bf16 a[128:131], a[244:247], v[84:87], a[128:131]\n"
"ds_read_b64 a[204:205], v248 offset:0\n"
"v_cvt_pk_bf16_f32 v30, v12, v13\n"
"v_mfma_f32_16x16x32_bf16 a[124:127], a[244:247], v[52:55], a[124:127]\n"
"v_exp_f32 v45, v45\n"
"v_mfma_f32_16x16x32_bf16 a[120:123], a[244:247], v[20:23], a[120:123]\n"
"v_add_f32 v216, v216, v14\n"
"v_add_f32 v216, v216, v15\n"
"v_mfma_f32_16x16x32_bf16 a[144:147], a[248:251], v[20:23], a[144:147]\n"
"v_exp_f32 v46, v46\n"
"v_mfma_f32_16x16x32_bf16 a[148:151], a[248:251], v[52:55], a[148:151]\n"
"ds_read_b64 a[206:207], v249 offset:0\n"
"v_cvt_pk_bf16_f32 v31, v14, v15\n"
"v_mfma_f32_16x16x32_bf16 a[152:155], a[248:251], v[84:87], a[152:155]\n"
"buffer_load_dwordx4 v241, s[48:51], s84 offen offset:1024 nt lds\n"
"v_exp_f32 v47, v47\n"
"v_mfma_f32_16x16x32_bf16 a[156:159], a[248:251], v[116:119], a[156:159]\n"
"v_add_f32 v217, v217, v44\n"
"v_add_f32 v217, v217, v45\n"
"v_mfma_f32_16x16x32_bf16 a[160:163], a[248:251], v[148:151], a[160:163]\n"
"v_exp_f32 v76, v76\n"
"v_mfma_f32_16x16x32_bf16 a[164:167], a[248:251], v[180:183], a[164:167]\n"
"v_cvt_pk_bf16_f32 v62, v44, v45\n"
"v_add_f32 v217, v217, v46\n"
"v_mfma_f32_16x16x32_bf16 a[188:191], a[252:255], v[180:183], a[188:191]\n"
"v_exp_f32 v77, v77\n"
"v_mfma_f32_16x16x32_bf16 a[184:187], a[252:255], v[148:151], a[184:187]\n"
"v_add_f32 v217, v217, v47\n"
"v_cvt_pk_bf16_f32 v63, v46, v47\n"
"v_mfma_f32_16x16x32_bf16 a[180:183], a[252:255], v[116:119], a[180:183]\n"
"v_exp_f32 v78, v78\n"
"v_mfma_f32_16x16x32_bf16 a[176:179], a[252:255], v[84:87], a[176:179]\n"
"v_add_f32 v218, v218, v76\n"
"v_add_f32 v218, v218, v77\n"
"v_mfma_f32_16x16x32_bf16 a[172:175], a[252:255], v[52:55], a[172:175]\n"
"s_waitcnt lgkmcnt(0)\n"
"v_exp_f32 v79, v79\n"
"v_mfma_f32_16x16x32_bf16 a[168:171], a[252:255], v[20:23], a[168:171]\n"
"v_cvt_pk_bf16_f32 v94, v76, v77\n"
"v_add_f32 v218, v218, v78\n"
"v_mfma_f32_16x16x32_bf16 v[4:7], a[192:195], v[0:3], v[192:195]\n"
"v_exp_f32 v108, v108\n"
"v_mfma_f32_16x16x32_bf16 v[4:7], a[196:199], v[8:11], v[4:7]\n"
"ds_read_b64 a[208:209], v222 offset:4096\n"
"v_add_f32 v218, v218, v79\n"
"v_mfma_f32_16x16x32_bf16 v[4:7], a[200:203], v[16:19], v[4:7]\n"
"v_exp_f32 v109, v109\n"
"v_mfma_f32_16x16x32_bf16 v[4:7], a[204:207], v[24:27], v[4:7]\n"
"s_add_u32 m0, s62, 0xd000\n"
"ds_read_b64 a[210:211], v223 offset:4096\n"
"v_cvt_pk_bf16_f32 v95, v78, v79\n"
"v_mfma_f32_16x16x32_bf16 v[36:39], a[204:207], v[56:59], v[196:199]\n"
"s_add_u32 s84, s84, 0x1000\n"
"buffer_load_dwordx4 v240, s[48:51], s84 offen nt lds\n"
"v_exp_f32 v110, v110\n"
"v_mfma_f32_16x16x32_bf16 v[36:39], a[200:203], v[48:51], v[36:39]\n"
"ds_read_b64 a[212:213], v224 offset:4096\n"
"v_add_f32 v219, v219, v108\n"
"v_mfma_f32_16x16x32_bf16 v[36:39], a[196:199], v[40:43], v[36:39]\n"
"v_exp_f32 v111, v111\n"
"v_mfma_f32_16x16x32_bf16 v[36:39], a[192:195], v[32:35], v[36:39]\n"
"ds_read_b64 a[214:215], v225 offset:4096\n"
"v_add_f32 v219, v219, v109\n"
"v_mfma_f32_16x16x32_bf16 v[68:71], a[192:195], v[64:67], v[200:203]\n"
"v_exp_f32 v140, v140\n"
"v_mfma_f32_16x16x32_bf16 v[68:71], a[196:199], v[72:75], v[68:71]\n"
"ds_read_b64 a[216:217], v246 offset:4096\n"
"v_cvt_pk_bf16_f32 v126, v108, v109\n"
"v_mfma_f32_16x16x32_bf16 v[68:71], a[200:203], v[80:83], v[68:71]\n"
"v_exp_f32 v141, v141\n"
"v_mfma_f32_16x16x32_bf16 v[68:71], a[204:207], v[88:91], v[68:71]\n"
"ds_read_b64 a[218:219], v247 offset:4096\n"
"v_add_f32 v219, v219, v110\n"
"v_mfma_f32_16x16x32_bf16 v[100:103], a[204:207], v[120:123], v[204:207]\n"
"v_exp_f32 v142, v142\n"
"v_mfma_f32_16x16x32_bf16 v[100:103], a[200:203], v[112:115], v[100:103]\n"
"ds_read_b64 a[220:221], v248 offset:4096\n"
"v_add_f32 v219, v219, v111\n"
"v_mfma_f32_16x16x32_bf16 v[100:103], a[196:199], v[104:107], v[100:103]\n"
"v_exp_f32 v143, v143\n"
"v_mfma_f32_16x16x32_bf16 v[100:103], a[192:195], v[96:99], v[100:103]\n"
"ds_read_b64 a[222:223], v249 offset:4096\n"
"v_cvt_pk_bf16_f32 v127, v110, v111\n"
"v_mfma_f32_16x16x32_bf16 v[132:135], a[192:195], v[128:131], v[208:211]\n"
"v_exp_f32 v172, v172\n"
"v_mfma_f32_16x16x32_bf16 v[132:135], a[196:199], v[136:139], v[132:135]\n"
"ds_read_b64_tr_b16 a[224:225], v226 offset:49152\n"
"v_add_f32 v220, v220, v140\n"
"v_mfma_f32_16x16x32_bf16 v[132:135], a[200:203], v[144:147], v[132:135]\n"
"buffer_load_dwordx4 v241, s[48:51], s84 offen offset:1024 nt lds\n"
"v_exp_f32 v173, v173\n"
"v_mfma_f32_16x16x32_bf16 v[132:135], a[204:207], v[152:155], v[132:135]\n"
"ds_read_b64_tr_b16 a[226:227], v226 offset:53248\n"
"v_add_f32 v220, v220, v141\n"
"v_cvt_pk_bf16_f32 v158, v140, v141\n"
"v_mfma_f32_16x16x32_bf16 v[164:167], a[204:207], v[184:187], v[212:215]\n"
"v_exp_f32 v174, v174\n"
"v_mfma_f32_16x16x32_bf16 v[164:167], a[200:203], v[176:179], v[164:167]\n"
"ds_read_b64_tr_b16 a[228:229], v227 offset:49152\n"
"v_add_f32 v220, v220, v142\n"
"v_mfma_f32_16x16x32_bf16 v[164:167], a[196:199], v[168:171], v[164:167]\n"
"s_waitcnt lgkmcnt(3)\n"
"v_exp_f32 v175, v175\n"
"v_mfma_f32_16x16x32_bf16 v[164:167], a[192:195], v[160:163], v[164:167]\n"
"ds_read_b64_tr_b16 a[230:231], v227 offset:53248\n"
"v_add_f32 v220, v220, v143\n"
"v_cvt_pk_bf16_f32 v159, v142, v143\n"
"v_mfma_f32_16x16x32_bf16 v[12:15], a[208:211], v[0:3], v[192:195]\n"
"v_exp_f32 v4, v4\n"
"v_mfma_f32_16x16x32_bf16 v[12:15], a[212:215], v[8:11], v[12:15]\n"
"ds_read_b64_tr_b16 a[232:233], v228 offset:49152\n"
"v_add_f32 v221, v221, v172\n"
"v_mfma_f32_16x16x32_bf16 v[12:15], a[216:219], v[16:19], v[12:15]\n"
"v_exp_f32 v5, v5\n"
"v_mfma_f32_16x16x32_bf16 v[12:15], a[220:223], v[24:27], v[12:15]\n"
"ds_read_b64_tr_b16 a[234:235], v228 offset:53248\n"
"v_add_f32 v221, v221, v173\n"
"v_cvt_pk_bf16_f32 v190, v172, v173\n"
"v_mfma_f32_16x16x32_bf16 v[44:47], a[220:223], v[56:59], v[196:199]\n"
"v_exp_f32 v6, v6\n"
"v_mfma_f32_16x16x32_bf16 v[44:47], a[216:219], v[48:51], v[44:47]\n"
"ds_read_b64_tr_b16 a[236:237], v229 offset:49152\n"
"v_add_f32 v221, v221, v174\n"
"v_mfma_f32_16x16x32_bf16 v[44:47], a[212:215], v[40:43], v[44:47]\n"
"v_exp_f32 v7, v7\n"
"v_mfma_f32_16x16x32_bf16 v[44:47], a[208:211], v[32:35], v[44:47]\n"
"s_add_u32 m0, s63, 0x8000\n"
"ds_read_b64_tr_b16 a[238:239], v229 offset:53248\n"
"v_add_f32 v221, v221, v175\n"
"v_cvt_pk_bf16_f32 v191, v174, v175\n"
"v_mfma_f32_16x16x32_bf16 v[76:79], a[208:211], v[64:67], v[200:203]\n"
"s_add_u32 s85, s85, 0x1000\n"
"s_and_b32 s85, s85, 0x1ffff\n"
"buffer_load_dwordx4 v242, s[52:55], s85 offen nt lds\n"
"v_exp_f32 v36, v36\n"
"v_mfma_f32_16x16x32_bf16 v[76:79], a[212:215], v[72:75], v[76:79]\n"
"ds_read_b64_tr_b16 a[240:241], v230 offset:49152\n"
"v_add_f32 v216, v216, v4\n"
"v_mfma_f32_16x16x32_bf16 v[76:79], a[216:219], v[80:83], v[76:79]\n"
"v_exp_f32 v37, v37\n"
"v_mfma_f32_16x16x32_bf16 v[76:79], a[220:223], v[88:91], v[76:79]\n"
"ds_read_b64_tr_b16 a[242:243], v230 offset:53248\n"
"v_add_f32 v216, v216, v5\n"
"v_cvt_pk_bf16_f32 v20, v4, v5\n"
"v_mfma_f32_16x16x32_bf16 v[108:111], a[220:223], v[120:123], v[204:207]\n"
"v_exp_f32 v38, v38\n"
"v_mfma_f32_16x16x32_bf16 v[108:111], a[216:219], v[112:115], v[108:111]\n"
"ds_read_b64_tr_b16 a[244:245], v231 offset:49152\n"
"v_add_f32 v216, v216, v6\n"
"v_mfma_f32_16x16x32_bf16 v[108:111], a[212:215], v[104:107], v[108:111]\n"
"v_exp_f32 v39, v39\n"
"v_mfma_f32_16x16x32_bf16 v[108:111], a[208:211], v[96:99], v[108:111]\n"
"ds_read_b64_tr_b16 a[246:247], v231 offset:53248\n"
"v_add_f32 v216, v216, v7\n"
"v_cvt_pk_bf16_f32 v21, v6, v7\n"
"v_mfma_f32_16x16x32_bf16 v[140:143], a[208:211], v[128:131], v[208:211]\n"
"v_exp_f32 v68, v68\n"
"v_mfma_f32_16x16x32_bf16 v[140:143], a[212:215], v[136:139], v[140:143]\n"
"ds_read_b64_tr_b16 a[248:249], v232 offset:49152\n"
"v_add_f32 v217, v217, v36\n"
"v_mfma_f32_16x16x32_bf16 v[140:143], a[216:219], v[144:147], v[140:143]\n"
"v_exp_f32 v69, v69\n"
"v_mfma_f32_16x16x32_bf16 v[140:143], a[220:223], v[152:155], v[140:143]\n"
"ds_read_b64_tr_b16 a[250:251], v232 offset:53248\n"
"v_add_f32 v217, v217, v37\n"
"v_cvt_pk_bf16_f32 v52, v36, v37\n"
"v_mfma_f32_16x16x32_bf16 v[172:175], a[220:223], v[184:187], v[212:215]\n"
"buffer_load_dwordx4 v242, s[52:55], s85 offen offset:2048 nt lds\n"
"v_exp_f32 v70, v70\n"
"v_mfma_f32_16x16x32_bf16 v[172:175], a[216:219], v[176:179], v[172:175]\n"
"ds_read_b64_tr_b16 a[252:253], v233 offset:49152\n"
"v_add_f32 v217, v217, v38\n"
"v_mfma_f32_16x16x32_bf16 v[172:175], a[212:215], v[168:171], v[172:175]\n"
"s_waitcnt lgkmcnt(3)\n"
"v_exp_f32 v71, v71\n"
"v_mfma_f32_16x16x32_bf16 v[172:175], a[208:211], v[160:163], v[172:175]\n"
"ds_read_b64_tr_b16 a[254:255], v233 offset:53248\n"
"v_add_f32 v217, v217, v39\n"
"v_cvt_pk_bf16_f32 v53, v38, v39\n"
"v_mfma_f32_16x16x32_bf16 a[0:3], a[224:227], v[28:31], a[0:3]\n"
"v_exp_f32 v100, v100\n"
"v_mfma_f32_16x16x32_bf16 a[4:7], a[224:227], v[60:63], a[4:7]\n"
"v_add_f32 v218, v218, v68\n"
"v_add_f32 v218, v218, v69\n"
"v_mfma_f32_16x16x32_bf16 a[8:11], a[224:227], v[92:95], a[8:11]\n"
"v_exp_f32 v101, v101\n"
"v_mfma_f32_16x16x32_bf16 a[12:15], a[224:227], v[124:127], a[12:15]\n"
"v_cvt_pk_bf16_f32 v84, v68, v69\n"
"v_add_f32 v218, v218, v70\n"
"v_mfma_f32_16x16x32_bf16 a[16:19], a[224:227], v[156:159], a[16:19]\n"
"s_memrealtime s[88:89]\n"
"v_exp_f32 v102, v102\n"
"v_mfma_f32_16x16x32_bf16 a[20:23], a[224:227], v[188:191], a[20:23]\n"
"v_add_f32 v218, v218, v71\n"
"v_cvt_pk_bf16_f32 v85, v70, v71\n"
"v_mfma_f32_16x16x32_bf16 a[44:47], a[228:231], v[188:191], a[44:47]\n"
"v_exp_f32 v103, v103\n"
"v_mfma_f32_16x16x32_bf16 a[40:43], a[228:231], v[156:159], a[40:43]\n"
"v_add_f32 v219, v219, v100\n"
"v_add_f32 v219, v219, v101\n"
"v_mfma_f32_16x16x32_bf16 a[36:39], a[228:231], v[124:127], a[36:39]\n"
"LP3_%=:\n"
"s_waitcnt lgkmcnt(0)\n"
"s_sub_u32 s89, s88, s86\n"
"s_cmp_ge_i32 s89, 0\n"
"s_cbranch_scc1 LQ3_%=\n"
"s_memrealtime s[88:89]\n"
"s_branch LP3_%=\n"
"LQ3_%=:\n"
"s_waitcnt lgkmcnt(0)\n"
"s_barrier\n"
"v_exp_f32 v132, v132\n"
"v_mfma_f32_16x16x32_bf16 a[32:35], a[228:231], v[92:95], a[32:35]\n"
"ds_read_b64 a[192:193], v222 offset:16384\n"
"v_cvt_pk_bf16_f32 v116, v100, v101\n"
"v_mfma_f32_16x16x32_bf16 a[28:31], a[228:231], v[60:63], a[28:31]\n"
"s_add_u32 s86, s86, 124\n"
"v_exp_f32 v133, v133\n"
"v_mfma_f32_16x16x32_bf16 a[24:27], a[228:231], v[28:31], a[24:27]\n"
"s_add_u32 m0, s63, 0x9000\n"
"v_add_f32 v219, v219, v102\n"
"v_add_f32 v219, v219, v103\n"
"v_mfma_f32_16x16x32_bf16 a[48:51], a[232:235], v[28:31], a[48:51]\n"
"s_add_u32 s85, s85, 0x1000\n"
"buffer_load_dwordx4 v242, s[52:55], s85 offen nt lds\n"
"v_exp_f32 v134, v134\n"
"v_mfma_f32_16x16x32_bf16 a[52:55], a[232:235], v[60:63], a[52:55]\n"
"ds_read_b64 a[194:195], v223 offset:16384\n"
"v_cvt_pk_bf16_f32 v117, v102, v103\n"
"v_mfma_f32_16x16x32_bf16 a[56:59], a[232:235], v[92:95], a[56:59]\n"
"v_exp_f32 v135, v135\n"
"v_mfma_f32_16x16x32_bf16 a[60:63], a[232:235], v[124:127], a[60:63]\n"
"v_add_f32 v220, v220, v132\n"
"v_add_f32 v220, v220, v133\n"
"v_mfma_f32_16x16x32_bf16 a[64:67], a[232:235], v[156:159], a[64:67]\n"
"v_exp_f32 v164, v164\n"
"v_mfma_f32_16x16x32_bf16 a[68:71], a[232:235], v[188:191], a[68:71]\n"
"ds_read_b64 a[196:197], v224 offset:16384\n"
"v_cvt_pk_bf16_f32 v148, v132, v133\n"
"v_mfma_f32_16x16x32_bf16 a[92:95], a[236:239], v[188:191], a[92:95]\n"
"v_exp_f32 v165, v165\n"
"v_mfma_f32_16x16x32_bf16 a[88:91], a[236:239], v[156:159], a[88:91]\n"
"v_add_f32 v220, v220, v134\n"
"v_add_f32 v220, v220, v135\n"
"v_mfma_f32_16x16x32_bf16 a[84:87], a[236:239], v[124:127], a[84:87]\n"
"v_exp_f32 v166, v166\n"
"v_mfma_f32_16x16x32_bf16 a[80:83], a[236:239], v[92:95], a[80:83]\n"
"ds_read_b64 a[198:199], v225 offset:16384\n"
"v_cvt_pk_bf16_f32 v149, v134, v135\n"
"v_mfma_f32_16x16x32_bf16 a[76:79], a[236:239], v[60:63], a[76:79]\n"
"v_exp_f32 v167, v167\n"
"v_mfma_f32_16x16x32_bf16 a[72:75], a[236:239], v[28:31], a[72:75]\n"
"v_add_f32 v221, v221, v164\n"
"v_add_f32 v221, v221, v165\n"
"v_mfma_f32_16x16x32_bf16 a[96:99], a[240:243], v[28:31], a[96:99]\n"
"buffer_load_dwordx4 v242, s[52:55], s85 offen offset:2048 nt lds\n"
"v_exp_f32 v12, v12\n"
"v_mfma_f32_16x16x32_bf16 a[100:103], a[240:243], v[60:63], a[100:103]\n"
"ds_read_b64 a[200:201], v246 offset:16384\n"
"v_cvt_pk_bf16_f32 v180, v164, v165\n"
"v_mfma_f32_16x16x32_bf16 a[104:107], a[240:243], v[92:95], a[104:107]\n"
"v_exp_f32 v13, v13\n"
"v_mfma_f32_16x16x32_bf16 a[108:111], a[240:243], v[124:127], a[108:111]\n"
"v_add_f32 v221, v221, v166\n"
"v_add_f32 v221, v221, v167\n"
"v_mfma_f32_16x16x32_bf16 a[112:115], a[240:243], v[156:159], a[112:115]\n"
"v_exp_f32 v14, v14\n"
"v_mfma_f32_16x16x32_bf16 a[116:119], a[240:243], v[188:191], a[116:119]\n"
"ds_read_b64 a[202:203], v247 offset:16384\n"
"v_cvt_pk_bf16_f32 v181, v166, v167\n"
"v_mfma_f32_16x16x32_bf16 a[140:143], a[244:247], v[188:191], a[140:143]\n"
"v_exp_f32 v15, v15\n"
"v_mfma_f32_16x16x32_bf16 a[136:139], a[244:247], v[156:159], a[136:139]\n"
"v_add_f32 v216, v216, v12\n"
"v_add_f32 v216, v216, v13\n"
"v_mfma_f32_16x16x32_bf16 a[132:135], a[244:247], v[124:127], a[132:135]\n"
"v_exp_f32 v44, v44\n"
"v_mfma_f32_16x16x32_bf16 a[128:131], a[244:247], v[92:95], a[128:131]\n"
"ds_read_b64 a[204:205], v248 offset:16384\n"
"v_cvt_pk_bf16_f32 v22, v12, v13\n"
"v_mfma_f32_16x16x32_bf16 a[124:127], a[244:247], v[60:63], a[124:127]\n"
"v_exp_f32 v45, v45\n"
"v_mfma_f32_16x16x32_bf16 a[120:123], a[244:247], v[28:31], a[120:123]\n"
"v_add_f32 v216, v216, v14\n"
"v_add_f32 v216, v216, v15\n"
"v_mfma_f32_16x16x32_bf16 a[144:147], a[248:251], v[28:31], a[144:147]\n"
"v_exp_f32 v46, v46\n"
"v_mfma_f32_16x16x32_bf16 a[148:151], a[248:251], v[60:63], a[148:151]\n"
"s_add_u32 m0, s62, 0x0\n"
"ds_read_b64 a[206:207], v249 offset:16384\n"
"v_cvt_pk_bf16_f32 v23, v14, v15\n"
"v_mfma_f32_16x16x32_bf16 a[152:155], a[248:251], v[92:95], a[152:155]\n"
"s_add_u32 s84, s84, 0x1000\n"
"s_and_b32 s84, s84, 0x1ffff\n"
"buffer_load_dwordx4 v240, s[48:51], s84 offen nt lds\n"
"v_exp_f32 v47, v47\n"
"v_mfma_f32_16x16x32_bf16 a[156:159], a[248:251], v[124:127], a[156:159]\n"
"v_add_f32 v217, v217, v44\n"
"v_add_f32 v217, v217, v45\n"
"v_mfma_f32_16x16x32_bf16 a[160:163], a[248:251], v[156:159], a[160:163]\n"
"v_exp_f32 v76, v76\n"
"v_mfma_f32_16x16x32_bf16 a[164:167], a[248:251], v[188:191], a[164:167]\n"
"v_cvt_pk_bf16_f32 v54, v44, v45\n"
"v_add_f32 v217, v217, v46\n"
"v_mfma_f32_16x16x32_bf16 a[188:191], a[252:255], v[188:191], a[188:191]\n"
"v_exp_f32 v77, v77\n"
"v_mfma_f32_16x16x32_bf16 a[184:187], a[252:255], v[156:159], a[184:187]\n"
"v_add_f32 v217, v217, v47\n"
"v_cvt_pk_bf16_f32 v55, v46, v47\n"
"v_mfma_f32_16x16x32_bf16 a[180:183], a[252:255], v[124:127], a[180:183]\n"
"v_exp_f32 v78, v78\n"
"v_mfma_f32_16x16x32_bf16 a[176:179], a[252:255], v[92:95], a[176:179]\n"
"v_add_f32 v218, v218, v76\n"
"v_add_f32 v218, v218, v77\n"
"v_mfma_f32_16x16x32_bf16 a[172:175], a[252:255], v[60:63], a[172:175]\n"
"s_waitcnt lgkmcnt(0)\n"
"v_exp_f32 v79, v79\n"
"v_mfma_f32_16x16x32_bf16 a[168:171], a[252:255], v[28:31], a[168:171]\n"
"v_cvt_pk_bf16_f32 v86, v76, v77\n"
"v_add_f32 v218, v218, v78\n"
"v_mfma_f32_16x16x32_bf16 v[4:7], a[192:195], v[0:3], v[192:195]\n"
"v_exp_f32 v108, v108\n"
"v_mfma_f32_16x16x32_bf16 v[4:7], a[196:199], v[8:11], v[4:7]\n"
"ds_read_b64 a[208:209], v222 offset:20480\n"
"v_add_f32 v218, v218, v79\n"
"v_mfma_f32_16x16x32_bf16 v[4:7], a[200:203], v[16:19], v[4:7]\n"
"buffer_load_dwordx4 v241, s[48:51], s84 offen offset:1024 nt lds\n"
"v_exp_f32 v109, v109\n"
"v_mfma_f32_16x16x32_bf16 v[4:7], a[204:207], v[24:27], v[4:7]\n"
"ds_read_b64 a[210:211], v223 offset:20480\n"
"v_cvt_pk_bf16_f32 v87, v78, v79\n"
"v_mfma_f32_16x16x32_bf16 v[36:39], a[204:207], v[56:59], v[196:199]\n"
"v_exp_f32 v110, v110\n"
"v_mfma_f32_16x16x32_bf16 v[36:39], a[200:203], v[48:51], v[36:39]\n"
"ds_read_b64 a[212:213], v224 offset:20480\n"
"v_add_f32 v219, v219, v108\n"
"v_mfma_f32_16x16x32_bf16 v[36:39], a[196:199], v[40:43], v[36:39]\n"
"v_exp_f32 v111, v111\n"
"v_mfma_f32_16x16x32_bf16 v[36:39], a[192:195], v[32:35], v[36:39]\n"
"ds_read_b64 a[214:215], v225 offset:20480\n"
"v_add_f32 v219, v219, v109\n"
"v_mfma_f32_16x16x32_bf16 v[68:71], a[192:195], v[64:67], v[200:203]\n"
"v_exp_f32 v140, v140\n"
"v_mfma_f32_16x16x32_bf16 v[68:71], a[196:199], v[72:75], v[68:71]\n"
"ds_read_b64 a[216:217], v246 offset:20480\n"
"v_cvt_pk_bf16_f32 v118, v108, v109\n"
"v_mfma_f32_16x16x32_bf16 v[68:71], a[200:203], v[80:83], v[68:71]\n"
"v_exp_f32 v141, v141\n"
"v_mfma_f32_16x16x32_bf16 v[68:71], a[204:207], v[88:91], v[68:71]\n"
"ds_read_b64 a[218:219], v247 offset:20480\n"
"v_add_f32 v219, v219, v110\n"
"v_mfma_f32_16x16x32_bf16 v[100:103], a[204:207], v[120:123], v[204:207]\n"
"v_exp_f32 v142, v142\n"
"v_mfma_f32_16x16x32_bf16 v[100:103], a[200:203], v[112:115], v[100:103]\n"
"ds_read_b64 a[220:221], v248 offset:20480\n"
"v_add_f32 v219, v219, v111\n"
"v_mfma_f32_16x16x32_bf16 v[100:103], a[196:199], v[104:107], v[100:103]\n"
"v_exp_f32 v143, v143\n"
"v_mfma_f32_16x16x32_bf16 v[100:103], a[192:195], v[96:99], v[100:103]\n"
"s_add_u32 m0, s62, 0x1000\n"
"ds_read_b64 a[222:223], v249 offset:20480\n"
"v_cvt_pk_bf16_f32 v119, v110, v111\n"
"v_mfma_f32_16x16x32_bf16 v[132:135], a[192:195], v[128:131], v[208:211]\n"
"s_add_u32 s84, s84, 0x1000\n"
"buffer_load_dwordx4 v240, s[48:51], s84 offen nt lds\n"
"v_exp_f32 v172, v172\n"
"v_mfma_f32_16x16x32_bf16 v[132:135], a[196:199], v[136:139], v[132:135]\n"
"ds_read_b64_tr_b16 a[224:225], v226 offset:0\n"
"v_add_f32 v220, v220, v140\n"
"v_mfma_f32_16x16x32_bf16 v[132:135], a[200:203], v[144:147], v[132:135]\n"
"v_exp_f32 v173, v173\n"
"v_mfma_f32_16x16x32_bf16 v[132:135], a[204:207], v[152:155], v[132:135]\n"
"ds_read_b64_tr_b16 a[226:227], v226 offset:4096\n"
"v_add_f32 v220, v220, v141\n"
"v_cvt_pk_bf16_f32 v150, v140, v141\n"
"v_mfma_f32_16x16x32_bf16 v[164:167], a[204:207], v[184:187], v[212:215]\n"
"v_exp_f32 v174, v174\n"
"v_mfma_f32_16x16x32_bf16 v[164:167], a[200:203], v[176:179], v[164:167]\n"
"ds_read_b64_tr_b16 a[228:229], v227 offset:0\n"
"v_add_f32 v220, v220, v142\n"
"v_mfma_f32_16x16x32_bf16 v[164:167], a[196:199], v[168:171], v[164:167]\n"
"s_waitcnt lgkmcnt(3)\n"
"v_exp_f32 v175, v175\n"
"v_mfma_f32_16x16x32_bf16 v[164:167], a[192:195], v[160:163], v[164:167]\n"
"ds_read_b64_tr_b16 a[230:231], v227 offset:4096\n"
"v_add_f32 v220, v220, v143\n"
"v_cvt_pk_bf16_f32 v151, v142, v143\n"
"v_mfma_f32_16x16x32_bf16 v[12:15], a[208:211], v[0:3], v[192:195]\n"
"v_exp_f32 v4, v4\n"
"v_mfma_f32_16x16x32_bf16 v[12:15], a[212:215], v[8:11], v[12:15]\n"
"ds_read_b64_tr_b16 a[232:233], v228 offset:0\n"
"v_add_f32 v221, v221, v172\n"
"v_mfma_f32_16x16x32_bf16 v[12:15], a[216:219], v[16:19], v[12:15]\n"
"v_exp_f32 v5, v5\n"
"v_mfma_f32_16x16x32_bf16 v[12:15], a[220:223], v[24:27], v[12:15]\n"
"ds_read_b64_tr_b16 a[234:235], v228 offset:4096\n"
"v_add_f32 v221, v221, v173\n"
"v_cvt_pk_bf16_f32 v182, v172, v173\n"
"v_mfma_f32_16x16x32_bf16 v[44:47], a[220:223], v[56:59], v[196:199]\n"
"v_exp_f32 v6, v6\n"
"v_mfma_f32_16x16x32_bf16 v[44:47], a[216:219], v[48:51], v[44:47]\n"
"ds_read_b64_tr_b16 a[236:237], v229 offset:0\n"
"v_add_f32 v221, v221, v174\n"
"v_mfma_f32_16x16x32_bf16 v[44:47], a[212:215], v[40:43], v[44:47]\n"
"buffer_load_dwordx4 v241, s[48:51], s84 offen offset:1024 nt lds\n"
"v_exp_f32 v7, v7\n"
"v_mfma_f32_16x16x32_bf16 v[44:47], a[208:211], v[32:35], v[44:47]\n"
"ds_read_b64_tr_b16 a[238:239], v229 offset:4096\n"
"v_add_f32 v221, v221, v175\n"
"v_cvt_pk_bf16_f32 v183, v174, v175\n"
"v_mfma_f32_16x16x32_bf16 v[76:79], a[208:211], v[64:67], v[200:203]\n"
"v_exp_f32 v36, v36\n"
"v_mfma_f32_16x16x32_bf16 v[76:79], a[212:215], v[72:75], v[76:79]\n"
"ds_read_b64_tr_b16 a[240:241], v230 offset:0\n"
"v_add_f32 v216, v216, v4\n"
"v_mfma_f32_16x16x32_bf16 v[76:79], a[216:219], v[80:83], v[76:79]\n"
"v_exp_f32 v37, v37\n"
"v_mfma_f32_16x16x32_bf16 v[76:79], a[220:223], v[88:91], v[76:79]\n"
"ds_read_b64_tr_b16 a[242:243], v230 offset:4096\n"
"v_add_f32 v216, v216, v5\n"
"v_cvt_pk_bf16_f32 v28, v4, v5\n"
"v_mfma_f32_16x16x32_bf16 v[108:111], a[220:223], v[120:123], v[204:207]\n"
"v_exp_f32 v38, v38\n"
"v_mfma_f32_16x16x32_bf16 v[108:111], a[216:219], v[112:115], v[108:111]\n"
"ds_read_b64_tr_b16 a[244:245], v231 offset:0\n"
"v_add_f32 v216, v216, v6\n"
"v_mfma_f32_16x16x32_bf16 v[108:111], a[212:215], v[104:107], v[108:111]\n"
"v_exp_f32 v39, v39\n"
"v_mfma_f32_16x16x32_bf16 v[108:111], a[208:211], v[96:99], v[108:111]\n"
"ds_read_b64_tr_b16 a[246:247], v231 offset:4096\n"
"v_add_f32 v216, v216, v7\n"
"v_cvt_pk_bf16_f32 v29, v6, v7\n"
"v_mfma_f32_16x16x32_bf16 v[140:143], a[208:211], v[128:131], v[208:211]\n"
"v_exp_f32 v68, v68\n"
"v_mfma_f32_16x16x32_bf16 v[140:143], a[212:215], v[136:139], v[140:143]\n"
"s_add_u32 m0, s63, 0xc000\n"
"ds_read_b64_tr_b16 a[248:249], v232 offset:0\n"
"v_add_f32 v217, v217, v36\n"
"v_mfma_f32_16x16x32_bf16 v[140:143], a[216:219], v[144:147], v[140:143]\n"
"s_add_u32 s85, s85, 0x1000\n"
"s_and_b32 s85, s85, 0x1ffff\n"
"buffer_load_dwordx4 v242, s[52:55], s85 offen nt lds\n"
"v_exp_f32 v69, v69\n"
"v_mfma_f32_16x16x32_bf16 v[140:143], a[220:223], v[152:155], v[140:143]\n"
"ds_read_b64_tr_b16 a[250:251], v232 offset:4096\n"
"v_add_f32 v217, v217, v37\n"
"v_cvt_pk_bf16_f32 v60, v36, v37\n"
"v_mfma_f32_16x16x32_bf16 v[172:175], a[220:223], v[184:187], v[212:215]\n"
"v_exp_f32 v70, v70\n"
"v_mfma_f32_16x16x32_bf16 v[172:175], a[216:219], v[176:179], v[172:175]\n"
"ds_read_b64_tr_b16 a[252:253], v233 offset:0\n"
"v_add_f32 v217, v217, v38\n"
"v_mfma_f32_16x16x32_bf16 v[172:175], a[212:215], v[168:171], v[172:175]\n"
"s_waitcnt lgkmcnt(3)\n"
"v_exp_f32 v71, v71\n"
"v_mfma_f32_16x16x32_bf16 v[172:175], a[208:211], v[160:163], v[172:175]\n"
"ds_read_b64_tr_b16 a[254:255], v233 offset:4096\n"
"v_add_f32 v217, v217, v39\n"
"v_cvt_pk_bf16_f32 v61, v38, v39\n"
"v_mfma_f32_16x16x32_bf16 a[0:3], a[224:227], v[20:23], a[0:3]\n"
"v_exp_f32 v100, v100\n"
"v_mfma_f32_16x16x32_bf16 a[4:7], a[224:227], v[52:55], a[4:7]\n"
"v_add_f32 v218, v218, v68\n"
"v_add_f32 v218, v218, v69\n"
"v_mfma_f32_16x16x32_bf16 a[8:11], a[224:227], v[84:87], a[8:11]\n"
"v_exp_f32 v101, v101\n"
"v_mfma_f32_16x16x32_bf16 a[12:15], a[224:227], v[116:119], a[12:15]\n"
"v_cvt_pk_bf16_f32 v92, v68, v69\n"
"v_add_f32 v218, v218, v70\n"
"v_mfma_f32_16x16x32_bf16 a[16:19], a[224:227], v[148:151], a[16:19]\n"
"s_memrealtime s[88:89]\n"
"v_exp_f32 v102, v102\n"
"v_mfma_f32_16x16x32_bf16 a[20:23], a[224:227], v[180:183], a[20:23]\n"
"v_add_f32 v218, v218, v71\n"
"v_cvt_pk_bf16_f32 v93, v70, v71\n"
"v_mfma_f32_16x16x32_bf16 a[44:47], a[228:231], v[180:183], a[44:47]\n"
"v_exp_f32 v103, v103\n"
"v_mfma_f32_16x16x32_bf16 a[40:43], a[228:231], v[148:151], a[40:43]\n"
"v_add_f32 v219, v219, v100\n"
"v_add_f32 v219, v219, v101\n"
"v_mfma_f32_16x16x32_bf16 a[36:39], a[228:231], v[116:119], a[36:39]\n"
"LP4_%=:\n"
"s_waitcnt lgkmcnt(0)\n"
"s_sub_u32 s89, s88, s86\n"
"s_cmp_ge_i32 s89, 0\n"
"s_cbranch_scc1 LQ4_%=\n"
"s_memrealtime s[88:89]\n"
"s_branch LP4_%=\n"
"LQ4_%=:\n"
"s_waitcnt vmcnt(13) lgkmcnt(0)\n"
"s_barrier\n"
"v_exp_f32 v132, v132\n"
"v_mfma_f32_16x16x32_bf16 a[32:35], a[228:231], v[84:87], a[32:35]\n"
"ds_read_b64 a[192:193], v222 offset:32768\n"
"v_cvt_pk_bf16_f32 v124, v100, v101\n"
"v_mfma_f32_16x16x32_bf16 a[28:31], a[228:231], v[52:55], a[28:31]\n"
"s_add_u32 s86, s86, 124\n"
"v_exp_f32 v133, v133\n"
"v_mfma_f32_16x16x32_bf16 a[24:27], a[228:231], v[20:23], a[24:27]\n"
"v_add_f32 v219, v219, v102\n"
"v_add_f32 v219, v219, v103\n"
"v_mfma_f32_16x16x32_bf16 a[48:51], a[232:235], v[20:23], a[48:51]\n"
"buffer_load_dwordx4 v242, s[52:55], s85 offen offset:2048 nt lds\n"
"v_exp_f32 v134, v134\n"
"v_mfma_f32_16x16x32_bf16 a[52:55], a[232:235], v[52:55], a[52:55]\n"
"ds_read_b64 a[194:195], v223 offset:32768\n"
"v_cvt_pk_bf16_f32 v125, v102, v103\n"
"v_mfma_f32_16x16x32_bf16 a[56:59], a[232:235], v[84:87], a[56:59]\n"
"v_exp_f32 v135, v135\n"
"v_mfma_f32_16x16x32_bf16 a[60:63], a[232:235], v[116:119], a[60:63]\n"
"v_add_f32 v220, v220, v132\n"
"v_add_f32 v220, v220, v133\n"
"v_mfma_f32_16x16x32_bf16 a[64:67], a[232:235], v[148:151], a[64:67]\n"
"v_exp_f32 v164, v164\n"
"v_mfma_f32_16x16x32_bf16 a[68:71], a[232:235], v[180:183], a[68:71]\n"
"ds_read_b64 a[196:197], v224 offset:32768\n"
"v_cvt_pk_bf16_f32 v156, v132, v133\n"
"v_mfma_f32_16x16x32_bf16 a[92:95], a[236:239], v[180:183], a[92:95]\n"
"v_exp_f32 v165, v165\n"
"v_mfma_f32_16x16x32_bf16 a[88:91], a[236:239], v[148:151], a[88:91]\n"
"v_add_f32 v220, v220, v134\n"
"v_add_f32 v220, v220, v135\n"
"v_mfma_f32_16x16x32_bf16 a[84:87], a[236:239], v[116:119], a[84:87]\n"
"v_exp_f32 v166, v166\n"
"v_mfma_f32_16x16x32_bf16 a[80:83], a[236:239], v[84:87], a[80:83]\n"
"s_add_u32 m0, s63, 0xd000\n"
"ds_read_b64 a[198:199], v225 offset:32768\n"
"v_cvt_pk_bf16_f32 v157, v134, v135\n"
"v_mfma_f32_16x16x32_bf16 a[76:79], a[236:239], v[52:55], a[76:79]\n"
"s_add_u32 s85, s85, 0x1000\n"
"buffer_load_dwordx4 v242, s[52:55], s85 offen nt lds\n"
"v_exp_f32 v167, v167\n"
"v_mfma_f32_16x16x32_bf16 a[72:75], a[236:239], v[20:23], a[72:75]\n"
"v_add_f32 v221, v221, v164\n"
"v_add_f32 v221, v221, v165\n"
"v_mfma_f32_16x16x32_bf16 a[96:99], a[240:243], v[20:23], a[96:99]\n"
"v_exp_f32 v12, v12\n"
"v_mfma_f32_16x16x32_bf16 a[100:103], a[240:243], v[52:55], a[100:103]\n"
"ds_read_b64 a[200:201], v246 offset:32768\n"
"v_cvt_pk_bf16_f32 v188, v164, v165\n"
"v_mfma_f32_16x16x32_bf16 a[104:107], a[240:243], v[84:87], a[104:107]\n"
"v_exp_f32 v13, v13\n"
"v_mfma_f32_16x16x32_bf16 a[108:111], a[240:243], v[116:119], a[108:111]\n"
"v_add_f32 v221, v221, v166\n"
"v_add_f32 v221, v221, v167\n"
"v_mfma_f32_16x16x32_bf16 a[112:115], a[240:243], v[148:151], a[112:115]\n"
"v_exp_f32 v14, v14\n"
"v_mfma_f32_16x16x32_bf16 a[116:119], a[240:243], v[180:183], a[116:119]\n"
"ds_read_b64 a[202:203], v247 offset:32768\n"
"v_cvt_pk_bf16_f32 v189, v166, v167\n"
"v_mfma_f32_16x16x32_bf16 a[140:143], a[244:247], v[180:183], a[140:143]\n"
"v_exp_f32 v15, v15\n"
"v_mfma_f32_16x16x32_bf16 a[136:139], a[244:247], v[148:151], a[136:139]\n"
"v_add_f32 v216, v216, v12\n"
"v_add_f32 v216, v216, v13\n"
"v_mfma_f32_16x16x32_bf16 a[132:135], a[244:247], v[116:119], a[132:135]\n"
"v_exp_f32 v44, v44\n"
"v_mfma_f32_16x16x32_bf16 a[128:131], a[244:247], v[84:87], a[128:131]\n"
"ds_read_b64 a[204:205], v248 offset:32768\n"
"v_cvt_pk_bf16_f32 v30, v12, v13\n"
"v_mfma_f32_16x16x32_bf16 a[124:127], a[244:247], v[52:55], a[124:127]\n"
"v_exp_f32 v45, v45\n"
"v_mfma_f32_16x16x32_bf16 a[120:123], a[244:247], v[20:23], a[120:123]\n"
"v_add_f32 v216, v216, v14\n"
"v_add_f32 v216, v216, v15\n"
"v_mfma_f32_16x16x32_bf16 a[144:147], a[248:251], v[20:23], a[144:147]\n"
"buffer_load_dwordx4 v242, s[52:55], s85 offen offset:2048 nt lds\n"
"v_exp_f32 v46, v46\n"
"v_mfma_f32_16x16x32_bf16 a[148:151], a[248:251], v[52:55], a[148:151]\n"
"ds_read_b64 a[206:207], v249 offset:32768\n"
"v_cvt_pk_bf16_f32 v31, v14, v15\n"
"v_mfma_f32_16x16x32_bf16 a[152:155], a[248:251], v[84:87], a[152:155]\n"
"v_exp_f32 v47, v47\n"
"v_mfma_f32_16x16x32_bf16 a[156:159], a[248:251], v[116:119], a[156:159]\n"
"v_add_f32 v217, v217, v44\n"
"v_add_f32 v217, v217, v45\n"
"v_mfma_f32_16x16x32_bf16 a[160:163], a[248:251], v[148:151], a[160:163]\n"
"v_exp_f32 v76, v76\n"
"v_mfma_f32_16x16x32_bf16 a[164:167], a[248:251], v[180:183], a[164:167]\n"
"v_cvt_pk_bf16_f32 v62, v44, v45\n"
"v_add_f32 v217, v217, v46\n"
"v_mfma_f32_16x16x32_bf16 a[188:191], a[252:255], v[180:183], a[188:191]\n"
"v_exp_f32 v77, v77\n"
"v_mfma_f32_16x16x32_bf16 a[184:187], a[252:255], v[148:151], a[184:187]\n"
"v_add_f32 v217, v217, v47\n"
"v_cvt_pk_bf16_f32 v63, v46, v47\n"
"v_mfma_f32_16x16x32_bf16 a[180:183], a[252:255], v[116:119], a[180:183]\n"
"v_exp_f32 v78, v78\n"
"v_mfma_f32_16x16x32_bf16 a[176:179], a[252:255], v[84:87], a[176:179]\n"
"v_add_f32 v218, v218, v76\n"
"v_add_f32 v218, v218, v77\n"
"v_mfma_f32_16x16x32_bf16 a[172:175], a[252:255], v[52:55], a[172:175]\n"
"s_waitcnt lgkmcnt(0)\n"
"v_exp_f32 v79, v79\n"
"v_mfma_f32_16x16x32_bf16 a[168:171], a[252:255], v[20:23], a[168:171]\n"
"v_cvt_pk_bf16_f32 v94, v76, v77\n"
"v_add_f32 v218, v218, v78\n"
"v_mfma_f32_16x16x32_bf16 v[4:7], a[192:195], v[0:3], v[192:195]\n"
"v_exp_f32 v108, v108\n"
"v_mfma_f32_16x16x32_bf16 v[4:7], a[196:199], v[8:11], v[4:7]\n"
"s_add_u32 m0, s62, 0x4000\n"
"ds_read_b64 a[208:209], v222 offset:36864\n"
"v_add_f32 v218, v218, v79\n"
"v_mfma_f32_16x16x32_bf16 v[4:7], a[200:203], v[16:19], v[4:7]\n"
"s_add_u32 s84, s84, 0x1000\n"
"s_and_b32 s84, s84, 0x1ffff\n"
"buffer_load_dwordx4 v240, s[48:51], s84 offen nt lds\n"
"v_exp_f32 v109, v109\n"
"v_mfma_f32_16x16x32_bf16 v[4:7], a[204:207], v[24:27], v[4:7]\n"
"ds_read_b64 a[210:211], v223 offset:36864\n"
"v_cvt_pk_bf16_f32 v95, v78, v79\n"
"v_mfma_f32_16x16x32_bf16 v[36:39], a[204:207], v[56:59], v[196:199]\n"
"v_exp_f32 v110, v110\n"
"v_mfma_f32_16x16x32_bf16 v[36:39], a[200:203], v[48:51], v[36:39]\n"
"ds_read_b64 a[212:213], v224 offset:36864\n"
"v_add_f32 v219, v219, v108\n"
"v_mfma_f32_16x16x32_bf16 v[36:39], a[196:199], v[40:43], v[36:39]\n"
"v_exp_f32 v111, v111\n"
"v_mfma_f32_16x16x32_bf16 v[36:39], a[192:195], v[32:35], v[36:39]\n"
"ds_read_b64 a[214:215], v225 offset:36864\n"
"v_add_f32 v219, v219, v109\n"
"v_mfma_f32_16x16x32_bf16 v[68:71], a[192:195], v[64:67], v[200:203]\n"
"v_exp_f32 v140, v140\n"
"v_mfma_f32_16x16x32_bf16 v[68:71], a[196:199], v[72:75], v[68:71]\n"
"ds_read_b64 a[216:217], v246 offset:36864\n"
"v_cvt_pk_bf16_f32 v126, v108, v109\n"
"v_mfma_f32_16x16x32_bf16 v[68:71], a[200:203], v[80:83], v[68:71]\n"
"v_exp_f32 v141, v141\n"
"v_mfma_f32_16x16x32_bf16 v[68:71], a[204:207], v[88:91], v[68:71]\n"
"ds_read_b64 a[218:219], v247 offset:36864\n"
"v_add_f32 v219, v219, v110\n"
"v_mfma_f32_16x16x32_bf16 v[100:103], a[204:207], v[120:123], v[204:207]\n"
"v_exp_f32 v142, v142\n"
"v_mfma_f32_16x16x32_bf16 v[100:103], a[200:203], v[112:115], v[100:103]\n"
"ds_read_b64 a[220:221], v248 offset:36864\n"
"v_add_f32 v219, v219, v111\n"
"v_mfma_f32_16x16x32_bf16 v[100:103], a[196:199], v[104:107], v[100:103]\n"
"buffer_load_dwordx4 v241, s[48:51], s84 offen offset:1024 nt lds\n"
"v_exp_f32 v143, v143\n"
"v_mfma_f32_16x16x32_bf16 v[100:103], a[192:195], v[96:99], v[100:103]\n"
"ds_read_b64 a[222:223], v249 offset:36864\n"
"v_cvt_pk_bf16_f32 v127, v110, v111\n"
"v_mfma_f32_16x16x32_bf16 v[132:135], a[192:195], v[128:131], v[208:211]\n"
"v_exp_f32 v172, v172\n"
"v_mfma_f32_16x16x32_bf16 v[132:135], a[196:199], v[136:139], v[132:135]\n"
"ds_read_b64_tr_b16 a[224:225], v226 offset:16384\n"
"v_add_f32 v220, v220, v140\n"
"v_mfma_f32_16x16x32_bf16 v[132:135], a[200:203], v[144:147], v[132:135]\n"
"v_exp_f32 v173, v173\n"
"v_mfma_f32_16x16x32_bf16 v[132:135], a[204:207], v[152:155], v[132:135]\n"
"ds_read_b64_tr_b16 a[226:227], v226 offset:20480\n"
"v_add_f32 v220, v220, v141\n"
"v_cvt_pk_bf16_f32 v158, v140, v141\n"
"v_mfma_f32_16x16x32_bf16 v[164:167], a[204:207], v[184:187], v[212:215]\n"
"v_exp_f32 v174, v174\n"
"v_mfma_f32_16x16x32_bf16 v[164:167], a[200:203], v[176:179], v[164:167]\n"
"ds_read_b64_tr_b16 a[228:229], v227 offset:16384\n"
"v_add_f32 v220, v220, v142\n"
"v_mfma_f32_16x16x32_bf16 v[164:167], a[196:199], v[168:171], v[164:167]\n"
"s_waitcnt lgkmcnt(3)\n"
"v_exp_f32 v175, v175\n"
"v_mfma_f32_16x16x32_bf16 v[164:167], a[192:195], v[160:163], v[164:167]\n"
"ds_read_b64_tr_b16 a[230:231], v227 offset:20480\n"
"v_add_f32 v220, v220, v143\n"
"v_cvt_pk_bf16_f32 v159, v142, v143\n"
"v_mfma_f32_16x16x32_bf16 v[12:15], a[208:211], v[0:3], v[192:195]\n"
"v_exp_f32 v4, v4\n"
"v_mfma_f32_16x16x32_bf16 v[12:15], a[212:215], v[8:11], v[12:15]\n"
"ds_read_b64_tr_b16 a[232:233], v228 offset:16384\n"
"v_add_f32 v221, v221, v172\n"
"v_mfma_f32_16x16x32_bf16 v[12:15], a[216:219], v[16:19], v[12:15]\n"
"v_exp_f32 v5, v5\n"
"v_mfma_f32_16x16x32_bf16 v[12:15], a[220:223], v[24:27], v[12:15]\n"
"s_add_u32 m0, s62, 0x5000\n"
"ds_read_b64_tr_b16 a[234:235], v228 offset:20480\n"
"v_add_f32 v221, v221, v173\n"
"v_cvt_pk_bf16_f32 v190, v172, v173\n"
"v_mfma_f32_16x16x32_bf16 v[44:47], a[220:223], v[56:59], v[196:199]\n"
"s_add_u32 s84, s84, 0x1000\n"
"buffer_load_dwordx4 v240, s[48:51], s84 offen nt lds\n"
"v_exp_f32 v6, v6\n"
"v_mfma_f32_16x16x32_bf16 v[44:47], a[216:219], v[48:51], v[44:47]\n"
"ds_read_b64_tr_b16 a[236:237], v229 offset:16384\n"
"v_add_f32 v221, v221, v174\n"
"v_mfma_f32_16x16x32_bf16 v[44:47], a[212:215], v[40:43], v[44:47]\n"
"v_exp_f32 v7, v7\n"
"v_mfma_f32_16x16x32_bf16 v[44:47], a[208:211], v[32:35], v[44:47]\n"
"ds_read_b64_tr_b16 a[238:239], v229 offset:20480\n"
"v_add_f32 v221, v221, v175\n"
"v_cvt_pk_bf16_f32 v191, v174, v175\n"
"v_mfma_f32_16x16x32_bf16 v[76:79], a[208:211], v[64:67], v[200:203]\n"
"v_exp_f32 v36, v36\n"
"v_mfma_f32_16x16x32_bf16 v[76:79], a[212:215], v[72:75], v[76:79]\n"
"ds_read_b64_tr_b16 a[240:241], v230 offset:16384\n"
"v_add_f32 v216, v216, v4\n"
"v_mfma_f32_16x16x32_bf16 v[76:79], a[216:219], v[80:83], v[76:79]\n"
"v_exp_f32 v37, v37\n"
"v_mfma_f32_16x16x32_bf16 v[76:79], a[220:223], v[88:91], v[76:79]\n"
"ds_read_b64_tr_b16 a[242:243], v230 offset:20480\n"
"v_add_f32 v216, v216, v5\n"
"v_cvt_pk_bf16_f32 v20, v4, v5\n"
"v_mfma_f32_16x16x32_bf16 v[108:111], a[220:223], v[120:123], v[204:207]\n"
"v_exp_f32 v38, v38\n"
"v_mfma_f32_16x16x32_bf16 v[108:111], a[216:219], v[112:115], v[108:111]\n"
"ds_read_b64_tr_b16 a[244:245], v231 offset:16384\n"
"v_add_f32 v216, v216, v6\n"
"v_mfma_f32_16x16x32_bf16 v[108:111], a[212:215], v[104:107], v[108:111]\n"
"v_exp_f32 v39, v39\n"
"v_mfma_f32_16x16x32_bf16 v[108:111], a[208:211], v[96:99], v[108:111]\n"
"ds_read_b64_tr_b16 a[246:247], v231 offset:20480\n"
"v_add_f32 v216, v216, v7\n"
"v_cvt_pk_bf16_f32 v21, v6, v7\n"
"v_mfma_f32_16x16x32_bf16 v[140:143], a[208:211], v[128:131], v[208:211]\n"
"buffer_load_dwordx4 v241, s[48:51], s84 offen offset:1024 nt lds\n"
"v_exp_f32 v68, v68\n"
"v_mfma_f32_16x16x32_bf16 v[140:143], a[212:215], v[136:139], v[140:143]\n"
"ds_read_b64_tr_b16 a[248:249], v232 offset:16384\n"
"v_add_f32 v217, v217, v36\n"
"v_mfma_f32_16x16x32_bf16 v[140:143], a[216:219], v[144:147], v[140:143]\n"
"v_exp_f32 v69, v69\n"
"v_mfma_f32_16x16x32_bf16 v[140:143], a[220:223], v[152:155], v[140:143]\n"
"ds_read_b64_tr_b16 a[250:251], v232 offset:20480\n"
"v_add_f32 v217, v217, v37\n"
"v_cvt_pk_bf16_f32 v52, v36, v37\n"
"v_mfma_f32_16x16x32_bf16 v[172:175], a[220:223], v[184:187], v[212:215]\n"
"v_exp_f32 v70, v70\n"
"v_mfma_f32_16x16x32_bf16 v[172:175], a[216:219], v[176:179], v[172:175]\n"
"ds_read_b64_tr_b16 a[252:253], v233 offset:16384\n"
"v_add_f32 v217, v217, v38\n"
"v_mfma_f32_16x16x32_bf16 v[172:175], a[212:215], v[168:171], v[172:175]\n"
"s_waitcnt lgkmcnt(3)\n"
"v_exp_f32 v71, v71\n"
"v_mfma_f32_16x16x32_bf16 v[172:175], a[208:211], v[160:163], v[172:175]\n"
"ds_read_b64_tr_b16 a[254:255], v233 offset:20480\n"
"v_add_f32 v217, v217, v39\n"
"v_cvt_pk_bf16_f32 v53, v38, v39\n"
"v_mfma_f32_16x16x32_bf16 a[0:3], a[224:227], v[28:31], a[0:3]\n"
"v_exp_f32 v100, v100\n"
"v_mfma_f32_16x16x32_bf16 a[4:7], a[224:227], v[60:63], a[4:7]\n"
"v_add_f32 v218, v218, v68\n"
"v_add_f32 v218, v218, v69\n"
"v_mfma_f32_16x16x32_bf16 a[8:11], a[224:227], v[92:95], a[8:11]\n"
"v_exp_f32 v101, v101\n"
"v_mfma_f32_16x16x32_bf16 a[12:15], a[224:227], v[124:127], a[12:15]\n"
"v_cvt_pk_bf16_f32 v84, v68, v69\n"
"v_add_f32 v218, v218, v70\n"
"v_mfma_f32_16x16x32_bf16 a[16:19], a[224:227], v[156:159], a[16:19]\n"
"s_memrealtime s[88:89]\n"
"v_exp_f32 v102, v102\n"
"v_mfma_f32_16x16x32_bf16 a[20:23], a[224:227], v[188:191], a[20:23]\n"
"v_add_f32 v218, v218, v71\n"
"v_cvt_pk_bf16_f32 v85, v70, v71\n"
"v_mfma_f32_16x16x32_bf16 a[44:47], a[228:231], v[188:191], a[44:47]\n"
"v_exp_f32 v103, v103\n"
"v_mfma_f32_16x16x32_bf16 a[40:43], a[228:231], v[156:159], a[40:43]\n"
"v_add_f32 v219, v219, v100\n"
"v_add_f32 v219, v219, v101\n"
"v_mfma_f32_16x16x32_bf16 a[36:39], a[228:231], v[124:127], a[36:39]\n"
"LP5_%=:\n"
"s_waitcnt lgkmcnt(0)\n"
"s_sub_u32 s89, s88, s86\n"
"s_cmp_ge_i32 s89, 0\n"
"s_cbranch_scc1 LQ5_%=\n"
"s_memrealtime s[88:89]\n"
"s_branch LP5_%=\n"
"LQ5_%=:\n"
"s_waitcnt vmcnt(12) lgkmcnt(0)\n"
"s_barrier\n"
"v_exp_f32 v132, v132\n"
"v_mfma_f32_16x16x32_bf16 a[32:35], a[228:231], v[92:95], a[32:35]\n"
"ds_read_b64 a[192:193], v222 offset:49152\n"
"v_cvt_pk_bf16_f32 v116, v100, v101\n"
"v_mfma_f32_16x16x32_bf16 a[28:31], a[228:231], v[60:63], a[28:31]\n"
"s_add_u32 s86, s86, 124\n"
"v_exp_f32 v133, v133\n"
"v_mfma_f32_16x16x32_bf16 a[24:27], a[228:231], v[28:31], a[24:27]\n"
"s_add_u32 m0, s63, 0x0\n"
"v_add_f32 v219, v219, v102\n"
"v_add_f32 v219, v219, v103\n"
"v_mfma_f32_16x16x32_bf16 a[48:51], a[232:235], v[28:31], a[48:51]\n"
"s_add_u32 s85, s85, 0x1000\n"
"s_and_b32 s85, s85, 0x1ffff\n"
"buffer_load_dwordx4 v242, s[52:55], s85 offen nt lds\n"
"v_exp_f32 v134, v134\n"
"v_mfma_f32_16x16x32_bf16 a[52:55], a[232:235], v[60:63], a[52:55]\n"
"ds_read_b64 a[194:195], v223 offset:49152\n"
"v_cvt_pk_bf16_f32 v117, v102, v103\n"
"v_mfma_f32_16x16x32_bf16 a[56:59], a[232:235], v[92:95], a[56:59]\n"
"v_exp_f32 v135, v135\n"
"v_mfma_f32_16x16x32_bf16 a[60:63], a[232:235], v[124:127], a[60:63]\n"
"v_add_f32 v220, v220, v132\n"
"v_add_f32 v220, v220, v133\n"
"v_mfma_f32_16x16x32_bf16 a[64:67], a[232:235], v[156:159], a[64:67]\n"
"v_exp_f32 v164, v164\n"
"v_mfma_f32_16x16x32_bf16 a[68:71], a[232:235], v[188:191], a[68:71]\n"
"ds_read_b64 a[196:197], v224 offset:49152\n"
"v_cvt_pk_bf16_f32 v148, v132, v133\n"
"v_mfma_f32_16x16x32_bf16 a[92:95], a[236:239], v[188:191], a[92:95]\n"
"v_exp_f32 v165, v165\n"
"v_mfma_f32_16x16x32_bf16 a[88:91], a[236:239], v[156:159], a[88:91]\n"
"v_add_f32 v220, v220, v134\n"
"v_add_f32 v220, v220, v135\n"
"v_mfma_f32_16x16x32_bf16 a[84:87], a[236:239], v[124:127], a[84:87]\n"
"buffer_load_dwordx4 v242, s[52:55], s85 offen offset:2048 nt lds\n"
"v_exp_f32 v166, v166\n"
"v_mfma_f32_16x16x32_bf16 a[80:83], a[236:239], v[92:95], a[80:83]\n"
"ds_read_b64 a[198:199], v225 offset:49152\n"
"v_cvt_pk_bf16_f32 v149, v134, v135\n"
"v_mfma_f32_16x16x32_bf16 a[76:79], a[236:239], v[60:63], a[76:79]\n"
"v_exp_f32 v167, v167\n"
"v_mfma_f32_16x16x32_bf16 a[72:75], a[236:239], v[28:31], a[72:75]\n"
"v_add_f32 v221, v221, v164\n"
"v_add_f32 v221, v221, v165\n"
"v_mfma_f32_16x16x32_bf16 a[96:99], a[240:243], v[28:31], a[96:99]\n"
"v_exp_f32 v12, v12\n"
"v_mfma_f32_16x16x32_bf16 a[100:103], a[240:243], v[60:63], a[100:103]\n"
"ds_read_b64 a[200:201], v246 offset:49152\n"
"v_cvt_pk_bf16_f32 v180, v164, v165\n"
"v_mfma_f32_16x16x32_bf16 a[104:107], a[240:243], v[92:95], a[104:107]\n"
"v_exp_f32 v13, v13\n"
"v_mfma_f32_16x16x32_bf16 a[108:111], a[240:243], v[124:127], a[108:111]\n"
"v_add_f32 v221, v221, v166\n"
"v_add_f32 v221, v221, v167\n"
"v_mfma_f32_16x16x32_bf16 a[112:115], a[240:243], v[156:159], a[112:115]\n"
"v_exp_f32 v14, v14\n"
"v_mfma_f32_16x16x32_bf16 a[116:119], a[240:243], v[188:191], a[116:119]\n"
"ds_read_b64 a[202:203], v247 offset:49152\n"
"v_cvt_pk_bf16_f32 v181, v166, v167\n"
"v_mfma_f32_16x16x32_bf16 a[140:143], a[244:247], v[188:191], a[140:143]\n"
"v_exp_f32 v15, v15\n"
"v_mfma_f32_16x16x32_bf16 a[136:139], a[244:247], v[156:159], a[136:139]\n"
"v_add_f32 v216, v216, v12\n"
"v_add_f32 v216, v216, v13\n"
"v_mfma_f32_16x16x32_bf16 a[132:135], a[244:247], v[124:127], a[132:135]\n"
"v_exp_f32 v44, v44\n"
"v_mfma_f32_16x16x32_bf16 a[128:131], a[244:247], v[92:95], a[128:131]\n"
"s_add_u32 m0, s63, 0x1000\n"
"ds_read_b64 a[204:205], v248 offset:49152\n"
"v_cvt_pk_bf16_f32 v22, v12, v13\n"
"v_mfma_f32_16x16x32_bf16 a[124:127], a[244:247], v[60:63], a[124:127]\n"
"s_add_u32 s85, s85, 0x1000\n"
"buffer_load_dwordx4 v242, s[52:55], s85 offen nt lds\n"
"v_exp_f32 v45, v45\n"
"v_mfma_f32_16x16x32_bf16 a[120:123], a[244:247], v[28:31], a[120:123]\n"
"v_add_f32 v216, v216, v14\n"
"v_add_f32 v216, v216, v15\n"
"v_mfma_f32_16x16x32_bf16 a[144:147], a[248:251], v[28:31], a[144:147]\n"
"v_exp_f32 v46, v46\n"
"v_mfma_f32_16x16x32_bf16 a[148:151], a[248:251], v[60:63], a[148:151]\n"
"ds_read_b64 a[206:207], v249 offset:49152\n"
"v_cvt_pk_bf16_f32 v23, v14, v15\n"
"v_mfma_f32_16x16x32_bf16 a[152:155], a[248:251], v[92:95], a[152:155]\n"
"v_exp_f32 v47, v47\n"
"v_mfma_f32_16x16x32_bf16 a[156:159], a[248:251], v[124:127], a[156:159]\n"
"v_add_f32 v217, v217, v44\n"
"v_add_f32 v217, v217, v45\n"
"v_mfma_f32_16x16x32_bf16 a[160:163], a[248:251], v[156:159], a[160:163]\n"
"v_exp_f32 v76, v76\n"
"v_mfma_f32_16x16x32_bf16 a[164:167], a[248:251], v[188:191], a[164:167]\n"
"v_cvt_pk_bf16_f32 v54, v44, v45\n"
"v_add_f32 v217, v217, v46\n"
"v_mfma_f32_16x16x32_bf16 a[188:191], a[252:255], v[188:191], a[188:191]\n"
"v_exp_f32 v77, v77\n"
"v_mfma_f32_16x16x32_bf16 a[184:187], a[252:255], v[156:159], a[184:187]\n"
"v_add_f32 v217, v217, v47\n"
"v_cvt_pk_bf16_f32 v55, v46, v47\n"
"v_mfma_f32_16x16x32_bf16 a[180:183], a[252:255], v[124:127], a[180:183]\n"
"v_exp_f32 v78, v78\n"
"v_mfma_f32_16x16x32_bf16 a[176:179], a[252:255], v[92:95], a[176:179]\n"
"v_add_f32 v218, v218, v76\n"
"v_add_f32 v218, v218, v77\n"
"v_mfma_f32_16x16x32_bf16 a[172:175], a[252:255], v[60:63], a[172:175]\n"
"s_waitcnt lgkmcnt(0)\n"
"v_exp_f32 v79, v79\n"
"v_mfma_f32_16x16x32_bf16 a[168:171], a[252:255], v[28:31], a[168:171]\n"
"v_cvt_pk_bf16_f32 v86, v76, v77\n"
"v_add_f32 v218, v218, v78\n"
"v_mfma_f32_16x16x32_bf16 v[4:7], a[192:195], v[0:3], v[192:195]\n"
"v_exp_f32 v108, v108\n"
"v_mfma_f32_16x16x32_bf16 v[4:7], a[196:199], v[8:11], v[4:7]\n"
"ds_read_b64 a[208:209], v222 offset:53248\n"
"v_add_f32 v218, v218, v79\n"
"v_mfma_f32_16x16x32_bf16 v[4:7], a[200:203], v[16:19], v[4:7]\n"
"buffer_load_dwordx4 v242, s[52:55], s85 offen offset:2048 nt lds\n"
"v_exp_f32 v109, v109\n"
"v_mfma_f32_16x16x32_bf16 v[4:7], a[204:207], v[24:27], v[4:7]\n"
"ds_read_b64 a[210:211], v223 offset:53248\n"
"v_cvt_pk_bf16_f32 v87, v78, v79\n"
"v_mfma_f32_16x16x32_bf16 v[36:39], a[204:207], v[56:59], v[196:199]\n"
"v_exp_f32 v110, v110\n"
"v_mfma_f32_16x16x32_bf16 v[36:39], a[200:203], v[48:51], v[36:39]\n"
"ds_read_b64 a[212:213], v224 offset:53248\n"
"v_add_f32 v219, v219, v108\n"
"v_mfma_f32_16x16x32_bf16 v[36:39], a[196:199], v[40:43], v[36:39]\n"
"v_exp_f32 v111, v111\n"
"v_mfma_f32_16x16x32_bf16 v[36:39], a[192:195], v[32:35], v[36:39]\n"
"ds_read_b64 a[214:215], v225 offset:53248\n"
"v_add_f32 v219, v219, v109\n"
"v_mfma_f32_16x16x32_bf16 v[68:71], a[192:195], v[64:67], v[200:203]\n"
"v_exp_f32 v140, v140\n"
"v_mfma_f32_16x16x32_bf16 v[68:71], a[196:199], v[72:75], v[68:71]\n"
"ds_read_b64 a[216:217], v246 offset:53248\n"
"v_cvt_pk_bf16_f32 v118, v108, v109\n"
"v_mfma_f32_16x16x32_bf16 v[68:71], a[200:203], v[80:83], v[68:71]\n"
"v_exp_f32 v141, v141\n"
"v_mfma_f32_16x16x32_bf16 v[68:71], a[204:207], v[88:91], v[68:71]\n"
"s_add_u32 m0, s62, 0x8000\n"
"ds_read_b64 a[218:219], v247 offset:53248\n"
"v_add_f32 v219, v219, v110\n"
"v_mfma_f32_16x16x32_bf16 v[100:103], a[204:207], v[120:123], v[204:207]\n"
"s_add_u32 s84, s84, 0x1000\n"
"s_and_b32 s84, s84, 0x1ffff\n"
"buffer_load_dwordx4 v240, s[48:51], s84 offen nt lds\n"
"v_exp_f32 v142, v142\n"
"v_mfma_f32_16x16x32_bf16 v[100:103], a[200:203], v[112:115], v[100:103]\n"
"ds_read_b64 a[220:221], v248 offset:53248\n"
"v_add_f32 v219, v219, v111\n"
"v_mfma_f32_16x16x32_bf16 v[100:103], a[196:199], v[104:107], v[100:103]\n"
"v_exp_f32 v143, v143\n"
"v_mfma_f32_16x16x32_bf16 v[100:103], a[192:195], v[96:99], v[100:103]\n"
"ds_read_b64 a[222:223], v249 offset:53248\n"
"v_cvt_pk_bf16_f32 v119, v110, v111\n"
"v_mfma_f32_16x16x32_bf16 v[132:135], a[192:195], v[128:131], v[208:211]\n"
"v_exp_f32 v172, v172\n"
"v_mfma_f32_16x16x32_bf16 v[132:135], a[196:199], v[136:139], v[132:135]\n"
"ds_read_b64_tr_b16 a[224:225], v226 offset:32768\n"
"v_add_f32 v220, v220, v140\n"
"v_mfma_f32_16x16x32_bf16 v[132:135], a[200:203], v[144:147], v[132:135]\n"
"v_exp_f32 v173, v173\n"
"v_mfma_f32_16x16x32_bf16 v[132:135], a[204:207], v[152:155], v[132:135]\n"
"ds_read_b64_tr_b16 a[226:227], v226 offset:36864\n"
"v_add_f32 v220, v220, v141\n"
"v_cvt_pk_bf16_f32 v150, v140, v141\n"
"v_mfma_f32_16x16x32_bf16 v[164:167], a[204:207], v[184:187], v[212:215]\n"
"v_exp_f32 v174, v174\n"
"v_mfma_f32_16x16x32_bf16 v[164:167], a[200:203], v[176:179], v[164:167]\n"
"ds_read_b64_tr_b16 a[228:229], v227 offset:32768\n"
"v_add_f32 v220, v220, v142\n"
"v_mfma_f32_16x16x32_bf16 v[164:167], a[196:199], v[168:171], v[164:167]\n"
"s_waitcnt lgkmcnt(3)\n"
"v_exp_f32 v175, v175\n"
"v_mfma_f32_16x16x32_bf16 v[164:167], a[192:195], v[160:163], v[164:167]\n"
"ds_read_b64_tr_b16 a[230:231], v227 offset:36864\n"
"v_add_f32 v220, v220, v143\n"
"v_cvt_pk_bf16_f32 v151, v142, v143\n"
"v_mfma_f32_16x16x32_bf16 v[12:15], a[208:211], v[0:3], v[192:195]\n"
"v_exp_f32 v4, v4\n"
"v_mfma_f32_16x16x32_bf16 v[12:15], a[212:215], v[8:11], v[12:15]\n"
"ds_read_b64_tr_b16 a[232:233], v228 offset:32768\n"
"v_add_f32 v221, v221, v172\n"
"v_mfma_f32_16x16x32_bf16 v[12:15], a[216:219], v[16:19], v[12:15]\n"
"buffer_load_dwordx4 v241, s[48:51], s84 offen offset:1024 nt lds\n"
"v_exp_f32 v5, v5\n"
"v_mfma_f32_16x16x32_bf16 v[12:15], a[220:223], v[24:27], v[12:15]\n"
"ds_read_b64_tr_b16 a[234:235], v228 offset:36864\n"
"v_add_f32 v221, v221, v173\n"
"v_cvt_pk_bf16_f32 v182, v172, v173\n"
"v_mfma_f32_16x16x32_bf16 v[44:47], a[220:223], v[56:59], v[196:199]\n"
"v_exp_f32 v6, v6\n"
"v_mfma_f32_16x16x32_bf16 v[44:47], a[216:219], v[48:51], v[44:47]\n"
"ds_read_b64_tr_b16 a[236:237], v229 offset:32768\n"
"v_add_f32 v221, v221, v174\n"
"v_mfma_f32_16x16x32_bf16 v[44:47], a[212:215], v[40:43], v[44:47]\n"
"v_exp_f32 v7, v7\n"
"v_mfma_f32_16x16x32_bf16 v[44:47], a[208:211], v[32:35], v[44:47]\n"
"ds_read_b64_tr_b16 a[238:239], v229 offset:36864\n"
"v_add_f32 v221, v221, v175\n"
"v_cvt_pk_bf16_f32 v183, v174, v175\n"
"v_mfma_f32_16x16x32_bf16 v[76:79], a[208:211], v[64:67], v[200:203]\n"
"v_exp_f32 v36, v36\n"
"v_mfma_f32_16x16x32_bf16 v[76:79], a[212:215], v[72:75], v[76:79]\n"
"ds_read_b64_tr_b16 a[240:241], v230 offset:32768\n"
"v_add_f32 v216, v216, v4\n"
"v_mfma_f32_16x16x32_bf16 v[76:79], a[216:219], v[80:83], v[76:79]\n"
"v_exp_f32 v37, v37\n"
"v_mfma_f32_16x16x32_bf16 v[76:79], a[220:223], v[88:91], v[76:79]\n"
"ds_read_b64_tr_b16 a[242:243], v230 offset:36864\n"
"v_add_f32 v216, v216, v5\n"
"v_cvt_pk_bf16_f32 v28, v4, v5\n"
"v_mfma_f32_16x16x32_bf16 v[108:111], a[220:223], v[120:123], v[204:207]\n"
"v_exp_f32 v38, v38\n"
"v_mfma_f32_16x16x32_bf16 v[108:111], a[216:219], v[112:115], v[108:111]\n"
"s_add_u32 m0, s62, 0x9000\n"
"ds_read_b64_tr_b16 a[244:245], v231 offset:32768\n"
"v_add_f32 v216, v216, v6\n"
"v_mfma_f32_16x16x32_bf16 v[108:111], a[212:215], v[104:107], v[108:111]\n"
"s_add_u32 s84, s84, 0x1000\n"
"buffer_load_dwordx4 v240, s[48:51], s84 offen nt lds\n"
"v_exp_f32 v39, v39\n"
"v_mfma_f32_16x16x32_bf16 v[108:111], a[208:211], v[96:99], v[108:111]\n"
"ds_read_b64_tr_b16 a[246:247], v231 offset:36864\n"
"v_add_f32 v216, v216, v7\n"
"v_cvt_pk_bf16_f32 v29, v6, v7\n"
"v_mfma_f32_16x16x32_bf16 v[140:143], a[208:211], v[128:131], v[208:211]\n"
"v_exp_f32 v68, v68\n"
"v_mfma_f32_16x16x32_bf16 v[140:143], a[212:215], v[136:139], v[140:143]\n"
"ds_read_b64_tr_b16 a[248:249], v232 offset:32768\n"
"v_add_f32 v217, v217, v36\n"
"v_mfma_f32_16x16x32_bf16 v[140:143], a[216:219], v[144:147], v[140:143]\n"
"v_exp_f32 v69, v69\n"
"v_mfma_f32_16x16x32_bf16 v[140:143], a[220:223], v[152:155], v[140:143]\n"
"ds_read_b64_tr_b16 a[250:251], v232 offset:36864\n"
"v_add_f32 v217, v217, v37\n"
"v_cvt_pk_bf16_f32 v60, v36, v37\n"
"v_mfma_f32_16x16x32_bf16 v[172:175], a[220:223], v[184:187], v[212:215]\n"
"v_exp_f32 v70, v70\n"
"v_mfma_f32_16x16x32_bf16 v[172:175], a[216:219], v[176:179], v[172:175]\n"
"ds_read_b64_tr_b16 a[252:253], v233 offset:32768\n"
"v_add_f32 v217, v217, v38\n"
"v_mfma_f32_16x16x32_bf16 v[172:175], a[212:215], v[168:171], v[172:175]\n"
"s_waitcnt lgkmcnt(3)\n"
"v_exp_f32 v71, v71\n"
"v_mfma_f32_16x16x32_bf16 v[172:175], a[208:211], v[160:163], v[172:175]\n"
"ds_read_b64_tr_b16 a[254:255], v233 offset:36864\n"
"v_add_f32 v217, v217, v39\n"
"v_cvt_pk_bf16_f32 v61, v38, v39\n"
"v_mfma_f32_16x16x32_bf16 a[0:3], a[224:227], v[20:23], a[0:3]\n"
"v_exp_f32 v100, v100\n"
"v_mfma_f32_16x16x32_bf16 a[4:7], a[224:227], v[52:55], a[4:7]\n"
"v_add_f32 v218, v218, v68\n"
"v_add_f32 v218, v218, v69\n"
"v_mfma_f32_16x16x32_bf16 a[8:11], a[224:227], v[84:87], a[8:11]\n"
"v_exp_f32 v101, v101\n"
"v_mfma_f32_16x16x32_bf16 a[12:15], a[224:227], v[116:119], a[12:15]\n"
"v_cvt_pk_bf16_f32 v92, v68, v69\n"
"v_add_f32 v218, v218, v70\n"
"v_mfma_f32_16x16x32_bf16 a[16:19], a[224:227], v[148:151], a[16:19]\n"
"s_memrealtime s[88:89]\n"
"v_exp_f32 v102, v102\n"
"v_mfma_f32_16x16x32_bf16 a[20:23], a[224:227], v[180:183], a[20:23]\n"
"v_add_f32 v218, v218, v71\n"
"v_cvt_pk_bf16_f32 v93, v70, v71\n"
"v_mfma_f32_16x16x32_bf16 a[44:47], a[228:231], v[180:183], a[44:47]\n"
"v_exp_f32 v103, v103\n"
"v_mfma_f32_16x16x32_bf16 a[40:43], a[228:231], v[148:151], a[40:43]\n"
"v_add_f32 v219, v219, v100\n"
"v_add_f32 v219, v219, v101\n"
"v_mfma_f32_16x16x32_bf16 a[36:39], a[228:231], v[116:119], a[36:39]\n"
"LP6_%=:\n"
"s_waitcnt lgkmcnt(0)\n"
"s_sub_u32 s89, s88, s86\n"
"s_cmp_ge_i32 s89, 0\n"
"s_cbranch_scc1 LQ6_%=\n"
"s_memrealtime s[88:89]\n"
"s_branch LP6_%=\n"
"LQ6_%=:\n"
"s_waitcnt vmcnt(11) lgkmcnt(0)\n"
"s_barrier\n"
"v_exp_f32 v132, v132\n"
"v_mfma_f32_16x16x32_bf16 a[32:35], a[228:231], v[84:87], a[32:35]\n"
"ds_read_b64 a[192:193], v222 offset:0\n"
"v_cvt_pk_bf16_f32 v124, v100, v101\n"
"v_mfma_f32_16x16x32_bf16 a[28:31], a[228:231], v[52:55], a[28:31]\n"
"s_add_u32 s86, s86, 124\n"
"v_exp_f32 v133, v133\n"
"v_mfma_f32_16x16x32_bf16 a[24:27], a[228:231], v[20:23], a[24:27]\n"
"v_add_f32 v219, v219, v102\n"
"v_add_f32 v219, v219, v103\n"
"v_mfma_f32_16x16x32_bf16 a[48:51], a[232:235], v[20:23], a[48:51]\n"
"buffer_load_dwordx4 v241, s[48:51], s84 offen offset:1024 nt lds\n"
"v_exp_f32 v134, v134\n"
"v_mfma_f32_16x16x32_bf16 a[52:55], a[232:235], v[52:55], a[52:55]\n"
"ds_read_b64 a[194:195], v223 offset:0\n"
"v_cvt_pk_bf16_f32 v125, v102, v103\n"
"v_mfma_f32_16x16x32_bf16 a[56:59], a[232:235], v[84:87], a[56:59]\n"
"v_exp_f32 v135, v135\n"
"v_mfma_f32_16x16x32_bf16 a[60:63], a[232:235], v[116:119], a[60:63]\n"
"v_add_f32 v220, v220, v132\n"
"v_add_f32 v220, v220, v133\n"
"v_mfma_f32_16x16x32_bf16 a[64:67], a[232:235], v[148:151], a[64:67]\n"
"v_exp_f32 v164, v164\n"
"v_mfma_f32_16x16x32_bf16 a[68:71], a[232:235], v[180:183], a[68:71]\n"
"s_add_u32 m0, s63, 0x4000\n"
"ds_read_b64 a[196:197], v224 offset:0\n"
"v_cvt_pk_bf16_f32 v156, v132, v133\n"
"v_mfma_f32_16x16x32_bf16 a[92:95], a[236:239], v[180:183], a[92:95]\n"
"s_add_u32 s85, s85, 0x1000\n"
"s_and_b32 s85, s85, 0x1ffff\n"
"buffer_load_dwordx4 v242, s[52:55], s85 offen nt lds\n"
"v_exp_f32 v165, v165\n"
"v_mfma_f32_16x16x32_bf16 a[88:91], a[236:239], v[148:151], a[88:91]\n"
"v_add_f32 v220, v220, v134\n"
"v_add_f32 v220, v220, v135\n"
"v_mfma_f32_16x16x32_bf16 a[84:87], a[236:239], v[116:119], a[84:87]\n"
"v_exp_f32 v166, v166\n"
"v_mfma_f32_16x16x32_bf16 a[80:83], a[236:239], v[84:87], a[80:83]\n"
"ds_read_b64 a[198:199], v225 offset:0\n"
"v_cvt_pk_bf16_f32 v157, v134, v135\n"
"v_mfma_f32_16x16x32_bf16 a[76:79], a[236:239], v[52:55], a[76:79]\n"
"v_exp_f32 v167, v167\n"
"v_mfma_f32_16x16x32_bf16 a[72:75], a[236:239], v[20:23], a[72:75]\n"
"v_add_f32 v221, v221, v164\n"
"v_add_f32 v221, v221, v165\n"
"v_mfma_f32_16x16x32_bf16 a[96:99], a[240:243], v[20:23], a[96:99]\n"
"v_exp_f32 v12, v12\n"
"v_mfma_f32_16x16x32_bf16 a[100:103], a[240:243], v[52:55], a[100:103]\n"
"ds_read_b64 a[200:201], v246 offset:0\n"
"v_cvt_pk_bf16_f32 v188, v164, v165\n"
"v_mfma_f32_16x16x32_bf16 a[104:107], a[240:243], v[84:87], a[104:107]\n"
"v_exp_f32 v13, v13\n"
"v_mfma_f32_16x16x32_bf16 a[108:111], a[240:243], v[116:119], a[108:111]\n"
"v_add_f32 v221, v221, v166\n"
"v_add_f32 v221, v221, v167\n"
"v_mfma_f32_16x16x32_bf16 a[112:115], a[240:243], v[148:151], a[112:115]\n"
"v_exp_f32 v14, v14\n"
"v_mfma_f32_16x16x32_bf16 a[116:119], a[240:243], v[180:183], a[116:119]\n"
"ds_read_b64 a[202:203], v247 offset:0\n"
"v_cvt_pk_bf16_f32 v189, v166, v167\n"
"v_mfma_f32_16x16x32_bf16 a[140:143], a[244:247], v[180:183], a[140:143]\n"
"v_exp_f32 v15, v15\n"
"v_mfma_f32_16x16x32_bf16 a[136:139], a[244:247], v[148:151], a[136:139]\n"
"v_add_f32 v216, v216, v12\n"
"v_add_f32 v216, v216, v13\n"
"v_mfma_f32_16x16x32_bf16 a[132:135], a[244:247], v[116:119], a[132:135]\n"
"buffer_load_dwordx4 v242, s[52:55], s85 offen offset:2048 nt lds\n"
"v_exp_f32 v44, v44\n"
"v_mfma_f32_16x16x32_bf16 a[128:131], a[244:247], v[84:87], a[128:131]\n"
"ds_read_b64 a[204:205], v248 offset:0\n"
"v_cvt_pk_bf16_f32 v30, v12, v13\n"
"v_mfma_f32_16x16x32_bf16 a[124:127], a[244:247], v[52:55], a[124:127]\n"
"v_exp_f32 v45, v45\n"
"v_mfma_f32_16x16x32_bf16 a[120:123], a[244:247], v[20:23], a[120:123]\n"
"v_add_f32 v216, v216, v14\n"
"v_add_f32 v216, v216, v15\n"
"v_mfma_f32_16x16x32_bf16 a[144:147], a[248:251], v[20:23], a[144:147]\n"
"v_exp_f32 v46, v46\n"
"v_mfma_f32_16x16x32_bf16 a[148:151], a[248:251], v[52:55], a[148:151]\n"
"ds_read_b64 a[206:207], v249 offset:0\n"
"v_cvt_pk_bf16_f32 v31, v14, v15\n"
"v_mfma_f32_16x16x32_bf16 a[152:155], a[248:251], v[84:87], a[152:155]\n"
"v_exp_f32 v47, v47\n"
"v_mfma_f32_16x16x32_bf16 a[156:159], a[248:251], v[116:119], a[156:159]\n"
"v_add_f32 v217, v217, v44\n"
"v_add_f32 v217, v217, v45\n"
"v_mfma_f32_16x16x32_bf16 a[160:163], a[248:251], v[148:151], a[160:163]\n"
"v_exp_f32 v76, v76\n"
"v_mfma_f32_16x16x32_bf16 a[164:167], a[248:251], v[180:183], a[164:167]\n"
"v_cvt_pk_bf16_f32 v62, v44, v45\n"
"v_add_f32 v217, v217, v46\n"
"v_mfma_f32_16x16x32_bf16 a[188:191], a[252:255], v[180:183], a[188:191]\n"
"v_exp_f32 v77, v77\n"
"v_mfma_f32_16x16x32_bf16 a[184:187], a[252:255], v[148:151], a[184:187]\n"
"s_add_u32 m0, s63, 0x5000\n"
"v_add_f32 v217, v217, v47\n"
"v_cvt_pk_bf16_f32 v63, v46, v47\n"
"v_mfma_f32_16x16x32_bf16 a[180:183], a[252:255], v[116:119], a[180:183]\n"
"s_add_u32 s85, s85, 0x1000\n"
"buffer_load_dwordx4 v242, s[52:55], s85 offen nt lds\n"
"v_exp_f32 v78, v78\n"
"v_mfma_f32_16x16x32_bf16 a[176:179], a[252:255], v[84:87], a[176:179]\n"
"v_add_f32 v218, v218, v76\n"
"v_add_f32 v218, v218, v77\n"
"v_mfma_f32_16x16x32_bf16 a[172:175], a[252:255], v[52:55], a[172:175]\n"
"s_waitcnt lgkmcnt(0)\n"
"v_exp_f32 v79, v79\n"
"v_mfma_f32_16x16x32_bf16 a[168:171], a[252:255], v[20:23], a[168:171]\n"
"v_cvt_pk_bf16_f32 v94, v76, v77\n"
"v_add_f32 v218, v218, v78\n"
"v_mfma_f32_16x16x32_bf16 v[4:7], a[192:195], v[0:3], v[192:195]\n"
"v_exp_f32 v108, v108\n"
"v_mfma_f32_16x16x32_bf16 v[4:7], a[196:199], v[8:11], v[4:7]\n"
"ds_read_b64 a[208:209], v222 offset:4096\n"
"v_add_f32 v218, v218, v79\n"
"v_mfma_f32_16x16x32_bf16 v[4:7], a[200:203], v[16:19], v[4:7]\n"
"v_exp_f32 v109, v109\n"
"v_mfma_f32_16x16x32_bf16 v[4:7], a[204:207], v[24:27], v[4:7]\n"
"ds_read_b64 a[210:211], v223 offset:4096\n"
"v_cvt_pk_bf16_f32 v95, v78, v79\n"
"v_mfma_f32_16x16x32_bf16 v[36:39], a[204:207], v[56:59], v[196:199]\n"
"v_exp_f32 v110, v110\n"
"v_mfma_f32_16x16x32_bf16 v[36:39], a[200:203], v[48:51], v[36:39]\n"
"ds_read_b64 a[212:213], v224 offset:4096\n"
"v_add_f32 v219, v219, v108\n"
"v_mfma_f32_16x16x32_bf16 v[36:39], a[196:199], v[40:43], v[36:39]\n"
"v_exp_f32 v111, v111\n"
"v_mfma_f32_16x16x32_bf16 v[36:39], a[192:195], v[32:35], v[36:39]\n"
"ds_read_b64 a[214:215], v225 offset:4096\n"
"v_add_f32 v219, v219, v109\n"
"v_mfma_f32_16x16x32_bf16 v[68:71], a[192:195], v[64:67], v[200:203]\n"
"v_exp_f32 v140, v140\n"
"v_mfma_f32_16x16x32_bf16 v[68:71], a[196:199], v[72:75], v[68:71]\n"
"ds_read_b64 a[216:217], v246 offset:4096\n"
"v_cvt_pk_bf16_f32 v126, v108, v109\n"
"v_mfma_f32_16x16x32_bf16 v[68:71], a[200:203], v[80:83], v[68:71]\n"
"buffer_load_dwordx4 v242, s[52:55], s85 offen offset:2048 nt lds\n"
"v_exp_f32 v141, v141\n"
"v_mfma_f32_16x16x32_bf16 v[68:71], a[204:207], v[88:91], v[68:71]\n"
"ds_read_b64 a[218:219], v247 offset:4096\n"
"v_add_f32 v219, v219, v110\n"
"v_mfma_f32_16x16x32_bf16 v[100:103], a[204:207], v[120:123], v[204:207]\n"
"v_exp_f32 v142, v142\n"
"v_mfma_f32_16x16x32_bf16 v[100:103], a[200:203], v[112:115], v[100:103]\n"
"ds_read_b64 a[220:221], v248 offset:4096\n"
"v_add_f32 v219, v219, v111\n"
"v_mfma_f32_16x16x32_bf16 v[100:103], a[196:199], v[104:107], v[100:103]\n"
"v_exp_f32 v143, v143\n"
"v_mfma_f32_16x16x32_bf16 v[100:103], a[192:195], v[96:99], v[100:103]\n"
"ds_read_b64 a[222:223], v249 offset:4096\n"
"v_cvt_pk_bf16_f32 v127, v110, v111\n"
"v_mfma_f32_16x16x32_bf16 v[132:135], a[192:195], v[128:131], v[208:211]\n"
"v_exp_f32 v172, v172\n"
"v_mfma_f32_16x16x32_bf16 v[132:135], a[196:199], v[136:139], v[132:135]\n"
"ds_read_b64_tr_b16 a[224:225], v226 offset:49152\n"
"v_add_f32 v220, v220, v140\n"
"v_mfma_f32_16x16x32_bf16 v[132:135], a[200:203], v[144:147], v[132:135]\n"
"v_exp_f32 v173, v173\n"
"v_mfma_f32_16x16x32_bf16 v[132:135], a[204:207], v[152:155], v[132:135]\n"
"ds_read_b64_tr_b16 a[226:227], v226 offset:53248\n"
"v_add_f32 v220, v220, v141\n"
"v_cvt_pk_bf16_f32 v158, v140, v141\n"
"v_mfma_f32_16x16x32_bf16 v[164:167], a[204:207], v[184:187], v[212:215]\n"
"v_exp_f32 v174, v174\n"
"v_mfma_f32_16x16x32_bf16 v[164:167], a[200:203], v[176:179], v[164:167]\n"
"ds_read_b64_tr_b16 a[228:229], v227 offset:49152\n"
"v_add_f32 v220, v220, v142\n"
"v_mfma_f32_16x16x32_bf16 v[164:167], a[196:199], v[168:171], v[164:167]\n"
"s_waitcnt lgkmcnt(3)\n"
"v_exp_f32 v175, v175\n"
"v_mfma_f32_16x16x32_bf16 v[164:167], a[192:195], v[160:163], v[164:167]\n"
"s_add_u32 m0, s62, 0xc000\n"
"ds_read_b64_tr_b16 a[230:231], v227 offset:53248\n"
"v_add_f32 v220, v220, v143\n"
"v_cvt_pk_bf16_f32 v159, v142, v143\n"
"v_mfma_f32_16x16x32_bf16 v[12:15], a[208:211], v[0:3], v[192:195]\n"
"s_add_u32 s84, s84, 0x1000\n"
"s_and_b32 s84, s84, 0x1ffff\n"
"buffer_load_dwordx4 v240, s[48:51], s84 offen nt lds\n"
"v_exp_f32 v4, v4\n"
"v_mfma_f32_16x16x32_bf16 v[12:15], a[212:215], v[8:11], v[12:15]\n"
"ds_read_b64_tr_b16 a[232:233], v228 offset:49152\n"
"v_add_f32 v221, v221, v172\n"
"v_mfma_f32_16x16x32_bf16 v[12:15], a[216:219], v[16:19], v[12:15]\n"
"v_exp_f32 v5, v5\n"
"v_mfma_f32_16x16x32_bf16 v[12:15], a[220:223], v[24:27], v[12:15]\n"
"ds_read_b64_tr_b16 a[234:235], v228 offset:53248\n"
"v_add_f32 v221, v221, v173\n"
"v_cvt_pk_bf16_f32 v190, v172, v173\n"
"v_mfma_f32_16x16x32_bf16 v[44:47], a[220:223], v[56:59], v[196:199]\n"
"v_exp_f32 v6, v6\n"
"v_mfma_f32_16x16x32_bf16 v[44:47], a[216:219], v[48:51], v[44:47]\n"
"ds_read_b64_tr_b16 a[236:237], v229 offset:49152\n"
"v_add_f32 v221, v221, v174\n"
"v_mfma_f32_16x16x32_bf16 v[44:47], a[212:215], v[40:43], v[44:47]\n"
"v_exp_f32 v7, v7\n"
"v_mfma_f32_16x16x32_bf16 v[44:47], a[208:211], v[32:35], v[44:47]\n"
"ds_read_b64_tr_b16 a[238:239], v229 offset:53248\n"
"v_add_f32 v221, v221, v175\n"
"v_cvt_pk_bf16_f32 v191, v174, v175\n"
"v_mfma_f32_16x16x32_bf16 v[76:79], a[208:211], v[64:67], v[200:203]\n"
"v_exp_f32 v36, v36\n"
"v_mfma_f32_16x16x32_bf16 v[76:79], a[212:215], v[72:75], v[76:79]\n"
"ds_read_b64_tr_b16 a[240:241], v230 offset:49152\n"
"v_add_f32 v216, v216, v4\n"
"v_mfma_f32_16x16x32_bf16 v[76:79], a[216:219], v[80:83], v[76:79]\n"
"v_exp_f32 v37, v37\n"
"v_mfma_f32_16x16x32_bf16 v[76:79], a[220:223], v[88:91], v[76:79]\n"
"ds_read_b64_tr_b16 a[242:243], v230 offset:53248\n"
"v_add_f32 v216, v216, v5\n"
"v_cvt_pk_bf16_f32 v20, v4, v5\n"
"v_mfma_f32_16x16x32_bf16 v[108:111], a[220:223], v[120:123], v[204:207]\n"
"v_exp_f32 v38, v38\n"
"v_mfma_f32_16x16x32_bf16 v[108:111], a[216:219], v[112:115], v[108:111]\n"
"ds_read_b64_tr_b16 a[244:245], v231 offset:49152\n"
"v_add_f32 v216, v216, v6\n"
"v_mfma_f32_16x16x32_bf16 v[108:111], a[212:215], v[104:107], v[108:111]\n"
"buffer_load_dwordx4 v241, s[48:51], s84 offen offset:1024 nt lds\n"
"v_exp_f32 v39, v39\n"
"v_mfma_f32_16x16x32_bf16 v[108:111], a[208:211], v[96:99], v[108:111]\n"
"ds_read_b64_tr_b16 a[246:247], v231 offset:53248\n"
"v_add_f32 v216, v216, v7\n"
"v_cvt_pk_bf16_f32 v21, v6, v7\n"
"v_mfma_f32_16x16x32_bf16 v[140:143], a[208:211], v[128:131], v[208:211]\n"
"v_exp_f32 v68, v68\n"
"v_mfma_f32_16x16x32_bf16 v[140:143], a[212:215], v[136:139], v[140:143]\n"
"ds_read_b64_tr_b16 a[248:249], v232 offset:49152\n"
"v_add_f32 v217, v217, v36\n"
"v_mfma_f32_16x16x32_bf16 v[140:143], a[216:219], v[144:147], v[140:143]\n"
"v_exp_f32 v69, v69\n"
"v_mfma_f32_16x16x32_bf16 v[140:143], a[220:223], v[152:155], v[140:143]\n"
"ds_read_b64_tr_b16 a[250:251], v232 offset:53248\n"
"v_add_f32 v217, v217, v37\n"
"v_cvt_pk_bf16_f32 v52, v36, v37\n"
"v_mfma_f32_16x16x32_bf16 v[172:175], a[220:223], v[184:187], v[212:215]\n"
"v_exp_f32 v70, v70\n"
"v_mfma_f32_16x16x32_bf16 v[172:175], a[216:219], v[176:179], v[172:175]\n"
"ds_read_b64_tr_b16 a[252:253], v233 offset:49152\n"
"v_add_f32 v217, v217, v38\n"
"v_mfma_f32_16x16x32_bf16 v[172:175], a[212:215], v[168:171], v[172:175]\n"
"s_waitcnt lgkmcnt(3)\n"
"v_exp_f32 v71, v71\n"
"v_mfma_f32_16x16x32_bf16 v[172:175], a[208:211], v[160:163], v[172:175]\n"
"ds_read_b64_tr_b16 a[254:255], v233 offset:53248\n"
"v_add_f32 v217, v217, v39\n"
"v_cvt_pk_bf16_f32 v53, v38, v39\n"
"v_mfma_f32_16x16x32_bf16 a[0:3], a[224:227], v[28:31], a[0:3]\n"
"v_exp_f32 v100, v100\n"
"v_mfma_f32_16x16x32_bf16 a[4:7], a[224:227], v[60:63], a[4:7]\n"
"s_add_u32 m0, s62, 0xd000\n"
"v_add_f32 v218, v218, v68\n"
"v_add_f32 v218, v218, v69\n"
"v_mfma_f32_16x16x32_bf16 a[8:11], a[224:227], v[92:95], a[8:11]\n"
"s_add_u32 s84, s84, 0x1000\n"
"buffer_load_dwordx4 v240, s[48:51], s84 offen nt lds\n"
"v_exp_f32 v101, v101\n"
"v_mfma_f32_16x16x32_bf16 a[12:15], a[224:227], v[124:127], a[12:15]\n"
"v_cvt_pk_bf16_f32 v84, v68, v69\n"
"v_add_f32 v218, v218, v70\n"
"v_mfma_f32_16x16x32_bf16 a[16:19], a[224:227], v[156:159], a[16:19]\n"
"s_memrealtime s[88:89]\n"
"v_exp_f32 v102, v102\n"
"v_mfma_f32_16x16x32_bf16 a[20:23], a[224:227], v[188:191], a[20:23]\n"
"v_add_f32 v218, v218, v71\n"
"v_cvt_pk_bf16_f32 v85, v70, v71\n"
"v_mfma_f32_16x16x32_bf16 a[44:47], a[228:231], v[188:191], a[44:47]\n"
"v_exp_f32 v103, v103\n"
"v_mfma_f32_16x16x32_bf16 a[40:43], a[228:231], v[156:159], a[40:43]\n"
"v_add_f32 v219, v219, v100\n"
"v_add_f32 v219, v219, v101\n"
"v_mfma_f32_16x16x32_bf16 a[36:39], a[228:231], v[124:127], a[36:39]\n"
"LP7_%=:\n"
"s_waitcnt lgkmcnt(0)\n"
"s_sub_u32 s89, s88, s86\n"
"s_cmp_ge_i32 s89, 0\n"
"s_cbranch_scc1 LQ7_%=\n"
"s_memrealtime s[88:89]\n"
"s_branch LP7_%=\n"
"LQ7_%=:\n"
"s_waitcnt vmcnt(11) lgkmcnt(0)\n"
"s_barrier\n"
"v_exp_f32 v132, v132\n"
"v_mfma_f32_16x16x32_bf16 a[32:35], a[228:231], v[92:95], a[32:35]\n"
"ds_read_b64 a[192:193], v222 offset:16384\n"
"v_cvt_pk_bf16_f32 v116, v100, v101\n"
"v_mfma_f32_16x16x32_bf16 a[28:31], a[228:231], v[60:63], a[28:31]\n"
"s_add_u32 s86, s86, 124\n"
"v_exp_f32 v133, v133\n"
"v_mfma_f32_16x16x32_bf16 a[24:27], a[228:231], v[28:31], a[24:27]\n"
"v_add_f32 v219, v219, v102\n"
"v_add_f32 v219, v219, v103\n"
"v_mfma_f32_16x16x32_bf16 a[48:51], a[232:235], v[28:31], a[48:51]\n"
"v_exp_f32 v134, v134\n"
"v_mfma_f32_16x16x32_bf16 a[52:55], a[232:235], v[60:63], a[52:55]\n"
"ds_read_b64 a[194:195], v223 offset:16384\n"
"v_cvt_pk_bf16_f32 v117, v102, v103\n"
"v_mfma_f32_16x16x32_bf16 a[56:59], a[232:235], v[92:95], a[56:59]\n"
"v_exp_f32 v135, v135\n"
"v_mfma_f32_16x16x32_bf16 a[60:63], a[232:235], v[124:127], a[60:63]\n"
"v_add_f32 v220, v220, v132\n"
"v_add_f32 v220, v220, v133\n"
"v_mfma_f32_16x16x32_bf16 a[64:67], a[232:235], v[156:159], a[64:67]\n"
"buffer_load_dwordx4 v241, s[48:51], s84 offen offset:1024 nt lds\n"
"v_exp_f32 v164, v164\n"
"v_mfma_f32_16x16x32_bf16 a[68:71], a[232:235], v[188:191], a[68:71]\n"
"ds_read_b64 a[196:197], v224 offset:16384\n"
"v_cvt_pk_bf16_f32 v148, v132, v133\n"
"v_mfma_f32_16x16x32_bf16 a[92:95], a[236:239], v[188:191], a[92:95]\n"
"v_exp_f32 v165, v165\n"
"v_mfma_f32_16x16x32_bf16 a[88:91], a[236:239], v[156:159], a[88:91]\n"
"v_add_f32 v220, v220, v134\n"
"v_add_f32 v220, v220, v135\n"
"v_mfma_f32_16x16x32_bf16 a[84:87], a[236:239], v[124:127], a[84:87]\n"
"v_exp_f32 v166, v166\n"
"v_mfma_f32_16x16x32_bf16 a[80:83], a[236:239], v[92:95], a[80:83]\n"
"ds_read_b64 a[198:199], v225 offset:16384\n"
"v_cvt_pk_bf16_f32 v149, v134, v135\n"
"v_mfma_f32_16x16x32_bf16 a[76:79], a[236:239], v[60:63], a[76:79]\n"
"v_exp_f32 v167, v167\n"
"v_mfma_f32_16x16x32_bf16 a[72:75], a[236:239], v[28:31], a[72:75]\n"
"v_add_f32 v221, v221, v164\n"
"v_add_f32 v221, v221, v165\n"
"v_mfma_f32_16x16x32_bf16 a[96:99], a[240:243], v[28:31], a[96:99]\n"
"v_exp_f32 v12, v12\n"
"v_mfma_f32_16x16x32_bf16 a[100:103], a[240:243], v[60:63], a[100:103]\n"
"ds_read_b64 a[200:201], v246 offset:16384\n"
"v_cvt_pk_bf16_f32 v180, v164, v165\n"
"v_mfma_f32_16x16x32_bf16 a[104:107], a[240:243], v[92:95], a[104:107]\n"
"v_exp_f32 v13, v13\n"
"v_mfma_f32_16x16x32_bf16 a[108:111], a[240:243], v[124:127], a[108:111]\n"
"v_add_f32 v221, v221, v166\n"
"v_add_f32 v221, v221, v167\n"
"v_mfma_f32_16x16x32_bf16 a[112:115], a[240:243], v[156:159], a[112:115]\n"
"v_exp_f32 v14, v14\n"
"v_mfma_f32_16x16x32_bf16 a[116:119], a[240:243], v[188:191], a[116:119]\n"
"s_add_u32 m0, s63, 0x8000\n"
"ds_read_b64 a[202:203], v247 offset:16384\n"
"v_cvt_pk_bf16_f32 v181, v166, v167\n"
"v_mfma_f32_16x16x32_bf16 a[140:143], a[244:247], v[188:191], a[140:143]\n"
"s_add_u32 s85, s85, 0x1000\n"
"s_and_b32 s85, s85, 0x1ffff\n"
"buffer_load_dwordx4 v242, s[52:55], s85 offen nt lds\n"
"v_exp_f32 v15, v15\n"
"v_mfma_f32_16x16x32_bf16 a[136:139], a[244:247], v[156:159], a[136:139]\n"
"v_add_f32 v216, v216, v12\n"
"v_add_f32 v216, v216, v13\n"
"v_mfma_f32_16x16x32_bf16 a[132:135], a[244:247], v[124:127], a[132:135]\n"
"v_exp_f32 v44, v44\n"
"v_mfma_f32_16x16x32_bf16 a[128:131], a[244:247], v[92:95], a[128:131]\n"
"ds_read_b64 a[204:205], v248 offset:16384\n"
"v_cvt_pk_bf16_f32 v22, v12, v13\n"
"v_mfma_f32_16x16x32_bf16 a[124:127], a[244:247], v[60:63], a[124:127]\n"
"v_exp_f32 v45, v45\n"
"v_mfma_f32_16x16x32_bf16 a[120:123], a[244:247], v[28:31], a[120:123]\n"
"v_add_f32 v216, v216, v14\n"
"v_add_f32 v216, v216, v15\n"
"v_mfma_f32_16x16x32_bf16 a[144:147], a[248:251], v[28:31], a[144:147]\n"
"v_exp_f32 v46, v46\n"
"v_mfma_f32_16x16x32_bf16 a[148:151], a[248:251], v[60:63], a[148:151]\n"
"ds_read_b64 a[206:207], v249 offset:16384\n"
"v_cvt_pk_bf16_f32 v23, v14, v15\n"
"v_mfma_f32_16x16x32_bf16 a[152:155], a[248:251], v[92:95], a[152:155]\n"
"v_exp_f32 v47, v47\n"
"v_mfma_f32_16x16x32_bf16 a[156:159], a[248:251], v[124:127], a[156:159]\n"
"v_add_f32 v217, v217, v44\n"
"v_add_f32 v217, v217, v45\n"
"v_mfma_f32_16x16x32_bf16 a[160:163], a[248:251], v[156:159], a[160:163]\n"
"v_exp_f32 v76, v76\n"
"v_mfma_f32_16x16x32_bf16 a[164:167], a[248:251], v[188:191], a[164:167]\n"
"v_cvt_pk_bf16_f32 v54, v44, v45\n"
"v_add_f32 v217, v217, v46\n"
"v_mfma_f32_16x16x32_bf16 a[188:191], a[252:255], v[188:191], a[188:191]\n"
"buffer_load_dwordx4 v242, s[52:55], s85 offen offset:2048 nt lds\n"
"v_exp_f32 v77, v77\n"
"v_mfma_f32_16x16x32_bf16 a[184:187], a[252:255], v[156:159], a[184:187]\n"
"v_add_f32 v217, v217, v47\n"
"v_cvt_pk_bf16_f32 v55, v46, v47\n"
"v_mfma_f32_16x16x32_bf16 a[180:183], a[252:255], v[124:127], a[180:183]\n"
"v_exp_f32 v78, v78\n"
"v_mfma_f32_16x16x32_bf16 a[176:179], a[252:255], v[92:95], a[176:179]\n"
"v_add_f32 v218, v218, v76\n"
"v_add_f32 v218, v218, v77\n"
"v_mfma_f32_16x16x32_bf16 a[172:175], a[252:255], v[60:63], a[172:175]\n"
"s_waitcnt lgkmcnt(0)\n"
"v_exp_f32 v79, v79\n"
"v_mfma_f32_16x16x32_bf16 a[168:171], a[252:255], v[28:31], a[168:171]\n"
"v_cvt_pk_bf16_f32 v86, v76, v77\n"
"v_add_f32 v218, v218, v78\n"
"v_mfma_f32_16x16x32_bf16 v[4:7], a[192:195], v[0:3], v[192:195]\n"
"v_exp_f32 v108, v108\n"
"v_mfma_f32_16x16x32_bf16 v[4:7], a[196:199], v[8:11], v[4:7]\n"
"ds_read_b64 a[208:209], v222 offset:20480\n"
"v_add_f32 v218, v218, v79\n"
"v_mfma_f32_16x16x32_bf16 v[4:7], a[200:203], v[16:19], v[4:7]\n"
"v_exp_f32 v109, v109\n"
"v_mfma_f32_16x16x32_bf16 v[4:7], a[204:207], v[24:27], v[4:7]\n"
"ds_read_b64 a[210:211], v223 offset:20480\n"
"v_cvt_pk_bf16_f32 v87, v78, v79\n"
"v_mfma_f32_16x16x32_bf16 v[36:39], a[204:207], v[56:59], v[196:199]\n"
"v_exp_f32 v110, v110\n"
"v_mfma_f32_16x16x32_bf16 v[36:39], a[200:203], v[48:51], v[36:39]\n"
"ds_read_b64 a[212:213], v224 offset:20480\n"
"v_add_f32 v219, v219, v108\n"
"v_mfma_f32_16x16x32_bf16 v[36:39], a[196:199], v[40:43], v[36:39]\n"
"v_exp_f32 v111, v111\n"
"v_mfma_f32_16x16x32_bf16 v[36:39], a[192:195], v[32:35], v[36:39]\n"
"s_add_u32 m0, s63, 0x9000\n"
"ds_read_b64 a[214:215], v225 offset:20480\n"
"v_add_f32 v219, v219, v109\n"
"v_mfma_f32_16x16x32_bf16 v[68:71], a[192:195], v[64:67], v[200:203]\n"
"s_add_u32 s85, s85, 0x1000\n"
"buffer_load_dwordx4 v242, s[52:55], s85 offen nt lds\n"
"v_exp_f32 v140, v140\n"
"v_mfma_f32_16x16x32_bf16 v[68:71], a[196:199], v[72:75], v[68:71]\n"
"ds_read_b64 a[216:217], v246 offset:20480\n"
"v_cvt_pk_bf16_f32 v118, v108, v109\n"
"v_mfma_f32_16x16x32_bf16 v[68:71], a[200:203], v[80:83], v[68:71]\n"
"v_exp_f32 v141, v141\n"
"v_mfma_f32_16x16x32_bf16 v[68:71], a[204:207], v[88:91], v[68:71]\n"
"ds_read_b64 a[218:219], v247 offset:20480\n"
"v_add_f32 v219, v219, v110\n"
"v_mfma_f32_16x16x32_bf16 v[100:103], a[204:207], v[120:123], v[204:207]\n"
"v_exp_f32 v142, v142\n"
"v_mfma_f32_16x16x32_bf16 v[100:103], a[200:203], v[112:115], v[100:103]\n"
"ds_read_b64 a[220:221], v248 offset:20480\n"
"v_add_f32 v219, v219, v111\n"
"v_mfma_f32_16x16x32_bf16 v[100:103], a[196:199], v[104:107], v[100:103]\n"
"v_exp_f32 v143, v143\n"
"v_mfma_f32_16x16x32_bf16 v[100:103], a[192:195], v[96:99], v[100:103]\n"
"ds_read_b64 a[222:223], v249 offset:20480\n"
"v_cvt_pk_bf16_f32 v119, v110, v111\n"
"v_mfma_f32_16x16x32_bf16 v[132:135], a[192:195], v[128:131], v[208:211]\n"
"v_exp_f32 v172, v172\n"
"v_mfma_f32_16x16x32_bf16 v[132:135], a[196:199], v[136:139], v[132:135]\n"
"ds_read_b64_tr_b16 a[224:225], v226 offset:0\n"
"v_add_f32 v220, v220, v140\n"
"v_mfma_f32_16x16x32_bf16 v[132:135], a[200:203], v[144:147], v[132:135]\n"
"v_exp_f32 v173, v173\n"
"v_mfma_f32_16x16x32_bf16 v[132:135], a[204:207], v[152:155], v[132:135]\n"
"ds_read_b64_tr_b16 a[226:227], v226 offset:4096\n"
"v_add_f32 v220, v220, v141\n"
"v_cvt_pk_bf16_f32 v150, v140, v141\n"
"v_mfma_f32_16x16x32_bf16 v[164:167], a[204:207], v[184:187], v[212:215]\n"
"v_exp_f32 v174, v174\n"
"v_mfma_f32_16x16x32_bf16 v[164:167], a[200:203], v[176:179], v[164:167]\n"
"ds_read_b64_tr_b16 a[228:229], v227 offset:0\n"
"v_add_f32 v220, v220, v142\n"
"v_mfma_f32_16x16x32_bf16 v[164:167], a[196:199], v[168:171], v[164:167]\n"
"s_waitcnt lgkmcnt(3)\n"
"v_exp_f32 v175, v175\n"
"v_mfma_f32_16x16x32_bf16 v[164:167], a[192:195], v[160:163], v[164:167]\n"
"ds_read_b64_tr_b16 a[230:231], v227 offset:4096\n"
"v_add_f32 v220, v220, v143\n"
"v_cvt_pk_bf16_f32 v151, v142, v143\n"
"v_mfma_f32_16x16x32_bf16 v[12:15], a[208:211], v[0:3], v[192:195]\n"
"buffer_load_dwordx4 v242, s[52:55], s85 offen offset:2048 nt lds\n"
"v_exp_f32 v4, v4\n"
"v_mfma_f32_16x16x32_bf16 v[12:15], a[212:215], v[8:11], v[12:15]\n"
"ds_read_b64_tr_b16 a[232:233], v228 offset:0\n"
"v_add_f32 v221, v221, v172\n"
"v_mfma_f32_16x16x32_bf16 v[12:15], a[216:219], v[16:19], v[12:15]\n"
"v_exp_f32 v5, v5\n"
"v_mfma_f32_16x16x32_bf16 v[12:15], a[220:223], v[24:27], v[12:15]\n"
"ds_read_b64_tr_b16 a[234:235], v228 offset:4096\n"
"v_add_f32 v221, v221, v173\n"
"v_cvt_pk_bf16_f32 v182, v172, v173\n"
"v_mfma_f32_16x16x32_bf16 v[44:47], a[220:223], v[56:59], v[196:199]\n"
"v_exp_f32 v6, v6\n"
"v_mfma_f32_16x16x32_bf16 v[44:47], a[216:219], v[48:51], v[44:47]\n"
"ds_read_b64_tr_b16 a[236:237], v229 offset:0\n"
"v_add_f32 v221, v221, v174\n"
"v_mfma_f32_16x16x32_bf16 v[44:47], a[212:215], v[40:43], v[44:47]\n"
"v_exp_f32 v7, v7\n"
"v_mfma_f32_16x16x32_bf16 v[44:47], a[208:211], v[32:35], v[44:47]\n"
"ds_read_b64_tr_b16 a[238:239], v229 offset:4096\n"
"v_add_f32 v221, v221, v175\n"
"v_cvt_pk_bf16_f32 v183, v174, v175\n"
"v_mfma_f32_16x16x32_bf16 v[76:79], a[208:211], v[64:67], v[200:203]\n"
"v_exp_f32 v36, v36\n"
"v_mfma_f32_16x16x32_bf16 v[76:79], a[212:215], v[72:75], v[76:79]\n"
"ds_read_b64_tr_b16 a[240:241], v230 offset:0\n"
"v_add_f32 v216, v216, v4\n"
"v_mfma_f32_16x16x32_bf16 v[76:79], a[216:219], v[80:83], v[76:79]\n"
"v_exp_f32 v37, v37\n"
"v_mfma_f32_16x16x32_bf16 v[76:79], a[220:223], v[88:91], v[76:79]\n"
"s_add_u32 m0, s62, 0x0\n"
"ds_read_b64_tr_b16 a[242:243], v230 offset:4096\n"
"v_add_f32 v216, v216, v5\n"
"v_cvt_pk_bf16_f32 v28, v4, v5\n"
"v_mfma_f32_16x16x32_bf16 v[108:111], a[220:223], v[120:123], v[204:207]\n"
"s_add_u32 s84, s84, 0x1000\n"
"s_and_b32 s84, s84, 0x1ffff\n"
"buffer_load_dwordx4 v240, s[48:51], s84 offen nt lds\n"
"v_exp_f32 v38, v38\n"
"v_mfma_f32_16x16x32_bf16 v[108:111], a[216:219], v[112:115], v[108:111]\n"
"ds_read_b64_tr_b16 a[244:245], v231 offset:0\n"
"v_add_f32 v216, v216, v6\n"
"v_mfma_f32_16x16x32_bf16 v[108:111], a[212:215], v[104:107], v[108:111]\n"
"v_exp_f32 v39, v39\n"
"v_mfma_f32_16x16x32_bf16 v[108:111], a[208:211], v[96:99], v[108:111]\n"
"ds_read_b64_tr_b16 a[246:247], v231 offset:4096\n"
"v_add_f32 v216, v216, v7\n"
"v_cvt_pk_bf16_f32 v29, v6, v7\n"
"v_mfma_f32_16x16x32_bf16 v[140:143], a[208:211], v[128:131], v[208:211]\n"
"v_exp_f32 v68, v68\n"
"v_mfma_f32_16x16x32_bf16 v[140:143], a[212:215], v[136:139], v[140:143]\n"
"ds_read_b64_tr_b16 a[248:249], v232 offset:0\n"
"v_add_f32 v217, v217, v36\n"
"v_mfma_f32_16x16x32_bf16 v[140:143], a[216:219], v[144:147], v[140:143]\n"
"v_exp_f32 v69, v69\n"
"v_mfma_f32_16x16x32_bf16 v[140:143], a[220:223], v[152:155], v[140:143]\n"
"ds_read_b64_tr_b16 a[250:251], v232 offset:4096\n"
"v_add_f32 v217, v217, v37\n"
"v_cvt_pk_bf16_f32 v60, v36, v37\n"
"v_mfma_f32_16x16x32_bf16 v[172:175], a[220:223], v[184:187], v[212:215]\n"
"v_exp_f32 v70, v70\n"
"v_mfma_f32_16x16x32_bf16 v[172:175], a[216:219], v[176:179], v[172:175]\n"
"ds_read_b64_tr_b16 a[252:253], v233 offset:0\n"
"v_add_f32 v217, v217, v38\n"
"v_mfma_f32_16x16x32_bf16 v[172:175], a[212:215], v[168:171], v[172:175]\n"
"s_waitcnt lgkmcnt(3)\n"
"v_exp_f32 v71, v71\n"
"v_mfma_f32_16x16x32_bf16 v[172:175], a[208:211], v[160:163], v[172:175]\n"
"ds_read_b64_tr_b16 a[254:255], v233 offset:4096\n"
"v_add_f32 v217, v217, v39\n"
"v_cvt_pk_bf16_f32 v61, v38, v39\n"
"v_mfma_f32_16x16x32_bf16 a[0:3], a[224:227], v[20:23], a[0:3]\n"
"buffer_load_dwordx4 v241, s[48:51], s84 offen offset:1024 nt lds\n"
"v_exp_f32 v100, v100\n"
"v_mfma_f32_16x16x32_bf16 a[4:7], a[224:227], v[52:55], a[4:7]\n"
"v_add_f32 v218, v218, v68\n"
"v_add_f32 v218, v218, v69\n"
"v_mfma_f32_16x16x32_bf16 a[8:11], a[224:227], v[84:87], a[8:11]\n"
"v_exp_f32 v101, v101\n"
"v_mfma_f32_16x16x32_bf16 a[12:15], a[224:227], v[116:119], a[12:15]\n"
"v_cvt_pk_bf16_f32 v92, v68, v69\n"
"v_add_f32 v218, v218, v70\n"
"v_mfma_f32_16x16x32_bf16 a[16:19], a[224:227], v[148:151], a[16:19]\n"
"s_memrealtime s[88:89]\n"
"v_exp_f32 v102, v102\n"
"v_mfma_f32_16x16x32_bf16 a[20:23], a[224:227], v[180:183], a[20:23]\n"
"v_add_f32 v218, v218, v71\n"
"v_cvt_pk_bf16_f32 v93, v70, v71\n"
"v_mfma_f32_16x16x32_bf16 a[44:47], a[228:231], v[180:183], a[44:47]\n"
"v_exp_f32 v103, v103\n"
"v_mfma_f32_16x16x32_bf16 a[40:43], a[228:231], v[148:151], a[40:43]\n"
"v_add_f32 v219, v219, v100\n"
"v_add_f32 v219, v219, v101\n"
"v_mfma_f32_16x16x32_bf16 a[36:39], a[228:231], v[116:119], a[36:39]\n"
"LP8_%=:\n"
"s_waitcnt lgkmcnt(0)\n"
"s_sub_u32 s89, s88, s86\n"
"s_cmp_ge_i32 s89, 0\n"
"s_cbranch_scc1 LQ8_%=\n"
"s_memrealtime s[88:89]\n"
"s_branch LP8_%=\n"
"LQ8_%=:\n"
"s_waitcnt vmcnt(10) lgkmcnt(0)\n"
"s_barrier\n"
"v_exp_f32 v132, v132\n"
"v_mfma_f32_16x16x32_bf16 a[32:35], a[228:231], v[84:87], a[32:35]\n"
"ds_read_b64 a[192:193], v222 offset:32768\n"
"v_cvt_pk_bf16_f32 v124, v100, v101\n"
"v_mfma_f32_16x16x32_bf16 a[28:31], a[228:231], v[52:55], a[28:31]\n"
"s_add_u32 s86, s86, 124\n"
"v_exp_f32 v133, v133\n"
"v_mfma_f32_16x16x32_bf16 a[24:27], a[228:231], v[20:23], a[24:27]\n"
"v_add_f32 v219, v219, v102\n"
"v_add_f32 v219, v219, v103\n"
"v_mfma_f32_16x16x32_bf16 a[48:51], a[232:235], v[20:23], a[48:51]\n"
"v_exp_f32 v134, v134\n"
"v_mfma_f32_16x16x32_bf16 a[52:55], a[232:235], v[52:55], a[52:55]\n"
"s_add_u32 m0, s62, 0x1000\n"
"ds_read_b64 a[194:195], v223 offset:32768\n"
"v_cvt_pk_bf16_f32 v125, v102, v103\n"
"v_mfma_f32_16x16x32_bf16 a[56:59], a[232:235], v[84:87], a[56:59]\n"
"s_add_u32 s84, s84, 0x1000\n"
"buffer_load_dwordx4 v240, s[48:51], s84 offen nt lds\n"
"v_exp_f32 v135, v135\n"
"v_mfma_f32_16x16x32_bf16 a[60:63], a[232:235], v[116:119], a[60:63]\n"
"v_add_f32 v220, v220, v132\n"
"v_add_f32 v220, v220, v133\n"
"v_mfma_f32_16x16x32_bf16 a[64:67], a[232:235], v[148:151], a[64:67]\n"
"v_exp_f32 v164, v164\n"
"v_mfma_f32_16x16x32_bf16 a[68:71], a[232:235], v[180:183], a[68:71]\n"
"ds_read_b64 a[196:197], v224 offset:32768\n"
"v_cvt_pk_bf16_f32 v156, v132, v133\n"
"v_mfma_f32_16x16x32_bf16 a[92:95], a[236:239], v[180:183], a[92:95]\n"
"v_exp_f32 v165, v165\n"
"v_mfma_f32_16x16x32_bf16 a[88:91], a[236:239], v[148:151], a[88:91]\n"
"v_add_f32 v220, v220, v134\n"
"v_add_f32 v220, v220, v135\n"
"v_mfma_f32_16x16x32_bf16 a[84:87], a[236:239], v[116:119], a[84:87]\n"
"v_exp_f32 v166, v166\n"
"v_mfma_f32_16x16x32_bf16 a[80:83], a[236:239], v[84:87], a[80:83]\n"
"ds_read_b64 a[198:199], v225 offset:32768\n"
"v_cvt_pk_bf16_f32 v157, v134, v135\n"
"v_mfma_f32_16x16x32_bf16 a[76:79], a[236:239], v[52:55], a[76:79]\n"
"v_exp_f32 v167, v167\n"
"v_mfma_f32_16x16x32_bf16 a[72:75], a[236:239], v[20:23], a[72:75]\n"
"v_add_f32 v221, v221, v164\n"
"v_add_f32 v221, v221, v165\n"
"v_mfma_f32_16x16x32_bf16 a[96:99], a[240:243], v[20:23], a[96:99]\n"
"v_exp_f32 v12, v12\n"
"v_mfma_f32_16x16x32_bf16 a[100:103], a[240:243], v[52:55], a[100:103]\n"
"ds_read_b64 a[200:201], v246 offset:32768\n"
"v_cvt_pk_bf16_f32 v188, v164, v165\n"
"v_mfma_f32_16x16x32_bf16 a[104:107], a[240:243], v[84:87], a[104:107]\n"
"v_exp_f32 v13, v13\n"
"v_mfma_f32_16x16x32_bf16 a[108:111], a[240:243], v[116:119], a[108:111]\n"
"v_add_f32 v221, v221, v166\n"
"v_add_f32 v221, v221, v167\n"
"v_mfma_f32_16x16x32_bf16 a[112:115], a[240:243], v[148:151], a[112:115]\n"
"buffer_load_dwordx4 v241, s[48:51], s84 offen offset:1024 nt lds\n"
"v_exp_f32 v14, v14\n"
"v_mfma_f32_16x16x32_bf16 a[116:119], a[240:243], v[180:183], a[116:119]\n"
"ds_read_b64 a[202:203], v247 offset:32768\n"
"v_cvt_pk_bf16_f32 v189, v166, v167\n"
"v_mfma_f32_16x16x32_bf16 a[140:143], a[244:247], v[180:183], a[140:143]\n"
"v_exp_f32 v15, v15\n"
"v_mfma_f32_16x16x32_bf16 a[136:139], a[244:247], v[148:151], a[136:139]\n"
"v_add_f32 v216, v216, v12\n"
"v_add_f32 v216, v216, v13\n"
"v_mfma_f32_16x16x32_bf16 a[132:135], a[244:247], v[116:119], a[132:135]\n"
"v_exp_f32 v44, v44\n"
"v_mfma_f32_16x16x32_bf16 a[128:131], a[244:247], v[84:87], a[128:131]\n"
"ds_read_b64 a[204:205], v248 offset:32768\n"
"v_cvt_pk_bf16_f32 v30, v12, v13\n"
"v_mfma_f32_16x16x32_bf16 a[124:127], a[244:247], v[52:55], a[124:127]\n"
"v_exp_f32 v45, v45\n"
"v_mfma_f32_16x16x32_bf16 a[120:123], a[244:247], v[20:23], a[120:123]\n"
"v_add_f32 v216, v216, v14\n"
"v_add_f32 v216, v216, v15\n"
"v_mfma_f32_16x16x32_bf16 a[144:147], a[248:251], v[20:23], a[144:147]\n"
"v_exp_f32 v46, v46\n"
"v_mfma_f32_16x16x32_bf16 a[148:151], a[248:251], v[52:55], a[148:151]\n"
"ds_read_b64 a[206:207], v249 offset:32768\n"
"v_cvt_pk_bf16_f32 v31, v14, v15\n"
"v_mfma_f32_16x16x32_bf16 a[152:155], a[248:251], v[84:87], a[152:155]\n"
"v_exp_f32 v47, v47\n"
"v_mfma_f32_16x16x32_bf16 a[156:159], a[248:251], v[116:119], a[156:159]\n"
"s_add_u32 m0, s63, 0xc000\n"
"v_add_f32 v217, v217, v44\n"
"v_add_f32 v217, v217, v45\n"
"v_mfma_f32_16x16x32_bf16 a[160:163], a[248:251], v[148:151], a[160:163]\n"
"s_add_u32 s85, s85, 0x1000\n"
"s_and_b32 s85, s85, 0x1ffff\n"
"buffer_load_dwordx4 v242, s[52:55], s85 offen nt lds\n"
"v_exp_f32 v76, v76\n"
"v_mfma_f32_16x16x32_bf16 a[164:167], a[248:251], v[180:183], a[164:167]\n"
"v_cvt_pk_bf16_f32 v62, v44, v45\n"
"v_add_f32 v217, v217, v46\n"
"v_mfma_f32_16x16x32_bf16 a[188:191], a[252:255], v[180:183], a[188:191]\n"
"v_exp_f32 v77, v77\n"
"v_mfma_f32_16x16x32_bf16 a[184:187], a[252:255], v[148:151], a[184:187]\n"
"v_add_f32 v217, v217, v47\n"
"v_cvt_pk_bf16_f32 v63, v46, v47\n"
"v_mfma_f32_16x16x32_bf16 a[180:183], a[252:255], v[116:119], a[180:183]\n"
"v_exp_f32 v78, v78\n"
"v_mfma_f32_16x16x32_bf16 a[176:179], a[252:255], v[84:87], a[176:179]\n"
"v_add_f32 v218, v218, v76\n"
"v_add_f32 v218, v218, v77\n"
"v_mfma_f32_16x16x32_bf16 a[172:175], a[252:255], v[52:55], a[172:175]\n"
"s_waitcnt lgkmcnt(0)\n"
"v_exp_f32 v79, v79\n"
"v_mfma_f32_16x16x32_bf16 a[168:171], a[252:255], v[20:23], a[168:171]\n"
"v_cvt_pk_bf16_f32 v94, v76, v77\n"
"v_add_f32 v218, v218, v78\n"
"v_mfma_f32_16x16x32_bf16 v[4:7], a[192:195], v[0:3], v[192:195]\n"
"v_exp_f32 v108, v108\n"
"v_mfma_f32_16x16x32_bf16 v[4:7], a[196:199], v[8:11], v[4:7]\n"
"ds_read_b64 a[208:209], v222 offset:36864\n"
"v_add_f32 v218, v218, v79\n"
"v_mfma_f32_16x16x32_bf16 v[4:7], a[200:203], v[16:19], v[4:7]\n"
"v_exp_f32 v109, v109\n"
"v_mfma_f32_16x16x32_bf16 v[4:7], a[204:207], v[24:27], v[4:7]\n"
"ds_read_b64 a[210:211], v223 offset:36864\n"
"v_cvt_pk_bf16_f32 v95, v78, v79\n"
"v_mfma_f32_16x16x32_bf16 v[36:39], a[204:207], v[56:59], v[196:199]\n"
"v_exp_f32 v110, v110\n"
"v_mfma_f32_16x16x32_bf16 v[36:39], a[200:203], v[48:51], v[36:39]\n"
"ds_read_b64 a[212:213], v224 offset:36864\n"
"v_add_f32 v219, v219, v108\n"
"v_mfma_f32_16x16x32_bf16 v[36:39], a[196:199], v[40:43], v[36:39]\n"
"buffer_load_dwordx4 v242, s[52:55], s85 offen offset:2048 nt lds\n"
"v_exp_f32 v111, v111\n"
"v_mfma_f32_16x16x32_bf16 v[36:39], a[192:195], v[32:35], v[36:39]\n"
"ds_read_b64 a[214:215], v225 offset:36864\n"
"v_add_f32 v219, v219, v109\n"
"v_mfma_f32_16x16x32_bf16 v[68:71], a[192:195], v[64:67], v[200:203]\n"
"v_exp_f32 v140, v140\n"
"v_mfma_f32_16x16x32_bf16 v[68:71], a[196:199], v[72:75], v[68:71]\n"
"ds_read_b64 a[216:217], v246 offset:36864\n"
"v_cvt_pk_bf16_f32 v126, v108, v109\n"
"v_mfma_f32_16x16x32_bf16 v[68:71], a[200:203], v[80:83], v[68:71]\n"
"v_exp_f32 v141, v141\n"
"v_mfma_f32_16x16x32_bf16 v[68:71], a[204:207], v[88:91], v[68:71]\n"
"ds_read_b64 a[218:219], v247 offset:36864\n"
"v_add_f32 v219, v219, v110\n"
"v_mfma_f32_16x16x32_bf16 v[100:103], a[204:207], v[120:123], v[204:207]\n"
"v_exp_f32 v142, v142\n"
"v_mfma_f32_16x16x32_bf16 v[100:103], a[200:203], v[112:115], v[100:103]\n"
"ds_read_b64 a[220:221], v248 offset:36864\n"
"v_add_f32 v219, v219, v111\n"
"v_mfma_f32_16x16x32_bf16 v[100:103], a[196:199], v[104:107], v[100:103]\n"
"v_exp_f32 v143, v143\n"
"v_mfma_f32_16x16x32_bf16 v[100:103], a[192:195], v[96:99], v[100:103]\n"
"ds_read_b64 a[222:223], v249 offset:36864\n"
"v_cvt_pk_bf16_f32 v127, v110, v111\n"
"v_mfma_f32_16x16x32_bf16 v[132:135], a[192:195], v[128:131], v[208:211]\n"
"v_exp_f32 v172, v172\n"
"v_mfma_f32_16x16x32_bf16 v[132:135], a[196:199], v[136:139], v[132:135]\n"
"ds_read_b64_tr_b16 a[224:225], v226 offset:16384\n"
"v_add_f32 v220, v220, v140\n"
"v_mfma_f32_16x16x32_bf16 v[132:135], a[200:203], v[144:147], v[132:135]\n"
"v_exp_f32 v173, v173\n"
"v_mfma_f32_16x16x32_bf16 v[132:135], a[204:207], v[152:155], v[132:135]\n"
"s_add_u32 m0, s63, 0xd000\n"
"ds_read_b64_tr_b16 a[226:227], v226 offset:20480\n"
"v_add_f32 v220, v220, v141\n"
"v_cvt_pk_bf16_f32 v158, v140, v141\n"
"v_mfma_f32_16x16x32_bf16 v[164:167], a[204:207], v[184:187], v[212:215]\n"
"s_add_u32 s85, s85, 0x1000\n"
"buffer_load_dwordx4 v242, s[52:55], s85 offen nt lds\n"
"v_exp_f32 v174, v174\n"
"v_mfma_f32_16x16x32_bf16 v[164:167], a[200:203], v[176:179], v[164:167]\n"
"ds_read_b64_tr_b16 a[228:229], v227 offset:16384\n"
"v_add_f32 v220, v220, v142\n"
"v_mfma_f32_16x16x32_bf16 v[164:167], a[196:199], v[168:171], v[164:167]\n"
"s_waitcnt lgkmcnt(3)\n"
"v_exp_f32 v175, v175\n"
"v_mfma_f32_16x16x32_bf16 v[164:167], a[192:195], v[160:163], v[164:167]\n"
"ds_read_b64_tr_b16 a[230:231], v227 offset:20480\n"
"v_add_f32 v220, v220, v143\n"
"v_cvt_pk_bf16_f32 v159, v142, v143\n"
"v_mfma_f32_16x16x32_bf16 v[12:15], a[208:211], v[0:3], v[192:195]\n"
"v_exp_f32 v4, v4\n"
"v_mfma_f32_16x16x32_bf16 v[12:15], a[212:215], v[8:11], v[12:15]\n"
"ds_read_b64_tr_b16 a[232:233], v228 offset:16384\n"
"v_add_f32 v221, v221, v172\n"
"v_mfma_f32_16x16x32_bf16 v[12:15], a[216:219], v[16:19], v[12:15]\n"
"v_exp_f32 v5, v5\n"
"v_mfma_f32_16x16x32_bf16 v[12:15], a[220:223], v[24:27], v[12:15]\n"
"ds_read_b64_tr_b16 a[234:235], v228 offset:20480\n"
"v_add_f32 v221, v221, v173\n"
"v_cvt_pk_bf16_f32 v190, v172, v173\n"
"v_mfma_f32_16x16x32_bf16 v[44:47], a[220:223], v[56:59], v[196:199]\n"
"v_exp_f32 v6, v6\n"
"v_mfma_f32_16x16x32_bf16 v[44:47], a[216:219], v[48:51], v[44:47]\n"
"ds_read_b64_tr_b16 a[236:237], v229 offset:16384\n"
"v_add_f32 v221, v221, v174\n"
"v_mfma_f32_16x16x32_bf16 v[44:47], a[212:215], v[40:43], v[44:47]\n"
"v_exp_f32 v7, v7\n"
"v_mfma_f32_16x16x32_bf16 v[44:47], a[208:211], v[32:35], v[44:47]\n"
"ds_read_b64_tr_b16 a[238:239], v229 offset:20480\n"
"v_add_f32 v221, v221, v175\n"
"v_cvt_pk_bf16_f32 v191, v174, v175\n"
"v_mfma_f32_16x16x32_bf16 v[76:79], a[208:211], v[64:67], v[200:203]\n"
"v_exp_f32 v36, v36\n"
"v_mfma_f32_16x16x32_bf16 v[76:79], a[212:215], v[72:75], v[76:79]\n"
"ds_read_b64_tr_b16 a[240:241], v230 offset:16384\n"
"v_add_f32 v216, v216, v4\n"
"v_mfma_f32_16x16x32_bf16 v[76:79], a[216:219], v[80:83], v[76:79]\n"
"buffer_load_dwordx4 v242, s[52:55], s85 offen offset:2048 nt lds\n"
"v_exp_f32 v37, v37\n"
"v_mfma_f32_16x16x32_bf16 v[76:79], a[220:223], v[88:91], v[76:79]\n"
"ds_read_b64_tr_b16 a[242:243], v230 offset:20480\n"
"v_add_f32 v216, v216, v5\n"
"v_cvt_pk_bf16_f32 v20, v4, v5\n"
"v_mfma_f32_16x16x32_bf16 v[108:111], a[220:223], v[120:123], v[204:207]\n"
"v_exp_f32 v38, v38\n"
"v_mfma_f32_16x16x32_bf16 v[108:111], a[216:219], v[112:115], v[108:111]\n"
"ds_read_b64_tr_b16 a[244:245], v231 offset:16384\n"
"v_add_f32 v216, v216, v6\n"
"v_mfma_f32_16x16x32_bf16 v[108:111], a[212:215], v[104:107], v[108:111]\n"
"v_exp_f32 v39, v39\n"
"v_mfma_f32_16x16x32_bf16 v[108:111], a[208:211], v[96:99], v[108:111]\n"
"ds_read_b64_tr_b16 a[246:247], v231 offset:20480\n"
"v_add_f32 v216, v216, v7\n"
"v_cvt_pk_bf16_f32 v21, v6, v7\n"
"v_mfma_f32_16x16x32_bf16 v[140:143], a[208:211], v[128:131], v[208:211]\n"
"v_exp_f32 v68, v68\n"
"v_mfma_f32_16x16x32_bf16 v[140:143], a[212:215], v[136:139], v[140:143]\n"
"ds_read_b64_tr_b16 a[248:249], v232 offset:16384\n"
"v_add_f32 v217, v217, v36\n"
"v_mfma_f32_16x16x32_bf16 v[140:143], a[216:219], v[144:147], v[140:143]\n"
"v_exp_f32 v69, v69\n"
"v_mfma_f32_16x16x32_bf16 v[140:143], a[220:223], v[152:155], v[140:143]\n"
"ds_read_b64_tr_b16 a[250:251], v232 offset:20480\n"
"v_add_f32 v217, v217, v37\n"
"v_cvt_pk_bf16_f32 v52, v36, v37\n"
"v_mfma_f32_16x16x32_bf16 v[172:175], a[220:223], v[184:187], v[212:215]\n"
"v_exp_f32 v70, v70\n"
"v_mfma_f32_16x16x32_bf16 v[172:175], a[216:219], v[176:179], v[172:175]\n"
"ds_read_b64_tr_b16 a[252:253], v233 offset:16384\n"
"v_add_f32 v217, v217, v38\n"
"v_mfma_f32_16x16x32_bf16 v[172:175], a[212:215], v[168:171], v[172:175]\n"
"s_waitcnt lgkmcnt(3)\n"
"v_exp_f32 v71, v71\n"
"v_mfma_f32_16x16x32_bf16 v[172:175], a[208:211], v[160:163], v[172:175]\n"
"s_add_u32 m0, s62, 0x4000\n"
"ds_read_b64_tr_b16 a[254:255], v233 offset:20480\n"
"v_add_f32 v217, v217, v39\n"
"v_cvt_pk_bf16_f32 v53, v38, v39\n"
"v_mfma_f32_16x16x32_bf16 a[0:3], a[224:227], v[28:31], a[0:3]\n"
"s_add_u32 s84, s84, 0x1000\n"
"s_and_b32 s84, s84, 0x1ffff\n"
"buffer_load_dwordx4 v240, s[48:51], s84 offen nt lds\n"
"v_exp_f32 v100, v100\n"
"v_mfma_f32_16x16x32_bf16 a[4:7], a[224:227], v[60:63], a[4:7]\n"
"v_add_f32 v218, v218, v68\n"
"v_add_f32 v218, v218, v69\n"
"v_mfma_f32_16x16x32_bf16 a[8:11], a[224:227], v[92:95], a[8:11]\n"
"v_exp_f32 v101, v101\n"
"v_mfma_f32_16x16x32_bf16 a[12:15], a[224:227], v[124:127], a[12:15]\n"
"v_cvt_pk_bf16_f32 v84, v68, v69\n"
"v_add_f32 v218, v218, v70\n"
"v_mfma_f32_16x16x32_bf16 a[16:19], a[224:227], v[156:159], a[16:19]\n"
"s_memrealtime s[88:89]\n"
"v_exp_f32 v102, v102\n"
"v_mfma_f32_16x16x32_bf16 a[20:23], a[224:227], v[188:191], a[20:23]\n"
"v_add_f32 v218, v218, v71\n"
"v_cvt_pk_bf16_f32 v85, v70, v71\n"
"v_mfma_f32_16x16x32_bf16 a[44:47], a[228:231], v[188:191], a[44:47]\n"
"v_exp_f32 v103, v103\n"
"v_mfma_f32_16x16x32_bf16 a[40:43], a[228:231], v[156:159], a[40:43]\n"
"v_add_f32 v219, v219, v100\n"
"v_add_f32 v219, v219, v101\n"
"v_mfma_f32_16x16x32_bf16 a[36:39], a[228:231], v[124:127], a[36:39]\n"
"LP9_%=:\n"
"s_waitcnt lgkmcnt(0)\n"
"s_sub_u32 s89, s88, s86\n"
"s_cmp_ge_i32 s89, 0\n"
"s_cbranch_scc1 LQ9_%=\n"
"s_memrealtime s[88:89]\n"
"s_branch LP9_%=\n"
"LQ9_%=:\n"
"s_waitcnt vmcnt(9) lgkmcnt(0)\n"
"s_barrier\n"
"v_exp_f32 v132, v132\n"
"v_mfma_f32_16x16x32_bf16 a[32:35], a[228:231], v[92:95], a[32:35]\n"
"ds_read_b64 a[192:193], v222 offset:49152\n"
"v_cvt_pk_bf16_f32 v116, v100, v101\n"
"v_mfma_f32_16x16x32_bf16 a[28:31], a[228:231], v[60:63], a[28:31]\n"
"s_add_u32 s86, s86, 124\n"
"v_exp_f32 v133, v133\n"
"v_mfma_f32_16x16x32_bf16 a[24:27], a[228:231], v[28:31], a[24:27]\n"
"v_add_f32 v219, v219, v102\n"
"v_add_f32 v219, v219, v103\n"
"v_mfma_f32_16x16x32_bf16 a[48:51], a[232:235], v[28:31], a[48:51]\n"
"buffer_load_dwordx4 v241, s[48:51], s84 offen offset:1024 nt lds\n"
"v_exp_f32 v134, v134\n"
"v_mfma_f32_16x16x32_bf16 a[52:55], a[232:235], v[60:63], a[52:55]\n"
"ds_read_b64 a[194:195], v223 offset:49152\n"
"v_cvt_pk_bf16_f32 v117, v102, v103\n"
"v_mfma_f32_16x16x32_bf16 a[56:59], a[232:235], v[92:95], a[56:59]\n"
"v_exp_f32 v135, v135\n"
"v_mfma_f32_16x16x32_bf16 a[60:63], a[232:235], v[124:127], a[60:63]\n"
"v_add_f32 v220, v220, v132\n"
"v_add_f32 v220, v220, v133\n"
"v_mfma_f32_16x16x32_bf16 a[64:67], a[232:235], v[156:159], a[64:67]\n"
"v_exp_f32 v164, v164\n"
"v_mfma_f32_16x16x32_bf16 a[68:71], a[232:235], v[188:191], a[68:71]\n"
"ds_read_b64 a[196:197], v224 offset:49152\n"
"v_cvt_pk_bf16_f32 v148, v132, v133\n"
"v_mfma_f32_16x16x32_bf16 a[92:95], a[236:239], v[188:191], a[92:95]\n"
"v_exp_f32 v165, v165\n"
"v_mfma_f32_16x16x32_bf16 a[88:91], a[236:239], v[156:159], a[88:91]\n"
"v_add_f32 v220, v220, v134\n"
"v_add_f32 v220, v220, v135\n"
"v_mfma_f32_16x16x32_bf16 a[84:87], a[236:239], v[124:127], a[84:87]\n"
"v_exp_f32 v166, v166\n"
"v_mfma_f32_16x16x32_bf16 a[80:83], a[236:239], v[92:95], a[80:83]\n"
"ds_read_b64 a[198:199], v225 offset:49152\n"
"v_cvt_pk_bf16_f32 v149, v134, v135\n"
"v_mfma_f32_16x16x32_bf16 a[76:79], a[236:239], v[60:63], a[76:79]\n"
"v_exp_f32 v167, v167\n"
"v_mfma_f32_16x16x32_bf16 a[72:75], a[236:239], v[28:31], a[72:75]\n"
"v_add_f32 v221, v221, v164\n"
"v_add_f32 v221, v221, v165\n"
"v_mfma_f32_16x16x32_bf16 a[96:99], a[240:243], v[28:31], a[96:99]\n"
"v_exp_f32 v12, v12\n"
"v_mfma_f32_16x16x32_bf16 a[100:103], a[240:243], v[60:63], a[100:103]\n"
"s_add_u32 m0, s62, 0x5000\n"
"ds_read_b64 a[200:201], v246 offset:49152\n"
"v_cvt_pk_bf16_f32 v180, v164, v165\n"
"v_mfma_f32_16x16x32_bf16 a[104:107], a[240:243], v[92:95], a[104:107]\n"
"s_add_u32 s84, s84, 0x1000\n"
"buffer_load_dwordx4 v240, s[48:51], s84 offen nt lds\n"
"v_exp_f32 v13, v13\n"
"v_mfma_f32_16x16x32_bf16 a[108:111], a[240:243], v[124:127], a[108:111]\n"
"v_add_f32 v221, v221, v166\n"
"v_add_f32 v221, v221, v167\n"
"v_mfma_f32_16x16x32_bf16 a[112:115], a[240:243], v[156:159], a[112:115]\n"
"v_exp_f32 v14, v14\n"
"v_mfma_f32_16x16x32_bf16 a[116:119], a[240:243], v[188:191], a[116:119]\n"
"ds_read_b64 a[202:203], v247 offset:49152\n"
"v_cvt_pk_bf16_f32 v181, v166, v167\n"
"v_mfma_f32_16x16x32_bf16 a[140:143], a[244:247], v[188:191], a[140:143]\n"
"v_exp_f32 v15, v15\n"
"v_mfma_f32_16x16x32_bf16 a[136:139], a[244:247], v[156:159], a[136:139]\n"
"v_add_f32 v216, v216, v12\n"
"v_add_f32 v216, v216, v13\n"
"v_mfma_f32_16x16x32_bf16 a[132:135], a[244:247], v[124:127], a[132:135]\n"
"v_exp_f32 v44, v44\n"
"v_mfma_f32_16x16x32_bf16 a[128:131], a[244:247], v[92:95], a[128:131]\n"
"ds_read_b64 a[204:205], v248 offset:49152\n"
"v_cvt_pk_bf16_f32 v22, v12, v13\n"
"v_mfma_f32_16x16x32_bf16 a[124:127], a[244:247], v[60:63], a[124:127]\n"
"v_exp_f32 v45, v45\n"
"v_mfma_f32_16x16x32_bf16 a[120:123], a[244:247], v[28:31], a[120:123]\n"
"v_add_f32 v216, v216, v14\n"
"v_add_f32 v216, v216, v15\n"
"v_mfma_f32_16x16x32_bf16 a[144:147], a[248:251], v[28:31], a[144:147]\n"
"v_exp_f32 v46, v46\n"
"v_mfma_f32_16x16x32_bf16 a[148:151], a[248:251], v[60:63], a[148:151]\n"
"ds_read_b64 a[206:207], v249 offset:49152\n"
"v_cvt_pk_bf16_f32 v23, v14, v15\n"
"v_mfma_f32_16x16x32_bf16 a[152:155], a[248:251], v[92:95], a[152:155]\n"
"buffer_load_dwordx4 v241, s[48:51], s84 offen offset:1024 nt lds\n"
"v_exp_f32 v47, v47\n"
"v_mfma_f32_16x16x32_bf16 a[156:159], a[248:251], v[124:127], a[156:159]\n"
"v_add_f32 v217, v217, v44\n"
"v_add_f32 v217, v217, v45\n"
"v_mfma_f32_16x16x32_bf16 a[160:163], a[248:251], v[156:159], a[160:163]\n"
"v_exp_f32 v76, v76\n"
"v_mfma_f32_16x16x32_bf16 a[164:167], a[248:251], v[188:191], a[164:167]\n"
"v_cvt_pk_bf16_f32 v54, v44, v45\n"
"v_add_f32 v217, v217, v46\n"
"v_mfma_f32_16x16x32_bf16 a[188:191], a[252:255], v[188:191], a[188:191]\n"
"v_exp_f32 v77, v77\n"
"v_mfma_f32_16x16x32_bf16 a[184:187], a[252:255], v[156:159], a[184:187]\n"
"v_add_f32 v217, v217, v47\n"
"v_cvt_pk_bf16_f32 v55, v46, v47\n"
"v_mfma_f32_16x16x32_bf16 a[180:183], a[252:255], v[124:127], a[180:183]\n"
"v_exp_f32 v78, v78\n"
"v_mfma_f32_16x16x32_bf16 a[176:179], a[252:255], v[92:95], a[176:179]\n"
"v_add_f32 v218, v218, v76\n"
"v_add_f32 v218, v218, v77\n"
"v_mfma_f32_16x16x32_bf16 a[172:175], a[252:255], v[60:63], a[172:175]\n"
"s_waitcnt lgkmcnt(0)\n"
"v_exp_f32 v79, v79\n"
"v_mfma_f32_16x16x32_bf16 a[168:171], a[252:255], v[28:31], a[168:171]\n"
"v_cvt_pk_bf16_f32 v86, v76, v77\n"
"v_add_f32 v218, v218, v78\n"
"v_mfma_f32_16x16x32_bf16 v[4:7], a[192:195], v[0:3], v[192:195]\n"
"v_exp_f32 v108, v108\n"
"v_mfma_f32_16x16x32_bf16 v[4:7], a[196:199], v[8:11], v[4:7]\n"
"ds_read_b64 a[208:209], v222 offset:53248\n"
"v_add_f32 v218, v218, v79\n"
"v_mfma_f32_16x16x32_bf16 v[4:7], a[200:203], v[16:19], v[4:7]\n"
"v_exp_f32 v109, v109\n"
"v_mfma_f32_16x16x32_bf16 v[4:7], a[204:207], v[24:27], v[4:7]\n"
"s_add_u32 m0, s63, 0x0\n"
"ds_read_b64 a[210:211], v223 offset:53248\n"
"v_cvt_pk_bf16_f32 v87, v78, v79\n"
"v_mfma_f32_16x16x32_bf16 v[36:39], a[204:207], v[56:59], v[196:199]\n"
"s_add_u32 s85, s85, 0x1000\n"
"s_and_b32 s85, s85, 0x1ffff\n"
"buffer_load_dwordx4 v242, s[52:55], s85 offen nt lds\n"
"v_exp_f32 v110, v110\n"
"v_mfma_f32_16x16x32_bf16 v[36:39], a[200:203], v[48:51], v[36:39]\n"
"ds_read_b64 a[212:213], v224 offset:53248\n"
"v_add_f32 v219, v219, v108\n"
"v_mfma_f32_16x16x32_bf16 v[36:39], a[196:199], v[40:43], v[36:39]\n"
"v_exp_f32 v111, v111\n"
"v_mfma_f32_16x16x32_bf16 v[36:39], a[192:195], v[32:35], v[36:39]\n"
"ds_read_b64 a[214:215], v225 offset:53248\n"
"v_add_f32 v219, v219, v109\n"
"v_mfma_f32_16x16x32_bf16 v[68:71], a[192:195], v[64:67], v[200:203]\n"
"v_exp_f32 v140, v140\n"
"v_mfma_f32_16x16x32_bf16 v[68:71], a[196:199], v[72:75], v[68:71]\n"
"ds_read_b64 a[216:217], v246 offset:53248\n"
"v_cvt_pk_bf16_f32 v118, v108, v109\n"
"v_mfma_f32_16x16x32_bf16 v[68:71], a[200:203], v[80:83], v[68:71]\n"
"v_exp_f32 v141, v141\n"
"v_mfma_f32_16x16x32_bf16 v[68:71], a[204:207], v[88:91], v[68:71]\n"
"ds_read_b64 a[218:219], v247 offset:53248\n"
"v_add_f32 v219, v219, v110\n"
"v_mfma_f32_16x16x32_bf16 v[100:103], a[204:207], v[120:123], v[204:207]\n"
"v_exp_f32 v142, v142\n"
"v_mfma_f32_16x16x32_bf16 v[100:103], a[200:203], v[112:115], v[100:103]\n"
"ds_read_b64 a[220:221], v248 offset:53248\n"
"v_add_f32 v219, v219, v111\n"
"v_mfma_f32_16x16x32_bf16 v[100:103], a[196:199], v[104:107], v[100:103]\n"
"v_exp_f32 v143, v143\n"
"v_mfma_f32_16x16x32_bf16 v[100:103], a[192:195], v[96:99], v[100:103]\n"
"ds_read_b64 a[222:223], v249 offset:53248\n"
"v_cvt_pk_bf16_f32 v119, v110, v111\n"
"v_mfma_f32_16x16x32_bf16 v[132:135], a[192:195], v[128:131], v[208:211]\n"
"v_exp_f32 v172, v172\n"
"v_mfma_f32_16x16x32_bf16 v[132:135], a[196:199], v[136:139], v[132:135]\n"
"ds_read_b64_tr_b16 a[224:225], v226 offset:32768\n"
"v_add_f32 v220, v220, v140\n"
"v_mfma_f32_16x16x32_bf16 v[132:135], a[200:203], v[144:147], v[132:135]\n"
"buffer_load_dwordx4 v242, s[52:55], s85 offen offset:2048 nt lds\n"
"v_exp_f32 v173, v173\n"
"v_mfma_f32_16x16x32_bf16 v[132:135], a[204:207], v[152:155], v[132:135]\n"
"ds_read_b64_tr_b16 a[226:227], v226 offset:36864\n"
"v_add_f32 v220, v220, v141\n"
"v_cvt_pk_bf16_f32 v150, v140, v141\n"
"v_mfma_f32_16x16x32_bf16 v[164:167], a[204:207], v[184:187], v[212:215]\n"
"v_exp_f32 v174, v174\n"
"v_mfma_f32_16x16x32_bf16 v[164:167], a[200:203], v[176:179], v[164:167]\n"
"ds_read_b64_tr_b16 a[228:229], v227 offset:32768\n"
"v_add_f32 v220, v220, v142\n"
"v_mfma_f32_16x16x32_bf16 v[164:167], a[196:199], v[168:171], v[164:167]\n"
"s_waitcnt lgkmcnt(3)\n"
"v_exp_f32 v175, v175\n"
"v_mfma_f32_16x16x32_bf16 v[164:167], a[192:195], v[160:163], v[164:167]\n"
"ds_read_b64_tr_b16 a[230:231], v227 offset:36864\n"
"v_add_f32 v220, v220, v143\n"
"v_cvt_pk_bf16_f32 v151, v142, v143\n"
"v_mfma_f32_16x16x32_bf16 v[12:15], a[208:211], v[0:3], v[192:195]\n"
"v_exp_f32 v4, v4\n"
"v_mfma_f32_16x16x32_bf16 v[12:15], a[212:215], v[8:11], v[12:15]\n"
"ds_read_b64_tr_b16 a[232:233], v228 offset:32768\n"
"v_add_f32 v221, v221, v172\n"
"v_mfma_f32_16x16x32_bf16 v[12:15], a[216:219], v[16:19], v[12:15]\n"
"v_exp_f32 v5, v5\n"
"v_mfma_f32_16x16x32_bf16 v[12:15], a[220:223], v[24:27], v[12:15]\n"
"ds_read_b64_tr_b16 a[234:235], v228 offset:36864\n"
"v_add_f32 v221, v221, v173\n"
"v_cvt_pk_bf16_f32 v182, v172, v173\n"
"v_mfma_f32_16x16x32_bf16 v[44:47], a[220:223], v[56:59], v[196:199]\n"
"v_exp_f32 v6, v6\n"
"v_mfma_f32_16x16x32_bf16 v[44:47], a[216:219], v[48:51], v[44:47]\n"
"ds_read_b64_tr_b16 a[236:237], v229 offset:32768\n"
"v_add_f32 v221, v221, v174\n"
"v_mfma_f32_16x16x32_bf16 v[44:47], a[212:215], v[40:43], v[44:47]\n"
"v_exp_f32 v7, v7\n"
"v_mfma_f32_16x16x32_bf16 v[44:47], a[208:211], v[32:35], v[44:47]\n"
"s_add_u32 m0, s63, 0x1000\n"
"ds_read_b64_tr_b16 a[238:239], v229 offset:36864\n"
"v_add_f32 v221, v221, v175\n"
"v_cvt_pk_bf16_f32 v183, v174, v175\n"
"v_mfma_f32_16x16x32_bf16 v[76:79], a[208:211], v[64:67], v[200:203]\n"
"s_add_u32 s85, s85, 0x1000\n"
"buffer_load_dwordx4 v242, s[52:55], s85 offen nt lds\n"
"v_exp_f32 v36, v36\n"
"v_mfma_f32_16x16x32_bf16 v[76:79], a[212:215], v[72:75], v[76:79]\n"
"ds_read_b64_tr_b16 a[240:241], v230 offset:32768\n"
"v_add_f32 v216, v216, v4\n"
"v_mfma_f32_16x16x32_bf16 v[76:79], a[216:219], v[80:83], v[76:79]\n"
"v_exp_f32 v37, v37\n"
"v_mfma_f32_16x16x32_bf16 v[76:79], a[220:223], v[88:91], v[76:79]\n"
"ds_read_b64_tr_b16 a[242:243], v230 offset:36864\n"
"v_add_f32 v216, v216, v5\n"
"v_cvt_pk_bf16_f32 v28, v4, v5\n"
"v_mfma_f32_16x16x32_bf16 v[108:111], a[220:223], v[120:123], v[204:207]\n"
"v_exp_f32 v38, v38\n"
"v_mfma_f32_16x16x32_bf16 v[108:111], a[216:219], v[112:115], v[108:111]\n"
"ds_read_b64_tr_b16 a[244:245], v231 offset:32768\n"
"v_add_f32 v216, v216, v6\n"
"v_mfma_f32_16x16x32_bf16 v[108:111], a[212:215], v[104:107], v[108:111]\n"
"v_exp_f32 v39, v39\n"
"v_mfma_f32_16x16x32_bf16 v[108:111], a[208:211], v[96:99], v[108:111]\n"
"ds_read_b64_tr_b16 a[246:247], v231 offset:36864\n"
"v_add_f32 v216, v216, v7\n"
"v_cvt_pk_bf16_f32 v29, v6, v7\n"
"v_mfma_f32_16x16x32_bf16 v[140:143], a[208:211], v[128:131], v[208:211]\n"
"v_exp_f32 v68, v68\n"
"v_mfma_f32_16x16x32_bf16 v[140:143], a[212:215], v[136:139], v[140:143]\n"
"ds_read_b64_tr_b16 a[248:249], v232 offset:32768\n"
"v_add_f32 v217, v217, v36\n"
"v_mfma_f32_16x16x32_bf16 v[140:143], a[216:219], v[144:147], v[140:143]\n"
"v_exp_f32 v69, v69\n"
"v_mfma_f32_16x16x32_bf16 v[140:143], a[220:223], v[152:155], v[140:143]\n"
"ds_read_b64_tr_b16 a[250:251], v232 offset:36864\n"
"v_add_f32 v217, v217, v37\n"
"v_cvt_pk_bf16_f32 v60, v36, v37\n"
"v_mfma_f32_16x16x32_bf16 v[172:175], a[220:223], v[184:187], v[212:215]\n"
"buffer_load_dwordx4 v242, s[52:55], s85 offen offset:2048 nt lds\n"
"v_exp_f32 v70, v70\n"
"v_mfma_f32_16x16x32_bf16 v[172:175], a[216:219], v[176:179], v[172:175]\n"
"ds_read_b64_tr_b16 a[252:253], v233 offset:32768\n"
"v_add_f32 v217, v217, v38\n"
"v_mfma_f32_16x16x32_bf16 v[172:175], a[212:215], v[168:171], v[172:175]\n"
"s_waitcnt lgkmcnt(3)\n"
"v_exp_f32 v71, v71\n"
"v_mfma_f32_16x16x32_bf16 v[172:175], a[208:211], v[160:163], v[172:175]\n"
"ds_read_b64_tr_b16 a[254:255], v233 offset:36864\n"
"v_add_f32 v217, v217, v39\n"
"v_cvt_pk_bf16_f32 v61, v38, v39\n"
"v_mfma_f32_16x16x32_bf16 a[0:3], a[224:227], v[20:23], a[0:3]\n"
"v_exp_f32 v100, v100\n"
"v_mfma_f32_16x16x32_bf16 a[4:7], a[224:227], v[52:55], a[4:7]\n"
"v_add_f32 v218, v218, v68\n"
"v_add_f32 v218, v218, v69\n"
"v_mfma_f32_16x16x32_bf16 a[8:11], a[224:227], v[84:87], a[8:11]\n"
"v_exp_f32 v101, v101\n"
"v_mfma_f32_16x16x32_bf16 a[12:15], a[224:227], v[116:119], a[12:15]\n"
"v_cvt_pk_bf16_f32 v92, v68, v69\n"
"v_add_f32 v218, v218, v70\n"
"v_mfma_f32_16x16x32_bf16 a[16:19], a[224:227], v[148:151], a[16:19]\n"
"s_memrealtime s[88:89]\n"
"v_exp_f32 v102, v102\n"
"v_mfma_f32_16x16x32_bf16 a[20:23], a[224:227], v[180:183], a[20:23]\n"
"v_add_f32 v218, v218, v71\n"
"v_cvt_pk_bf16_f32 v93, v70, v71\n"
"v_mfma_f32_16x16x32_bf16 a[44:47], a[228:231], v[180:183], a[44:47]\n"
"v_exp_f32 v103, v103\n"
"v_mfma_f32_16x16x32_bf16 a[40:43], a[228:231], v[148:151], a[40:43]\n"
"v_add_f32 v219, v219, v100\n"
"v_add_f32 v219, v219, v101\n"
"v_mfma_f32_16x16x32_bf16 a[36:39], a[228:231], v[116:119], a[36:39]\n"
"LP10_%=:\n"
"s_waitcnt lgkmcnt(0)\n"
"s_sub_u32 s89, s88, s86\n"
"s_cmp_ge_i32 s89, 0\n"
"s_cbranch_scc1 LQ10_%=\n"
"s_memrealtime s[88:89]\n"
"s_branch LP10_%=\n"
"LQ10_%=:\n"
"s_waitcnt vmcnt(8) lgkmcnt(0)\n"
"s_barrier\n"
"v_exp_f32 v132, v132\n"
"v_mfma_f32_16x16x32_bf16 a[32:35], a[228:231], v[84:87], a[32:35]\n"
"ds_read_b64 a[192:193], v222 offset:0\n"
"v_cvt_pk_bf16_f32 v124, v100, v101\n"
"v_mfma_f32_16x16x32_bf16 a[28:31], a[228:231], v[52:55], a[28:31]\n"
"s_add_u32 s86, s86, 124\n"
"v_exp_f32 v133, v133\n"
"v_mfma_f32_16x16x32_bf16 a[24:27], a[228:231], v[20:23], a[24:27]\n"
"s_add_u32 m0, s62, 0x8000\n"
"v_add_f32 v219, v219, v102\n"
"v_add_f32 v219, v219, v103\n"
"v_mfma_f32_16x16x32_bf16 a[48:51], a[232:235], v[20:23], a[48:51]\n"
"s_add_u32 s84, s84, 0x1000\n"
"s_and_b32 s84, s84, 0x1ffff\n"
"buffer_load_dwordx4 v240, s[48:51], s84 offen nt lds\n"
"v_exp_f32 v134, v134\n"
"v_mfma_f32_16x16x32_bf16 a[52:55], a[232:235], v[52:55], a[52:55]\n"
"ds_read_b64 a[194:195], v223 offset:0\n"
"v_cvt_pk_bf16_f32 v125, v102, v103\n"
"v_mfma_f32_16x16x32_bf16 a[56:59], a[232:235], v[84:87], a[56:59]\n"
"v_exp_f32 v135, v135\n"
"v_mfma_f32_16x16x32_bf16 a[60:63], a[232:235], v[116:119], a[60:63]\n"
"v_add_f32 v220, v220, v132\n"
"v_add_f32 v220, v220, v133\n"
"v_mfma_f32_16x16x32_bf16 a[64:67], a[232:235], v[148:151], a[64:67]\n"
"v_exp_f32 v164, v164\n"
"v_mfma_f32_16x16x32_bf16 a[68:71], a[232:235], v[180:183], a[68:71]\n"
"ds_read_b64 a[196:197], v224 offset:0\n"
"v_cvt_pk_bf16_f32 v156, v132, v133\n"
"v_mfma_f32_16x16x32_bf16 a[92:95], a[236:239], v[180:183], a[92:95]\n"
"v_exp_f32 v165, v165\n"
"v_mfma_f32_16x16x32_bf16 a[88:91], a[236:239], v[148:151], a[88:91]\n"
"v_add_f32 v220, v220, v134\n"
"v_add_f32 v220, v220, v135\n"
"v_mfma_f32_16x16x32_bf16 a[84:87], a[236:239], v[116:119], a[84:87]\n"
"v_exp_f32 v166, v166\n"
"v_mfma_f32_16x16x32_bf16 a[80:83], a[236:239], v[84:87], a[80:83]\n"
"ds_read_b64 a[198:199], v225 offset:0\n"
"v_cvt_pk_bf16_f32 v157, v134, v135\n"
"v_mfma_f32_16x16x32_bf16 a[76:79], a[236:239], v[52:55], a[76:79]\n"
"v_exp_f32 v167, v167\n"
"v_mfma_f32_16x16x32_bf16 a[72:75], a[236:239], v[20:23], a[72:75]\n"
"v_add_f32 v221, v221, v164\n"
"v_add_f32 v221, v221, v165\n"
"v_mfma_f32_16x16x32_bf16 a[96:99], a[240:243], v[20:23], a[96:99]\n"
"buffer_load_dwordx4 v241, s[48:51], s84 offen offset:1024 nt lds\n"
"v_exp_f32 v12, v12\n"
"v_mfma_f32_16x16x32_bf16 a[100:103], a[240:243], v[52:55], a[100:103]\n"
"ds_read_b64 a[200:201], v246 offset:0\n"
"v_cvt_pk_bf16_f32 v188, v164, v165\n"
"v_mfma_f32_16x16x32_bf16 a[104:107], a[240:243], v[84:87], a[104:107]\n"
"v_exp_f32 v13, v13\n"
"v_mfma_f32_16x16x32_bf16 a[108:111], a[240:243], v[116:119], a[108:111]\n"
"v_add_f32 v221, v221, v166\n"
"v_add_f32 v221, v221, v167\n"
"v_mfma_f32_16x16x32_bf16 a[112:115], a[240:243], v[148:151], a[112:115]\n"
"s_add_u32 s90, s87, 0x200\n"
"v_exp_f32 v14, v14\n"
"v_mfma_f32_16x16x32_bf16 a[116:119], a[240:243], v[180:183], a[116:119]\n"
"s_add_u32 m0, s91, 0x20000\n"
"ds_read_b64 a[202:203], v247 offset:0\n"
"v_cvt_pk_bf16_f32 v189, v166, v167\n"
"v_mfma_f32_16x16x32_bf16 a[140:143], a[244:247], v[180:183], a[140:143]\n"
"buffer_load_dwordx4 v245, s[92:95], s90 offen lds\n"
"v_exp_f32 v15, v15\n"
"v_mfma_f32_16x16x32_bf16 a[136:139], a[244:247], v[148:151], a[136:139]\n"
"v_add_f32 v216, v216, v12\n"
"v_add_f32 v216, v216, v13\n"
"v_mfma_f32_16x16x32_bf16 a[132:135], a[244:247], v[116:119], a[132:135]\n"
"v_exp_f32 v44, v44\n"
"v_mfma_f32_16x16x32_bf16 a[128:131], a[244:247], v[84:87], a[128:131]\n"
"ds_read_b64 a[204:205], v248 offset:0\n"
"v_cvt_pk_bf16_f32 v30, v12, v13\n"
"v_mfma_f32_16x16x32_bf16 a[124:127], a[244:247], v[52:55], a[124:127]\n"
"v_exp_f32 v45, v45\n"
"v_mfma_f32_16x16x32_bf16 a[120:123], a[244:247], v[20:23], a[120:123]\n"
"v_add_f32 v216, v216, v14\n"
"v_add_f32 v216, v216, v15\n"
"v_mfma_f32_16x16x32_bf16 a[144:147], a[248:251], v[20:23], a[144:147]\n"
"buffer_load_dwordx4 v251, s[92:95], s90 offen offset:1024 lds\n"
"v_exp_f32 v46, v46\n"
"v_mfma_f32_16x16x32_bf16 a[148:151], a[248:251], v[52:55], a[148:151]\n"
"ds_read_b64 a[206:207], v249 offset:0\n"
"v_cvt_pk_bf16_f32 v31, v14, v15\n"
"v_mfma_f32_16x16x32_bf16 a[152:155], a[248:251], v[84:87], a[152:155]\n"
"v_exp_f32 v47, v47\n"
"v_mfma_f32_16x16x32_bf16 a[156:159], a[248:251], v[116:119], a[156:159]\n"
"v_add_f32 v217, v217, v44\n"
"v_add_f32 v217, v217, v45\n"
"v_mfma_f32_16x16x32_bf16 a[160:163], a[248:251], v[148:151], a[160:163]\n"
"v_exp_f32 v76, v76\n"
"v_mfma_f32_16x16x32_bf16 a[164:167], a[248:251], v[180:183], a[164:167]\n"
"v_cvt_pk_bf16_f32 v62, v44, v45\n"
"v_add_f32 v217, v217, v46\n"
"v_mfma_f32_16x16x32_bf16 a[188:191], a[252:255], v[180:183], a[188:191]\n"
"buffer_load_dwordx4 v252, s[92:95], s90 offen offset:2048 lds\n"
"v_exp_f32 v77, v77\n"
"v_mfma_f32_16x16x32_bf16 a[184:187], a[252:255], v[148:151], a[184:187]\n"
"v_add_f32 v217, v217, v47\n"
"v_cvt_pk_bf16_f32 v63, v46, v47\n"
"v_mfma_f32_16x16x32_bf16 a[180:183], a[252:255], v[116:119], a[180:183]\n"
"v_exp_f32 v78, v78\n"
"v_mfma_f32_16x16x32_bf16 a[176:179], a[252:255], v[84:87], a[176:179]\n"
"v_add_f32 v218, v218, v76\n"
"v_add_f32 v218, v218, v77\n"
"v_mfma_f32_16x16x32_bf16 a[172:175], a[252:255], v[52:55], a[172:175]\n"
"s_waitcnt lgkmcnt(0)\n"
"v_exp_f32 v79, v79\n"
"v_mfma_f32_16x16x32_bf16 a[168:171], a[252:255], v[20:23], a[168:171]\n"
"v_cvt_pk_bf16_f32 v94, v76, v77\n"
"v_add_f32 v218, v218, v78\n"
"v_mfma_f32_16x16x32_bf16 v[4:7], a[192:195], v[0:3], v[192:195]\n"
"v_exp_f32 v108, v108\n"
"v_mfma_f32_16x16x32_bf16 v[4:7], a[196:199], v[8:11], v[4:7]\n"
"ds_read_b64 a[208:209], v222 offset:4096\n"
"v_add_f32 v218, v218, v79\n"
"v_mfma_f32_16x16x32_bf16 v[4:7], a[200:203], v[16:19], v[4:7]\n"
"buffer_load_dwordx4 v253, s[92:95], s90 offen offset:3072 lds\n"
"v_exp_f32 v109, v109\n"
"v_mfma_f32_16x16x32_bf16 v[4:7], a[204:207], v[24:27], v[4:7]\n"
"ds_read_b64 a[210:211], v223 offset:4096\n"
"v_cvt_pk_bf16_f32 v95, v78, v79\n"
"v_mfma_f32_16x16x32_bf16 v[36:39], a[204:207], v[56:59], v[196:199]\n"
"v_exp_f32 v110, v110\n"
"v_mfma_f32_16x16x32_bf16 v[36:39], a[200:203], v[48:51], v[36:39]\n"
"ds_read_b64 a[212:213], v224 offset:4096\n"
"v_add_f32 v219, v219, v108\n"
"v_mfma_f32_16x16x32_bf16 v[36:39], a[196:199], v[40:43], v[36:39]\n"
"v_exp_f32 v111, v111\n"
"v_mfma_f32_16x16x32_bf16 v[36:39], a[192:195], v[32:35], v[36:39]\n"
"s_add_u32 m0, s62, 0x9000\n"
"ds_read_b64 a[214:215], v225 offset:4096\n"
"v_add_f32 v219, v219, v109\n"
"v_mfma_f32_16x16x32_bf16 v[68:71], a[192:195], v[64:67], v[200:203]\n"
"s_add_u32 s84, s84, 0x1000\n"
"buffer_load_dwordx4 v240, s[48:51], s84 offen nt lds\n"
"v_exp_f32 v140, v140\n"
"v_mfma_f32_16x16x32_bf16 v[68:71], a[196:199], v[72:75], v[68:71]\n"
"ds_read_b64 a[216:217], v246 offset:4096\n"
"v_cvt_pk_bf16_f32 v126, v108, v109\n"
"v_mfma_f32_16x16x32_bf16 v[68:71], a[200:203], v[80:83], v[68:71]\n"
"v_exp_f32 v141, v141\n"
"v_mfma_f32_16x16x32_bf16 v[68:71], a[204:207], v[88:91], v[68:71]\n"
"ds_read_b64 a[218:219], v247 offset:4096\n"
"v_add_f32 v219, v219, v110\n"
"v_mfma_f32_16x16x32_bf16 v[100:103], a[204:207], v[120:123], v[204:207]\n"
"v_exp_f32 v142, v142\n"
"v_mfma_f32_16x16x32_bf16 v[100:103], a[200:203], v[112:115], v[100:103]\n"
"ds_read_b64 a[220:221], v248 offset:4096\n"
"v_add_f32 v219, v219, v111\n"
"v_mfma_f32_16x16x32_bf16 v[100:103], a[196:199], v[104:107], v[100:103]\n"
"buffer_load_dwordx4 v241, s[48:51], s84 offen offset:1024 nt lds\n"
"v_exp_f32 v143, v143\n"
"v_mfma_f32_16x16x32_bf16 v[100:103], a[192:195], v[96:99], v[100:103]\n"
"ds_read_b64 a[222:223], v249 offset:4096\n"
"v_cvt_pk_bf16_f32 v127, v110, v111\n"
"v_mfma_f32_16x16x32_bf16 v[132:135], a[192:195], v[128:131], v[208:211]\n"
"v_exp_f32 v172, v172\n"
"v_mfma_f32_16x16x32_bf16 v[132:135], a[196:199], v[136:139], v[132:135]\n"
"ds_read_b64_tr_b16 a[224:225], v226 offset:49152\n"
"v_add_f32 v220, v220, v140\n"
"v_mfma_f32_16x16x32_bf16 v[132:135], a[200:203], v[144:147], v[132:135]\n"
"v_exp_f32 v173, v173\n"
"v_mfma_f32_16x16x32_bf16 v[132:135], a[204:207], v[152:155], v[132:135]\n"
"s_add_u32 m0, s63, 0x4000\n"
"ds_read_b64_tr_b16 a[226:227], v226 offset:53248\n"
"v_add_f32 v220, v220, v141\n"
"v_cvt_pk_bf16_f32 v158, v140, v141\n"
"v_mfma_f32_16x16x32_bf16 v[164:167], a[204:207], v[184:187], v[212:215]\n"
"s_add_u32 s85, s85, 0x1000\n"
"s_and_b32 s85, s85, 0x1ffff\n"
"buffer_load_dwordx4 v242, s[52:55], s85 offen nt lds\n"
"v_exp_f32 v174, v174\n"
"v_mfma_f32_16x16x32_bf16 v[164:167], a[200:203], v[176:179], v[164:167]\n"
"ds_read_b64_tr_b16 a[228:229], v227 offset:49152\n"
"v_add_f32 v220, v220, v142\n"
"v_mfma_f32_16x16x32_bf16 v[164:167], a[196:199], v[168:171], v[164:167]\n"
"s_waitcnt lgkmcnt(3)\n"
"v_exp_f32 v175, v175\n"
"v_mfma_f32_16x16x32_bf16 v[164:167], a[192:195], v[160:163], v[164:167]\n"
"ds_read_b64_tr_b16 a[230:231], v227 offset:53248\n"
"v_add_f32 v220, v220, v143\n"
"v_cvt_pk_bf16_f32 v159, v142, v143\n"
"v_mfma_f32_16x16x32_bf16 v[12:15], a[208:211], v[0:3], v[192:195]\n"
"v_exp_f32 v4, v4\n"
"v_mfma_f32_16x16x32_bf16 v[12:15], a[212:215], v[8:11], v[12:15]\n"
"ds_read_b64_tr_b16 a[232:233], v228 offset:49152\n"
"v_add_f32 v221, v221, v172\n"
"v_mfma_f32_16x16x32_bf16 v[12:15], a[216:219], v[16:19], v[12:15]\n"
"v_exp_f32 v5, v5\n"
"v_mfma_f32_16x16x32_bf16 v[12:15], a[220:223], v[24:27], v[12:15]\n"
"ds_read_b64_tr_b16 a[234:235], v228 offset:53248\n"
"v_add_f32 v221, v221, v173\n"
"v_cvt_pk_bf16_f32 v190, v172, v173\n"
"v_mfma_f32_16x16x32_bf16 v[44:47], a[220:223], v[56:59], v[196:199]\n"
"v_exp_f32 v6, v6\n"
"v_mfma_f32_16x16x32_bf16 v[44:47], a[216:219], v[48:51], v[44:47]\n"
"ds_read_b64_tr_b16 a[236:237], v229 offset:49152\n"
"v_add_f32 v221, v221, v174\n"
"v_mfma_f32_16x16x32_bf16 v[44:47], a[212:215], v[40:43], v[44:47]\n"
"buffer_load_dwordx4 v242, s[52:55], s85 offen offset:2048 nt lds\n"
"v_exp_f32 v7, v7\n"
"v_mfma_f32_16x16x32_bf16 v[44:47], a[208:211], v[32:35], v[44:47]\n"
"ds_read_b64_tr_b16 a[238:239], v229 offset:53248\n"
"v_add_f32 v221, v221, v175\n"
"v_cvt_pk_bf16_f32 v191, v174, v175\n"
"v_mfma_f32_16x16x32_bf16 v[76:79], a[208:211], v[64:67], v[200:203]\n"
"v_exp_f32 v36, v36\n"
"v_mfma_f32_16x16x32_bf16 v[76:79], a[212:215], v[72:75], v[76:79]\n"
"ds_read_b64_tr_b16 a[240:241], v230 offset:49152\n"
"v_add_f32 v216, v216, v4\n"
"v_mfma_f32_16x16x32_bf16 v[76:79], a[216:219], v[80:83], v[76:79]\n"
"v_exp_f32 v37, v37\n"
"v_mfma_f32_16x16x32_bf16 v[76:79], a[220:223], v[88:91], v[76:79]\n"
"ds_read_b64_tr_b16 a[242:243], v230 offset:53248\n"
"v_add_f32 v216, v216, v5\n"
"v_cvt_pk_bf16_f32 v20, v4, v5\n"
"v_mfma_f32_16x16x32_bf16 v[108:111], a[220:223], v[120:123], v[204:207]\n"
"v_exp_f32 v38, v38\n"
"v_mfma_f32_16x16x32_bf16 v[108:111], a[216:219], v[112:115], v[108:111]\n"
"ds_read_b64_tr_b16 a[244:245], v231 offset:49152\n"
"v_add_f32 v216, v216, v6\n"
"v_mfma_f32_16x16x32_bf16 v[108:111], a[212:215], v[104:107], v[108:111]\n"
"v_exp_f32 v39, v39\n"
"v_mfma_f32_16x16x32_bf16 v[108:111], a[208:211], v[96:99], v[108:111]\n"
"ds_read_b64_tr_b16 a[246:247], v231 offset:53248\n"
"v_add_f32 v216, v216, v7\n"
"v_cvt_pk_bf16_f32 v21, v6, v7\n"
"v_mfma_f32_16x16x32_bf16 v[140:143], a[208:211], v[128:131], v[208:211]\n"
"v_exp_f32 v68, v68\n"
"v_mfma_f32_16x16x32_bf16 v[140:143], a[212:215], v[136:139], v[140:143]\n"
"s_add_u32 m0, s63, 0x5000\n"
"ds_read_b64_tr_b16 a[248:249], v232 offset:49152\n"
"v_add_f32 v217, v217, v36\n"
"v_mfma_f32_16x16x32_bf16 v[140:143], a[216:219], v[144:147], v[140:143]\n"
"s_add_u32 s85, s85, 0x1000\n"
"buffer_load_dwordx4 v242, s[52:55], s85 offen nt lds\n"
"v_exp_f32 v69, v69\n"
"v_mfma_f32_16x16x32_bf16 v[140:143], a[220:223], v[152:155], v[140:143]\n"
"ds_read_b64_tr_b16 a[250:251], v232 offset:53248\n"
"v_add_f32 v217, v217, v37\n"
"v_cvt_pk_bf16_f32 v52, v36, v37\n"
"v_mfma_f32_16x16x32_bf16 v[172:175], a[220:223], v[184:187], v[212:215]\n"
"v_exp_f32 v70, v70\n"
"v_mfma_f32_16x16x32_bf16 v[172:175], a[216:219], v[176:179], v[172:175]\n"
"ds_read_b64_tr_b16 a[252:253], v233 offset:49152\n"
"v_add_f32 v217, v217, v38\n"
"v_mfma_f32_16x16x32_bf16 v[172:175], a[212:215], v[168:171], v[172:175]\n"
"s_waitcnt lgkmcnt(3)\n"
"v_exp_f32 v71, v71\n"
"v_mfma_f32_16x16x32_bf16 v[172:175], a[208:211], v[160:163], v[172:175]\n"
"ds_read_b64_tr_b16 a[254:255], v233 offset:53248\n"
"v_add_f32 v217, v217, v39\n"
"v_cvt_pk_bf16_f32 v53, v38, v39\n"
"v_mfma_f32_16x16x32_bf16 a[0:3], a[224:227], v[28:31], a[0:3]\n"
"v_exp_f32 v100, v100\n"
"v_mfma_f32_16x16x32_bf16 a[4:7], a[224:227], v[60:63], a[4:7]\n"
"v_add_f32 v218, v218, v68\n"
"v_add_f32 v218, v218, v69\n"
"v_mfma_f32_16x16x32_bf16 a[8:11], a[224:227], v[92:95], a[8:11]\n"
"v_exp_f32 v101, v101\n"
"v_mfma_f32_16x16x32_bf16 a[12:15], a[224:227], v[124:127], a[12:15]\n"
"v_cvt_pk_bf16_f32 v84, v68, v69\n"
"v_add_f32 v218, v218, v70\n"
"v_mfma_f32_16x16x32_bf16 a[16:19], a[224:227], v[156:159], a[16:19]\n"
"s_memrealtime s[88:89]\n"
"v_exp_f32 v102, v102\n"
"v_mfma_f32_16x16x32_bf16 a[20:23], a[224:227], v[188:191], a[20:23]\n"
"v_add_f32 v218, v218, v71\n"
"v_cvt_pk_bf16_f32 v85, v70, v71\n"
"v_mfma_f32_16x16x32_bf16 a[44:47], a[228:231], v[188:191], a[44:47]\n"
"v_exp_f32 v103, v103\n"
"v_mfma_f32_16x16x32_bf16 a[40:43], a[228:231], v[156:159], a[40:43]\n"
"v_add_f32 v219, v219, v100\n"
"v_add_f32 v219, v219, v101\n"
"v_mfma_f32_16x16x32_bf16 a[36:39], a[228:231], v[124:127], a[36:39]\n"
"LP11_%=:\n"
"s_waitcnt lgkmcnt(0)\n"
"s_sub_u32 s89, s88, s86\n"
"s_cmp_ge_i32 s89, 0\n"
"s_cbranch_scc1 LQ11_%=\n"
"s_memrealtime s[88:89]\n"
"s_branch LP11_%=\n"
"LQ11_%=:\n"
"s_waitcnt vmcnt(11) lgkmcnt(0)\n"
"s_barrier\n"
"v_exp_f32 v132, v132\n"
"v_mfma_f32_16x16x32_bf16 a[32:35], a[228:231], v[92:95], a[32:35]\n"
"ds_read_b64 a[192:193], v222 offset:16384\n"
"v_cvt_pk_bf16_f32 v116, v100, v101\n"
"v_mfma_f32_16x16x32_bf16 a[28:31], a[228:231], v[60:63], a[28:31]\n"
"s_add_u32 s86, s86, 124\n"
"v_exp_f32 v133, v133\n"
"v_mfma_f32_16x16x32_bf16 a[24:27], a[228:231], v[28:31], a[24:27]\n"
"v_add_f32 v219, v219, v102\n"
"v_add_f32 v219, v219, v103\n"
"v_mfma_f32_16x16x32_bf16 a[48:51], a[232:235], v[28:31], a[48:51]\n"
"buffer_load_dwordx4 v242, s[52:55], s85 offen offset:2048 nt lds\n"
"v_exp_f32 v134, v134\n"
"v_mfma_f32_16x16x32_bf16 a[52:55], a[232:235], v[60:63], a[52:55]\n"
"ds_read_b64 a[194:195], v223 offset:16384\n"
"v_cvt_pk_bf16_f32 v117, v102, v103\n"
"v_mfma_f32_16x16x32_bf16 a[56:59], a[232:235], v[92:95], a[56:59]\n"
"v_exp_f32 v135, v135\n"
"v_mfma_f32_16x16x32_bf16 a[60:63], a[232:235], v[124:127], a[60:63]\n"
"v_add_f32 v220, v220, v132\n"
"v_add_f32 v220, v220, v133\n"
"v_mfma_f32_16x16x32_bf16 a[64:67], a[232:235], v[156:159], a[64:67]\n"
"v_exp_f32 v164, v164\n"
"v_mfma_f32_16x16x32_bf16 a[68:71], a[232:235], v[188:191], a[68:71]\n"
"ds_read_b64 a[196:197], v224 offset:16384\n"
"v_cvt_pk_bf16_f32 v148, v132, v133\n"
"v_mfma_f32_16x16x32_bf16 a[92:95], a[236:239], v[188:191], a[92:95]\n"
"s_add_u32 s90, s87, s41\n"
"v_exp_f32 v165, v165\n"
"v_mfma_f32_16x16x32_bf16 a[88:91], a[236:239], v[156:159], a[88:91]\n"
"s_add_u32 m0, s91, 0x24000\n"
"v_add_f32 v220, v220, v134\n"
"v_add_f32 v220, v220, v135\n"
"v_mfma_f32_16x16x32_bf16 a[84:87], a[236:239], v[124:127], a[84:87]\n"
"buffer_load_dwordx4 v245, s[92:95], s90 offen lds\n"
"v_exp_f32 v166, v166\n"
"v_mfma_f32_16x16x32_bf16 a[80:83], a[236:239], v[92:95], a[80:83]\n"
"ds_read_b64 a[198:199], v225 offset:16384\n"
"v_cvt_pk_bf16_f32 v149, v134, v135\n"
"v_mfma_f32_16x16x32_bf16 a[76:79], a[236:239], v[60:63], a[76:79]\n"
"v_exp_f32 v167, v167\n"
"v_mfma_f32_16x16x32_bf16 a[72:75], a[236:239], v[28:31], a[72:75]\n"
"v_add_f32 v221, v221, v164\n"
"v_add_f32 v221, v221, v165\n"
"v_mfma_f32_16x16x32_bf16 a[96:99], a[240:243], v[28:31], a[96:99]\n"
"v_exp_f32 v12, v12\n"
"v_mfma_f32_16x16x32_bf16 a[100:103], a[240:243], v[60:63], a[100:103]\n"
"ds_read_b64 a[200:201], v246 offset:16384\n"
"v_cvt_pk_bf16_f32 v180, v164, v165\n"
"v_mfma_f32_16x16x32_bf16 a[104:107], a[240:243], v[92:95], a[104:107]\n"
"buffer_load_dwordx4 v251, s[92:95], s90 offen offset:1024 lds\n"
"v_exp_f32 v13, v13\n"
"v_mfma_f32_16x16x32_bf16 a[108:111], a[240:243], v[124:127], a[108:111]\n"
"v_add_f32 v221, v221, v166\n"
"v_add_f32 v221, v221, v167\n"
"v_mfma_f32_16x16x32_bf16 a[112:115], a[240:243], v[156:159], a[112:115]\n"
"v_exp_f32 v14, v14\n"
"v_mfma_f32_16x16x32_bf16 a[116:119], a[240:243], v[188:191], a[116:119]\n"
"ds_read_b64 a[202:203], v247 offset:16384\n"
"v_cvt_pk_bf16_f32 v181, v166, v167\n"
"v_mfma_f32_16x16x32_bf16 a[140:143], a[244:247], v[188:191], a[140:143]\n"
"v_exp_f32 v15, v15\n"
"v_mfma_f32_16x16x32_bf16 a[136:139], a[244:247], v[156:159], a[136:139]\n"
"v_add_f32 v216, v216, v12\n"
"v_add_f32 v216, v216, v13\n"
"v_mfma_f32_16x16x32_bf16 a[132:135], a[244:247], v[124:127], a[132:135]\n"
"buffer_load_dwordx4 v252, s[92:95], s90 offen offset:2048 lds\n"
"v_exp_f32 v44, v44\n"
"v_mfma_f32_16x16x32_bf16 a[128:131], a[244:247], v[92:95], a[128:131]\n"
"ds_read_b64 a[204:205], v248 offset:16384\n"
"v_cvt_pk_bf16_f32 v22, v12, v13\n"
"v_mfma_f32_16x16x32_bf16 a[124:127], a[244:247], v[60:63], a[124:127]\n"
"v_exp_f32 v45, v45\n"
"v_mfma_f32_16x16x32_bf16 a[120:123], a[244:247], v[28:31], a[120:123]\n"
"v_add_f32 v216, v216, v14\n"
"v_add_f32 v216, v216, v15\n"
"v_mfma_f32_16x16x32_bf16 a[144:147], a[248:251], v[28:31], a[144:147]\n"
"v_exp_f32 v46, v46\n"
"v_mfma_f32_16x16x32_bf16 a[148:151], a[248:251], v[60:63], a[148:151]\n"
"ds_read_b64 a[206:207], v249 offset:16384\n"
"v_cvt_pk_bf16_f32 v23, v14, v15\n"
"v_mfma_f32_16x16x32_bf16 a[152:155], a[248:251], v[92:95], a[152:155]\n"
"buffer_load_dwordx4 v253, s[92:95], s90 offen offset:3072 lds\n"
"v_exp_f32 v47, v47\n"
"v_mfma_f32_16x16x32_bf16 a[156:159], a[248:251], v[124:127], a[156:159]\n"
"v_add_f32 v217, v217, v44\n"
"v_add_f32 v217, v217, v45\n"
"v_mfma_f32_16x16x32_bf16 a[160:163], a[248:251], v[156:159], a[160:163]\n"
"v_exp_f32 v76, v76\n"
"v_mfma_f32_16x16x32_bf16 a[164:167], a[248:251], v[188:191], a[164:167]\n"
"v_cvt_pk_bf16_f32 v54, v44, v45\n"
"v_add_f32 v217, v217, v46\n"
"v_mfma_f32_16x16x32_bf16 a[188:191], a[252:255], v[188:191], a[188:191]\n"
"v_exp_f32 v77, v77\n"
"v_mfma_f32_16x16x32_bf16 a[184:187], a[252:255], v[156:159], a[184:187]\n"
"s_add_u32 m0, s62, 0xc000\n"
"v_add_f32 v217, v217, v47\n"
"v_cvt_pk_bf16_f32 v55, v46, v47\n"
"v_mfma_f32_16x16x32_bf16 a[180:183], a[252:255], v[124:127], a[180:183]\n"
"s_add_u32 s84, s84, 0x1000\n"
"s_and_b32 s84, s84, 0x1ffff\n"
"buffer_load_dwordx4 v240, s[48:51], s84 offen nt lds\n"
"v_exp_f32 v78, v78\n"
"v_mfma_f32_16x16x32_bf16 a[176:179], a[252:255], v[92:95], a[176:179]\n"
"v_add_f32 v218, v218, v76\n"
"v_add_f32 v218, v218, v77\n"
"v_mfma_f32_16x16x32_bf16 a[172:175], a[252:255], v[60:63], a[172:175]\n"
"s_waitcnt lgkmcnt(0)\n"
"v_exp_f32 v79, v79\n"
"v_mfma_f32_16x16x32_bf16 a[168:171], a[252:255], v[28:31], a[168:171]\n"
"v_cvt_pk_bf16_f32 v86, v76, v77\n"
"v_add_f32 v218, v218, v78\n"
"v_mfma_f32_16x16x32_bf16 v[4:7], a[192:195], v[0:3], v[192:195]\n"
"v_exp_f32 v108, v108\n"
"v_mfma_f32_16x16x32_bf16 v[4:7], a[196:199], v[8:11], v[4:7]\n"
"ds_read_b64 a[208:209], v222 offset:20480\n"
"v_add_f32 v218, v218, v79\n"
"v_mfma_f32_16x16x32_bf16 v[4:7], a[200:203], v[16:19], v[4:7]\n"
"buffer_load_dwordx4 v241, s[48:51], s84 offen offset:1024 nt lds\n"
"v_exp_f32 v109, v109\n"
"v_mfma_f32_16x16x32_bf16 v[4:7], a[204:207], v[24:27], v[4:7]\n"
"ds_read_b64 a[210:211], v223 offset:20480\n"
"v_cvt_pk_bf16_f32 v87, v78, v79\n"
"v_mfma_f32_16x16x32_bf16 v[36:39], a[204:207], v[56:59], v[196:199]\n"
"v_exp_f32 v110, v110\n"
"v_mfma_f32_16x16x32_bf16 v[36:39], a[200:203], v[48:51], v[36:39]\n"
"ds_read_b64 a[212:213], v224 offset:20480\n"
"v_add_f32 v219, v219, v108\n"
"v_mfma_f32_16x16x32_bf16 v[36:39], a[196:199], v[40:43], v[36:39]\n"
"v_exp_f32 v111, v111\n"
"v_mfma_f32_16x16x32_bf16 v[36:39], a[192:195], v[32:35], v[36:39]\n"
"s_add_u32 m0, s62, 0xd000\n"
"ds_read_b64 a[214:215], v225 offset:20480\n"
"v_add_f32 v219, v219, v109\n"
"v_mfma_f32_16x16x32_bf16 v[68:71], a[192:195], v[64:67], v[200:203]\n"
"s_add_u32 s84, s84, 0x1000\n"
"buffer_load_dwordx4 v240, s[48:51], s84 offen nt lds\n"
"v_exp_f32 v140, v140\n"
"v_mfma_f32_16x16x32_bf16 v[68:71], a[196:199], v[72:75], v[68:71]\n"
"ds_read_b64 a[216:217], v246 offset:20480\n"
"v_cvt_pk_bf16_f32 v118, v108, v109\n"
"v_mfma_f32_16x16x32_bf16 v[68:71], a[200:203], v[80:83], v[68:71]\n"
"v_exp_f32 v141, v141\n"
"v_mfma_f32_16x16x32_bf16 v[68:71], a[204:207], v[88:91], v[68:71]\n"
"ds_read_b64 a[218:219], v247 offset:20480\n"
"v_add_f32 v219, v219, v110\n"
"v_mfma_f32_16x16x32_bf16 v[100:103], a[204:207], v[120:123], v[204:207]\n"
"v_exp_f32 v142, v142\n"
"v_mfma_f32_16x16x32_bf16 v[100:103], a[200:203], v[112:115], v[100:103]\n"
"ds_read_b64 a[220:221], v248 offset:20480\n"
"v_add_f32 v219, v219, v111\n"
"v_mfma_f32_16x16x32_bf16 v[100:103], a[196:199], v[104:107], v[100:103]\n"
"buffer_load_dwordx4 v241, s[48:51], s84 offen offset:1024 nt lds\n"
"v_exp_f32 v143, v143\n"
"v_mfma_f32_16x16x32_bf16 v[100:103], a[192:195], v[96:99], v[100:103]\n"
"ds_read_b64 a[222:223], v249 offset:20480\n"
"v_cvt_pk_bf16_f32 v119, v110, v111\n"
"v_mfma_f32_16x16x32_bf16 v[132:135], a[192:195], v[128:131], v[208:211]\n"
"v_exp_f32 v172, v172\n"
"v_mfma_f32_16x16x32_bf16 v[132:135], a[196:199], v[136:139], v[132:135]\n"
"ds_read_b64_tr_b16 a[224:225], v226 offset:0\n"
"v_add_f32 v220, v220, v140\n"
"v_mfma_f32_16x16x32_bf16 v[132:135], a[200:203], v[144:147], v[132:135]\n"
"v_exp_f32 v173, v173\n"
"v_mfma_f32_16x16x32_bf16 v[132:135], a[204:207], v[152:155], v[132:135]\n"
"ds_read_b64_tr_b16 a[226:227], v226 offset:4096\n"
"v_add_f32 v220, v220, v141\n"
"v_cvt_pk_bf16_f32 v150, v140, v141\n"
"v_mfma_f32_16x16x32_bf16 v[164:167], a[204:207], v[184:187], v[212:215]\n"
"v_exp_f32 v174, v174\n"
"v_mfma_f32_16x16x32_bf16 v[164:167], a[200:203], v[176:179], v[164:167]\n"
"ds_read_b64_tr_b16 a[228:229], v227 offset:0\n"
"v_add_f32 v220, v220, v142\n"
"v_mfma_f32_16x16x32_bf16 v[164:167], a[196:199], v[168:171], v[164:167]\n"
"s_waitcnt lgkmcnt(3)\n"
"v_exp_f32 v175, v175\n"
"v_mfma_f32_16x16x32_bf16 v[164:167], a[192:195], v[160:163], v[164:167]\n"
"ds_read_b64_tr_b16 a[230:231], v227 offset:4096\n"
"v_add_f32 v220, v220, v143\n"
"v_cvt_pk_bf16_f32 v151, v142, v143\n"
"v_mfma_f32_16x16x32_bf16 v[12:15], a[208:211], v[0:3], v[192:195]\n"
"v_exp_f32 v4, v4\n"
"v_mfma_f32_16x16x32_bf16 v[12:15], a[212:215], v[8:11], v[12:15]\n"
"ds_read_b64_tr_b16 a[232:233], v228 offset:0\n"
"v_add_f32 v221, v221, v172\n"
"v_mfma_f32_16x16x32_bf16 v[12:15], a[216:219], v[16:19], v[12:15]\n"
"v_exp_f32 v5, v5\n"
"v_mfma_f32_16x16x32_bf16 v[12:15], a[220:223], v[24:27], v[12:15]\n"
"s_add_u32 m0, s63, 0x8000\n"
"ds_read_b64_tr_b16 a[234:235], v228 offset:4096\n"
"v_add_f32 v221, v221, v173\n"
"v_cvt_pk_bf16_f32 v182, v172, v173\n"
"v_mfma_f32_16x16x32_bf16 v[44:47], a[220:223], v[56:59], v[196:199]\n"
"s_add_u32 s85, s85, 0x1000\n"
"s_and_b32 s85, s85, 0x1ffff\n"
"buffer_load_dwordx4 v242, s[52:55], s85 offen nt lds\n"
"v_exp_f32 v6, v6\n"
"v_mfma_f32_16x16x32_bf16 v[44:47], a[216:219], v[48:51], v[44:47]\n"
"ds_read_b64_tr_b16 a[236:237], v229 offset:0\n"
"v_add_f32 v221, v221, v174\n"
"v_mfma_f32_16x16x32_bf16 v[44:47], a[212:215], v[40:43], v[44:47]\n"
"v_exp_f32 v7, v7\n"
"v_mfma_f32_16x16x32_bf16 v[44:47], a[208:211], v[32:35], v[44:47]\n"
"ds_read_b64_tr_b16 a[238:239], v229 offset:4096\n"
"v_add_f32 v221, v221, v175\n"
"v_cvt_pk_bf16_f32 v183, v174, v175\n"
"v_mfma_f32_16x16x32_bf16 v[76:79], a[208:211], v[64:67], v[200:203]\n"
"v_exp_f32 v36, v36\n"
"v_mfma_f32_16x16x32_bf16 v[76:79], a[212:215], v[72:75], v[76:79]\n"
"ds_read_b64_tr_b16 a[240:241], v230 offset:0\n"
"v_add_f32 v216, v216, v4\n"
"v_mfma_f32_16x16x32_bf16 v[76:79], a[216:219], v[80:83], v[76:79]\n"
"v_exp_f32 v37, v37\n"
"v_mfma_f32_16x16x32_bf16 v[76:79], a[220:223], v[88:91], v[76:79]\n"
"ds_read_b64_tr_b16 a[242:243], v230 offset:4096\n"
"v_add_f32 v216, v216, v5\n"
"v_cvt_pk_bf16_f32 v28, v4, v5\n"
"v_mfma_f32_16x16x32_bf16 v[108:111], a[220:223], v[120:123], v[204:207]\n"
"v_exp_f32 v38, v38\n"
"v_mfma_f32_16x16x32_bf16 v[108:111], a[216:219], v[112:115], v[108:111]\n"
"ds_read_b64_tr_b16 a[244:245], v231 offset:0\n"
"v_add_f32 v216, v216, v6\n"
"v_mfma_f32_16x16x32_bf16 v[108:111], a[212:215], v[104:107], v[108:111]\n"
"v_exp_f32 v39, v39\n"
"v_mfma_f32_16x16x32_bf16 v[108:111], a[208:211], v[96:99], v[108:111]\n"
"ds_read_b64_tr_b16 a[246:247], v231 offset:4096\n"
"v_add_f32 v216, v216, v7\n"
"v_cvt_pk_bf16_f32 v29, v6, v7\n"
"v_mfma_f32_16x16x32_bf16 v[140:143], a[208:211], v[128:131], v[208:211]\n"
"buffer_load_dwordx4 v242, s[52:55], s85 offen offset:2048 nt lds\n"
"v_exp_f32 v68, v68\n"
"v_mfma_f32_16x16x32_bf16 v[140:143], a[212:215], v[136:139], v[140:143]\n"
"ds_read_b64_tr_b16 a[248:249], v232 offset:0\n"
"v_add_f32 v217, v217, v36\n"
"v_mfma_f32_16x16x32_bf16 v[140:143], a[216:219], v[144:147], v[140:143]\n"
"v_exp_f32 v69, v69\n"
"v_mfma_f32_16x16x32_bf16 v[140:143], a[220:223], v[152:155], v[140:143]\n"
"ds_read_b64_tr_b16 a[250:251], v232 offset:4096\n"
"v_add_f32 v217, v217, v37\n"
"v_cvt_pk_bf16_f32 v60, v36, v37\n"
"v_mfma_f32_16x16x32_bf16 v[172:175], a[220:223], v[184:187], v[212:215]\n"
"v_exp_f32 v70, v70\n"
"v_mfma_f32_16x16x32_bf16 v[172:175], a[216:219], v[176:179], v[172:175]\n"
"ds_read_b64_tr_b16 a[252:253], v233 offset:0\n"
"v_add_f32 v217, v217, v38\n"
"v_mfma_f32_16x16x32_bf16 v[172:175], a[212:215], v[168:171], v[172:175]\n"
"s_waitcnt lgkmcnt(3)\n"
"v_exp_f32 v71, v71\n"
"v_mfma_f32_16x16x32_bf16 v[172:175], a[208:211], v[160:163], v[172:175]\n"
"ds_read_b64_tr_b16 a[254:255], v233 offset:4096\n"
"v_add_f32 v217, v217, v39\n"
"v_cvt_pk_bf16_f32 v61, v38, v39\n"
"v_mfma_f32_16x16x32_bf16 a[0:3], a[224:227], v[20:23], a[0:3]\n"
"v_exp_f32 v100, v100\n"
"v_mfma_f32_16x16x32_bf16 a[4:7], a[224:227], v[52:55], a[4:7]\n"
"v_add_f32 v218, v218, v68\n"
"v_add_f32 v218, v218, v69\n"
"v_mfma_f32_16x16x32_bf16 a[8:11], a[224:227], v[84:87], a[8:11]\n"
"v_exp_f32 v101, v101\n"
"v_mfma_f32_16x16x32_bf16 a[12:15], a[224:227], v[116:119], a[12:15]\n"
"v_cvt_pk_bf16_f32 v92, v68, v69\n"
"v_add_f32 v218, v218, v70\n"
"v_mfma_f32_16x16x32_bf16 a[16:19], a[224:227], v[148:151], a[16:19]\n"
"s_memrealtime s[88:89]\n"
"v_exp_f32 v102, v102\n"
"v_mfma_f32_16x16x32_bf16 a[20:23], a[224:227], v[180:183], a[20:23]\n"
"v_add_f32 v218, v218, v71\n"
"v_cvt_pk_bf16_f32 v93, v70, v71\n"
"v_mfma_f32_16x16x32_bf16 a[44:47], a[228:231], v[180:183], a[44:47]\n"
"v_exp_f32 v103, v103\n"
"v_mfma_f32_16x16x32_bf16 a[40:43], a[228:231], v[148:151], a[40:43]\n"
"v_add_f32 v219, v219, v100\n"
"v_add_f32 v219, v219, v101\n"
"v_mfma_f32_16x16x32_bf16 a[36:39], a[228:231], v[116:119], a[36:39]\n"
"LP12_%=:\n"
"s_waitcnt lgkmcnt(0)\n"
"s_sub_u32 s89, s88, s86\n"
"s_cmp_ge_i32 s89, 0\n"
"s_cbranch_scc1 LQ12_%=\n"
"s_memrealtime s[88:89]\n"
"s_branch LP12_%=\n"
"LQ12_%=:\n"
"s_waitcnt vmcnt(10) lgkmcnt(0)\n"
"s_barrier\n"
"v_exp_f32 v132, v132\n"
"v_mfma_f32_16x16x32_bf16 a[32:35], a[228:231], v[84:87], a[32:35]\n"
"ds_read_b64 a[192:193], v222 offset:32768\n"
"v_cvt_pk_bf16_f32 v124, v100, v101\n"
"v_mfma_f32_16x16x32_bf16 a[28:31], a[228:231], v[52:55], a[28:31]\n"
"s_add_u32 s86, s86, 124\n"
"v_exp_f32 v133, v133\n"
"v_mfma_f32_16x16x32_bf16 a[24:27], a[228:231], v[20:23], a[24:27]\n"
"s_add_u32 m0, s63, 0x9000\n"
"v_add_f32 v219, v219, v102\n"
"v_add_f32 v219, v219, v103\n"
"v_mfma_f32_16x16x32_bf16 a[48:51], a[232:235], v[20:23], a[48:51]\n"
"s_add_u32 s85, s85, 0x1000\n"
"buffer_load_dwordx4 v242, s[52:55], s85 offen nt lds\n"
"v_exp_f32 v134, v134\n"
"v_mfma_f32_16x16x32_bf16 a[52:55], a[232:235], v[52:55], a[52:55]\n"
"ds_read_b64 a[194:195], v223 offset:32768\n"
"v_cvt_pk_bf16_f32 v125, v102, v103\n"
"v_mfma_f32_16x16x32_bf16 a[56:59], a[232:235], v[84:87], a[56:59]\n"
"v_exp_f32 v135, v135\n"
"v_mfma_f32_16x16x32_bf16 a[60:63], a[232:235], v[116:119], a[60:63]\n"
"v_add_f32 v220, v220, v132\n"
"v_add_f32 v220, v220, v133\n"
"v_mfma_f32_16x16x32_bf16 a[64:67], a[232:235], v[148:151], a[64:67]\n"
"v_exp_f32 v164, v164\n"
"v_mfma_f32_16x16x32_bf16 a[68:71], a[232:235], v[180:183], a[68:71]\n"
"ds_read_b64 a[196:197], v224 offset:32768\n"
"v_cvt_pk_bf16_f32 v156, v132, v133\n"
"v_mfma_f32_16x16x32_bf16 a[92:95], a[236:239], v[180:183], a[92:95]\n"
"v_exp_f32 v165, v165\n"
"v_mfma_f32_16x16x32_bf16 a[88:91], a[236:239], v[148:151], a[88:91]\n"
"v_add_f32 v220, v220, v134\n"
"v_add_f32 v220, v220, v135\n"
"v_mfma_f32_16x16x32_bf16 a[84:87], a[236:239], v[116:119], a[84:87]\n"
"buffer_load_dwordx4 v242, s[52:55], s85 offen offset:2048 nt lds\n"
"v_exp_f32 v166, v166\n"
"v_mfma_f32_16x16x32_bf16 a[80:83], a[236:239], v[84:87], a[80:83]\n"
"ds_read_b64 a[198:199], v225 offset:32768\n"
"v_cvt_pk_bf16_f32 v157, v134, v135\n"
"v_mfma_f32_16x16x32_bf16 a[76:79], a[236:239], v[52:55], a[76:79]\n"
"v_exp_f32 v167, v167\n"
"v_mfma_f32_16x16x32_bf16 a[72:75], a[236:239], v[20:23], a[72:75]\n"
"v_add_f32 v221, v221, v164\n"
"v_add_f32 v221, v221, v165\n"
"v_mfma_f32_16x16x32_bf16 a[96:99], a[240:243], v[20:23], a[96:99]\n"
"s_add_u32 s90, s87, s42\n"
"v_exp_f32 v12, v12\n"
"v_mfma_f32_16x16x32_bf16 a[100:103], a[240:243], v[52:55], a[100:103]\n"
"s_add_u32 m0, s91, 0x0\n"
"ds_read_b64 a[200:201], v246 offset:32768\n"
"v_cvt_pk_bf16_f32 v188, v164, v165\n"
"v_mfma_f32_16x16x32_bf16 a[104:107], a[240:243], v[84:87], a[104:107]\n"
"buffer_load_dwordx4 v245, s[92:95], s90 offen lds\n"
"v_exp_f32 v13, v13\n"
"v_mfma_f32_16x16x32_bf16 a[108:111], a[240:243], v[116:119], a[108:111]\n"
"v_add_f32 v221, v221, v166\n"
"v_add_f32 v221, v221, v167\n"
"v_mfma_f32_16x16x32_bf16 a[112:115], a[240:243], v[148:151], a[112:115]\n"
"v_exp_f32 v14, v14\n"
"v_mfma_f32_16x16x32_bf16 a[116:119], a[240:243], v[180:183], a[116:119]\n"
"ds_read_b64 a[202:203], v247 offset:32768\n"
"v_cvt_pk_bf16_f32 v189, v166, v167\n"
"v_mfma_f32_16x16x32_bf16 a[140:143], a[244:247], v[180:183], a[140:143]\n"
"v_exp_f32 v15, v15\n"
"v_mfma_f32_16x16x32_bf16 a[136:139], a[244:247], v[148:151], a[136:139]\n"
"v_add_f32 v216, v216, v12\n"
"v_add_f32 v216, v216, v13\n"
"v_mfma_f32_16x16x32_bf16 a[132:135], a[244:247], v[116:119], a[132:135]\n"
"buffer_load_dwordx4 v251, s[92:95], s90 offen offset:1024 lds\n"
"v_exp_f32 v44, v44\n"
"v_mfma_f32_16x16x32_bf16 a[128:131], a[244:247], v[84:87], a[128:131]\n"
"ds_read_b64 a[204:205], v248 offset:32768\n"
"v_cvt_pk_bf16_f32 v30, v12, v13\n"
"v_mfma_f32_16x16x32_bf16 a[124:127], a[244:247], v[52:55], a[124:127]\n"
"v_exp_f32 v45, v45\n"
"v_mfma_f32_16x16x32_bf16 a[120:123], a[244:247], v[20:23], a[120:123]\n"
"v_add_f32 v216, v216, v14\n"
"v_add_f32 v216, v216, v15\n"
"v_mfma_f32_16x16x32_bf16 a[144:147], a[248:251], v[20:23], a[144:147]\n"
"v_exp_f32 v46, v46\n"
"v_mfma_f32_16x16x32_bf16 a[148:151], a[248:251], v[52:55], a[148:151]\n"
"ds_read_b64 a[206:207], v249 offset:32768\n"
"v_cvt_pk_bf16_f32 v31, v14, v15\n"
"v_mfma_f32_16x16x32_bf16 a[152:155], a[248:251], v[84:87], a[152:155]\n"
"buffer_load_dwordx4 v252, s[92:95], s90 offen offset:2048 lds\n"
"v_exp_f32 v47, v47\n"
"v_mfma_f32_16x16x32_bf16 a[156:159], a[248:251], v[116:119], a[156:159]\n"
"v_add_f32 v217, v217, v44\n"
"v_add_f32 v217, v217, v45\n"
"v_mfma_f32_16x16x32_bf16 a[160:163], a[248:251], v[148:151], a[160:163]\n"
"v_exp_f32 v76, v76\n"
"v_mfma_f32_16x16x32_bf16 a[164:167], a[248:251], v[180:183], a[164:167]\n"
"v_cvt_pk_bf16_f32 v62, v44, v45\n"
"v_add_f32 v217, v217, v46\n"
"v_mfma_f32_16x16x32_bf16 a[188:191], a[252:255], v[180:183], a[188:191]\n"
"v_exp_f32 v77, v77\n"
"v_mfma_f32_16x16x32_bf16 a[184:187], a[252:255], v[148:151], a[184:187]\n"
"v_add_f32 v217, v217, v47\n"
"v_cvt_pk_bf16_f32 v63, v46, v47\n"
"v_mfma_f32_16x16x32_bf16 a[180:183], a[252:255], v[116:119], a[180:183]\n"
"buffer_load_dwordx4 v253, s[92:95], s90 offen offset:3072 lds\n"
"v_exp_f32 v78, v78\n"
"v_mfma_f32_16x16x32_bf16 a[176:179], a[252:255], v[84:87], a[176:179]\n"
"v_add_f32 v218, v218, v76\n"
"v_add_f32 v218, v218, v77\n"
"v_mfma_f32_16x16x32_bf16 a[172:175], a[252:255], v[52:55], a[172:175]\n"
"s_waitcnt lgkmcnt(0)\n"
"v_exp_f32 v79, v79\n"
"v_mfma_f32_16x16x32_bf16 a[168:171], a[252:255], v[20:23], a[168:171]\n"
"v_cvt_pk_bf16_f32 v94, v76, v77\n"
"v_add_f32 v218, v218, v78\n"
"v_mfma_f32_16x16x32_bf16 v[4:7], a[192:195], v[0:3], v[192:195]\n"
"s_add_u32 s90, s87, s43\n"
"v_exp_f32 v108, v108\n"
"v_mfma_f32_16x16x32_bf16 v[4:7], a[196:199], v[8:11], v[4:7]\n"
"s_add_u32 m0, s91, 0x10000\n"
"ds_read_b64 a[208:209], v222 offset:36864\n"
"v_add_f32 v218, v218, v79\n"
"v_mfma_f32_16x16x32_bf16 v[4:7], a[200:203], v[16:19], v[4:7]\n"
"buffer_load_dwordx4 v245, s[92:95], s90 offen lds\n"
"v_exp_f32 v109, v109\n"
"v_mfma_f32_16x16x32_bf16 v[4:7], a[204:207], v[24:27], v[4:7]\n"
"ds_read_b64 a[210:211], v223 offset:36864\n"
"v_cvt_pk_bf16_f32 v95, v78, v79\n"
"v_mfma_f32_16x16x32_bf16 v[36:39], a[204:207], v[56:59], v[196:199]\n"
"v_exp_f32 v110, v110\n"
"v_mfma_f32_16x16x32_bf16 v[36:39], a[200:203], v[48:51], v[36:39]\n"
"ds_read_b64 a[212:213], v224 offset:36864\n"
"v_add_f32 v219, v219, v108\n"
"v_mfma_f32_16x16x32_bf16 v[36:39], a[196:199], v[40:43], v[36:39]\n"
"v_exp_f32 v111, v111\n"
"v_mfma_f32_16x16x32_bf16 v[36:39], a[192:195], v[32:35], v[36:39]\n"
"ds_read_b64 a[214:215], v225 offset:36864\n"
"v_add_f32 v219, v219, v109\n"
"v_mfma_f32_16x16x32_bf16 v[68:71], a[192:195], v[64:67], v[200:203]\n"
"buffer_load_dwordx4 v251, s[92:95], s90 offen offset:1024 lds\n"
"v_exp_f32 v140, v140\n"
"v_mfma_f32_16x16x32_bf16 v[68:71], a[196:199], v[72:75], v[68:71]\n"
"ds_read_b64 a[216:217], v246 offset:36864\n"
"v_cvt_pk_bf16_f32 v126, v108, v109\n"
"v_mfma_f32_16x16x32_bf16 v[68:71], a[200:203], v[80:83], v[68:71]\n"
"v_exp_f32 v141, v141\n"
"v_mfma_f32_16x16x32_bf16 v[68:71], a[204:207], v[88:91], v[68:71]\n"
"ds_read_b64 a[218:219], v247 offset:36864\n"
"v_add_f32 v219, v219, v110\n"
"v_mfma_f32_16x16x32_bf16 v[100:103], a[204:207], v[120:123], v[204:207]\n"
"v_exp_f32 v142, v142\n"
"v_mfma_f32_16x16x32_bf16 v[100:103], a[200:203], v[112:115], v[100:103]\n"
"ds_read_b64 a[220:221], v248 offset:36864\n"
"v_add_f32 v219, v219, v111\n"
"v_mfma_f32_16x16x32_bf16 v[100:103], a[196:199], v[104:107], v[100:103]\n"
"buffer_load_dwordx4 v252, s[92:95], s90 offen offset:2048 lds\n"
"v_exp_f32 v143, v143\n"
"v_mfma_f32_16x16x32_bf16 v[100:103], a[192:195], v[96:99], v[100:103]\n"
"ds_read_b64 a[222:223], v249 offset:36864\n"
"v_cvt_pk_bf16_f32 v127, v110, v111\n"
"v_mfma_f32_16x16x32_bf16 v[132:135], a[192:195], v[128:131], v[208:211]\n"
"v_exp_f32 v172, v172\n"
"v_mfma_f32_16x16x32_bf16 v[132:135], a[196:199], v[136:139], v[132:135]\n"
"ds_read_b64_tr_b16 a[224:225], v226 offset:16384\n"
"v_add_f32 v220, v220, v140\n"
"v_mfma_f32_16x16x32_bf16 v[132:135], a[200:203], v[144:147], v[132:135]\n"
"v_exp_f32 v173, v173\n"
"v_mfma_f32_16x16x32_bf16 v[132:135], a[204:207], v[152:155], v[132:135]\n"
"ds_read_b64_tr_b16 a[226:227], v226 offset:20480\n"
"v_add_f32 v220, v220, v141\n"
"v_cvt_pk_bf16_f32 v158, v140, v141\n"
"v_mfma_f32_16x16x32_bf16 v[164:167], a[204:207], v[184:187], v[212:215]\n"
"buffer_load_dwordx4 v253, s[92:95], s90 offen offset:3072 lds\n"
"v_exp_f32 v174, v174\n"
"v_mfma_f32_16x16x32_bf16 v[164:167], a[200:203], v[176:179], v[164:167]\n"
"ds_read_b64_tr_b16 a[228:229], v227 offset:16384\n"
"v_add_f32 v220, v220, v142\n"
"v_mfma_f32_16x16x32_bf16 v[164:167], a[196:199], v[168:171], v[164:167]\n"
"s_waitcnt lgkmcnt(3)\n"
"v_exp_f32 v175, v175\n"
"v_mfma_f32_16x16x32_bf16 v[164:167], a[192:195], v[160:163], v[164:167]\n"
"ds_read_b64_tr_b16 a[230:231], v227 offset:20480\n"
"v_add_f32 v220, v220, v143\n"
"v_cvt_pk_bf16_f32 v159, v142, v143\n"
"v_mfma_f32_16x16x32_bf16 v[12:15], a[208:211], v[0:3], v[192:195]\n"
"v_exp_f32 v4, v4\n"
"v_mfma_f32_16x16x32_bf16 v[12:15], a[212:215], v[8:11], v[12:15]\n"
"s_add_u32 m0, s63, 0xc000\n"
"ds_read_b64_tr_b16 a[232:233], v228 offset:16384\n"
"v_add_f32 v221, v221, v172\n"
"v_mfma_f32_16x16x32_bf16 v[12:15], a[216:219], v[16:19], v[12:15]\n"
"s_add_u32 s85, s85, 0x1000\n"
"s_and_b32 s85, s85, 0x1ffff\n"
"buffer_load_dwordx4 v242, s[52:55], s85 offen nt lds\n"
"v_exp_f32 v5, v5\n"
"v_mfma_f32_16x16x32_bf16 v[12:15], a[220:223], v[24:27], v[12:15]\n"
"ds_read_b64_tr_b16 a[234:235], v228 offset:20480\n"
"v_add_f32 v221, v221, v173\n"
"v_cvt_pk_bf16_f32 v190, v172, v173\n"
"v_mfma_f32_16x16x32_bf16 v[44:47], a[220:223], v[56:59], v[196:199]\n"
"v_exp_f32 v6, v6\n"
"v_mfma_f32_16x16x32_bf16 v[44:47], a[216:219], v[48:51], v[44:47]\n"
"ds_read_b64_tr_b16 a[236:237], v229 offset:16384\n"
"v_add_f32 v221, v221, v174\n"
"v_mfma_f32_16x16x32_bf16 v[44:47], a[212:215], v[40:43], v[44:47]\n"
"v_exp_f32 v7, v7\n"
"v_mfma_f32_16x16x32_bf16 v[44:47], a[208:211], v[32:35], v[44:47]\n"
"ds_read_b64_tr_b16 a[238:239], v229 offset:20480\n"
"v_add_f32 v221, v221, v175\n"
"v_cvt_pk_bf16_f32 v191, v174, v175\n"
"v_mfma_f32_16x16x32_bf16 v[76:79], a[208:211], v[64:67], v[200:203]\n"
"buffer_load_dwordx4 v242, s[52:55], s85 offen offset:2048 nt lds\n"
"v_exp_f32 v36, v36\n"
"v_mfma_f32_16x16x32_bf16 v[76:79], a[212:215], v[72:75], v[76:79]\n"
"ds_read_b64_tr_b16 a[240:241], v230 offset:16384\n"
"v_add_f32 v216, v216, v4\n"
"v_mfma_f32_16x16x32_bf16 v[76:79], a[216:219], v[80:83], v[76:79]\n"
"v_exp_f32 v37, v37\n"
"v_mfma_f32_16x16x32_bf16 v[76:79], a[220:223], v[88:91], v[76:79]\n"
"ds_read_b64_tr_b16 a[242:243], v230 offset:20480\n"
"v_add_f32 v216, v216, v5\n"
"v_cvt_pk_bf16_f32 v20, v4, v5\n"
"v_mfma_f32_16x16x32_bf16 v[108:111], a[220:223], v[120:123], v[204:207]\n"
"v_exp_f32 v38, v38\n"
"v_mfma_f32_16x16x32_bf16 v[108:111], a[216:219], v[112:115], v[108:111]\n"
"s_add_u32 m0, s63, 0xd000\n"
"ds_read_b64_tr_b16 a[244:245], v231 offset:16384\n"
"v_add_f32 v216, v216, v6\n"
"v_mfma_f32_16x16x32_bf16 v[108:111], a[212:215], v[104:107], v[108:111]\n"
"s_add_u32 s85, s85, 0x1000\n"
"buffer_load_dwordx4 v242, s[52:55], s85 offen nt lds\n"
"v_exp_f32 v39, v39\n"
"v_mfma_f32_16x16x32_bf16 v[108:111], a[208:211], v[96:99], v[108:111]\n"
"ds_read_b64_tr_b16 a[246:247], v231 offset:20480\n"
"v_add_f32 v216, v216, v7\n"
"v_cvt_pk_bf16_f32 v21, v6, v7\n"
"v_mfma_f32_16x16x32_bf16 v[140:143], a[208:211], v[128:131], v[208:211]\n"
"v_exp_f32 v68, v68\n"
"v_mfma_f32_16x16x32_bf16 v[140:143], a[212:215], v[136:139], v[140:143]\n"
"ds_read_b64_tr_b16 a[248:249], v232 offset:16384\n"
"v_add_f32 v217, v217, v36\n"
"v_mfma_f32_16x16x32_bf16 v[140:143], a[216:219], v[144:147], v[140:143]\n"
"v_exp_f32 v69, v69\n"
"v_mfma_f32_16x16x32_bf16 v[140:143], a[220:223], v[152:155], v[140:143]\n"
"ds_read_b64_tr_b16 a[250:251], v232 offset:20480\n"
"v_add_f32 v217, v217, v37\n"
"v_cvt_pk_bf16_f32 v52, v36, v37\n"
"v_mfma_f32_16x16x32_bf16 v[172:175], a[220:223], v[184:187], v[212:215]\n"
"buffer_load_dwordx4 v242, s[52:55], s85 offen offset:2048 nt lds\n"
"v_exp_f32 v70, v70\n"
"v_mfma_f32_16x16x32_bf16 v[172:175], a[216:219], v[176:179], v[172:175]\n"
"ds_read_b64_tr_b16 a[252:253], v233 offset:16384\n"
"v_add_f32 v217, v217, v38\n"
"v_mfma_f32_16x16x32_bf16 v[172:175], a[212:215], v[168:171], v[172:175]\n"
"s_waitcnt lgkmcnt(3)\n"
"v_exp_f32 v71, v71\n"
"v_mfma_f32_16x16x32_bf16 v[172:175], a[208:211], v[160:163], v[172:175]\n"
"ds_read_b64_tr_b16 a[254:255], v233 offset:20480\n"
"v_add_f32 v217, v217, v39\n"
"v_cvt_pk_bf16_f32 v53, v38, v39\n"
"v_mfma_f32_16x16x32_bf16 a[0:3], a[224:227], v[28:31], a[0:3]\n"
"v_exp_f32 v100, v100\n"
"v_mfma_f32_16x16x32_bf16 a[4:7], a[224:227], v[60:63], a[4:7]\n"
"v_add_f32 v218, v218, v68\n"
"v_add_f32 v218, v218, v69\n"
"v_mfma_f32_16x16x32_bf16 a[8:11], a[224:227], v[92:95], a[8:11]\n"
"v_exp_f32 v101, v101\n"
"v_mfma_f32_16x16x32_bf16 a[12:15], a[224:227], v[124:127], a[12:15]\n"
"v_cvt_pk_bf16_f32 v84, v68, v69\n"
"v_add_f32 v218, v218, v70\n"
"v_mfma_f32_16x16x32_bf16 a[16:19], a[224:227], v[156:159], a[16:19]\n"
"s_memrealtime s[88:89]\n"
"v_exp_f32 v102, v102\n"
"v_mfma_f32_16x16x32_bf16 a[20:23], a[224:227], v[188:191], a[20:23]\n"
"v_add_f32 v218, v218, v71\n"
"v_cvt_pk_bf16_f32 v85, v70, v71\n"
"v_mfma_f32_16x16x32_bf16 a[44:47], a[228:231], v[188:191], a[44:47]\n"
"v_exp_f32 v103, v103\n"
"v_mfma_f32_16x16x32_bf16 a[40:43], a[228:231], v[156:159], a[40:43]\n"
"v_add_f32 v219, v219, v100\n"
"v_add_f32 v219, v219, v101\n"
"v_mfma_f32_16x16x32_bf16 a[36:39], a[228:231], v[124:127], a[36:39]\n"
"LP13_%=:\n"
"s_waitcnt lgkmcnt(0)\n"
"s_sub_u32 s89, s88, s86\n"
"s_cmp_ge_i32 s89, 0\n"
"s_cbranch_scc1 LQ13_%=\n"
"s_memrealtime s[88:89]\n"
"s_branch LP13_%=\n"
"LQ13_%=:\n"
"s_waitcnt vmcnt(12) lgkmcnt(0)\n"
"s_barrier\n"
"v_exp_f32 v132, v132\n"
"v_mfma_f32_16x16x32_bf16 a[32:35], a[228:231], v[92:95], a[32:35]\n"
"ds_read_b64 a[192:193], v222 offset:49152\n"
"v_cvt_pk_bf16_f32 v116, v100, v101\n"
"v_mfma_f32_16x16x32_bf16 a[28:31], a[228:231], v[60:63], a[28:31]\n"
"s_add_u32 s86, s86, 124\n"
"v_exp_f32 v133, v133\n"
"v_mfma_f32_16x16x32_bf16 a[24:27], a[228:231], v[28:31], a[24:27]\n"
"ds_read_b64 a[194:195], v223 offset:49152\n"
"v_add_f32 v219, v219, v102\n"
"v_mfma_f32_16x16x32_bf16 a[48:51], a[232:235], v[28:31], a[48:51]\n"
"v_exp_f32 v134, v134\n"
"v_mfma_f32_16x16x32_bf16 a[52:55], a[232:235], v[60:63], a[52:55]\n"
"ds_read_b64 a[196:197], v224 offset:49152\n"
"v_add_f32 v219, v219, v103\n"
"v_mfma_f32_16x16x32_bf16 a[56:59], a[232:235], v[92:95], a[56:59]\n"
"v_exp_f32 v135, v135\n"
"v_mfma_f32_16x16x32_bf16 a[60:63], a[232:235], v[124:127], a[60:63]\n"
"ds_read_b64 a[198:199], v225 offset:49152\n"
"v_cvt_pk_bf16_f32 v117, v102, v103\n"
"v_mfma_f32_16x16x32_bf16 a[64:67], a[232:235], v[156:159], a[64:67]\n"
"v_exp_f32 v164, v164\n"
"v_mfma_f32_16x16x32_bf16 a[68:71], a[232:235], v[188:191], a[68:71]\n"
"ds_read_b64 a[200:201], v246 offset:49152\n"
"v_add_f32 v220, v220, v132\n"
"v_mfma_f32_16x16x32_bf16 a[92:95], a[236:239], v[188:191], a[92:95]\n"
"s_add_u32 s90, s87, s65\n"
"v_exp_f32 v165, v165\n"
"v_mfma_f32_16x16x32_bf16 a[88:91], a[236:239], v[156:159], a[88:91]\n"
"s_add_u32 m0, s91, 0x4000\n"
"ds_read_b64 a[202:203], v247 offset:49152\n"
"v_add_f32 v220, v220, v133\n"
"v_cvt_pk_bf16_f32 v148, v132, v133\n"
"v_mfma_f32_16x16x32_bf16 a[84:87], a[236:239], v[124:127], a[84:87]\n"
"buffer_load_dwordx4 v245, s[92:95], s90 offen lds\n"
"v_exp_f32 v166, v166\n"
"v_mfma_f32_16x16x32_bf16 a[80:83], a[236:239], v[92:95], a[80:83]\n"
"ds_read_b64 a[204:205], v248 offset:49152\n"
"v_add_f32 v220, v220, v134\n"
"v_mfma_f32_16x16x32_bf16 a[76:79], a[236:239], v[60:63], a[76:79]\n"
"v_exp_f32 v167, v167\n"
"v_mfma_f32_16x16x32_bf16 a[72:75], a[236:239], v[28:31], a[72:75]\n"
"ds_read_b64 a[206:207], v249 offset:49152\n"
"v_add_f32 v220, v220, v135\n"
"v_cvt_pk_bf16_f32 v149, v134, v135\n"
"v_mfma_f32_16x16x32_bf16 a[96:99], a[240:243], v[28:31], a[96:99]\n"
"v_exp_f32 v12, v12\n"
"v_mfma_f32_16x16x32_bf16 a[100:103], a[240:243], v[60:63], a[100:103]\n"
"ds_read_b64 a[208:209], v222 offset:53248\n"
"v_add_f32 v221, v221, v164\n"
"v_mfma_f32_16x16x32_bf16 a[104:107], a[240:243], v[92:95], a[104:107]\n"
"buffer_load_dwordx4 v251, s[92:95], s90 offen offset:1024 lds\n"
"v_exp_f32 v13, v13\n"
"v_mfma_f32_16x16x32_bf16 a[108:111], a[240:243], v[124:127], a[108:111]\n"
"ds_read_b64 a[210:211], v223 offset:53248\n"
"v_add_f32 v221, v221, v165\n"
"v_cvt_pk_bf16_f32 v180, v164, v165\n"
"v_mfma_f32_16x16x32_bf16 a[112:115], a[240:243], v[156:159], a[112:115]\n"
"v_exp_f32 v14, v14\n"
"v_mfma_f32_16x16x32_bf16 a[116:119], a[240:243], v[188:191], a[116:119]\n"
"ds_read_b64 a[212:213], v224 offset:53248\n"
"v_add_f32 v221, v221, v166\n"
"v_mfma_f32_16x16x32_bf16 a[140:143], a[244:247], v[188:191], a[140:143]\n"
"v_exp_f32 v15, v15\n"
"v_mfma_f32_16x16x32_bf16 a[136:139], a[244:247], v[156:159], a[136:139]\n"
"ds_read_b64 a[214:215], v225 offset:53248\n"
"v_add_f32 v221, v221, v167\n"
"v_cvt_pk_bf16_f32 v181, v166, v167\n"
"v_mfma_f32_16x16x32_bf16 a[132:135], a[244:247], v[124:127], a[132:135]\n"
"buffer_load_dwordx4 v252, s[92:95], s90 offen offset:2048 lds\n"
"v_exp_f32 v44, v44\n"
"v_mfma_f32_16x16x32_bf16 a[128:131], a[244:247], v[92:95], a[128:131]\n"
"ds_read_b64 a[216:217], v246 offset:53248\n"
"v_add_f32 v216, v216, v12\n"
"v_mfma_f32_16x16x32_bf16 a[124:127], a[244:247], v[60:63], a[124:127]\n"
"v_exp_f32 v45, v45\n"
"v_mfma_f32_16x16x32_bf16 a[120:123], a[244:247], v[28:31], a[120:123]\n"
"ds_read_b64 a[218:219], v247 offset:53248\n"
"v_add_f32 v216, v216, v13\n"
"v_cvt_pk_bf16_f32 v22, v12, v13\n"
"v_mfma_f32_16x16x32_bf16 a[144:147], a[248:251], v[28:31], a[144:147]\n"
"v_exp_f32 v46, v46\n"
"v_mfma_f32_16x16x32_bf16 a[148:151], a[248:251], v[60:63], a[148:151]\n"
"ds_read_b64 a[220:221], v248 offset:53248\n"
"v_add_f32 v216, v216, v14\n"
"v_mfma_f32_16x16x32_bf16 a[152:155], a[248:251], v[92:95], a[152:155]\n"
"buffer_load_dwordx4 v253, s[92:95], s90 offen offset:3072 lds\n"
"v_exp_f32 v47, v47\n"
"v_mfma_f32_16x16x32_bf16 a[156:159], a[248:251], v[124:127], a[156:159]\n"
"ds_read_b64 a[222:223], v249 offset:53248\n"
"v_add_f32 v216, v216, v15\n"
"v_cvt_pk_bf16_f32 v23, v14, v15\n"
"v_mfma_f32_16x16x32_bf16 a[160:163], a[248:251], v[156:159], a[160:163]\n"
"v_exp_f32 v76, v76\n"
"v_mfma_f32_16x16x32_bf16 a[164:167], a[248:251], v[188:191], a[164:167]\n"
"v_add_f32 v217, v217, v44\n"
"v_add_f32 v217, v217, v45\n"
"v_mfma_f32_16x16x32_bf16 a[188:191], a[252:255], v[188:191], a[188:191]\n"
"v_exp_f32 v77, v77\n"
"v_mfma_f32_16x16x32_bf16 a[184:187], a[252:255], v[156:159], a[184:187]\n"
"v_cvt_pk_bf16_f32 v54, v44, v45\n"
"v_add_f32 v217, v217, v46\n"
"v_mfma_f32_16x16x32_bf16 a[180:183], a[252:255], v[124:127], a[180:183]\n"
"v_exp_f32 v78, v78\n"
"v_mfma_f32_16x16x32_bf16 a[176:179], a[252:255], v[92:95], a[176:179]\n"
"v_add_f32 v217, v217, v47\n"
"v_cvt_pk_bf16_f32 v55, v46, v47\n"
"v_mfma_f32_16x16x32_bf16 a[172:175], a[252:255], v[60:63], a[172:175]\n"
"s_waitcnt lgkmcnt(8)\n"
"v_exp_f32 v79, v79\n"
"v_mfma_f32_16x16x32_bf16 a[168:171], a[252:255], v[28:31], a[168:171]\n"
"v_add_f32 v218, v218, v76\n"
"v_add_f32 v218, v218, v77\n"
"s_mov_b64 s[74:75], 0\n"
"s_mov_b32 s72, 0x7b800000\n"
"s_mov_b64 s[88:89], 0\n"
"s_mov_b32 s86, 0x4b800000\n"
"s_waitcnt lgkmcnt(0)\n"
"v_mfma_f32_16x16x32_bf16 v[4:7], a[192:195], v[0:3], v[192:195]\n"
"v_cvt_pk_bf16_f32 v86, v76, v77\n"
"v_add_f32 v218, v218, v78\n"
"v_mfma_f32_16x16x32_bf16 v[4:7], a[196:199], v[8:11], v[4:7]\n"
"ds_read_b64_tr_b16 a[224:225], v226 offset:32768\n"
"v_mfma_f32_16x16x32_bf16 v[4:7], a[200:203], v[16:19], v[4:7]\n"
"v_exp_f32 v108, v108\n"
"v_mfma_f32_16x16x32_bf16 v[4:7], a[204:207], v[24:27], v[4:7]\n"
"ds_read_b64_tr_b16 a[226:227], v226 offset:36864\n"
"v_add_f32 v218, v218, v79\n"
"v_mfma_f32_16x16x32_bf16 v[12:15], a[220:223], v[24:27], v[192:195]\n"
"v_cvt_pk_bf16_f32 v87, v78, v79\n"
"v_mfma_f32_16x16x32_bf16 v[12:15], a[216:219], v[16:19], v[12:15]\n"
"ds_read_b64_tr_b16 a[228:229], v227 offset:32768\n"
"v_mfma_f32_16x16x32_bf16 v[12:15], a[212:215], v[8:11], v[12:15]\n"
"v_exp_f32 v109, v109\n"
"v_mfma_f32_16x16x32_bf16 v[12:15], a[208:211], v[0:3], v[12:15]\n"
"ds_read_b64_tr_b16 a[230:231], v227 offset:36864\n"
"v_add_f32 v219, v219, v108\n"
"v_mfma_f32_16x16x32_bf16 v[36:39], a[192:195], v[32:35], v[196:199]\n"
"v_exp_f32 v110, v110\n"
"v_mfma_f32_16x16x32_bf16 v[36:39], a[196:199], v[40:43], v[36:39]\n"
"ds_read_b64_tr_b16 a[232:233], v228 offset:32768\n"
"v_add_f32 v219, v219, v109\n"
"v_mfma_f32_16x16x32_bf16 v[36:39], a[200:203], v[48:51], v[36:39]\n"
"v_cvt_pk_bf16_f32 v118, v108, v109\n"
"v_mfma_f32_16x16x32_bf16 v[36:39], a[204:207], v[56:59], v[36:39]\n"
"ds_read_b64_tr_b16 a[234:235], v228 offset:36864\n"
"v_mfma_f32_16x16x32_bf16 v[44:47], a[220:223], v[56:59], v[196:199]\n"
"v_exp_f32 v111, v111\n"
"v_mfma_f32_16x16x32_bf16 v[44:47], a[216:219], v[48:51], v[44:47]\n"
"ds_read_b64_tr_b16 a[236:237], v229 offset:32768\n"
"v_add_f32 v219, v219, v110\n"
"v_mfma_f32_16x16x32_bf16 v[44:47], a[212:215], v[40:43], v[44:47]\n"
"v_exp_f32 v140, v140\n"
"v_mfma_f32_16x16x32_bf16 v[44:47], a[208:211], v[32:35], v[44:47]\n"
"ds_read_b64_tr_b16 a[238:239], v229 offset:36864\n"
"v_add_f32 v219, v219, v111\n"
"v_mfma_f32_16x16x32_bf16 v[68:71], a[192:195], v[64:67], v[200:203]\n"
"v_cvt_pk_bf16_f32 v119, v110, v111\n"
"v_mfma_f32_16x16x32_bf16 v[68:71], a[196:199], v[72:75], v[68:71]\n"
"ds_read_b64_tr_b16 a[240:241], v230 offset:32768\n"
"v_mfma_f32_16x16x32_bf16 v[68:71], a[200:203], v[80:83], v[68:71]\n"
"v_exp_f32 v141, v141\n"
"v_mfma_f32_16x16x32_bf16 v[68:71], a[204:207], v[88:91], v[68:71]\n"
"ds_read_b64_tr_b16 a[242:243], v230 offset:36864\n"
"v_add_f32 v220, v220, v140\n"
"v_mfma_f32_16x16x32_bf16 v[76:79], a[220:223], v[88:91], v[200:203]\n"
"v_exp_f32 v142, v142\n"
"v_mfma_f32_16x16x32_bf16 v[76:79], a[216:219], v[80:83], v[76:79]\n"
"ds_read_b64_tr_b16 a[244:245], v231 offset:32768\n"
"v_add_f32 v220, v220, v141\n"
"v_mfma_f32_16x16x32_bf16 v[76:79], a[212:215], v[72:75], v[76:79]\n"
"v_cvt_pk_bf16_f32 v150, v140, v141\n"
"v_mfma_f32_16x16x32_bf16 v[76:79], a[208:211], v[64:67], v[76:79]\n"
"ds_read_b64_tr_b16 a[246:247], v231 offset:36864\n"
"v_mfma_f32_16x16x32_bf16 v[100:103], a[192:195], v[96:99], v[204:207]\n"
"v_exp_f32 v143, v143\n"
"v_mfma_f32_16x16x32_bf16 v[100:103], a[196:199], v[104:107], v[100:103]\n"
"ds_read_b64_tr_b16 a[248:249], v232 offset:32768\n"
"v_add_f32 v220, v220, v142\n"
"v_mfma_f32_16x16x32_bf16 v[100:103], a[200:203], v[112:115], v[100:103]\n"
"v_exp_f32 v172, v172\n"
"v_mfma_f32_16x16x32_bf16 v[100:103], a[204:207], v[120:123], v[100:103]\n"
"ds_read_b64_tr_b16 a[250:251], v232 offset:36864\n"
"v_add_f32 v220, v220, v143\n"
"v_mfma_f32_16x16x32_bf16 v[108:111], a[220:223], v[120:123], v[204:207]\n"
"v_cvt_pk_bf16_f32 v151, v142, v143\n"
"v_mfma_f32_16x16x32_bf16 v[108:111], a[216:219], v[112:115], v[108:111]\n"
"ds_read_b64_tr_b16 a[252:253], v233 offset:32768\n"
"v_mfma_f32_16x16x32_bf16 v[108:111], a[212:215], v[104:107], v[108:111]\n"
"v_exp_f32 v173, v173\n"
"v_mfma_f32_16x16x32_bf16 v[108:111], a[208:211], v[96:99], v[108:111]\n"
"ds_read_b64_tr_b16 a[254:255], v233 offset:36864\n"
"v_add_f32 v221, v221, v172\n"
"v_mfma_f32_16x16x32_bf16 v[132:135], a[192:195], v[128:131], v[208:211]\n"
"v_exp_f32 v174, v174\n"
"v_mfma_f32_16x16x32_bf16 v[132:135], a[196:199], v[136:139], v[132:135]\n"
"v_add_f32 v221, v221, v173\n"
"v_cvt_pk_bf16_f32 v182, v172, v173\n"
"v_mfma_f32_16x16x32_bf16 v[132:135], a[200:203], v[144:147], v[132:135]\n"
"v_exp_f32 v175, v175\n"
"v_mfma_f32_16x16x32_bf16 v[132:135], a[204:207], v[152:155], v[132:135]\n"
"s_nop 0\n"
"v_add_f32 v221, v221, v174\n"
"v_mfma_f32_16x16x32_bf16 v[140:143], a[220:223], v[152:155], v[208:211]\n"
"v_add_f32 v221, v221, v175\n"
"v_cvt_pk_bf16_f32 v183, v174, v175\n"
"v_mfma_f32_16x16x32_bf16 v[140:143], a[216:219], v[144:147], v[140:143]\n"
"v_exp_f32 v4, v4\n"
"v_mfma_f32_16x16x32_bf16 v[140:143], a[212:215], v[136:139], v[140:143]\n"
"v_exp_f32 v5, v5\n"
"v_mfma_f32_16x16x32_bf16 v[140:143], a[208:211], v[128:131], v[140:143]\n"
"v_exp_f32 v6, v6\n"
"v_mfma_f32_16x16x32_bf16 v[164:167], a[192:195], v[160:163], v[212:215]\n"
"v_exp_f32 v7, v7\n"
"v_mfma_f32_16x16x32_bf16 v[164:167], a[196:199], v[168:171], v[164:167]\n"
"v_add_f32 v216, v216, v4\n"
"v_add_f32 v216, v216, v5\n"
"v_mfma_f32_16x16x32_bf16 v[164:167], a[200:203], v[176:179], v[164:167]\n"
"v_add_f32 v216, v216, v6\n"
"v_add_f32 v216, v216, v7\n"
"v_mfma_f32_16x16x32_bf16 v[164:167], a[204:207], v[184:187], v[164:167]\n"
"v_cvt_pk_bf16_f32 v28, v4, v5\n"
"v_cvt_pk_bf16_f32 v29, v6, v7\n"
"v_mfma_f32_16x16x32_bf16 v[172:175], a[220:223], v[184:187], v[212:215]\n"
"v_exp_f32 v12, v12\n"
"v_mfma_f32_16x16x32_bf16 v[172:175], a[216:219], v[176:179], v[172:175]\n"
"v_exp_f32 v13, v13\n"
"v_mfma_f32_16x16x32_bf16 v[172:175], a[212:215], v[168:171], v[172:175]\n"
"v_exp_f32 v14, v14\n"
"v_mfma_f32_16x16x32_bf16 v[172:175], a[208:211], v[160:163], v[172:175]\n"
"v_exp_f32 v15, v15\n"
"s_waitcnt vmcnt(4) lgkmcnt(0)\n"
"s_barrier\n"
"v_mfma_f32_16x16x32_bf16 a[0:3], a[224:227], v[20:23], a[0:3]\n"
"ds_read_b64_tr_b16 a[192:193], v226 offset:49152\n"
"ds_read_b64_tr_b16 a[194:195], v226 offset:53248\n"
"v_mfma_f32_16x16x32_bf16 a[24:27], a[228:231], v[20:23], a[24:27]\n"
"ds_read_b64_tr_b16 a[196:197], v227 offset:49152\n"
"ds_read_b64_tr_b16 a[198:199], v227 offset:53248\n"
"v_mfma_f32_16x16x32_bf16 a[48:51], a[232:235], v[20:23], a[48:51]\n"
"ds_read_b64_tr_b16 a[200:201], v228 offset:49152\n"
"ds_read_b64_tr_b16 a[202:203], v228 offset:53248\n"
"v_mfma_f32_16x16x32_bf16 a[72:75], a[236:239], v[20:23], a[72:75]\n"
"ds_read_b64_tr_b16 a[204:205], v229 offset:49152\n"
"ds_read_b64_tr_b16 a[206:207], v229 offset:53248\n"
"v_mfma_f32_16x16x32_bf16 a[96:99], a[240:243], v[20:23], a[96:99]\n"
"ds_read_b64_tr_b16 a[208:209], v230 offset:49152\n"
"ds_read_b64_tr_b16 a[210:211], v230 offset:53248\n"
"v_mfma_f32_16x16x32_bf16 a[120:123], a[244:247], v[20:23], a[120:123]\n"
"ds_read_b64_tr_b16 a[212:213], v231 offset:49152\n"
"ds_read_b64_tr_b16 a[214:215], v231 offset:53248\n"
"v_mfma_f32_16x16x32_bf16 a[144:147], a[248:251], v[20:23], a[144:147]\n"
"ds_read_b64_tr_b16 a[216:217], v232 offset:49152\n"
"ds_read_b64_tr_b16 a[218:219], v232 offset:53248\n"
"v_mfma_f32_16x16x32_bf16 a[168:171], a[252:255], v[20:23], a[168:171]\n"
"ds_read_b64_tr_b16 a[220:221], v233 offset:49152\n"
"ds_read_b64_tr_b16 a[222:223], v233 offset:53248\n"
"v_add_f32 v216, v216, v12\n"
"v_add_f32 v216, v216, v13\n"
"v_add_f32 v216, v216, v14\n"
"v_add_f32 v216, v216, v15\n"
"v_cvt_pk_bf16_f32 v30, v12, v13\n"
"v_cvt_pk_bf16_f32 v31, v14, v15\n"
"v_mov_b32 v128, v216\n"
"s_nop 1\n"
"v_permlane32_swap_b32 v216, v128\n"
"v_add_f32 v216, v216, v128\n"
"v_mov_b32 v128, v216\n"
"s_nop 1\n"
"v_permlane16_swap_b32 v216, v128\n"
"v_add_f32 v216, v216, v128\n"
"v_cmp_nge_f32 vcc, s72, v216\n"
"s_or_b64 s[74:75], s[74:75], vcc\n"
"v_cmp_nge_f32 vcc, s86, v216\n"
"s_or_b64 s[88:89], s[88:89], vcc\n"
"v_rcp_f32 v216, v216\n"
"s_nop 0\n"
"s_waitcnt lgkmcnt(0)\n"
"s_barrier\n"
"v_mfma_f32_16x16x32_bf16 a[0:3], a[192:195], v[28:31], a[0:3]\n"
"v_and_b32 v206, 3, v244\n"
"v_lshlrev_b32 v206, 3, v206\n"
"v_lshrrev_b32 v202, 2, v244\n"
"v_and_b32 v202, 3, v202\n"
"v_lshrrev_b32 v202, 1, v202\n"
"v_mfma_f32_16x16x32_bf16 a[24:27], a[196:199], v[28:31], a[24:27]\n"
"v_lshrrev_b32 v205, 4, v244\n"
"v_lshlrev_b32 v205, 1, v205\n"
"v_add_u32 v205, v202, v205\n"
"v_and_b32 v205, 3, v205\n"
"v_xor_b32 v205, s77, v205\n"
"v_mfma_f32_16x16x32_bf16 a[48:51], a[200:203], v[28:31], a[48:51]\n"
"v_lshlrev_b32 v205, 5, v205\n"
"v_lshrrev_b32 v204, 2, v244\n"
"v_and_b32 v204, 3, v204\n"
"v_lshrrev_b32 v224, 4, v244\n"
"v_lshlrev_b32 v224, 2, v224\n"
"v_mfma_f32_16x16x32_bf16 a[72:75], a[204:207], v[28:31], a[72:75]\n"
"v_add_u32 v224, v204, v224\n"
"v_lshlrev_b32 v224, 7, v224\n"
"v_add_u32 v224, v205, v224\n"
"v_add_u32 v224, v206, v224\n"
"v_add_u32 v224, s76, v224\n"
"v_mfma_f32_16x16x32_bf16 a[96:99], a[208:211], v[28:31], a[96:99]\n"
"v_add_u32 v225, 0x10000, v224\n"
"v_add_u32 v226, 0x20000, v224\n"
"s_add_u32 s73, s76, 0xa000\n"
"v_add_u32 v227, s73, v243\n"
"v_add_u32 v228, 0x10000, v227\n"
"v_mfma_f32_16x16x32_bf16 a[120:123], a[212:215], v[28:31], a[120:123]\n"
"s_lshl_b32 s73, s70, 16\n"
"v_add_u32 v229, s73, v227\n"
"s_lshl_b32 s73, s77, 10\n"
"v_add_u32 v230, s73, v243\n"
"v_add_u32 v231, 0x1fc00, v230\n"
"v_mfma_f32_16x16x32_bf16 a[144:147], a[216:219], v[28:31], a[144:147]\n"
"v_add_u32 v232, 0x3f800, v230\n"
"v_add_u32 v233, 0x5f400, v230\n"
"s_mul_i32 s73, s77, 6144\n"
"s_add_u32 s50, s73, s76\n"
"v_and_b32 v238, 7, v244\n"
"v_mfma_f32_16x16x32_bf16 a[168:171], a[220:223], v[28:31], a[168:171]\n"
"v_lshlrev_b32 v238, 7, v238\n"
"v_lshlrev_b32 v239, 11, v244\n"
"s_add_u32 s60, s36, 1\n"
"s_addc_u32 s61, s37, 0\n"
"s_lshr_b32 s63, %[hq0], 2\n"
"s_xor_b32 s63, s63, s71\n"
"s_lshl_b32 s48, %[hq0], 12\n"
"s_add_u32 s48, s48, 0x1000\n"
"s_add_u32 s68, s38, 0x0\n"
"s_nop 3\n"
"v_accvgpr_read_b32 v0, a0\n"
"v_accvgpr_read_b32 v1, a1\n"
"v_accvgpr_read_b32 v2, a2\n"
"v_accvgpr_read_b32 v3, a3\n"
"v_accvgpr_read_b32 v32, a24\n"
"v_accvgpr_read_b32 v33, a25\n"
"v_accvgpr_read_b32 v34, a26\n"
"v_accvgpr_read_b32 v35, a27\n"
"v_mul_f32 v0, v0, v216\n"
"v_mul_f32 v1, v1, v216\n"
"v_mul_f32 v2, v2, v216\n"
"v_mul_f32 v3, v3, v216\n"
"v_mul_f32 v32, v32, v216\n"
"v_mul_f32 v33, v33, v216\n"
"v_mul_f32 v34, v34, v216\n"
"v_mul_f32 v35, v35, v216\n"
"v_cvt_pk_bf16_f32 v0, v0, v1\n"
"v_cvt_pk_bf16_f32 v1, v2, v3\n"
"v_cvt_pk_bf16_f32 v2, v32, v33\n"
"v_cvt_pk_bf16_f32 v3, v34, v35\n"
"buffer_store_dwordx4 v[0:3], v243, s[56:59], s68 offen sc1\n"
"v_exp_f32 v36, v36\n"
"v_exp_f32 v37, v37\n"
"v_exp_f32 v38, v38\n"
"v_exp_f32 v39, v39\n"
"v_add_f32 v217, v217, v36\n"
"v_add_f32 v217, v217, v37\n"
"v_add_f32 v217, v217, v38\n"
"v_add_f32 v217, v217, v39\n"
"v_cvt_pk_bf16_f32 v60, v36, v37\n"
"v_cvt_pk_bf16_f32 v61, v38, v39\n"
"v_exp_f32 v44, v44\n"
"v_exp_f32 v45, v45\n"
"v_exp_f32 v46, v46\n"
"v_exp_f32 v47, v47\n"
"v_add_f32 v217, v217, v44\n"
"v_add_f32 v217, v217, v45\n"
"v_add_f32 v217, v217, v46\n"
"v_add_f32 v217, v217, v47\n"
"v_cvt_pk_bf16_f32 v62, v44, v45\n"
"v_cvt_pk_bf16_f32 v63, v46, v47\n"
"v_mov_b32 v129, v217\n"
"s_nop 1\n"
"v_permlane32_swap_b32 v217, v129\n"
"v_add_f32 v217, v217, v129\n"
"v_mov_b32 v129, v217\n"
"s_nop 1\n"
"v_permlane16_swap_b32 v217, v129\n"
"v_add_f32 v217, v217, v129\n"
"v_cmp_nge_f32 vcc, s72, v217\n"
"s_or_b64 s[74:75], s[74:75], vcc\n"
"v_cmp_nge_f32 vcc, s86, v217\n"
"s_or_b64 s[88:89], s[88:89], vcc\n"
"v_rcp_f32 v217, v217\n"
"s_nop 0\n"
"s_nop 0\n"
"v_mfma_f32_16x16x32_bf16 a[4:7], a[224:227], v[52:55], a[4:7]\n"
"v_accvgpr_read_b32 v8, a48\n"
"v_accvgpr_read_b32 v9, a49\n"
"v_accvgpr_read_b32 v10, a50\n"
"v_mfma_f32_16x16x32_bf16 a[4:7], a[192:195], v[60:63], a[4:7]\n"
"v_accvgpr_read_b32 v11, a51\n"
"v_accvgpr_read_b32 v40, a72\n"
"v_accvgpr_read_b32 v41, a73\n"
"v_mfma_f32_16x16x32_bf16 a[28:31], a[196:199], v[60:63], a[28:31]\n"
"v_accvgpr_read_b32 v42, a74\n"
"v_accvgpr_read_b32 v43, a75\n"
"v_mul_f32 v8, v8, v216\n"
"v_mfma_f32_16x16x32_bf16 a[28:31], a[228:231], v[52:55], a[28:31]\n"
"v_mul_f32 v9, v9, v216\n"
"v_mul_f32 v10, v10, v216\n"
"v_mul_f32 v11, v11, v216\n"
"v_mfma_f32_16x16x32_bf16 a[52:55], a[232:235], v[52:55], a[52:55]\n"
"v_mul_f32 v40, v40, v216\n"
"v_mul_f32 v41, v41, v216\n"
"v_mul_f32 v42, v42, v216\n"
"v_mfma_f32_16x16x32_bf16 a[52:55], a[200:203], v[60:63], a[52:55]\n"
"v_mul_f32 v43, v43, v216\n"
"v_cvt_pk_bf16_f32 v8, v8, v9\n"
"v_cvt_pk_bf16_f32 v9, v10, v11\n"
"v_cvt_pk_bf16_f32 v10, v40, v41\n"
"v_cvt_pk_bf16_f32 v11, v42, v43\n"
"buffer_store_dwordx4 v[8:11], v243, s[56:59], s68 offen offset:1024 sc1\n"
"v_mfma_f32_16x16x32_bf16 a[76:79], a[204:207], v[60:63], a[76:79]\n"
"v_accvgpr_read_b32 v16, a96\n"
"v_accvgpr_read_b32 v17, a97\n"
"v_accvgpr_read_b32 v18, a98\n"
"v_mfma_f32_16x16x32_bf16 a[76:79], a[236:239], v[52:55], a[76:79]\n"
"v_accvgpr_read_b32 v19, a99\n"
"v_accvgpr_read_b32 v48, a120\n"
"v_accvgpr_read_b32 v49, a121\n"
"v_mfma_f32_16x16x32_bf16 a[100:103], a[240:243], v[52:55], a[100:103]\n"
"v_accvgpr_read_b32 v50, a122\n"
"v_accvgpr_read_b32 v51, a123\n"
"v_mul_f32 v16, v16, v216\n"
"v_mfma_f32_16x16x32_bf16 a[100:103], a[208:211], v[60:63], a[100:103]\n"
"v_mul_f32 v17, v17, v216\n"
"v_mul_f32 v18, v18, v216\n"
"v_mul_f32 v19, v19, v216\n"
"v_mfma_f32_16x16x32_bf16 a[124:127], a[212:215], v[60:63], a[124:127]\n"
"v_mul_f32 v48, v48, v216\n"
"v_mul_f32 v49, v49, v216\n"
"v_mul_f32 v50, v50, v216\n"
"v_mfma_f32_16x16x32_bf16 a[124:127], a[244:247], v[52:55], a[124:127]\n"
"v_mul_f32 v51, v51, v216\n"
"v_cvt_pk_bf16_f32 v16, v16, v17\n"
"v_cvt_pk_bf16_f32 v17, v18, v19\n"
"v_cvt_pk_bf16_f32 v18, v48, v49\n"
"v_cvt_pk_bf16_f32 v19, v50, v51\n"
"buffer_store_dwordx4 v[16:19], v243, s[56:59], s68 offen offset:2048 sc1\n"
"v_mfma_f32_16x16x32_bf16 a[148:151], a[248:251], v[52:55], a[148:151]\n"
"v_accvgpr_read_b32 v24, a144\n"
"v_accvgpr_read_b32 v25, a145\n"
"v_accvgpr_read_b32 v26, a146\n"
"v_mfma_f32_16x16x32_bf16 a[148:151], a[216:219], v[60:63], a[148:151]\n"
"v_accvgpr_read_b32 v27, a147\n"
"v_accvgpr_read_b32 v56, a168\n"
"v_accvgpr_read_b32 v57, a169\n"
"v_mfma_f32_16x16x32_bf16 a[172:175], a[220:223], v[60:63], a[172:175]\n"
"v_accvgpr_read_b32 v58, a170\n"
"v_accvgpr_read_b32 v59, a171\n"
"v_mul_f32 v24, v24, v216\n"
"v_mfma_f32_16x16x32_bf16 a[172:175], a[252:255], v[52:55], a[172:175]\n"
"v_mul_f32 v25, v25, v216\n"
"v_mul_f32 v26, v26, v216\n"
"v_mul_f32 v27, v27, v216\n"
"v_mul_f32 v56, v56, v216\n"
"v_mul_f32 v57, v57, v216\n"
"v_mul_f32 v58, v58, v216\n"
"v_mul_f32 v59, v59, v216\n"
"v_cvt_pk_bf16_f32 v24, v24, v25\n"
"v_cvt_pk_bf16_f32 v25, v26, v27\n"
"v_cvt_pk_bf16_f32 v26, v56, v57\n"
"v_cvt_pk_bf16_f32 v27, v58, v59\n"
"buffer_store_dwordx4 v[24:27], v243, s[56:59], s68 offen offset:3072 sc1\n"
"s_cmp_lg_u32 s63, 0\n"
"s_cbranch_scc1 LO0_%=\n"
"ds_write_b128 v229, v[0:3] offset:0\n"
"ds_write_b128 v229, v[8:11] offset:6144\n"
"ds_write_b128 v229, v[16:19] offset:12288\n"
"ds_write_b128 v229, v[24:27] offset:18432\n"
"LO0_%=:\n"
"s_add_u32 s68, s38, 0x20000\n"
"v_accvgpr_read_b32 v64, a4\n"
"v_accvgpr_read_b32 v65, a5\n"
"v_accvgpr_read_b32 v66, a6\n"
"v_accvgpr_read_b32 v67, a7\n"
"v_accvgpr_read_b32 v96, a28\n"
"v_accvgpr_read_b32 v97, a29\n"
"v_accvgpr_read_b32 v98, a30\n"
"v_accvgpr_read_b32 v99, a31\n"
"v_mul_f32 v64, v64, v217\n"
"v_mul_f32 v65, v65, v217\n"
"v_mul_f32 v66, v66, v217\n"
"v_mul_f32 v67, v67, v217\n"
"v_mul_f32 v96, v96, v217\n"
"v_mul_f32 v97, v97, v217\n"
"v_mul_f32 v98, v98, v217\n"
"v_mul_f32 v99, v99, v217\n"
"v_cvt_pk_bf16_f32 v64, v64, v65\n"
"v_cvt_pk_bf16_f32 v65, v66, v67\n"
"v_cvt_pk_bf16_f32 v66, v96, v97\n"
"v_cvt_pk_bf16_f32 v67, v98, v99\n"
"buffer_store_dwordx4 v[64:67], v243, s[56:59], s68 offen sc1\n"
"v_exp_f32 v68, v68\n"
"v_exp_f32 v69, v69\n"
"v_exp_f32 v70, v70\n"
"v_exp_f32 v71, v71\n"
"v_add_f32 v218, v218, v68\n"
"v_add_f32 v218, v218, v69\n"
"v_add_f32 v218, v218, v70\n"
"v_add_f32 v218, v218, v71\n"
"v_cvt_pk_bf16_f32 v92, v68, v69\n"
"v_cvt_pk_bf16_f32 v93, v70, v71\n"
"v_exp_f32 v76, v76\n"
"v_exp_f32 v77, v77\n"
"v_exp_f32 v78, v78\n"
"v_exp_f32 v79, v79\n"
"v_add_f32 v218, v218, v76\n"
"v_add_f32 v218, v218, v77\n"
"v_add_f32 v218, v218, v78\n"
"v_add_f32 v218, v218, v79\n"
"v_cvt_pk_bf16_f32 v94, v76, v77\n"
"v_cvt_pk_bf16_f32 v95, v78, v79\n"
"v_mov_b32 v130, v218\n"
"s_nop 1\n"
"v_permlane32_swap_b32 v218, v130\n"
"v_add_f32 v218, v218, v130\n"
"v_mov_b32 v130, v218\n"
"s_nop 1\n"
"v_permlane16_swap_b32 v218, v130\n"
"v_add_f32 v218, v218, v130\n"
"v_cmp_nge_f32 vcc, s72, v218\n"
"s_or_b64 s[74:75], s[74:75], vcc\n"
"v_cmp_nge_f32 vcc, s86, v218\n"
"s_or_b64 s[88:89], s[88:89], vcc\n"
"v_rcp_f32 v218, v218\n"
"s_nop 0\n"
"s_nop 0\n"
"v_mfma_f32_16x16x32_bf16 a[8:11], a[224:227], v[84:87], a[8:11]\n"
"v_accvgpr_read_b32 v72, a52\n"
"v_accvgpr_read_b32 v73, a53\n"
"v_accvgpr_read_b32 v74, a54\n"
"v_mfma_f32_16x16x32_bf16 a[8:11], a[192:195], v[92:95], a[8:11]\n"
"v_accvgpr_read_b32 v75, a55\n"
"v_accvgpr_read_b32 v104, a76\n"
"v_accvgpr_read_b32 v105, a77\n"
"v_mfma_f32_16x16x32_bf16 a[32:35], a[196:199], v[92:95], a[32:35]\n"
"v_accvgpr_read_b32 v106, a78\n"
"v_accvgpr_read_b32 v107, a79\n"
"v_mul_f32 v72, v72, v217\n"
"v_mfma_f32_16x16x32_bf16 a[32:35], a[228:231], v[84:87], a[32:35]\n"
"v_mul_f32 v73, v73, v217\n"
"v_mul_f32 v74, v74, v217\n"
"v_mul_f32 v75, v75, v217\n"
"v_mfma_f32_16x16x32_bf16 a[56:59], a[232:235], v[84:87], a[56:59]\n"
"v_mul_f32 v104, v104, v217\n"
"v_mul_f32 v105, v105, v217\n"
"v_mul_f32 v106, v106, v217\n"
"v_mfma_f32_16x16x32_bf16 a[56:59], a[200:203], v[92:95], a[56:59]\n"
"v_mul_f32 v107, v107, v217\n"
"v_cvt_pk_bf16_f32 v72, v72, v73\n"
"v_cvt_pk_bf16_f32 v73, v74, v75\n"
"v_cvt_pk_bf16_f32 v74, v104, v105\n"
"v_cvt_pk_bf16_f32 v75, v106, v107\n"
"buffer_store_dwordx4 v[72:75], v243, s[56:59], s68 offen offset:1024 sc1\n"
"v_mfma_f32_16x16x32_bf16 a[80:83], a[204:207], v[92:95], a[80:83]\n"
"v_accvgpr_read_b32 v80, a100\n"
"v_accvgpr_read_b32 v81, a101\n"
"v_accvgpr_read_b32 v82, a102\n"
"v_mfma_f32_16x16x32_bf16 a[80:83], a[236:239], v[84:87], a[80:83]\n"
"v_accvgpr_read_b32 v83, a103\n"
"v_accvgpr_read_b32 v112, a124\n"
"v_accvgpr_read_b32 v113, a125\n"
"v_mfma_f32_16x16x32_bf16 a[104:107], a[240:243], v[84:87], a[104:107]\n"
"v_accvgpr_read_b32 v114, a126\n"
"v_accvgpr_read_b32 v115, a127\n"
"v_mul_f32 v80, v80, v217\n"
"v_mfma_f32_16x16x32_bf16 a[104:107], a[208:211], v[92:95], a[104:107]\n"
"v_mul_f32 v81, v81, v217\n"
"v_mul_f32 v82, v82, v217\n"
"v_mul_f32 v83, v83, v217\n"
"v_mfma_f32_16x16x32_bf16 a[128:131], a[212:215], v[92:95], a[128:131]\n"
"v_mul_f32 v112, v112, v217\n"
"v_mul_f32 v113, v113, v217\n"
"v_mul_f32 v114, v114, v217\n"
"v_mfma_f32_16x16x32_bf16 a[128:131], a[244:247], v[84:87], a[128:131]\n"
"v_mul_f32 v115, v115, v217\n"
"v_cvt_pk_bf16_f32 v80, v80, v81\n"
"v_cvt_pk_bf16_f32 v81, v82, v83\n"
"v_cvt_pk_bf16_f32 v82, v112, v113\n"
"v_cvt_pk_bf16_f32 v83, v114, v115\n"
"buffer_store_dwordx4 v[80:83], v243, s[56:59], s68 offen offset:2048 sc1\n"
"v_mfma_f32_16x16x32_bf16 a[152:155], a[248:251], v[84:87], a[152:155]\n"
"v_accvgpr_read_b32 v88, a148\n"
"v_accvgpr_read_b32 v89, a149\n"
"v_accvgpr_read_b32 v90, a150\n"
"v_mfma_f32_16x16x32_bf16 a[152:155], a[216:219], v[92:95], a[152:155]\n"
"v_accvgpr_read_b32 v91, a151\n"
"v_accvgpr_read_b32 v120, a172\n"
"v_accvgpr_read_b32 v121, a173\n"
"v_mfma_f32_16x16x32_bf16 a[176:179], a[220:223], v[92:95], a[176:179]\n"
"v_accvgpr_read_b32 v122, a174\n"
"v_accvgpr_read_b32 v123, a175\n"
"v_mul_f32 v88, v88, v217\n"
"v_mfma_f32_16x16x32_bf16 a[176:179], a[252:255], v[84:87], a[176:179]\n"
"v_mul_f32 v89, v89, v217\n"
"v_mul_f32 v90, v90, v217\n"
"v_mul_f32 v91, v91, v217\n"
"v_mul_f32 v120, v120, v217\n"
"v_mul_f32 v121, v121, v217\n"
"v_mul_f32 v122, v122, v217\n"
"v_mul_f32 v123, v123, v217\n"
"v_cvt_pk_bf16_f32 v88, v88, v89\n"
"v_cvt_pk_bf16_f32 v89, v90, v91\n"
"v_cvt_pk_bf16_f32 v90, v120, v121\n"
"v_cvt_pk_bf16_f32 v91, v122, v123\n"
"buffer_store_dwordx4 v[88:91], v243, s[56:59], s68 offen offset:3072 sc1\n"
"s_cmp_lg_u32 s63, 0\n"
"s_cbranch_scc1 LO1_%=\n"
"ds_write_b128 v229, v[64:67] offset:1024\n"
"ds_write_b128 v229, v[72:75] offset:7168\n"
"ds_write_b128 v229, v[80:83] offset:13312\n"
"ds_write_b128 v229, v[88:91] offset:19456\n"
"LO1_%=:\n"
"s_add_u32 s68, s38, 0x40000\n"
"v_accvgpr_read_b32 v0, a8\n"
"v_accvgpr_read_b32 v1, a9\n"
"v_accvgpr_read_b32 v2, a10\n"
"v_accvgpr_read_b32 v3, a11\n"
"v_accvgpr_read_b32 v32, a32\n"
"v_accvgpr_read_b32 v33, a33\n"
"v_accvgpr_read_b32 v34, a34\n"
"v_accvgpr_read_b32 v35, a35\n"
"v_mul_f32 v0, v0, v218\n"
"v_mul_f32 v1, v1, v218\n"
"v_mul_f32 v2, v2, v218\n"
"v_mul_f32 v3, v3, v218\n"
"v_mul_f32 v32, v32, v218\n"
"v_mul_f32 v33, v33, v218\n"
"v_mul_f32 v34, v34, v218\n"
"v_mul_f32 v35, v35, v218\n"
"v_cvt_pk_bf16_f32 v0, v0, v1\n"
"v_cvt_pk_bf16_f32 v1, v2, v3\n"
"v_cvt_pk_bf16_f32 v2, v32, v33\n"
"v_cvt_pk_bf16_f32 v3, v34, v35\n"
"buffer_store_dwordx4 v[0:3], v243, s[56:59], s68 offen sc1\n"
"v_exp_f32 v100, v100\n"
"v_exp_f32 v101, v101\n"
"v_exp_f32 v102, v102\n"
"v_exp_f32 v103, v103\n"
"v_add_f32 v219, v219, v100\n"
"v_add_f32 v219, v219, v101\n"
"v_add_f32 v219, v219, v102\n"
"v_add_f32 v219, v219, v103\n"
"v_cvt_pk_bf16_f32 v124, v100, v101\n"
"v_cvt_pk_bf16_f32 v125, v102, v103\n"
"v_exp_f32 v108, v108\n"
"v_exp_f32 v109, v109\n"
"v_exp_f32 v110, v110\n"
"v_exp_f32 v111, v111\n"
"v_add_f32 v219, v219, v108\n"
"v_add_f32 v219, v219, v109\n"
"v_add_f32 v219, v219, v110\n"
"v_add_f32 v219, v219, v111\n"
"v_cvt_pk_bf16_f32 v126, v108, v109\n"
"v_cvt_pk_bf16_f32 v127, v110, v111\n"
"v_mov_b32 v131, v219\n"
"s_nop 1\n"
"v_permlane32_swap_b32 v219, v131\n"
"v_add_f32 v219, v219, v131\n"
"v_mov_b32 v131, v219\n"
"s_nop 1\n"
"v_permlane16_swap_b32 v219, v131\n"
"v_add_f32 v219, v219, v131\n"
"v_cmp_nge_f32 vcc, s72, v219\n"
"s_or_b64 s[74:75], s[74:75], vcc\n"
"v_cmp_nge_f32 vcc, s86, v219\n"
"s_or_b64 s[88:89], s[88:89], vcc\n"
"v_rcp_f32 v219, v219\n"
"s_nop 0\n"
"s_nop 0\n"
"v_mfma_f32_16x16x32_bf16 a[12:15], a[224:227], v[116:119], a[12:15]\n"
"v_accvgpr_read_b32 v8, a56\n"
"v_accvgpr_read_b32 v9, a57\n"
"v_accvgpr_read_b32 v10, a58\n"
"v_mfma_f32_16x16x32_bf16 a[12:15], a[192:195], v[124:127], a[12:15]\n"
"v_accvgpr_read_b32 v11, a59\n"
"v_accvgpr_read_b32 v40, a80\n"
"v_accvgpr_read_b32 v41, a81\n"
"v_mfma_f32_16x16x32_bf16 a[36:39], a[196:199], v[124:127], a[36:39]\n"
"v_accvgpr_read_b32 v42, a82\n"
"v_accvgpr_read_b32 v43, a83\n"
"v_mul_f32 v8, v8, v218\n"
"v_mfma_f32_16x16x32_bf16 a[36:39], a[228:231], v[116:119], a[36:39]\n"
"v_mul_f32 v9, v9, v218\n"
"v_mul_f32 v10, v10, v218\n"
"v_mul_f32 v11, v11, v218\n"
"v_mfma_f32_16x16x32_bf16 a[60:63], a[232:235], v[116:119], a[60:63]\n"
"v_mul_f32 v40, v40, v218\n"
"v_mul_f32 v41, v41, v218\n"
"v_mul_f32 v42, v42, v218\n"
"v_mfma_f32_16x16x32_bf16 a[60:63], a[200:203], v[124:127], a[60:63]\n"
"v_mul_f32 v43, v43, v218\n"
"v_cvt_pk_bf16_f32 v8, v8, v9\n"
"v_cvt_pk_bf16_f32 v9, v10, v11\n"
"v_cvt_pk_bf16_f32 v10, v40, v41\n"
"v_cvt_pk_bf16_f32 v11, v42, v43\n"
"buffer_store_dwordx4 v[8:11], v243, s[56:59], s68 offen offset:1024 sc1\n"
"v_mfma_f32_16x16x32_bf16 a[84:87], a[204:207], v[124:127], a[84:87]\n"
"v_accvgpr_read_b32 v16, a104\n"
"v_accvgpr_read_b32 v17, a105\n"
"v_accvgpr_read_b32 v18, a106\n"
"v_mfma_f32_16x16x32_bf16 a[84:87], a[236:239], v[116:119], a[84:87]\n"
"v_accvgpr_read_b32 v19, a107\n"
"v_accvgpr_read_b32 v48, a128\n"
"v_accvgpr_read_b32 v49, a129\n"
"v_mfma_f32_16x16x32_bf16 a[108:111], a[240:243], v[116:119], a[108:111]\n"
"v_accvgpr_read_b32 v50, a130\n"
"v_accvgpr_read_b32 v51, a131\n"
"v_mul_f32 v16, v16, v218\n"
"v_mfma_f32_16x16x32_bf16 a[108:111], a[208:211], v[124:127], a[108:111]\n"
"v_mul_f32 v17, v17, v218\n"
"v_mul_f32 v18, v18, v218\n"
"v_mul_f32 v19, v19, v218\n"
"v_mfma_f32_16x16x32_bf16 a[132:135], a[212:215], v[124:127], a[132:135]\n"
"v_mul_f32 v48, v48, v218\n"
"v_mul_f32 v49, v49, v218\n"
"v_mul_f32 v50, v50, v218\n"
"v_mfma_f32_16x16x32_bf16 a[132:135], a[244:247], v[116:119], a[132:135]\n"
"v_mul_f32 v51, v51, v218\n"
"v_cvt_pk_bf16_f32 v16, v16, v17\n"
"v_cvt_pk_bf16_f32 v17, v18, v19\n"
"v_cvt_pk_bf16_f32 v18, v48, v49\n"
"v_cvt_pk_bf16_f32 v19, v50, v51\n"
"buffer_store_dwordx4 v[16:19], v243, s[56:59], s68 offen offset:2048 sc1\n"
"v_mfma_f32_16x16x32_bf16 a[156:159], a[248:251], v[116:119], a[156:159]\n"
"v_accvgpr_read_b32 v24, a152\n"
"v_accvgpr_read_b32 v25, a153\n"
"v_accvgpr_read_b32 v26, a154\n"
"v_mfma_f32_16x16x32_bf16 a[156:159], a[216:219], v[124:127], a[156:159]\n"
"v_accvgpr_read_b32 v27, a155\n"
"v_accvgpr_read_b32 v56, a176\n"
"v_accvgpr_read_b32 v57, a177\n"
"v_mfma_f32_16x16x32_bf16 a[180:183], a[220:223], v[124:127], a[180:183]\n"
"v_accvgpr_read_b32 v58, a178\n"
"v_accvgpr_read_b32 v59, a179\n"
"v_mul_f32 v24, v24, v218\n"
"v_mfma_f32_16x16x32_bf16 a[180:183], a[252:255], v[116:119], a[180:183]\n"
"v_mul_f32 v25, v25, v218\n"
"v_mul_f32 v26, v26, v218\n"
"v_mul_f32 v27, v27, v218\n"
"v_mul_f32 v56, v56, v218\n"
"v_mul_f32 v57, v57, v218\n"
"v_mul_f32 v58, v58, v218\n"
"v_mul_f32 v59, v59, v218\n"
"v_cvt_pk_bf16_f32 v24, v24, v25\n"
"v_cvt_pk_bf16_f32 v25, v26, v27\n"
"v_cvt_pk_bf16_f32 v26, v56, v57\n"
"v_cvt_pk_bf16_f32 v27, v58, v59\n"
"buffer_store_dwordx4 v[24:27], v243, s[56:59], s68 offen offset:3072 sc1\n"
"s_cmp_lg_u32 s63, 0\n"
"s_cbranch_scc1 LO2_%=\n"
"ds_write_b128 v229, v[0:3] offset:2048\n"
"ds_write_b128 v229, v[8:11] offset:8192\n"
"ds_write_b128 v229, v[16:19] offset:14336\n"
"ds_write_b128 v229, v[24:27] offset:20480\n"
"LO2_%=:\n"
"s_add_u32 s68, s38, 0x60000\n"
"v_accvgpr_read_b32 v64, a12\n"
"v_accvgpr_read_b32 v65, a13\n"
"v_accvgpr_read_b32 v66, a14\n"
"v_accvgpr_read_b32 v67, a15\n"
"v_accvgpr_read_b32 v96, a36\n"
"v_accvgpr_read_b32 v97, a37\n"
"v_accvgpr_read_b32 v98, a38\n"
"v_accvgpr_read_b32 v99, a39\n"
"v_mul_f32 v64, v64, v219\n"
"v_mul_f32 v65, v65, v219\n"
"v_mul_f32 v66, v66, v219\n"
"v_mul_f32 v67, v67, v219\n"
"v_mul_f32 v96, v96, v219\n"
"v_mul_f32 v97, v97, v219\n"
"v_mul_f32 v98, v98, v219\n"
"v_mul_f32 v99, v99, v219\n"
"v_cvt_pk_bf16_f32 v64, v64, v65\n"
"v_cvt_pk_bf16_f32 v65, v66, v67\n"
"v_cvt_pk_bf16_f32 v66, v96, v97\n"
"v_cvt_pk_bf16_f32 v67, v98, v99\n"
"buffer_store_dwordx4 v[64:67], v243, s[56:59], s68 offen sc1\n"
"v_exp_f32 v132, v132\n"
"v_exp_f32 v133, v133\n"
"v_exp_f32 v134, v134\n"
"v_exp_f32 v135, v135\n"
"v_add_f32 v220, v220, v132\n"
"v_add_f32 v220, v220, v133\n"
"v_add_f32 v220, v220, v134\n"
"v_add_f32 v220, v220, v135\n"
"v_cvt_pk_bf16_f32 v156, v132, v133\n"
"v_cvt_pk_bf16_f32 v157, v134, v135\n"
"v_exp_f32 v140, v140\n"
"v_exp_f32 v141, v141\n"
"v_exp_f32 v142, v142\n"
"v_exp_f32 v143, v143\n"
"v_add_f32 v220, v220, v140\n"
"v_add_f32 v220, v220, v141\n"
"v_add_f32 v220, v220, v142\n"
"v_add_f32 v220, v220, v143\n"
"v_cvt_pk_bf16_f32 v158, v140, v141\n"
"v_cvt_pk_bf16_f32 v159, v142, v143\n"
"v_mov_b32 v136, v220\n"
"s_nop 1\n"
"v_permlane32_swap_b32 v220, v136\n"
"v_add_f32 v220, v220, v136\n"
"v_mov_b32 v136, v220\n"
"s_nop 1\n"
"v_permlane16_swap_b32 v220, v136\n"
"v_add_f32 v220, v220, v136\n"
"v_cmp_nge_f32 vcc, s72, v220\n"
"s_or_b64 s[74:75], s[74:75], vcc\n"
"v_cmp_nge_f32 vcc, s86, v220\n"
"s_or_b64 s[88:89], s[88:89], vcc\n"
"v_rcp_f32 v220, v220\n"
"s_nop 0\n"
"s_nop 0\n"
"v_mfma_f32_16x16x32_bf16 a[16:19], a[224:227], v[148:151], a[16:19]\n"
"v_accvgpr_read_b32 v72, a60\n"
"v_accvgpr_read_b32 v73, a61\n"
"v_accvgpr_read_b32 v74, a62\n"
"v_mfma_f32_16x16x32_bf16 a[16:19], a[192:195], v[156:159], a[16:19]\n"
"v_accvgpr_read_b32 v75, a63\n"
"v_accvgpr_read_b32 v104, a84\n"
"v_accvgpr_read_b32 v105, a85\n"
"v_mfma_f32_16x16x32_bf16 a[40:43], a[196:199], v[156:159], a[40:43]\n"
"v_accvgpr_read_b32 v106, a86\n"
"v_accvgpr_read_b32 v107, a87\n"
"v_mul_f32 v72, v72, v219\n"
"v_mfma_f32_16x16x32_bf16 a[40:43], a[228:231], v[148:151], a[40:43]\n"
"v_mul_f32 v73, v73, v219\n"
"v_mul_f32 v74, v74, v219\n"
"v_mul_f32 v75, v75, v219\n"
"v_mfma_f32_16x16x32_bf16 a[64:67], a[232:235], v[148:151], a[64:67]\n"
"v_mul_f32 v104, v104, v219\n"
"v_mul_f32 v105, v105, v219\n"
"v_mul_f32 v106, v106, v219\n"
"v_mfma_f32_16x16x32_bf16 a[64:67], a[200:203], v[156:159], a[64:67]\n"
"v_mul_f32 v107, v107, v219\n"
"v_cvt_pk_bf16_f32 v72, v72, v73\n"
"v_cvt_pk_bf16_f32 v73, v74, v75\n"
"v_cvt_pk_bf16_f32 v74, v104, v105\n"
"v_cvt_pk_bf16_f32 v75, v106, v107\n"
"buffer_store_dwordx4 v[72:75], v243, s[56:59], s68 offen offset:1024 sc1\n"
"v_mfma_f32_16x16x32_bf16 a[88:91], a[204:207], v[156:159], a[88:91]\n"
"v_accvgpr_read_b32 v80, a108\n"
"v_accvgpr_read_b32 v81, a109\n"
"v_accvgpr_read_b32 v82, a110\n"
"v_mfma_f32_16x16x32_bf16 a[88:91], a[236:239], v[148:151], a[88:91]\n"
"v_accvgpr_read_b32 v83, a111\n"
"v_accvgpr_read_b32 v112, a132\n"
"v_accvgpr_read_b32 v113, a133\n"
"v_mfma_f32_16x16x32_bf16 a[112:115], a[240:243], v[148:151], a[112:115]\n"
"v_accvgpr_read_b32 v114, a134\n"
"v_accvgpr_read_b32 v115, a135\n"
"v_mul_f32 v80, v80, v219\n"
"v_mfma_f32_16x16x32_bf16 a[112:115], a[208:211], v[156:159], a[112:115]\n"
"v_mul_f32 v81, v81, v219\n"
"v_mul_f32 v82, v82, v219\n"
"v_mul_f32 v83, v83, v219\n"
"v_mfma_f32_16x16x32_bf16 a[136:139], a[212:215], v[156:159], a[136:139]\n"
"v_mul_f32 v112, v112, v219\n"
"v_mul_f32 v113, v113, v219\n"
"v_mul_f32 v114, v114, v219\n"
"v_mfma_f32_16x16x32_bf16 a[136:139], a[244:247], v[148:151], a[136:139]\n"
"v_mul_f32 v115, v115, v219\n"
"v_cvt_pk_bf16_f32 v80, v80, v81\n"
"v_cvt_pk_bf16_f32 v81, v82, v83\n"
"v_cvt_pk_bf16_f32 v82, v112, v113\n"
"v_cvt_pk_bf16_f32 v83, v114, v115\n"
"buffer_store_dwordx4 v[80:83], v243, s[56:59], s68 offen offset:2048 sc1\n"
"v_mfma_f32_16x16x32_bf16 a[160:163], a[248:251], v[148:151], a[160:163]\n"
"v_accvgpr_read_b32 v88, a156\n"
"v_accvgpr_read_b32 v89, a157\n"
"v_accvgpr_read_b32 v90, a158\n"
"v_mfma_f32_16x16x32_bf16 a[160:163], a[216:219], v[156:159], a[160:163]\n"
"v_accvgpr_read_b32 v91, a159\n"
"v_accvgpr_read_b32 v120, a180\n"
"v_accvgpr_read_b32 v121, a181\n"
"v_mfma_f32_16x16x32_bf16 a[184:187], a[220:223], v[156:159], a[184:187]\n"
"v_accvgpr_read_b32 v122, a182\n"
"v_accvgpr_read_b32 v123, a183\n"
"v_mul_f32 v88, v88, v219\n"
"v_mfma_f32_16x16x32_bf16 a[184:187], a[252:255], v[148:151], a[184:187]\n"
"v_mul_f32 v89, v89, v219\n"
"v_mul_f32 v90, v90, v219\n"
"v_mul_f32 v91, v91, v219\n"
"v_mul_f32 v120, v120, v219\n"
"v_mul_f32 v121, v121, v219\n"
"v_mul_f32 v122, v122, v219\n"
"v_mul_f32 v123, v123, v219\n"
"v_cvt_pk_bf16_f32 v88, v88, v89\n"
"v_cvt_pk_bf16_f32 v89, v90, v91\n"
"v_cvt_pk_bf16_f32 v90, v120, v121\n"
"v_cvt_pk_bf16_f32 v91, v122, v123\n"
"buffer_store_dwordx4 v[88:91], v243, s[56:59], s68 offen offset:3072 sc1\n"
"s_cmp_lg_u32 s63, 0\n"
"s_cbranch_scc1 LO3_%=\n"
"ds_write_b128 v229, v[64:67] offset:3072\n"
"ds_write_b128 v229, v[72:75] offset:9216\n"
"ds_write_b128 v229, v[80:83] offset:15360\n"
"ds_write_b128 v229, v[88:91] offset:21504\n"
"LO3_%=:\n"
"s_add_u32 s68, s38, 0x80000\n"
"v_accvgpr_read_b32 v0, a16\n"
"v_accvgpr_read_b32 v1, a17\n"
"v_accvgpr_read_b32 v2, a18\n"
"v_accvgpr_read_b32 v3, a19\n"
"v_accvgpr_read_b32 v32, a40\n"
"v_accvgpr_read_b32 v33, a41\n"
"v_accvgpr_read_b32 v34, a42\n"
"v_accvgpr_read_b32 v35, a43\n"
"v_mul_f32 v0, v0, v220\n"
"v_mul_f32 v1, v1, v220\n"
"v_mul_f32 v2, v2, v220\n"
"v_mul_f32 v3, v3, v220\n"
"v_mul_f32 v32, v32, v220\n"
"v_mul_f32 v33, v33, v220\n"
"v_mul_f32 v34, v34, v220\n"
"v_mul_f32 v35, v35, v220\n"
"v_cvt_pk_bf16_f32 v0, v0, v1\n"
"v_cvt_pk_bf16_f32 v1, v2, v3\n"
"v_cvt_pk_bf16_f32 v2, v32, v33\n"
"v_cvt_pk_bf16_f32 v3, v34, v35\n"
"buffer_store_dwordx4 v[0:3], v243, s[56:59], s68 offen sc1\n"
"v_exp_f32 v164, v164\n"
"v_exp_f32 v165, v165\n"
"v_exp_f32 v166, v166\n"
"v_exp_f32 v167, v167\n"
"v_add_f32 v221, v221, v164\n"
"v_add_f32 v221, v221, v165\n"
"v_add_f32 v221, v221, v166\n"
"v_add_f32 v221, v221, v167\n"
"v_cvt_pk_bf16_f32 v188, v164, v165\n"
"v_cvt_pk_bf16_f32 v189, v166, v167\n"
"v_exp_f32 v172, v172\n"
"v_exp_f32 v173, v173\n"
"v_exp_f32 v174, v174\n"
"v_exp_f32 v175, v175\n"
"v_add_f32 v221, v221, v172\n"
"v_add_f32 v221, v221, v173\n"
"v_add_f32 v221, v221, v174\n"
"v_add_f32 v221, v221, v175\n"
"v_cvt_pk_bf16_f32 v190, v172, v173\n"
"v_cvt_pk_bf16_f32 v191, v174, v175\n"
"v_mov_b32 v137, v221\n"
"s_nop 1\n"
"v_permlane32_swap_b32 v221, v137\n"
"v_add_f32 v221, v221, v137\n"
"v_mov_b32 v137, v221\n"
"s_nop 1\n"
"v_permlane16_swap_b32 v221, v137\n"
"v_add_f32 v221, v221, v137\n"
"v_cmp_nge_f32 vcc, s72, v221\n"
"s_or_b64 s[74:75], s[74:75], vcc\n"
"v_cmp_nge_f32 vcc, s86, v221\n"
"s_or_b64 s[88:89], s[88:89], vcc\n"
"v_rcp_f32 v221, v221\n"
"s_nop 0\n"
"s_nop 0\n"
"v_mfma_f32_16x16x32_bf16 a[20:23], a[224:227], v[180:183], a[20:23]\n"
"v_accvgpr_read_b32 v8, a64\n"
"v_accvgpr_read_b32 v9, a65\n"
"v_accvgpr_read_b32 v10, a66\n"
"v_mfma_f32_16x16x32_bf16 a[20:23], a[192:195], v[188:191], a[20:23]\n"
"v_accvgpr_read_b32 v11, a67\n"
"v_accvgpr_read_b32 v40, a88\n"
"v_accvgpr_read_b32 v41, a89\n"
"v_mfma_f32_16x16x32_bf16 a[44:47], a[196:199], v[188:191], a[44:47]\n"
"v_accvgpr_read_b32 v42, a90\n"
"v_accvgpr_read_b32 v43, a91\n"
"v_mul_f32 v8, v8, v220\n"
"v_mfma_f32_16x16x32_bf16 a[44:47], a[228:231], v[180:183], a[44:47]\n"
"v_mul_f32 v9, v9, v220\n"
"v_mul_f32 v10, v10, v220\n"
"v_mul_f32 v11, v11, v220\n"
"v_mfma_f32_16x16x32_bf16 a[68:71], a[232:235], v[180:183], a[68:71]\n"
"v_mul_f32 v40, v40, v220\n"
"v_mul_f32 v41, v41, v220\n"
"v_mul_f32 v42, v42, v220\n"
"v_mfma_f32_16x16x32_bf16 a[68:71], a[200:203], v[188:191], a[68:71]\n"
"v_mul_f32 v43, v43, v220\n"
"v_cvt_pk_bf16_f32 v8, v8, v9\n"
"v_cvt_pk_bf16_f32 v9, v10, v11\n"
"v_cvt_pk_bf16_f32 v10, v40, v41\n"
"v_cvt_pk_bf16_f32 v11, v42, v43\n"
"buffer_store_dwordx4 v[8:11], v243, s[56:59], s68 offen offset:1024 sc1\n"
"v_mfma_f32_16x16x32_bf16 a[92:95], a[204:207], v[188:191], a[92:95]\n"
"v_accvgpr_read_b32 v16, a112\n"
"v_accvgpr_read_b32 v17, a113\n"
"v_accvgpr_read_b32 v18, a114\n"
"v_mfma_f32_16x16x32_bf16 a[92:95], a[236:239], v[180:183], a[92:95]\n"
"v_accvgpr_read_b32 v19, a115\n"
"v_accvgpr_read_b32 v48, a136\n"
"v_accvgpr_read_b32 v49, a137\n"
"v_mfma_f32_16x16x32_bf16 a[116:119], a[240:243], v[180:183], a[116:119]\n"
"v_accvgpr_read_b32 v50, a138\n"
"v_accvgpr_read_b32 v51, a139\n"
"v_mul_f32 v16, v16, v220\n"
"v_mfma_f32_16x16x32_bf16 a[116:119], a[208:211], v[188:191], a[116:119]\n"
"v_mul_f32 v17, v17, v220\n"
"v_mul_f32 v18, v18, v220\n"
"v_mul_f32 v19, v19, v220\n"
"v_mfma_f32_16x16x32_bf16 a[140:143], a[212:215], v[188:191], a[140:143]\n"
"v_mul_f32 v48, v48, v220\n"
"v_mul_f32 v49, v49, v220\n"
"v_mul_f32 v50, v50, v220\n"
"v_mfma_f32_16x16x32_bf16 a[140:143], a[244:247], v[180:183], a[140:143]\n"
"v_mul_f32 v51, v51, v220\n"
"v_cvt_pk_bf16_f32 v16, v16, v17\n"
"v_cvt_pk_bf16_f32 v17, v18, v19\n"
"v_cvt_pk_bf16_f32 v18, v48, v49\n"
"v_cvt_pk_bf16_f32 v19, v50, v51\n"
"buffer_store_dwordx4 v[16:19], v243, s[56:59], s68 offen offset:2048 sc1\n"
"v_mfma_f32_16x16x32_bf16 a[164:167], a[248:251], v[180:183], a[164:167]\n"
"v_accvgpr_read_b32 v24, a160\n"
"v_accvgpr_read_b32 v25, a161\n"
"v_accvgpr_read_b32 v26, a162\n"
"v_mfma_f32_16x16x32_bf16 a[164:167], a[216:219], v[188:191], a[164:167]\n"
"v_accvgpr_read_b32 v27, a163\n"
"v_accvgpr_read_b32 v56, a184\n"
"v_accvgpr_read_b32 v57, a185\n"
"v_mfma_f32_16x16x32_bf16 a[188:191], a[220:223], v[188:191], a[188:191]\n"
"v_accvgpr_read_b32 v58, a186\n"
"v_accvgpr_read_b32 v59, a187\n"
"v_mul_f32 v24, v24, v220\n"
"v_mfma_f32_16x16x32_bf16 a[188:191], a[252:255], v[180:183], a[188:191]\n"
"v_mul_f32 v25, v25, v220\n"
"v_mul_f32 v26, v26, v220\n"
"v_mul_f32 v27, v27, v220\n"
"v_mul_f32 v56, v56, v220\n"
"v_mul_f32 v57, v57, v220\n"
"v_mul_f32 v58, v58, v220\n"
"v_mul_f32 v59, v59, v220\n"
"v_cvt_pk_bf16_f32 v24, v24, v25\n"
"v_cvt_pk_bf16_f32 v25, v26, v27\n"
"v_cvt_pk_bf16_f32 v26, v56, v57\n"
"v_cvt_pk_bf16_f32 v27, v58, v59\n"
"buffer_store_dwordx4 v[24:27], v243, s[56:59], s68 offen offset:3072 sc1\n"
"s_cmp_lg_u32 s63, 0\n"
"s_cbranch_scc1 LO4_%=\n"
"ds_write_b128 v229, v[0:3] offset:4096\n"
"ds_write_b128 v229, v[8:11] offset:10240\n"
"ds_write_b128 v229, v[16:19] offset:16384\n"
"ds_write_b128 v229, v[24:27] offset:22528\n"
"LO4_%=:\n"
"s_add_u32 s68, s38, 0xa0000\n"
"v_accvgpr_read_b32 v64, a20\n"
"v_accvgpr_read_b32 v65, a21\n"
"v_accvgpr_read_b32 v66, a22\n"
"v_accvgpr_read_b32 v67, a23\n"
"v_accvgpr_read_b32 v96, a44\n"
"v_accvgpr_read_b32 v97, a45\n"
"v_accvgpr_read_b32 v98, a46\n"
"v_accvgpr_read_b32 v99, a47\n"
"v_mul_f32 v64, v64, v221\n"
"v_mul_f32 v65, v65, v221\n"
"v_mul_f32 v66, v66, v221\n"
"v_mul_f32 v67, v67, v221\n"
"v_mul_f32 v96, v96, v221\n"
"v_mul_f32 v97, v97, v221\n"
"v_mul_f32 v98, v98, v221\n"
"v_mul_f32 v99, v99, v221\n"
"v_cvt_pk_bf16_f32 v64, v64, v65\n"
"v_cvt_pk_bf16_f32 v65, v66, v67\n"
"v_cvt_pk_bf16_f32 v66, v96, v97\n"
"v_cvt_pk_bf16_f32 v67, v98, v99\n"
"buffer_store_dwordx4 v[64:67], v243, s[56:59], s68 offen sc1\n"
"ds_read_b64_tr_b16 a[192:193], v226 offset:0\n"
"ds_read_b64_tr_b16 a[194:195], v226 offset:2048\n"
"ds_read_b64_tr_b16 a[196:197], v226 offset:4096\n"
"ds_read_b64_tr_b16 a[198:199], v226 offset:6144\n"
"ds_read_b64_tr_b16 a[200:201], v226 offset:8192\n"
"ds_read_b64_tr_b16 a[202:203], v226 offset:10240\n"
"ds_read_b64_tr_b16 a[204:205], v226 offset:12288\n"
"ds_read_b64_tr_b16 a[206:207], v226 offset:14336\n"
"v_accvgpr_read_b32 v72, a68\n"
"v_accvgpr_read_b32 v73, a69\n"
"v_accvgpr_read_b32 v74, a70\n"
"v_accvgpr_read_b32 v75, a71\n"
"v_accvgpr_read_b32 v104, a92\n"
"v_accvgpr_read_b32 v105, a93\n"
"v_accvgpr_read_b32 v106, a94\n"
"v_accvgpr_read_b32 v107, a95\n"
"v_mul_f32 v72, v72, v221\n"
"v_mul_f32 v73, v73, v221\n"
"v_mul_f32 v74, v74, v221\n"
"v_mul_f32 v75, v75, v221\n"
"v_mul_f32 v104, v104, v221\n"
"v_mul_f32 v105, v105, v221\n"
"v_mul_f32 v106, v106, v221\n"
"v_mul_f32 v107, v107, v221\n"
"v_cvt_pk_bf16_f32 v72, v72, v73\n"
"v_cvt_pk_bf16_f32 v73, v74, v75\n"
"v_cvt_pk_bf16_f32 v74, v104, v105\n"
"v_cvt_pk_bf16_f32 v75, v106, v107\n"
"buffer_store_dwordx4 v[72:75], v243, s[56:59], s68 offen offset:1024 sc1\n"
"v_accvgpr_read_b32 v80, a116\n"
"v_accvgpr_read_b32 v81, a117\n"
"v_accvgpr_read_b32 v82, a118\n"
"v_accvgpr_read_b32 v83, a119\n"
"v_accvgpr_read_b32 v112, a140\n"
"v_accvgpr_read_b32 v113, a141\n"
"v_accvgpr_read_b32 v114, a142\n"
"v_accvgpr_read_b32 v115, a143\n"
"v_mul_f32 v80, v80, v221\n"
"v_mul_f32 v81, v81, v221\n"
"v_mul_f32 v82, v82, v221\n"
"v_mul_f32 v83, v83, v221\n"
"v_mul_f32 v112, v112, v221\n"
"v_mul_f32 v113, v113, v221\n"
"v_mul_f32 v114, v114, v221\n"
"v_mul_f32 v115, v115, v221\n"
"v_cvt_pk_bf16_f32 v80, v80, v81\n"
"v_cvt_pk_bf16_f32 v81, v82, v83\n"
"v_cvt_pk_bf16_f32 v82, v112, v113\n"
"v_cvt_pk_bf16_f32 v83, v114, v115\n"
"buffer_store_dwordx4 v[80:83], v243, s[56:59], s68 offen offset:2048 sc1\n"
"v_accvgpr_read_b32 v88, a164\n"
"v_accvgpr_read_b32 v89, a165\n"
"v_accvgpr_read_b32 v90, a166\n"
"v_accvgpr_read_b32 v91, a167\n"
"v_accvgpr_read_b32 v120, a188\n"
"v_accvgpr_read_b32 v121, a189\n"
"v_accvgpr_read_b32 v122, a190\n"
"v_accvgpr_read_b32 v123, a191\n"
"v_mul_f32 v88, v88, v221\n"
"v_mul_f32 v89, v89, v221\n"
"v_mul_f32 v90, v90, v221\n"
"v_mul_f32 v91, v91, v221\n"
"v_mul_f32 v120, v120, v221\n"
"v_mul_f32 v121, v121, v221\n"
"v_mul_f32 v122, v122, v221\n"
"v_mul_f32 v123, v123, v221\n"
"v_cvt_pk_bf16_f32 v88, v88, v89\n"
"v_cvt_pk_bf16_f32 v89, v90, v91\n"
"v_cvt_pk_bf16_f32 v90, v120, v121\n"
"v_cvt_pk_bf16_f32 v91, v122, v123\n"
"buffer_store_dwordx4 v[88:91], v243, s[56:59], s68 offen offset:3072 sc1\n"
"s_cmp_lg_u32 s63, 0\n"
"s_cbranch_scc1 LO5_%=\n"
"ds_write_b128 v229, v[64:67] offset:5120\n"
"ds_write_b128 v229, v[72:75] offset:11264\n"
"ds_write_b128 v229, v[80:83] offset:17408\n"
"ds_write_b128 v229, v[88:91] offset:23552\n"
"LO5_%=:\n"
"s_cmp_lg_u64 s[88:89], 0\n"
"s_cbranch_scc1 LBIG%=\n"
"LBIGR%=:\n"
"LCOMMON%=:\n"
"s_waitcnt lgkmcnt(0)\n"
"s_waitcnt lgkmcnt(0)\n"
"s_barrier\n"
"ds_read_b128 a[0:3], v227 offset:0\n"
"ds_read_b128 a[24:27], v227 offset:6144\n"
"ds_read_b128 a[48:51], v227 offset:12288\n"
"ds_read_b128 a[72:75], v227 offset:18432\n"
"ds_read_b128 a[4:7], v227 offset:1024\n"
"ds_read_b128 a[28:31], v227 offset:7168\n"
"ds_read_b128 a[52:55], v227 offset:13312\n"
"ds_read_b128 a[76:79], v227 offset:19456\n"
"ds_read_b128 a[8:11], v227 offset:2048\n"
"ds_read_b128 a[32:35], v227 offset:8192\n"
"ds_read_b128 a[56:59], v227 offset:14336\n"
"ds_read_b128 a[80:83], v227 offset:20480\n"
"ds_read_b128 a[12:15], v227 offset:3072\n"
"ds_read_b128 a[36:39], v227 offset:9216\n"
"ds_read_b128 a[60:63], v227 offset:15360\n"
"ds_read_b128 a[84:87], v227 offset:21504\n"
"ds_read_b128 a[16:19], v227 offset:4096\n"
"ds_read_b128 a[40:43], v227 offset:10240\n"
"ds_read_b128 a[64:67], v227 offset:16384\n"
"ds_read_b128 a[88:91], v227 offset:22528\n"
"ds_read_b128 a[20:23], v227 offset:5120\n"
"ds_read_b128 a[44:47], v227 offset:11264\n"
"ds_read_b128 a[68:71], v227 offset:17408\n"
"ds_read_b128 a[92:95], v227 offset:23552\n"
"s_waitcnt lgkmcnt(15)\n"
"v_mfma_f32_16x16x32_bf16 v[0:3], a[192:195], a[0:3], 0\n"
"v_mfma_f32_16x16x32_bf16 v[0:3], a[196:199], a[24:27], v[0:3]\n"
"v_mfma_f32_16x16x32_bf16 v[0:3], a[200:203], a[48:51], v[0:3]\n"
"v_mfma_f32_16x16x32_bf16 v[0:3], a[204:207], a[72:75], v[0:3]\n"
"v_mfma_f32_16x16x32_bf16 v[4:7], a[204:207], a[76:79], 0\n"
"v_mfma_f32_16x16x32_bf16 v[4:7], a[200:203], a[52:55], v[4:7]\n"
"v_mfma_f32_16x16x32_bf16 v[4:7], a[196:199], a[28:31], v[4:7]\n"
"v_mfma_f32_16x16x32_bf16 v[4:7], a[192:195], a[4:7], v[4:7]\n"
"s_waitcnt lgkmcnt(12)\n"
"v_mfma_f32_16x16x32_bf16 v[8:11], a[192:195], a[8:11], 0\n"
"v_mfma_f32_16x16x32_bf16 v[8:11], a[196:199], a[32:35], v[8:11]\n"
"v_mfma_f32_16x16x32_bf16 v[8:11], a[200:203], a[56:59], v[8:11]\n"
"s_barrier\n"
"ds_read_b64_tr_b16 a[208:209], v226 offset:16384\n"
"v_mfma_f32_16x16x32_bf16 v[8:11], a[204:207], a[80:83], v[8:11]\n"
"s_add_u32 s87, s87, 0x28000\n"
"s_cmp_eq_u32 s87, 0x140000\n"
"s_cselect_b32 s87, 0, s87\n"
"s_add_u32 s90, s87, 0x200\n"
"s_waitcnt lgkmcnt(9)\n"
"ds_read_b64_tr_b16 a[210:211], v226 offset:18432\n"
"v_mfma_f32_16x16x32_bf16 v[12:15], a[204:207], a[84:87], 0\n"
"s_add_u32 m0, s91, 0x20000\n"
"s_nop 0\n"
"buffer_load_dwordx4 v245, s[92:95], s90 offen lds\n"
"v_mfma_f32_16x16x32_bf16 v[12:15], a[200:203], a[60:63], v[12:15]\n"
"ds_read_b64_tr_b16 a[212:213], v226 offset:20480\n"
"v_mfma_f32_16x16x32_bf16 v[12:15], a[196:199], a[36:39], v[12:15]\n"
"ds_read_b64_tr_b16 a[214:215], v226 offset:22528\n"
"v_mfma_f32_16x16x32_bf16 v[12:15], a[192:195], a[12:15], v[12:15]\n"
"s_waitcnt lgkmcnt(8)\n"
"ds_read_b64_tr_b16 a[216:217], v226 offset:24576\n"
"v_mfma_f32_16x16x32_bf16 v[16:19], a[192:195], a[16:19], 0\n"
"buffer_load_dwordx4 v251, s[92:95], s90 offen offset:1024 lds\n"
"v_mfma_f32_16x16x32_bf16 v[16:19], a[196:199], a[40:43], v[16:19]\n"
"ds_read_b64_tr_b16 a[218:219], v226 offset:26624\n"
"v_mfma_f32_16x16x32_bf16 v[16:19], a[200:203], a[64:67], v[16:19]\n"
"ds_read_b64_tr_b16 a[220:221], v226 offset:28672\n"
"v_mfma_f32_16x16x32_bf16 v[16:19], a[204:207], a[88:91], v[16:19]\n"
"s_waitcnt lgkmcnt(7)\n"
"ds_read_b64_tr_b16 a[222:223], v226 offset:30720\n"
"v_mfma_f32_16x16x32_bf16 v[20:23], a[204:207], a[92:95], 0\n"
"buffer_load_dwordx4 v252, s[92:95], s90 offen offset:2048 lds\n"
"v_mfma_f32_16x16x32_bf16 v[20:23], a[200:203], a[68:71], v[20:23]\n"
"v_mfma_f32_16x16x32_bf16 v[20:23], a[196:199], a[44:47], v[20:23]\n"
"s_waitcnt lgkmcnt(4)\n"
"v_mfma_f32_16x16x32_bf16 v[20:23], a[192:195], a[20:23], v[20:23]\n"
"s_waitcnt vmcnt(3)\n"
"v_mov_b32 v246, s60\n"
"v_mov_b32 v247, s61\n"
"s_mov_b64 exec, 1\n"
"buffer_store_dwordx2 v[246:247], v239, s[56:59], s64 offen sc1\n"
"s_mov_b64 exec, -1\n"
"v_mfma_f32_16x16x32_bf16 v[24:27], a[208:211], a[0:3], 0\n"
"s_add_u32 m0, s91, 0x20000\n"
"s_nop 0\n"
"buffer_load_dwordx4 v253, s[92:95], s90 offen offset:3072 lds\n"
"s_waitcnt lgkmcnt(2)\n"
"v_mfma_f32_16x16x32_bf16 v[24:27], a[212:215], a[24:27], v[24:27]\n"
"s_waitcnt lgkmcnt(0)\n"
"v_mfma_f32_16x16x32_bf16 v[24:27], a[216:219], a[48:51], v[24:27]\n"
"v_mfma_f32_16x16x32_bf16 v[24:27], a[220:223], a[72:75], v[24:27]\n"
"v_mfma_f32_16x16x32_bf16 v[28:31], a[220:223], a[76:79], 0\n"
"v_mfma_f32_16x16x32_bf16 v[28:31], a[216:219], a[52:55], v[28:31]\n"
"v_mfma_f32_16x16x32_bf16 v[28:31], a[212:215], a[28:31], v[28:31]\n"
"v_mfma_f32_16x16x32_bf16 v[28:31], a[208:211], a[4:7], v[28:31]\n"
"v_mfma_f32_16x16x32_bf16 v[32:35], a[208:211], a[8:11], 0\n"
"v_mfma_f32_16x16x32_bf16 v[32:35], a[212:215], a[32:35], v[32:35]\n"
"v_mfma_f32_16x16x32_bf16 v[32:35], a[216:219], a[56:59], v[32:35]\n"
"s_barrier\n"
"ds_read_b64_tr_b16 a[192:193], v224 offset:0\n"
"v_mfma_f32_16x16x32_bf16 v[32:35], a[220:223], a[80:83], v[32:35]\n"
"s_add_u32 s90, s87, s41\n"
"s_add_u32 m0, s91, 0x24000\n"
"ds_read_b64_tr_b16 a[194:195], v224 offset:2048\n"
"v_mfma_f32_16x16x32_bf16 v[36:39], a[220:223], a[84:87], 0\n"
"buffer_load_dwordx4 v245, s[92:95], s90 offen lds\n"
"v_mfma_f32_16x16x32_bf16 v[36:39], a[216:219], a[60:63], v[36:39]\n"
"ds_read_b64_tr_b16 a[196:197], v224 offset:4096\n"
"v_mfma_f32_16x16x32_bf16 v[36:39], a[212:215], a[36:39], v[36:39]\n"
"ds_read_b64_tr_b16 a[198:199], v224 offset:6144\n"
"v_mfma_f32_16x16x32_bf16 v[36:39], a[208:211], a[12:15], v[36:39]\n"
"ds_read_b64_tr_b16 a[200:201], v224 offset:8192\n"
"v_mfma_f32_16x16x32_bf16 v[40:43], a[208:211], a[16:19], 0\n"
"buffer_load_dwordx4 v251, s[92:95], s90 offen offset:1024 lds\n"
"v_mfma_f32_16x16x32_bf16 v[40:43], a[212:215], a[40:43], v[40:43]\n"
"ds_read_b64_tr_b16 a[202:203], v224 offset:10240\n"
"v_mfma_f32_16x16x32_bf16 v[40:43], a[216:219], a[64:67], v[40:43]\n"
"ds_read_b64_tr_b16 a[204:205], v224 offset:12288\n"
"v_mfma_f32_16x16x32_bf16 v[40:43], a[220:223], a[88:91], v[40:43]\n"
"ds_read_b64_tr_b16 a[206:207], v224 offset:14336\n"
"v_mfma_f32_16x16x32_bf16 v[44:47], a[220:223], a[92:95], 0\n"
"buffer_load_dwordx4 v252, s[92:95], s90 offen offset:2048 lds\n"
"v_mfma_f32_16x16x32_bf16 v[44:47], a[216:219], a[68:71], v[44:47]\n"
"v_mfma_f32_16x16x32_bf16 v[44:47], a[212:215], a[44:47], v[44:47]\n"
"s_waitcnt lgkmcnt(4)\n"
"v_mfma_f32_16x16x32_bf16 v[44:47], a[208:211], a[20:23], v[44:47]\n"
"v_mfma_f32_16x16x32_bf16 v[48:51], a[192:195], a[0:3], 0\n"
"s_add_u32 m0, s91, 0x24000\n"
"s_nop 0\n"
"buffer_load_dwordx4 v253, s[92:95], s90 offen offset:3072 lds\n"
"s_waitcnt lgkmcnt(2)\n"
"v_mfma_f32_16x16x32_bf16 v[48:51], a[196:199], a[24:27], v[48:51]\n"
"s_waitcnt lgkmcnt(0)\n"
"ds_read_b128 a[96:99], v228 offset:0\n"
"v_mfma_f32_16x16x32_bf16 v[48:51], a[200:203], a[48:51], v[48:51]\n"
"ds_read_b128 a[120:123], v228 offset:6144\n"
"v_mfma_f32_16x16x32_bf16 v[48:51], a[204:207], a[72:75], v[48:51]\n"
"ds_read_b128 a[144:147], v228 offset:12288\n"
"v_mfma_f32_16x16x32_bf16 v[52:55], a[204:207], a[76:79], 0\n"
"v_mfma_f32_16x16x32_bf16 v[52:55], a[200:203], a[52:55], v[52:55]\n"
"ds_read_b128 a[168:171], v228 offset:18432\n"
"v_mfma_f32_16x16x32_bf16 v[52:55], a[196:199], a[28:31], v[52:55]\n"
"ds_read_b128 a[100:103], v228 offset:1024\n"
"v_mfma_f32_16x16x32_bf16 v[52:55], a[192:195], a[4:7], v[52:55]\n"
"ds_read_b128 a[124:127], v228 offset:7168\n"
"v_mfma_f32_16x16x32_bf16 v[56:59], a[192:195], a[8:11], 0\n"
"v_mfma_f32_16x16x32_bf16 v[56:59], a[196:199], a[32:35], v[56:59]\n"
"ds_read_b128 a[148:151], v228 offset:13312\n"
"v_mfma_f32_16x16x32_bf16 v[56:59], a[200:203], a[56:59], v[56:59]\n"
"s_barrier\n"
"ds_read_b64_tr_b16 a[208:209], v225 offset:0\n"
"v_mfma_f32_16x16x32_bf16 v[56:59], a[204:207], a[80:83], v[56:59]\n"
"s_add_u32 s90, s87, s42\n"
"s_add_u32 m0, s91, 0x0\n"
"ds_read_b64_tr_b16 a[210:211], v225 offset:2048\n"
"v_mfma_f32_16x16x32_bf16 v[60:63], a[204:207], a[84:87], 0\n"
"buffer_load_dwordx4 v245, s[92:95], s90 offen lds\n"
"v_mfma_f32_16x16x32_bf16 v[60:63], a[200:203], a[60:63], v[60:63]\n"
"ds_read_b64_tr_b16 a[212:213], v225 offset:4096\n"
"v_mfma_f32_16x16x32_bf16 v[60:63], a[196:199], a[36:39], v[60:63]\n"
"ds_read_b64_tr_b16 a[214:215], v225 offset:6144\n"
"v_mfma_f32_16x16x32_bf16 v[60:63], a[192:195], a[12:15], v[60:63]\n"
"ds_read_b64_tr_b16 a[216:217], v225 offset:8192\n"
"v_mfma_f32_16x16x32_bf16 v[64:67], a[192:195], a[16:19], 0\n"
"buffer_load_dwordx4 v251, s[92:95], s90 offen offset:1024 lds\n"
"v_mfma_f32_16x16x32_bf16 v[64:67], a[196:199], a[40:43], v[64:67]\n"
"ds_read_b64_tr_b16 a[218:219], v225 offset:10240\n"
"v_mfma_f32_16x16x32_bf16 v[64:67], a[200:203], a[64:67], v[64:67]\n"
"ds_read_b64_tr_b16 a[220:221], v225 offset:12288\n"
"v_mfma_f32_16x16x32_bf16 v[64:67], a[204:207], a[88:91], v[64:67]\n"
"ds_read_b64_tr_b16 a[222:223], v225 offset:14336\n"
"v_mfma_f32_16x16x32_bf16 v[68:71], a[204:207], a[92:95], 0\n"
"buffer_load_dwordx4 v252, s[92:95], s90 offen offset:2048 lds\n"
"v_mfma_f32_16x16x32_bf16 v[68:71], a[200:203], a[68:71], v[68:71]\n"
"ds_read_b128 a[172:175], v228 offset:19456\n"
"v_mfma_f32_16x16x32_bf16 v[68:71], a[196:199], a[44:47], v[68:71]\n"
"s_waitcnt lgkmcnt(5)\n"
"v_mfma_f32_16x16x32_bf16 v[68:71], a[192:195], a[20:23], v[68:71]\n"
"v_mfma_f32_16x16x32_bf16 v[72:75], a[208:211], a[0:3], 0\n"
"s_add_u32 m0, s91, 0x0\n"
"s_nop 0\n"
"buffer_load_dwordx4 v253, s[92:95], s90 offen offset:3072 lds\n"
"s_waitcnt lgkmcnt(3)\n"
"v_mfma_f32_16x16x32_bf16 v[72:75], a[212:215], a[24:27], v[72:75]\n"
"s_waitcnt lgkmcnt(1)\n"
"ds_read_b128 a[104:107], v228 offset:2048\n"
"v_mfma_f32_16x16x32_bf16 v[72:75], a[216:219], a[48:51], v[72:75]\n"
"ds_read_b128 a[128:131], v228 offset:8192\n"
"v_mfma_f32_16x16x32_bf16 v[72:75], a[220:223], a[72:75], v[72:75]\n"
"ds_read_b128 a[152:155], v228 offset:14336\n"
"v_mfma_f32_16x16x32_bf16 v[76:79], a[220:223], a[76:79], 0\n"
"v_mfma_f32_16x16x32_bf16 v[76:79], a[216:219], a[52:55], v[76:79]\n"
"ds_read_b128 a[176:179], v228 offset:20480\n"
"v_mfma_f32_16x16x32_bf16 v[76:79], a[212:215], a[28:31], v[76:79]\n"
"ds_read_b128 a[108:111], v228 offset:3072\n"
"v_mfma_f32_16x16x32_bf16 v[76:79], a[208:211], a[4:7], v[76:79]\n"
"ds_read_b128 a[132:135], v228 offset:9216\n"
"v_mfma_f32_16x16x32_bf16 v[80:83], a[208:211], a[8:11], 0\n"
"v_mfma_f32_16x16x32_bf16 v[80:83], a[212:215], a[32:35], v[80:83]\n"
"ds_read_b128 a[156:159], v228 offset:15360\n"
"v_mfma_f32_16x16x32_bf16 v[80:83], a[216:219], a[56:59], v[80:83]\n"
"s_barrier\n"
"ds_read_b64_tr_b16 a[192:193], v224 offset:16384\n"
"v_mfma_f32_16x16x32_bf16 v[80:83], a[220:223], a[80:83], v[80:83]\n"
"s_add_u32 s90, s87, s43\n"
"s_add_u32 m0, s91, 0x10000\n"
"ds_read_b64_tr_b16 a[194:195], v224 offset:18432\n"
"v_mfma_f32_16x16x32_bf16 v[84:87], a[220:223], a[84:87], 0\n"
"buffer_load_dwordx4 v245, s[92:95], s90 offen lds\n"
"v_mfma_f32_16x16x32_bf16 v[84:87], a[216:219], a[60:63], v[84:87]\n"
"ds_read_b64_tr_b16 a[196:197], v224 offset:20480\n"
"v_mfma_f32_16x16x32_bf16 v[84:87], a[212:215], a[36:39], v[84:87]\n"
"ds_read_b64_tr_b16 a[198:199], v224 offset:22528\n"
"v_mfma_f32_16x16x32_bf16 v[84:87], a[208:211], a[12:15], v[84:87]\n"
"ds_read_b64_tr_b16 a[200:201], v224 offset:24576\n"
"v_mfma_f32_16x16x32_bf16 v[88:91], a[208:211], a[16:19], 0\n"
"buffer_load_dwordx4 v251, s[92:95], s90 offen offset:1024 lds\n"
"v_mfma_f32_16x16x32_bf16 v[88:91], a[212:215], a[40:43], v[88:91]\n"
"ds_read_b64_tr_b16 a[202:203], v224 offset:26624\n"
"v_mfma_f32_16x16x32_bf16 v[88:91], a[216:219], a[64:67], v[88:91]\n"
"ds_read_b64_tr_b16 a[204:205], v224 offset:28672\n"
"v_mfma_f32_16x16x32_bf16 v[88:91], a[220:223], a[88:91], v[88:91]\n"
"ds_read_b64_tr_b16 a[206:207], v224 offset:30720\n"
"v_mfma_f32_16x16x32_bf16 v[92:95], a[220:223], a[92:95], 0\n"
"buffer_load_dwordx4 v252, s[92:95], s90 offen offset:2048 lds\n"
"v_mfma_f32_16x16x32_bf16 v[92:95], a[216:219], a[68:71], v[92:95]\n"
"ds_read_b128 a[180:183], v228 offset:21504\n"
"s_add_u32 s72, %[hq0], 2\n"
"s_and_b32 s72, s72, 7\n"
"s_lshl_b32 s52, 3, s72\n"
"s_mov_b32 s53, 0\n"
"v_mfma_f32_16x16x32_bf16 v[92:95], a[212:215], a[44:47], v[92:95]\n"
"s_waitcnt lgkmcnt(5)\n"
"v_mfma_f32_16x16x32_bf16 v[92:95], a[208:211], a[20:23], v[92:95]\n"
"buffer_load_dwordx2 v[248:249], v238, s[56:59], s66 offen sc1\n"
"v_mfma_f32_16x16x32_bf16 v[96:99], a[192:195], a[0:3], 0\n"
"s_add_u32 m0, s91, 0x10000\n"
"s_nop 0\n"
"buffer_load_dwordx4 v253, s[92:95], s90 offen offset:3072 lds\n"
"s_waitcnt lgkmcnt(3)\n"
"v_mfma_f32_16x16x32_bf16 v[96:99], a[196:199], a[24:27], v[96:99]\n"
"s_waitcnt lgkmcnt(1)\n"
"ds_read_b128 a[112:115], v228 offset:4096\n"
"v_mfma_f32_16x16x32_bf16 v[96:99], a[200:203], a[48:51], v[96:99]\n"
"ds_read_b128 a[136:139], v228 offset:10240\n"
"v_mfma_f32_16x16x32_bf16 v[96:99], a[204:207], a[72:75], v[96:99]\n"
"ds_read_b128 a[160:163], v228 offset:16384\n"
"v_mfma_f32_16x16x32_bf16 v[100:103], a[204:207], a[76:79], 0\n"
"v_mfma_f32_16x16x32_bf16 v[100:103], a[200:203], a[52:55], v[100:103]\n"
"ds_read_b128 a[184:187], v228 offset:22528\n"
"v_mfma_f32_16x16x32_bf16 v[100:103], a[196:199], a[28:31], v[100:103]\n"
"ds_read_b128 a[116:119], v228 offset:5120\n"
"v_mfma_f32_16x16x32_bf16 v[100:103], a[192:195], a[4:7], v[100:103]\n"
"ds_read_b128 a[140:143], v228 offset:11264\n"
"v_mfma_f32_16x16x32_bf16 v[104:107], a[192:195], a[8:11], 0\n"
"v_mfma_f32_16x16x32_bf16 v[104:107], a[196:199], a[32:35], v[104:107]\n"
"ds_read_b128 a[164:167], v228 offset:17408\n"
"v_mfma_f32_16x16x32_bf16 v[104:107], a[200:203], a[56:59], v[104:107]\n"
"s_waitcnt vmcnt(13)\n"
"s_barrier\n"
"ds_read_b64_tr_b16 a[208:209], v226 offset:0\n"
"v_mfma_f32_16x16x32_bf16 v[104:107], a[204:207], a[80:83], v[104:107]\n"
"s_add_u32 s90, s87, s65\n"
"s_add_u32 m0, s91, 0x4000\n"
"ds_read_b64_tr_b16 a[210:211], v226 offset:2048\n"
"v_mfma_f32_16x16x32_bf16 v[108:111], a[204:207], a[84:87], 0\n"
"buffer_load_dwordx4 v245, s[92:95], s90 offen lds\n"
"v_mfma_f32_16x16x32_bf16 v[108:111], a[200:203], a[60:63], v[108:111]\n"
"ds_read_b64_tr_b16 a[212:213], v226 offset:4096\n"
"v_mfma_f32_16x16x32_bf16 v[108:111], a[196:199], a[36:39], v[108:111]\n"
"ds_read_b64_tr_b16 a[214:215], v226 offset:6144\n"
"v_mfma_f32_16x16x32_bf16 v[108:111], a[192:195], a[12:15], v[108:111]\n"
"ds_read_b64_tr_b16 a[216:217], v226 offset:8192\n"
"v_mfma_f32_16x16x32_bf16 v[112:115], a[192:195], a[16:19], 0\n"
"buffer_load_dwordx4 v251, s[92:95], s90 offen offset:1024 lds\n"
"v_mfma_f32_16x16x32_bf16 v[112:115], a[196:199], a[40:43], v[112:115]\n"
"ds_read_b64_tr_b16 a[218:219], v226 offset:10240\n"
"v_mfma_f32_16x16x32_bf16 v[112:115], a[200:203], a[64:67], v[112:115]\n"
"ds_read_b64_tr_b16 a[220:221], v226 offset:12288\n"
"v_mfma_f32_16x16x32_bf16 v[112:115], a[204:207], a[88:91], v[112:115]\n"
"ds_read_b64_tr_b16 a[222:223], v226 offset:14336\n"
"v_mfma_f32_16x16x32_bf16 v[116:119], a[204:207], a[92:95], 0\n"
"buffer_load_dwordx4 v252, s[92:95], s90 offen offset:2048 lds\n"
"v_mfma_f32_16x16x32_bf16 v[116:119], a[200:203], a[68:71], v[116:119]\n"
"ds_read_b128 a[188:191], v228 offset:23552\n"
"v_mfma_f32_16x16x32_bf16 v[116:119], a[196:199], a[44:47], v[116:119]\n"
"s_waitcnt lgkmcnt(5)\n"
"v_mfma_f32_16x16x32_bf16 v[116:119], a[192:195], a[20:23], v[116:119]\n"
"v_mfma_f32_16x16x32_bf16 v[0:3], a[208:211], a[96:99], v[0:3]\n"
"s_add_u32 m0, s91, 0x4000\n"
"s_nop 0\n"
"buffer_load_dwordx4 v253, s[92:95], s90 offen offset:3072 lds\n"
"s_waitcnt lgkmcnt(3)\n"
"v_mfma_f32_16x16x32_bf16 v[0:3], a[212:215], a[120:123], v[0:3]\n"
"s_waitcnt lgkmcnt(1)\n"
"v_mfma_f32_16x16x32_bf16 v[0:3], a[216:219], a[144:147], v[0:3]\n"
"v_mfma_f32_16x16x32_bf16 v[0:3], a[220:223], a[168:171], v[0:3]\n"
"v_mfma_f32_16x16x32_bf16 v[4:7], a[220:223], a[172:175], v[4:7]\n"
"v_mfma_f32_16x16x32_bf16 v[4:7], a[216:219], a[148:151], v[4:7]\n"
"v_mfma_f32_16x16x32_bf16 v[4:7], a[212:215], a[124:127], v[4:7]\n"
"v_mfma_f32_16x16x32_bf16 v[4:7], a[208:211], a[100:103], v[4:7]\n"
"v_mfma_f32_16x16x32_bf16 v[8:11], a[208:211], a[104:107], v[8:11]\n"
"v_mfma_f32_16x16x32_bf16 v[8:11], a[212:215], a[128:131], v[8:11]\n"
"v_mfma_f32_16x16x32_bf16 v[8:11], a[216:219], a[152:155], v[8:11]\n"
"s_waitcnt vmcnt(13)\n"
"s_barrier\n"
"ds_read_b64_tr_b16 a[192:193], v226 offset:16384\n"
"v_mfma_f32_16x16x32_bf16 v[8:11], a[220:223], a[176:179], v[8:11]\n"
"s_add_u32 s87, s87, 0x28000\n"
"s_cmp_eq_u32 s87, 0x140000\n"
"s_cselect_b32 s87, 0, s87\n"
"s_add_u32 s90, s87, 0x200\n"
"s_add_u32 m0, s91, 0x20000\n"
"ds_read_b64_tr_b16 a[194:195], v226 offset:18432\n"
"v_mfma_f32_16x16x32_bf16 v[12:15], a[220:223], a[180:183], v[12:15]\n"
"buffer_load_dwordx4 v245, s[92:95], s90 offen lds\n"
"v_mfma_f32_16x16x32_bf16 v[12:15], a[216:219], a[156:159], v[12:15]\n"
"ds_read_b64_tr_b16 a[196:197], v226 offset:20480\n"
"v_mfma_f32_16x16x32_bf16 v[12:15], a[212:215], a[132:135], v[12:15]\n"
"ds_read_b64_tr_b16 a[198:199], v226 offset:22528\n"
"v_mfma_f32_16x16x32_bf16 v[12:15], a[208:211], a[108:111], v[12:15]\n"
"ds_read_b64_tr_b16 a[200:201], v226 offset:24576\n"
"v_mfma_f32_16x16x32_bf16 v[16:19], a[208:211], a[112:115], v[16:19]\n"
"buffer_load_dwordx4 v251, s[92:95], s90 offen offset:1024 lds\n"
"v_mfma_f32_16x16x32_bf16 v[16:19], a[212:215], a[136:139], v[16:19]\n"
"ds_read_b64_tr_b16 a[202:203], v226 offset:26624\n"
"v_mfma_f32_16x16x32_bf16 v[16:19], a[216:219], a[160:163], v[16:19]\n"
"s_waitcnt lgkmcnt(6)\n"
"ds_read_b64_tr_b16 a[204:205], v226 offset:28672\n"
"v_mfma_f32_16x16x32_bf16 v[16:19], a[220:223], a[184:187], v[16:19]\n"
"ds_read_b64_tr_b16 a[206:207], v226 offset:30720\n"
"v_mfma_f32_16x16x32_bf16 v[20:23], a[220:223], a[188:191], v[20:23]\n"
"buffer_load_dwordx4 v252, s[92:95], s90 offen offset:2048 lds\n"
"v_mfma_f32_16x16x32_bf16 v[20:23], a[216:219], a[164:167], v[20:23]\n"
"v_mfma_f32_16x16x32_bf16 v[20:23], a[212:215], a[140:143], v[20:23]\n"
"s_waitcnt lgkmcnt(4)\n"
"v_mfma_f32_16x16x32_bf16 v[20:23], a[208:211], a[116:119], v[20:23]\n"
"s_waitcnt vmcnt(8)\n"
"s_mov_b32 s62, 0\n"
"LA6%=:\n"
"v_cmp_ne_u64 vcc, s[60:61], v[248:249]\n"
"s_and_b64 vcc, vcc, s[52:53]\n"
"s_cbranch_scc0 LB6%=\n"
"s_sleep 0\n"
"buffer_load_dwordx2 v[248:249], v238, s[56:59], s66 offen sc1\n"
"s_add_u32 s62, s62, 1\n"
"s_cmp_lt_u32 s62, 0x100000\n"
"s_waitcnt vmcnt(0)\n"
"s_cbranch_scc1 LA6%=\n"
"LB6%=:\n"
"v_mfma_f32_16x16x32_bf16 v[24:27], a[192:195], a[96:99], v[24:27]\n"
"s_add_u32 s48, s48, 0x1000\n"
"s_and_b32 s48, s48, 0x7fff\n"
"s_add_u32 s49, s48, s39\n"
"s_add_u32 m0, s50, 0x14000\n"
"s_nop 0\n"
"buffer_load_dwordx4 v230, s[56:59], s49 offen sc1 lds\n"
"s_waitcnt lgkmcnt(2)\n"
"v_mfma_f32_16x16x32_bf16 v[24:27], a[196:199], a[120:123], v[24:27]\n"
"s_waitcnt lgkmcnt(0)\n"
"v_mfma_f32_16x16x32_bf16 v[24:27], a[200:203], a[144:147], v[24:27]\n"
"v_mfma_f32_16x16x32_bf16 v[24:27], a[204:207], a[168:171], v[24:27]\n"
"v_mfma_f32_16x16x32_bf16 v[28:31], a[204:207], a[172:175], v[28:31]\n"
"buffer_load_dwordx4 v231, s[56:59], s49 offen offset:1024 sc1 lds\n"
"v_mfma_f32_16x16x32_bf16 v[28:31], a[200:203], a[148:151], v[28:31]\n"
"v_mfma_f32_16x16x32_bf16 v[28:31], a[196:199], a[124:127], v[28:31]\n"
"v_mfma_f32_16x16x32_bf16 v[28:31], a[192:195], a[100:103], v[28:31]\n"
"s_add_u32 m0, s50, 0x14000\n"
"v_mfma_f32_16x16x32_bf16 v[32:35], a[192:195], a[104:107], v[32:35]\n"
"buffer_load_dwordx4 v232, s[56:59], s49 offen offset:2048 sc1 lds\n"
"v_mfma_f32_16x16x32_bf16 v[32:35], a[196:199], a[128:131], v[32:35]\n"
"v_mfma_f32_16x16x32_bf16 v[32:35], a[200:203], a[152:155], v[32:35]\n"
"s_barrier\n"
"ds_read_b64_tr_b16 a[208:209], v224 offset:0\n"
"v_mfma_f32_16x16x32_bf16 v[32:35], a[204:207], a[176:179], v[32:35]\n"
"ds_read_b64_tr_b16 a[210:211], v224 offset:2048\n"
"v_mfma_f32_16x16x32_bf16 v[36:39], a[204:207], a[180:183], v[36:39]\n"
"buffer_load_dwordx4 v233, s[56:59], s49 offen offset:3072 sc1 lds\n"
"v_mfma_f32_16x16x32_bf16 v[36:39], a[200:203], a[156:159], v[36:39]\n"
"ds_read_b64_tr_b16 a[212:213], v224 offset:4096\n"
"v_mfma_f32_16x16x32_bf16 v[36:39], a[196:199], a[132:135], v[36:39]\n"
"s_add_u32 s49, s49, 0x80000\n"
"ds_read_b64_tr_b16 a[214:215], v224 offset:6144\n"
"v_mfma_f32_16x16x32_bf16 v[36:39], a[192:195], a[108:111], v[36:39]\n"
"s_add_u32 m0, s50, 0x15000\n"
"ds_read_b64_tr_b16 a[216:217], v224 offset:8192\n"
"v_mfma_f32_16x16x32_bf16 v[40:43], a[192:195], a[112:115], v[40:43]\n"
"buffer_load_dwordx4 v230, s[56:59], s49 offen sc1 lds\n"
"v_mfma_f32_16x16x32_bf16 v[40:43], a[196:199], a[136:139], v[40:43]\n"
"ds_read_b64_tr_b16 a[218:219], v224 offset:10240\n"
"v_mfma_f32_16x16x32_bf16 v[40:43], a[200:203], a[160:163], v[40:43]\n"
"ds_read_b64_tr_b16 a[220:221], v224 offset:12288\n"
"v_mfma_f32_16x16x32_bf16 v[40:43], a[204:207], a[184:187], v[40:43]\n"
"ds_read_b64_tr_b16 a[222:223], v224 offset:14336\n"
"v_mfma_f32_16x16x32_bf16 v[44:47], a[204:207], a[188:191], v[44:47]\n"
"buffer_load_dwordx4 v231, s[56:59], s49 offen offset:1024 sc1 lds\n"
"v_mfma_f32_16x16x32_bf16 v[44:47], a[200:203], a[164:167], v[44:47]\n"
"v_mfma_f32_16x16x32_bf16 v[44:47], a[196:199], a[140:143], v[44:47]\n"
"s_waitcnt lgkmcnt(4)\n"
"v_mfma_f32_16x16x32_bf16 v[44:47], a[192:195], a[116:119], v[44:47]\n"
"v_mfma_f32_16x16x32_bf16 v[48:51], a[208:211], a[96:99], v[48:51]\n"
"s_add_u32 m0, s91, 0x20000\n"
"s_nop 0\n"
"buffer_load_dwordx4 v253, s[92:95], s90 offen offset:3072 lds\n"
"s_waitcnt lgkmcnt(2)\n"
"v_mfma_f32_16x16x32_bf16 v[48:51], a[212:215], a[120:123], v[48:51]\n"
"s_waitcnt lgkmcnt(0)\n"
"v_mfma_f32_16x16x32_bf16 v[48:51], a[216:219], a[144:147], v[48:51]\n"
"s_add_u32 s90, s87, s41\n"
"v_mfma_f32_16x16x32_bf16 v[48:51], a[220:223], a[168:171], v[48:51]\n"
"s_add_u32 m0, s91, 0x24000\n"
"v_mfma_f32_16x16x32_bf16 v[52:55], a[220:223], a[172:175], v[52:55]\n"
"buffer_load_dwordx4 v245, s[92:95], s90 offen lds\n"
"v_mfma_f32_16x16x32_bf16 v[52:55], a[216:219], a[148:151], v[52:55]\n"
"v_mfma_f32_16x16x32_bf16 v[52:55], a[212:215], a[124:127], v[52:55]\n"
"v_mfma_f32_16x16x32_bf16 v[52:55], a[208:211], a[100:103], v[52:55]\n"
"v_mfma_f32_16x16x32_bf16 v[56:59], a[208:211], a[104:107], v[56:59]\n"
"buffer_load_dwordx4 v251, s[92:95], s90 offen offset:1024 lds\n"
"v_mfma_f32_16x16x32_bf16 v[56:59], a[212:215], a[128:131], v[56:59]\n"
"v_mfma_f32_16x16x32_bf16 v[56:59], a[216:219], a[152:155], v[56:59]\n"
"s_waitcnt vmcnt(16)\n"
"s_barrier\n"
"ds_read_b64_tr_b16 a[192:193], v225 offset:0\n"
"v_mfma_f32_16x16x32_bf16 v[56:59], a[220:223], a[176:179], v[56:59]\n"
"ds_read_b64_tr_b16 a[194:195], v225 offset:2048\n"
"v_mfma_f32_16x16x32_bf16 v[60:63], a[220:223], a[180:183], v[60:63]\n"
"buffer_load_dwordx4 v252, s[92:95], s90 offen offset:2048 lds\n"
"v_mfma_f32_16x16x32_bf16 v[60:63], a[216:219], a[156:159], v[60:63]\n"
"ds_read_b64_tr_b16 a[196:197], v225 offset:4096\n"
"v_mfma_f32_16x16x32_bf16 v[60:63], a[212:215], a[132:135], v[60:63]\n"
"ds_read_b64_tr_b16 a[198:199], v225 offset:6144\n"
"v_mfma_f32_16x16x32_bf16 v[60:63], a[208:211], a[108:111], v[60:63]\n"
"ds_read_b64_tr_b16 a[200:201], v225 offset:8192\n"
"v_mfma_f32_16x16x32_bf16 v[64:67], a[208:211], a[112:115], v[64:67]\n"
"buffer_load_dwordx4 v253, s[92:95], s90 offen offset:3072 lds\n"
"v_mfma_f32_16x16x32_bf16 v[64:67], a[212:215], a[136:139], v[64:67]\n"
"ds_read_b64_tr_b16 a[202:203], v225 offset:10240\n"
"v_mfma_f32_16x16x32_bf16 v[64:67], a[216:219], a[160:163], v[64:67]\n"
"s_add_u32 s90, s87, s42\n"
"ds_read_b64_tr_b16 a[204:205], v225 offset:12288\n"
"v_mfma_f32_16x16x32_bf16 v[64:67], a[220:223], a[184:187], v[64:67]\n"
"s_add_u32 m0, s91, 0x0\n"
"ds_read_b64_tr_b16 a[206:207], v225 offset:14336\n"
"v_mfma_f32_16x16x32_bf16 v[68:71], a[220:223], a[188:191], v[68:71]\n"
"buffer_load_dwordx4 v245, s[92:95], s90 offen lds\n"
"v_mfma_f32_16x16x32_bf16 v[68:71], a[216:219], a[164:167], v[68:71]\n"
"v_mfma_f32_16x16x32_bf16 v[68:71], a[212:215], a[140:143], v[68:71]\n"
"s_waitcnt lgkmcnt(4)\n"
"v_mfma_f32_16x16x32_bf16 v[68:71], a[208:211], a[116:119], v[68:71]\n"
"v_mfma_f32_16x16x32_bf16 v[72:75], a[192:195], a[96:99], v[72:75]\n"
"s_add_u32 m0, s91, 0x0\n"
"s_nop 0\n"
"buffer_load_dwordx4 v251, s[92:95], s90 offen offset:1024 lds\n"
"s_waitcnt lgkmcnt(2)\n"
"v_mfma_f32_16x16x32_bf16 v[72:75], a[196:199], a[120:123], v[72:75]\n"
"s_waitcnt lgkmcnt(0)\n"
"v_mfma_f32_16x16x32_bf16 v[72:75], a[200:203], a[144:147], v[72:75]\n"
"v_mfma_f32_16x16x32_bf16 v[72:75], a[204:207], a[168:171], v[72:75]\n"
"v_mfma_f32_16x16x32_bf16 v[76:79], a[204:207], a[172:175], v[76:79]\n"
"buffer_load_dwordx4 v252, s[92:95], s90 offen offset:2048 lds\n"
"v_mfma_f32_16x16x32_bf16 v[76:79], a[200:203], a[148:151], v[76:79]\n"
"v_mfma_f32_16x16x32_bf16 v[76:79], a[196:199], a[124:127], v[76:79]\n"
"v_mfma_f32_16x16x32_bf16 v[76:79], a[192:195], a[100:103], v[76:79]\n"
"v_mfma_f32_16x16x32_bf16 v[80:83], a[192:195], a[104:107], v[80:83]\n"
"buffer_load_dwordx4 v253, s[92:95], s90 offen offset:3072 lds\n"
"v_mfma_f32_16x16x32_bf16 v[80:83], a[196:199], a[128:131], v[80:83]\n"
"v_mfma_f32_16x16x32_bf16 v[80:83], a[200:203], a[152:155], v[80:83]\n"
"s_waitcnt vmcnt(9)\n"
"s_barrier\n"
"ds_read_b64_tr_b16 a[208:209], v224 offset:16384\n"
"v_mfma_f32_16x16x32_bf16 v[80:83], a[204:207], a[176:179], v[80:83]\n"
"s_add_u32 s90, s87, s43\n"
"s_add_u32 m0, s91, 0x10000\n"
"ds_read_b64_tr_b16 a[210:211], v224 offset:18432\n"
"v_mfma_f32_16x16x32_bf16 v[84:87], a[204:207], a[180:183], v[84:87]\n"
"buffer_load_dwordx4 v245, s[92:95], s90 offen lds\n"
"v_mfma_f32_16x16x32_bf16 v[84:87], a[200:203], a[156:159], v[84:87]\n"
"ds_read_b64_tr_b16 a[212:213], v224 offset:20480\n"
"v_mfma_f32_16x16x32_bf16 v[84:87], a[196:199], a[132:135], v[84:87]\n"
"ds_read_b64_tr_b16 a[214:215], v224 offset:22528\n"
"v_mfma_f32_16x16x32_bf16 v[84:87], a[192:195], a[108:111], v[84:87]\n"
"ds_read_b64_tr_b16 a[216:217], v224 offset:24576\n"
"v_mfma_f32_16x16x32_bf16 v[88:91], a[192:195], a[112:115], v[88:91]\n"
"buffer_load_dwordx4 v251, s[92:95], s90 offen offset:1024 lds\n"
"v_mfma_f32_16x16x32_bf16 v[88:91], a[196:199], a[136:139], v[88:91]\n"
"ds_read_b64_tr_b16 a[218:219], v224 offset:26624\n"
"v_mfma_f32_16x16x32_bf16 v[88:91], a[200:203], a[160:163], v[88:91]\n"
"ds_read_b64_tr_b16 a[220:221], v224 offset:28672\n"
"v_mfma_f32_16x16x32_bf16 v[88:91], a[204:207], a[184:187], v[88:91]\n"
"ds_read_b64_tr_b16 a[222:223], v224 offset:30720\n"
"v_mfma_f32_16x16x32_bf16 v[92:95], a[204:207], a[188:191], v[92:95]\n"
"buffer_load_dwordx4 v252, s[92:95], s90 offen offset:2048 lds\n"
"v_mfma_f32_16x16x32_bf16 v[92:95], a[200:203], a[164:167], v[92:95]\n"
"ds_read_b128 a[0:3], v227 offset:40960\n"
"v_mfma_f32_16x16x32_bf16 v[92:95], a[196:199], a[140:143], v[92:95]\n"
"s_waitcnt lgkmcnt(5)\n"
"ds_read_b128 a[24:27], v227 offset:47104\n"
"v_mfma_f32_16x16x32_bf16 v[92:95], a[192:195], a[116:119], v[92:95]\n"
"ds_read_b128 a[48:51], v227 offset:53248\n"
"v_mfma_f32_16x16x32_bf16 v[96:99], a[208:211], a[96:99], v[96:99]\n"
"s_add_u32 s48, s48, 0x1000\n"
"s_and_b32 s48, s48, 0x7fff\n"
"s_add_u32 s49, s48, s39\n"
"s_add_u32 m0, s50, 0xa000\n"
"s_nop 0\n"
"buffer_load_dwordx4 v230, s[56:59], s49 offen sc1 lds\n"
"s_waitcnt lgkmcnt(5)\n"
"v_mfma_f32_16x16x32_bf16 v[96:99], a[212:215], a[120:123], v[96:99]\n"
"s_waitcnt lgkmcnt(3)\n"
"ds_read_b128 a[72:75], v227 offset:59392\n"
"v_mfma_f32_16x16x32_bf16 v[96:99], a[216:219], a[144:147], v[96:99]\n"
"ds_read_b128 a[4:7], v227 offset:41984\n"
"v_mfma_f32_16x16x32_bf16 v[96:99], a[220:223], a[168:171], v[96:99]\n"
"ds_read_b128 a[28:31], v227 offset:48128\n"
"v_mfma_f32_16x16x32_bf16 v[100:103], a[220:223], a[172:175], v[100:103]\n"
"buffer_load_dwordx4 v231, s[56:59], s49 offen offset:1024 sc1 lds\n"
"v_mfma_f32_16x16x32_bf16 v[100:103], a[216:219], a[148:151], v[100:103]\n"
"ds_read_b128 a[52:55], v227 offset:54272\n"
"v_mfma_f32_16x16x32_bf16 v[100:103], a[212:215], a[124:127], v[100:103]\n"
"ds_read_b128 a[76:79], v227 offset:60416\n"
"v_mfma_f32_16x16x32_bf16 v[100:103], a[208:211], a[100:103], v[100:103]\n"
"s_add_u32 m0, s50, 0xa000\n"
"ds_read_b128 a[8:11], v227 offset:43008\n"
"v_mfma_f32_16x16x32_bf16 v[104:107], a[208:211], a[104:107], v[104:107]\n"
"buffer_load_dwordx4 v232, s[56:59], s49 offen offset:2048 sc1 lds\n"
"v_mfma_f32_16x16x32_bf16 v[104:107], a[212:215], a[128:131], v[104:107]\n"
"ds_read_b128 a[32:35], v227 offset:49152\n"
"v_mfma_f32_16x16x32_bf16 v[104:107], a[216:219], a[152:155], v[104:107]\n"
"s_waitcnt vmcnt(14)\n"
"s_barrier\n"
"ds_read_b64_tr_b16 a[192:193], v226 offset:0\n"
"v_mfma_f32_16x16x32_bf16 v[104:107], a[220:223], a[176:179], v[104:107]\n"
"ds_read_b64_tr_b16 a[194:195], v226 offset:2048\n"
"v_mfma_f32_16x16x32_bf16 v[108:111], a[220:223], a[180:183], v[108:111]\n"
"buffer_load_dwordx4 v233, s[56:59], s49 offen offset:3072 sc1 lds\n"
"v_mfma_f32_16x16x32_bf16 v[108:111], a[216:219], a[156:159], v[108:111]\n"
"ds_read_b64_tr_b16 a[196:197], v226 offset:4096\n"
"v_mfma_f32_16x16x32_bf16 v[108:111], a[212:215], a[132:135], v[108:111]\n"
"s_add_u32 s49, s49, 0x80000\n"
"ds_read_b64_tr_b16 a[198:199], v226 offset:6144\n"
"v_mfma_f32_16x16x32_bf16 v[108:111], a[208:211], a[108:111], v[108:111]\n"
"s_add_u32 m0, s50, 0xb000\n"
"ds_read_b64_tr_b16 a[200:201], v226 offset:8192\n"
"v_mfma_f32_16x16x32_bf16 v[112:115], a[208:211], a[112:115], v[112:115]\n"
"buffer_load_dwordx4 v230, s[56:59], s49 offen sc1 lds\n"
"v_mfma_f32_16x16x32_bf16 v[112:115], a[212:215], a[136:139], v[112:115]\n"
"ds_read_b64_tr_b16 a[202:203], v226 offset:10240\n"
"v_mfma_f32_16x16x32_bf16 v[112:115], a[216:219], a[160:163], v[112:115]\n"
"ds_read_b64_tr_b16 a[204:205], v226 offset:12288\n"
"v_mfma_f32_16x16x32_bf16 v[112:115], a[220:223], a[184:187], v[112:115]\n"
"ds_read_b64_tr_b16 a[206:207], v226 offset:14336\n"
"v_mfma_f32_16x16x32_bf16 v[116:119], a[220:223], a[188:191], v[116:119]\n"
"buffer_load_dwordx4 v231, s[56:59], s49 offen offset:1024 sc1 lds\n"
"v_mfma_f32_16x16x32_bf16 v[116:119], a[216:219], a[164:167], v[116:119]\n"
"ds_read_b128 a[56:59], v227 offset:55296\n"
"s_add_u32 s72, %[hq0], 4\n"
"s_and_b32 s72, s72, 7\n"
"s_lshl_b32 s52, 3, s72\n"
"s_mov_b32 s53, 0\n"
"v_mfma_f32_16x16x32_bf16 v[116:119], a[212:215], a[140:143], v[116:119]\n"
"s_waitcnt lgkmcnt(5)\n"
"ds_read_b128 a[80:83], v227 offset:61440\n"
"v_mfma_f32_16x16x32_bf16 v[116:119], a[208:211], a[116:119], v[116:119]\n"
"ds_read_b128 a[12:15], v227 offset:44032\n"
"buffer_load_dwordx2 v[248:249], v238, s[56:59], s66 offen sc1\n"
"v_mfma_f32_16x16x32_bf16 v[0:3], a[192:195], a[0:3], v[0:3]\n"
"s_add_u32 m0, s91, 0x10000\n"
"s_nop 0\n"
"buffer_load_dwordx4 v253, s[92:95], s90 offen offset:3072 lds\n"
"s_waitcnt lgkmcnt(5)\n"
"v_mfma_f32_16x16x32_bf16 v[0:3], a[196:199], a[24:27], v[0:3]\n"
"s_waitcnt lgkmcnt(3)\n"
"ds_read_b128 a[36:39], v227 offset:50176\n"
"v_mfma_f32_16x16x32_bf16 v[0:3], a[200:203], a[48:51], v[0:3]\n"
"s_add_u32 s90, s87, s65\n"
"ds_read_b128 a[60:63], v227 offset:56320\n"
"v_mfma_f32_16x16x32_bf16 v[0:3], a[204:207], a[72:75], v[0:3]\n"
"s_add_u32 m0, s91, 0x4000\n"
"ds_read_b128 a[84:87], v227 offset:62464\n"
"v_mfma_f32_16x16x32_bf16 v[4:7], a[204:207], a[76:79], v[4:7]\n"
"buffer_load_dwordx4 v245, s[92:95], s90 offen lds\n"
"ds_read_b128 a[16:19], v227 offset:45056\n"
"v_mfma_f32_16x16x32_bf16 v[4:7], a[200:203], a[52:55], v[4:7]\n"
"ds_read_b128 a[40:43], v227 offset:51200\n"
"v_mfma_f32_16x16x32_bf16 v[4:7], a[196:199], a[28:31], v[4:7]\n"
"s_waitcnt lgkmcnt(6)\n"
"ds_read_b128 a[64:67], v227 offset:57344\n"
"v_mfma_f32_16x16x32_bf16 v[4:7], a[192:195], a[4:7], v[4:7]\n"
"ds_read_b128 a[88:91], v227 offset:63488\n"
"v_mfma_f32_16x16x32_bf16 v[8:11], a[192:195], a[8:11], v[8:11]\n"
"buffer_load_dwordx4 v251, s[92:95], s90 offen offset:1024 lds\n"
"ds_read_b128 a[20:23], v227 offset:46080\n"
"v_mfma_f32_16x16x32_bf16 v[8:11], a[196:199], a[32:35], v[8:11]\n"
"ds_read_b128 a[44:47], v227 offset:52224\n"
"v_mfma_f32_16x16x32_bf16 v[8:11], a[200:203], a[56:59], v[8:11]\n"
"s_waitcnt lgkmcnt(6)\n"
"s_waitcnt vmcnt(17)\n"
"s_barrier\n"
"ds_read_b64_tr_b16 a[208:209], v226 offset:16384\n"
"v_mfma_f32_16x16x32_bf16 v[8:11], a[204:207], a[80:83], v[8:11]\n"
"ds_read_b128 a[68:71], v227 offset:58368\n"
"ds_read_b64_tr_b16 a[210:211], v226 offset:18432\n"
"v_mfma_f32_16x16x32_bf16 v[12:15], a[204:207], a[84:87], v[12:15]\n"
"buffer_load_dwordx4 v252, s[92:95], s90 offen offset:2048 lds\n"
"ds_read_b128 a[92:95], v227 offset:64512\n"
"v_mfma_f32_16x16x32_bf16 v[12:15], a[200:203], a[60:63], v[12:15]\n"
"ds_read_b64_tr_b16 a[212:213], v226 offset:20480\n"
"v_mfma_f32_16x16x32_bf16 v[12:15], a[196:199], a[36:39], v[12:15]\n"
"s_waitcnt lgkmcnt(7)\n"
"ds_read_b64_tr_b16 a[214:215], v226 offset:22528\n"
"v_mfma_f32_16x16x32_bf16 v[12:15], a[192:195], a[12:15], v[12:15]\n"
"ds_read_b64_tr_b16 a[216:217], v226 offset:24576\n"
"v_mfma_f32_16x16x32_bf16 v[16:19], a[192:195], a[16:19], v[16:19]\n"
"buffer_load_dwordx4 v253, s[92:95], s90 offen offset:3072 lds\n"
"v_mfma_f32_16x16x32_bf16 v[16:19], a[196:199], a[40:43], v[16:19]\n"
"s_add_u32 s87, s87, 0x28000\n"
"s_cmp_eq_u32 s87, 0x140000\n"
"s_cselect_b32 s87, 0, s87\n"
"s_add_u32 s90, s87, 0x200\n"
"ds_read_b64_tr_b16 a[218:219], v226 offset:26624\n"
"v_mfma_f32_16x16x32_bf16 v[16:19], a[200:203], a[64:67], v[16:19]\n"
"s_waitcnt lgkmcnt(4)\n"
"ds_read_b64_tr_b16 a[220:221], v226 offset:28672\n"
"v_mfma_f32_16x16x32_bf16 v[16:19], a[204:207], a[88:91], v[16:19]\n"
"s_add_u32 m0, s91, 0x20000\n"
"ds_read_b64_tr_b16 a[222:223], v226 offset:30720\n"
"v_mfma_f32_16x16x32_bf16 v[20:23], a[204:207], a[92:95], v[20:23]\n"
"buffer_load_dwordx4 v245, s[92:95], s90 offen lds\n"
"v_mfma_f32_16x16x32_bf16 v[20:23], a[200:203], a[68:71], v[20:23]\n"
"v_and_b32 v200, 15, v244\n"
"v_lshrrev_b32 v201, 2, v200\n"
"v_mfma_f32_16x16x32_bf16 v[20:23], a[196:199], a[44:47], v[20:23]\n"
"s_waitcnt lgkmcnt(4)\n"
"v_lshrrev_b32 v202, 4, v244\n"
"v_and_b32 v203, 1, v244\n"
"v_mfma_f32_16x16x32_bf16 v[20:23], a[192:195], a[20:23], v[20:23]\n"
"v_lshlrev_b32 v203, 4, v203\n"
"v_mov_b32 v204, 0x3f80\n"
"v_mfma_f32_16x16x32_bf16 v[24:27], a[208:211], a[0:3], v[24:27]\n"
"s_add_u32 m0, s91, 0x20000\n"
"s_nop 0\n"
"buffer_load_dwordx4 v251, s[92:95], s90 offen offset:1024 lds\n"
"s_waitcnt lgkmcnt(2)\n"
"v_mfma_f32_16x16x32_bf16 v[24:27], a[212:215], a[24:27], v[24:27]\n"
"s_waitcnt lgkmcnt(0)\n"
"v_mfma_f32_16x16x32_bf16 v[24:27], a[216:219], a[48:51], v[24:27]\n"
"v_lshlrev_b32 v204, v203, v204\n"
"v_mfma_f32_16x16x32_bf16 v[24:27], a[220:223], a[72:75], v[24:27]\n"
"v_cmp_eq_u32 vcc, v201, v202\n"
"s_nop 1\n"
"v_cndmask_b32 v204, 0, v204, vcc\n"
"v_mfma_f32_16x16x32_bf16 v[28:31], a[220:223], a[76:79], v[28:31]\n"
"buffer_load_dwordx4 v252, s[92:95], s90 offen offset:2048 lds\n"
"v_mfma_f32_16x16x32_bf16 v[28:31], a[216:219], a[52:55], v[28:31]\n"
"v_and_b32 v203, 2, v244\n"
"v_mfma_f32_16x16x32_bf16 v[28:31], a[212:215], a[28:31], v[28:31]\n"
"v_cmp_eq_u32 vcc, 0, v203\n"
"s_nop 1\n"
"v_cndmask_b32 v192, 0, v204, vcc\n"
"v_mfma_f32_16x16x32_bf16 v[28:31], a[208:211], a[4:7], v[28:31]\n"
"v_sub_u32 v193, v204, v192\n"
"v_mov_b32 v194, 0\n"
"v_mfma_f32_16x16x32_bf16 v[32:35], a[208:211], a[8:11], v[32:35]\n"
"buffer_load_dwordx4 v253, s[92:95], s90 offen offset:3072 lds\n"
"v_mfma_f32_16x16x32_bf16 v[32:35], a[212:215], a[32:35], v[32:35]\n"
"v_mov_b32 v195, 0\n"
"v_mov_b32 v196, 0\n"
"v_mfma_f32_16x16x32_bf16 v[32:35], a[216:219], a[56:59], v[32:35]\n"
"s_waitcnt vmcnt(10)\n"
"s_barrier\n"
"ds_read_b64_tr_b16 a[192:193], v224 offset:0\n"
"v_mfma_f32_16x16x32_bf16 v[32:35], a[220:223], a[80:83], v[32:35]\n"
"s_add_u32 s90, s87, s41\n"
"s_add_u32 m0, s91, 0x24000\n"
"ds_read_b64_tr_b16 a[194:195], v224 offset:2048\n"
"v_mfma_f32_16x16x32_bf16 v[36:39], a[220:223], a[84:87], v[36:39]\n"
"buffer_load_dwordx4 v245, s[92:95], s90 offen lds\n"
"v_mfma_f32_16x16x32_bf16 v[36:39], a[216:219], a[60:63], v[36:39]\n"
"ds_read_b64_tr_b16 a[196:197], v224 offset:4096\n"
"v_mfma_f32_16x16x32_bf16 v[36:39], a[212:215], a[36:39], v[36:39]\n"
"ds_read_b64_tr_b16 a[198:199], v224 offset:6144\n"
"v_mfma_f32_16x16x32_bf16 v[36:39], a[208:211], a[12:15], v[36:39]\n"
"ds_read_b64_tr_b16 a[200:201], v224 offset:8192\n"
"v_mfma_f32_16x16x32_bf16 v[40:43], a[208:211], a[16:19], v[40:43]\n"
"buffer_load_dwordx4 v251, s[92:95], s90 offen offset:1024 lds\n"
"v_mfma_f32_16x16x32_bf16 v[40:43], a[212:215], a[40:43], v[40:43]\n"
"ds_read_b64_tr_b16 a[202:203], v224 offset:10240\n"
"v_mfma_f32_16x16x32_bf16 v[40:43], a[216:219], a[64:67], v[40:43]\n"
"ds_read_b64_tr_b16 a[204:205], v224 offset:12288\n"
"v_mfma_f32_16x16x32_bf16 v[40:43], a[220:223], a[88:91], v[40:43]\n"
"ds_read_b64_tr_b16 a[206:207], v224 offset:14336\n"
"v_mfma_f32_16x16x32_bf16 v[44:47], a[220:223], a[92:95], v[44:47]\n"
"buffer_load_dwordx4 v252, s[92:95], s90 offen offset:2048 lds\n"
"v_mfma_f32_16x16x32_bf16 v[44:47], a[216:219], a[68:71], v[44:47]\n"
"v_mov_b32 v197, 0\n"
"v_mov_b32 v198, v192\n"
"v_mfma_f32_16x16x32_bf16 v[44:47], a[212:215], a[44:47], v[44:47]\n"
"s_waitcnt lgkmcnt(4)\n"
"v_mov_b32 v199, v193\n"
"v_mfma_f32_16x16x32_bf16 v[44:47], a[208:211], a[20:23], v[44:47]\n"
"s_mov_b64 s[44:45], %[pout]\n"
"s_waitcnt vmcnt(12)\n"
"s_mov_b32 s62, 0\n"
"LA12%=:\n"
"v_cmp_ne_u64 vcc, s[60:61], v[248:249]\n"
"s_and_b64 vcc, vcc, s[52:53]\n"
"s_cbranch_scc0 LB12%=\n"
"s_sleep 0\n"
"buffer_load_dwordx2 v[248:249], v238, s[56:59], s66 offen sc1\n"
"s_add_u32 s62, s62, 1\n"
"s_cmp_lt_u32 s62, 0x100000\n"
"s_waitcnt vmcnt(0)\n"
"s_cbranch_scc1 LA12%=\n"
"LB12%=:\n"
"v_mfma_f32_16x16x32_bf16 v[48:51], a[192:195], a[0:3], v[48:51]\n"
"s_add_u32 s48, s48, 0x1000\n"
"s_and_b32 s48, s48, 0x7fff\n"
"s_add_u32 s49, s48, s39\n"
"s_add_u32 m0, s50, 0x1a000\n"
"s_nop 0\n"
"buffer_load_dwordx4 v230, s[56:59], s49 offen sc1 lds\n"
"s_waitcnt lgkmcnt(2)\n"
"v_mfma_f32_16x16x32_bf16 v[48:51], a[196:199], a[24:27], v[48:51]\n"
"s_waitcnt lgkmcnt(0)\n"
"ds_read_b128 a[96:99], v227 offset:0\n"
"v_mfma_f32_16x16x32_bf16 v[48:51], a[200:203], a[48:51], v[48:51]\n"
"ds_read_b128 a[120:123], v227 offset:6144\n"
"v_mfma_f32_16x16x32_bf16 v[48:51], a[204:207], a[72:75], v[48:51]\n"
"ds_read_b128 a[144:147], v227 offset:12288\n"
"v_mfma_f32_16x16x32_bf16 v[52:55], a[204:207], a[76:79], v[52:55]\n"
"buffer_load_dwordx4 v231, s[56:59], s49 offen offset:1024 sc1 lds\n"
"v_mfma_f32_16x16x32_bf16 v[52:55], a[200:203], a[52:55], v[52:55]\n"
"ds_read_b128 a[168:171], v227 offset:18432\n"
"v_mfma_f32_16x16x32_bf16 v[52:55], a[196:199], a[28:31], v[52:55]\n"
"ds_read_b128 a[100:103], v227 offset:1024\n"
"v_mfma_f32_16x16x32_bf16 v[52:55], a[192:195], a[4:7], v[52:55]\n"
"s_add_u32 m0, s50, 0x1a000\n"
"ds_read_b128 a[124:127], v227 offset:7168\n"
"v_mfma_f32_16x16x32_bf16 v[56:59], a[192:195], a[8:11], v[56:59]\n"
"buffer_load_dwordx4 v232, s[56:59], s49 offen offset:2048 sc1 lds\n"
"v_mfma_f32_16x16x32_bf16 v[56:59], a[196:199], a[32:35], v[56:59]\n"
"ds_read_b128 a[148:151], v227 offset:13312\n"
"v_mfma_f32_16x16x32_bf16 v[56:59], a[200:203], a[56:59], v[56:59]\n"
"s_waitcnt vmcnt(14)\n"
"s_barrier\n"
"ds_read_b64_tr_b16 a[208:209], v225 offset:0\n"
"v_mfma_f32_16x16x32_bf16 v[56:59], a[204:207], a[80:83], v[56:59]\n"
"ds_read_b64_tr_b16 a[210:211], v225 offset:2048\n"
"v_mfma_f32_16x16x32_bf16 v[60:63], a[204:207], a[84:87], v[60:63]\n"
"buffer_load_dwordx4 v233, s[56:59], s49 offen offset:3072 sc1 lds\n"
"v_mfma_f32_16x16x32_bf16 v[60:63], a[200:203], a[60:63], v[60:63]\n"
"ds_read_b64_tr_b16 a[212:213], v225 offset:4096\n"
"v_mfma_f32_16x16x32_bf16 v[60:63], a[196:199], a[36:39], v[60:63]\n"
"s_add_u32 s49, s49, 0x80000\n"
"ds_read_b64_tr_b16 a[214:215], v225 offset:6144\n"
"v_mfma_f32_16x16x32_bf16 v[60:63], a[192:195], a[12:15], v[60:63]\n"
"s_add_u32 m0, s50, 0x1b000\n"
"ds_read_b64_tr_b16 a[216:217], v225 offset:8192\n"
"v_mfma_f32_16x16x32_bf16 v[64:67], a[192:195], a[16:19], v[64:67]\n"
"buffer_load_dwordx4 v230, s[56:59], s49 offen sc1 lds\n"
"v_mfma_f32_16x16x32_bf16 v[64:67], a[196:199], a[40:43], v[64:67]\n"
"ds_read_b64_tr_b16 a[218:219], v225 offset:10240\n"
"v_mfma_f32_16x16x32_bf16 v[64:67], a[200:203], a[64:67], v[64:67]\n"
"ds_read_b64_tr_b16 a[220:221], v225 offset:12288\n"
"v_mfma_f32_16x16x32_bf16 v[64:67], a[204:207], a[88:91], v[64:67]\n"
"ds_read_b64_tr_b16 a[222:223], v225 offset:14336\n"
"v_mfma_f32_16x16x32_bf16 v[68:71], a[204:207], a[92:95], v[68:71]\n"
"buffer_load_dwordx4 v231, s[56:59], s49 offen offset:1024 sc1 lds\n"
"v_mfma_f32_16x16x32_bf16 v[68:71], a[200:203], a[68:71], v[68:71]\n"
"ds_read_b128 a[172:175], v227 offset:19456\n"
"v_mfma_f32_16x16x32_bf16 v[68:71], a[196:199], a[44:47], v[68:71]\n"
"s_waitcnt lgkmcnt(5)\n"
"s_and_b32 s45, s45, 0xffff\n"
"v_mfma_f32_16x16x32_bf16 v[68:71], a[192:195], a[20:23], v[68:71]\n"
"s_mov_b32 s46, 0x3c000\n"
"v_mfma_f32_16x16x32_bf16 v[72:75], a[208:211], a[0:3], v[72:75]\n"
"s_add_u32 m0, s91, 0x24000\n"
"s_nop 0\n"
"buffer_load_dwordx4 v253, s[92:95], s90 offen offset:3072 lds\n"
"s_waitcnt lgkmcnt(3)\n"
"v_mfma_f32_16x16x32_bf16 v[72:75], a[212:215], a[24:27], v[72:75]\n"
"s_waitcnt lgkmcnt(1)\n"
"ds_read_b128 a[104:107], v227 offset:2048\n"
"v_mfma_f32_16x16x32_bf16 v[72:75], a[216:219], a[48:51], v[72:75]\n"
"s_add_u32 s90, s87, s42\n"
"ds_read_b128 a[128:131], v227 offset:8192\n"
"v_mfma_f32_16x16x32_bf16 v[72:75], a[220:223], a[72:75], v[72:75]\n"
"s_add_u32 m0, s91, 0x0\n"
"ds_read_b128 a[152:155], v227 offset:14336\n"
"v_mfma_f32_16x16x32_bf16 v[76:79], a[220:223], a[76:79], v[76:79]\n"
"buffer_load_dwordx4 v245, s[92:95], s90 offen lds\n"
"v_mfma_f32_16x16x32_bf16 v[76:79], a[216:219], a[52:55], v[76:79]\n"
"ds_read_b128 a[176:179], v227 offset:20480\n"
"v_mfma_f32_16x16x32_bf16 v[76:79], a[212:215], a[28:31], v[76:79]\n"
"ds_read_b128 a[108:111], v227 offset:3072\n"
"v_mfma_f32_16x16x32_bf16 v[76:79], a[208:211], a[4:7], v[76:79]\n"
"ds_read_b128 a[132:135], v227 offset:9216\n"
"v_mfma_f32_16x16x32_bf16 v[80:83], a[208:211], a[8:11], v[80:83]\n"
"buffer_load_dwordx4 v251, s[92:95], s90 offen offset:1024 lds\n"
"v_mfma_f32_16x16x32_bf16 v[80:83], a[212:215], a[32:35], v[80:83]\n"
"ds_read_b128 a[156:159], v227 offset:15360\n"
"v_mfma_f32_16x16x32_bf16 v[80:83], a[216:219], a[56:59], v[80:83]\n"
"s_waitcnt vmcnt(16)\n"
"s_barrier\n"
"ds_read_b64_tr_b16 a[192:193], v224 offset:16384\n"
"v_mfma_f32_16x16x32_bf16 v[80:83], a[220:223], a[80:83], v[80:83]\n"
"ds_read_b64_tr_b16 a[194:195], v224 offset:18432\n"
"v_mfma_f32_16x16x32_bf16 v[84:87], a[220:223], a[84:87], v[84:87]\n"
"buffer_load_dwordx4 v252, s[92:95], s90 offen offset:2048 lds\n"
"v_mfma_f32_16x16x32_bf16 v[84:87], a[216:219], a[60:63], v[84:87]\n"
"ds_read_b64_tr_b16 a[196:197], v224 offset:20480\n"
"v_mfma_f32_16x16x32_bf16 v[84:87], a[212:215], a[36:39], v[84:87]\n"
"ds_read_b64_tr_b16 a[198:199], v224 offset:22528\n"
"v_mfma_f32_16x16x32_bf16 v[84:87], a[208:211], a[12:15], v[84:87]\n"
"ds_read_b64_tr_b16 a[200:201], v224 offset:24576\n"
"v_mfma_f32_16x16x32_bf16 v[88:91], a[208:211], a[16:19], v[88:91]\n"
"buffer_load_dwordx4 v253, s[92:95], s90 offen offset:3072 lds\n"
"v_mfma_f32_16x16x32_bf16 v[88:91], a[212:215], a[40:43], v[88:91]\n"
"ds_read_b64_tr_b16 a[202:203], v224 offset:26624\n"
"v_mfma_f32_16x16x32_bf16 v[88:91], a[216:219], a[64:67], v[88:91]\n"
"s_add_u32 s90, s87, s43\n"
"ds_read_b64_tr_b16 a[204:205], v224 offset:28672\n"
"v_mfma_f32_16x16x32_bf16 v[88:91], a[220:223], a[88:91], v[88:91]\n"
"s_add_u32 m0, s91, 0x10000\n"
"ds_read_b64_tr_b16 a[206:207], v224 offset:30720\n"
"v_mfma_f32_16x16x32_bf16 v[92:95], a[220:223], a[92:95], v[92:95]\n"
"buffer_load_dwordx4 v245, s[92:95], s90 offen lds\n"
"v_mfma_f32_16x16x32_bf16 v[92:95], a[216:219], a[68:71], v[92:95]\n"
"ds_read_b128 a[180:183], v227 offset:21504\n"
"v_mfma_f32_16x16x32_bf16 v[92:95], a[212:215], a[44:47], v[92:95]\n"
"s_waitcnt lgkmcnt(5)\n"
"s_mov_b32 s47, 0x20000\n"
"v_mfma_f32_16x16x32_bf16 v[92:95], a[208:211], a[20:23], v[92:95]\n"
"v_lshrrev_b32 v205, 4, v244\n"
"v_and_b32 v205, 1, v205\n"
"v_mfma_f32_16x16x32_bf16 v[96:99], a[192:195], a[0:3], v[96:99]\n"
"s_add_u32 m0, s91, 0x10000\n"
"s_nop 0\n"
"buffer_load_dwordx4 v251, s[92:95], s90 offen offset:1024 lds\n"
"s_waitcnt lgkmcnt(3)\n"
"v_mfma_f32_16x16x32_bf16 v[96:99], a[196:199], a[24:27], v[96:99]\n"
"s_waitcnt lgkmcnt(1)\n"
"ds_read_b128 a[112:115], v227 offset:4096\n"
"v_mfma_f32_16x16x32_bf16 v[96:99], a[200:203], a[48:51], v[96:99]\n"
"ds_read_b128 a[136:139], v227 offset:10240\n"
"v_mfma_f32_16x16x32_bf16 v[96:99], a[204:207], a[72:75], v[96:99]\n"
"ds_read_b128 a[160:163], v227 offset:16384\n"
"v_mfma_f32_16x16x32_bf16 v[100:103], a[204:207], a[76:79], v[100:103]\n"
"buffer_load_dwordx4 v252, s[92:95], s90 offen offset:2048 lds\n"
"v_mfma_f32_16x16x32_bf16 v[100:103], a[200:203], a[52:55], v[100:103]\n"
"ds_read_b128 a[184:187], v227 offset:22528\n"
"v_mfma_f32_16x16x32_bf16 v[100:103], a[196:199], a[28:31], v[100:103]\n"
"ds_read_b128 a[116:119], v227 offset:5120\n"
"v_mfma_f32_16x16x32_bf16 v[100:103], a[192:195], a[4:7], v[100:103]\n"
"ds_read_b128 a[140:143], v227 offset:11264\n"
"v_mfma_f32_16x16x32_bf16 v[104:107], a[192:195], a[8:11], v[104:107]\n"
"buffer_load_dwordx4 v253, s[92:95], s90 offen offset:3072 lds\n"
"v_mfma_f32_16x16x32_bf16 v[104:107], a[196:199], a[32:35], v[104:107]\n"
"ds_read_b128 a[164:167], v227 offset:17408\n"
"v_mfma_f32_16x16x32_bf16 v[104:107], a[200:203], a[56:59], v[104:107]\n"
"s_waitcnt vmcnt(18)\n"
"s_barrier\n"
"ds_read_b64_tr_b16 a[208:209], v226 offset:0\n"
"v_mfma_f32_16x16x32_bf16 v[104:107], a[204:207], a[80:83], v[104:107]\n"
"s_add_u32 s90, s87, s65\n"
"s_add_u32 m0, s91, 0x4000\n"
"ds_read_b64_tr_b16 a[210:211], v226 offset:2048\n"
"v_mfma_f32_16x16x32_bf16 v[108:111], a[204:207], a[84:87], v[108:111]\n"
"buffer_load_dwordx4 v245, s[92:95], s90 offen lds\n"
"v_mfma_f32_16x16x32_bf16 v[108:111], a[200:203], a[60:63], v[108:111]\n"
"ds_read_b64_tr_b16 a[212:213], v226 offset:4096\n"
"v_mfma_f32_16x16x32_bf16 v[108:111], a[196:199], a[36:39], v[108:111]\n"
"ds_read_b64_tr_b16 a[214:215], v226 offset:6144\n"
"v_mfma_f32_16x16x32_bf16 v[108:111], a[192:195], a[12:15], v[108:111]\n"
"ds_read_b64_tr_b16 a[216:217], v226 offset:8192\n"
"v_mfma_f32_16x16x32_bf16 v[112:115], a[192:195], a[16:19], v[112:115]\n"
"buffer_load_dwordx4 v251, s[92:95], s90 offen offset:1024 lds\n"
"v_mfma_f32_16x16x32_bf16 v[112:115], a[196:199], a[40:43], v[112:115]\n"
"ds_read_b64_tr_b16 a[218:219], v226 offset:10240\n"
"v_mfma_f32_16x16x32_bf16 v[112:115], a[200:203], a[64:67], v[112:115]\n"
"ds_read_b64_tr_b16 a[220:221], v226 offset:12288\n"
"v_mfma_f32_16x16x32_bf16 v[112:115], a[204:207], a[88:91], v[112:115]\n"
"ds_read_b64_tr_b16 a[222:223], v226 offset:14336\n"
"v_mfma_f32_16x16x32_bf16 v[116:119], a[204:207], a[92:95], v[116:119]\n"
"buffer_load_dwordx4 v252, s[92:95], s90 offen offset:2048 lds\n"
"v_mfma_f32_16x16x32_bf16 v[116:119], a[200:203], a[68:71], v[116:119]\n"
"ds_read_b128 a[188:191], v227 offset:23552\n"
"s_add_u32 s72, %[hq0], 6\n"
"s_and_b32 s72, s72, 7\n"
"s_lshl_b32 s52, 3, s72\n"
"s_mov_b32 s53, 0\n"
"v_mfma_f32_16x16x32_bf16 v[116:119], a[196:199], a[44:47], v[116:119]\n"
"s_waitcnt lgkmcnt(5)\n"
"v_lshlrev_b32 v205, 3, v205\n"
"v_and_b32 v203, 15, v244\n"
"v_mfma_f32_16x16x32_bf16 v[116:119], a[192:195], a[20:23], v[116:119]\n"
"v_and_b32 v203, 7, v203\n"
"v_lshrrev_b32 v202, 4, v244\n"
"buffer_load_dwordx2 v[248:249], v238, s[56:59], s66 offen sc1\n"
"v_mfma_f32_16x16x32_bf16 v[0:3], a[208:211], a[96:99], v[0:3]\n"
"s_add_u32 m0, s91, 0x4000\n"
"s_nop 0\n"
"buffer_load_dwordx4 v253, s[92:95], s90 offen offset:3072 lds\n"
"s_waitcnt lgkmcnt(3)\n"
"v_mfma_f32_16x16x32_bf16 v[0:3], a[212:215], a[120:123], v[0:3]\n"
"s_waitcnt lgkmcnt(1)\n"
"v_mfma_f32_16x16x32_bf16 v[0:3], a[216:219], a[144:147], v[0:3]\n"
"v_lshrrev_b32 v202, 1, v202\n"
"v_mov_b32 v204, s77\n"
"v_mfma_f32_16x16x32_bf16 v[0:3], a[220:223], a[168:171], v[0:3]\n"
"v_lshlrev_b32 v204, 1, v204\n"
"v_add_u32 v204, v202, v204\n"
"v_mfma_f32_16x16x32_bf16 v[4:7], a[220:223], a[172:175], v[4:7]\n"
"v_mfma_f32_16x16x32_bf16 v[4:7], a[216:219], a[148:151], v[4:7]\n"
"v_xor_b32 v204, v203, v204\n"
"v_lshlrev_b32 v204, 4, v204\n"
"v_mfma_f32_16x16x32_bf16 v[4:7], a[212:215], a[124:127], v[4:7]\n"
"v_and_b32 v234, 15, v244\n"
"v_lshlrev_b32 v234, 7, v234\n"
"v_mfma_f32_16x16x32_bf16 v[4:7], a[208:211], a[100:103], v[4:7]\n"
"v_add_u32 v234, v204, v234\n"
"v_add_u32 v234, v205, v234\n"
"v_mfma_f32_16x16x32_bf16 v[8:11], a[208:211], a[104:107], v[8:11]\n"
"v_mfma_f32_16x16x32_bf16 v[8:11], a[212:215], a[128:131], v[8:11]\n"
"v_add_u32 v234, s76, v234\n"
"v_add_u32 v234, 0x14000, v234\n"
"v_mfma_f32_16x16x32_bf16 v[8:11], a[216:219], a[152:155], v[8:11]\n"
"s_waitcnt vmcnt(13)\n"
"s_barrier\n"
"ds_read_b64_tr_b16 a[192:193], v226 offset:16384\n"
"v_mfma_f32_16x16x32_bf16 v[8:11], a[220:223], a[176:179], v[8:11]\n"
"s_add_u32 s87, s87, 0x28000\n"
"s_cmp_eq_u32 s87, 0x140000\n"
"s_cselect_b32 s87, 0, s87\n"
"s_add_u32 s90, s87, 0x200\n"
"s_add_u32 m0, s91, 0x20000\n"
"ds_read_b64_tr_b16 a[194:195], v226 offset:18432\n"
"v_mfma_f32_16x16x32_bf16 v[12:15], a[220:223], a[180:183], v[12:15]\n"
"buffer_load_dwordx4 v245, s[92:95], s90 offen lds\n"
"v_mfma_f32_16x16x32_bf16 v[12:15], a[216:219], a[156:159], v[12:15]\n"
"ds_read_b64_tr_b16 a[196:197], v226 offset:20480\n"
"v_mfma_f32_16x16x32_bf16 v[12:15], a[212:215], a[132:135], v[12:15]\n"
"ds_read_b64_tr_b16 a[198:199], v226 offset:22528\n"
"v_mfma_f32_16x16x32_bf16 v[12:15], a[208:211], a[108:111], v[12:15]\n"
"ds_read_b64_tr_b16 a[200:201], v226 offset:24576\n"
"v_mfma_f32_16x16x32_bf16 v[16:19], a[208:211], a[112:115], v[16:19]\n"
"buffer_load_dwordx4 v251, s[92:95], s90 offen offset:1024 lds\n"
"v_mfma_f32_16x16x32_bf16 v[16:19], a[212:215], a[136:139], v[16:19]\n"
"ds_read_b64_tr_b16 a[202:203], v226 offset:26624\n"
"v_mfma_f32_16x16x32_bf16 v[16:19], a[216:219], a[160:163], v[16:19]\n"
"s_waitcnt lgkmcnt(6)\n"
"ds_read_b64_tr_b16 a[204:205], v226 offset:28672\n"
"v_mfma_f32_16x16x32_bf16 v[16:19], a[220:223], a[184:187], v[16:19]\n"
"ds_read_b64_tr_b16 a[206:207], v226 offset:30720\n"
"v_mfma_f32_16x16x32_bf16 v[20:23], a[220:223], a[188:191], v[20:23]\n"
"buffer_load_dwordx4 v252, s[92:95], s90 offen offset:2048 lds\n"
"v_mfma_f32_16x16x32_bf16 v[20:23], a[216:219], a[164:167], v[20:23]\n"
"v_mov_b32 v205, s77\n"
"v_lshlrev_b32 v205, 10, v205\n"
"v_mfma_f32_16x16x32_bf16 v[20:23], a[212:215], a[140:143], v[20:23]\n"
"s_waitcnt lgkmcnt(4)\n"
"v_lshrrev_b32 v203, 3, v244\n"
"v_and_b32 v203, 7, v203\n"
"v_mfma_f32_16x16x32_bf16 v[20:23], a[208:211], a[116:119], v[20:23]\n"
"v_and_b32 v204, 7, v244\n"
"v_xor_b32 v204, v203, v204\n"
"v_mfma_f32_16x16x32_bf16 v[24:27], a[192:195], a[96:99], v[24:27]\n"
"s_add_u32 s48, s48, 0x1000\n"
"s_and_b32 s48, s48, 0x7fff\n"
"s_add_u32 s49, s48, s39\n"
"s_add_u32 m0, s50, 0x14000\n"
"s_nop 0\n"
"buffer_load_dwordx4 v230, s[56:59], s49 offen sc1 lds\n"
"s_waitcnt lgkmcnt(2)\n"
"v_mfma_f32_16x16x32_bf16 v[24:27], a[196:199], a[120:123], v[24:27]\n"
"s_waitcnt lgkmcnt(0)\n"
"v_mfma_f32_16x16x32_bf16 v[24:27], a[200:203], a[144:147], v[24:27]\n"
"v_lshlrev_b32 v204, 4, v204\n"
"v_lshrrev_b32 v235, 3, v244\n"
"v_mfma_f32_16x16x32_bf16 v[24:27], a[204:207], a[168:171], v[24:27]\n"
"v_lshlrev_b32 v235, 7, v235\n"
"v_add_u32 v235, v204, v235\n"
"v_mfma_f32_16x16x32_bf16 v[28:31], a[204:207], a[172:175], v[28:31]\n"
"buffer_load_dwordx4 v231, s[56:59], s49 offen offset:1024 sc1 lds\n"
"v_mfma_f32_16x16x32_bf16 v[28:31], a[200:203], a[148:151], v[28:31]\n"
"v_add_u32 v235, v205, v235\n"
"v_add_u32 v235, s76, v235\n"
"v_mfma_f32_16x16x32_bf16 v[28:31], a[196:199], a[124:127], v[28:31]\n"
"v_add_u32 v235, 0x14000, v235\n"
"v_and_b32 v207, 7, v244\n"
"v_mfma_f32_16x16x32_bf16 v[28:31], a[192:195], a[100:103], v[28:31]\n"
"s_add_u32 m0, s50, 0x14000\n"
"v_lshlrev_b32 v207, 4, v207\n"
"v_mov_b32 v206, s77\n"
"v_mfma_f32_16x16x32_bf16 v[32:35], a[192:195], a[104:107], v[32:35]\n"
"buffer_load_dwordx4 v232, s[56:59], s49 offen offset:2048 sc1 lds\n"
"v_mfma_f32_16x16x32_bf16 v[32:35], a[196:199], a[128:131], v[32:35]\n"
"v_lshlrev_b32 v206, 3, v206\n"
"v_lshrrev_b32 v240, 3, v244\n"
"v_mfma_f32_16x16x32_bf16 v[32:35], a[200:203], a[152:155], v[32:35]\n"
"s_waitcnt vmcnt(15)\n"
"s_barrier\n"
"ds_read_b64_tr_b16 a[208:209], v224 offset:0\n"
"v_mfma_f32_16x16x32_bf16 v[32:35], a[204:207], a[176:179], v[32:35]\n"
"ds_read_b64_tr_b16 a[210:211], v224 offset:2048\n"
"v_mfma_f32_16x16x32_bf16 v[36:39], a[204:207], a[180:183], v[36:39]\n"
"buffer_load_dwordx4 v233, s[56:59], s49 offen offset:3072 sc1 lds\n"
"v_mfma_f32_16x16x32_bf16 v[36:39], a[200:203], a[156:159], v[36:39]\n"
"ds_read_b64_tr_b16 a[212:213], v224 offset:4096\n"
"v_mfma_f32_16x16x32_bf16 v[36:39], a[196:199], a[132:135], v[36:39]\n"
"s_add_u32 s49, s49, 0x80000\n"
"ds_read_b64_tr_b16 a[214:215], v224 offset:6144\n"
"v_mfma_f32_16x16x32_bf16 v[36:39], a[192:195], a[108:111], v[36:39]\n"
"s_add_u32 m0, s50, 0x15000\n"
"ds_read_b64_tr_b16 a[216:217], v224 offset:8192\n"
"v_mfma_f32_16x16x32_bf16 v[40:43], a[192:195], a[112:115], v[40:43]\n"
"buffer_load_dwordx4 v230, s[56:59], s49 offen sc1 lds\n"
"v_mfma_f32_16x16x32_bf16 v[40:43], a[196:199], a[136:139], v[40:43]\n"
"ds_read_b64_tr_b16 a[218:219], v224 offset:10240\n"
"v_mfma_f32_16x16x32_bf16 v[40:43], a[200:203], a[160:163], v[40:43]\n"
"ds_read_b64_tr_b16 a[220:221], v224 offset:12288\n"
"v_mfma_f32_16x16x32_bf16 v[40:43], a[204:207], a[184:187], v[40:43]\n"
"ds_read_b64_tr_b16 a[222:223], v224 offset:14336\n"
"v_mfma_f32_16x16x32_bf16 v[44:47], a[204:207], a[188:191], v[44:47]\n"
"buffer_load_dwordx4 v231, s[56:59], s49 offen offset:1024 sc1 lds\n"
"v_mfma_f32_16x16x32_bf16 v[44:47], a[200:203], a[164:167], v[44:47]\n"
"v_add_u32 v240, v206, v240\n"
"v_mul_u32_u24 v240, 0xa00, v240\n"
"v_mfma_f32_16x16x32_bf16 v[44:47], a[196:199], a[140:143], v[44:47]\n"
"s_waitcnt lgkmcnt(4)\n"
"v_add_u32 v240, v207, v240\n"
"v_mfma_f32_16x16x32_bf16 v[44:47], a[192:195], a[116:119], v[44:47]\n"
"s_lshl_b32 s40, %[kq], 7\n"
"s_waitcnt vmcnt(10)\n"
"s_mov_b32 s62, 0\n"
"LA17%=:\n"
"v_cmp_ne_u64 vcc, s[60:61], v[248:249]\n"
"s_and_b64 vcc, vcc, s[52:53]\n"
"s_cbranch_scc0 LB17%=\n"
"s_sleep 0\n"
"buffer_load_dwordx2 v[248:249], v238, s[56:59], s66 offen sc1\n"
"s_add_u32 s62, s62, 1\n"
"s_cmp_lt_u32 s62, 0x100000\n"
"s_waitcnt vmcnt(0)\n"
"s_cbranch_scc1 LA17%=\n"
"LB17%=:\n"
"v_mfma_f32_16x16x32_bf16 v[48:51], a[208:211], a[96:99], v[48:51]\n"
"s_add_u32 m0, s91, 0x20000\n"
"s_nop 0\n"
"buffer_load_dwordx4 v253, s[92:95], s90 offen offset:3072 lds\n"
"s_waitcnt lgkmcnt(2)\n"
"v_mfma_f32_16x16x32_bf16 v[48:51], a[212:215], a[120:123], v[48:51]\n"
"s_waitcnt lgkmcnt(0)\n"
"ds_read_b128 a[0:3], v228 offset:0\n"
"v_mfma_f32_16x16x32_bf16 v[48:51], a[216:219], a[144:147], v[48:51]\n"
"s_add_u32 s90, s87, s41\n"
"ds_read_b128 a[24:27], v228 offset:6144\n"
"v_mfma_f32_16x16x32_bf16 v[48:51], a[220:223], a[168:171], v[48:51]\n"
"s_add_u32 m0, s91, 0x24000\n"
"ds_read_b128 a[48:51], v228 offset:12288\n"
"v_mfma_f32_16x16x32_bf16 v[52:55], a[220:223], a[172:175], v[52:55]\n"
"buffer_load_dwordx4 v245, s[92:95], s90 offen lds\n"
"v_mfma_f32_16x16x32_bf16 v[52:55], a[216:219], a[148:151], v[52:55]\n"
"ds_read_b128 a[72:75], v228 offset:18432\n"
"v_mfma_f32_16x16x32_bf16 v[52:55], a[212:215], a[124:127], v[52:55]\n"
"ds_read_b128 a[4:7], v228 offset:1024\n"
"v_mfma_f32_16x16x32_bf16 v[52:55], a[208:211], a[100:103], v[52:55]\n"
"ds_read_b128 a[28:31], v228 offset:7168\n"
"v_mfma_f32_16x16x32_bf16 v[56:59], a[208:211], a[104:107], v[56:59]\n"
"buffer_load_dwordx4 v251, s[92:95], s90 offen offset:1024 lds\n"
"v_mfma_f32_16x16x32_bf16 v[56:59], a[212:215], a[128:131], v[56:59]\n"
"ds_read_b128 a[52:55], v228 offset:13312\n"
"v_mfma_f32_16x16x32_bf16 v[56:59], a[216:219], a[152:155], v[56:59]\n"
"s_barrier\n"
"ds_read_b64_tr_b16 a[192:193], v225 offset:0\n"
"v_mfma_f32_16x16x32_bf16 v[56:59], a[220:223], a[176:179], v[56:59]\n"
"ds_read_b64_tr_b16 a[194:195], v225 offset:2048\n"
"v_mfma_f32_16x16x32_bf16 v[60:63], a[220:223], a[180:183], v[60:63]\n"
"buffer_load_dwordx4 v252, s[92:95], s90 offen offset:2048 lds\n"
"v_mfma_f32_16x16x32_bf16 v[60:63], a[216:219], a[156:159], v[60:63]\n"
"ds_read_b64_tr_b16 a[196:197], v225 offset:4096\n"
"v_mfma_f32_16x16x32_bf16 v[60:63], a[212:215], a[132:135], v[60:63]\n"
"ds_read_b64_tr_b16 a[198:199], v225 offset:6144\n"
"v_mfma_f32_16x16x32_bf16 v[60:63], a[208:211], a[108:111], v[60:63]\n"
"ds_read_b64_tr_b16 a[200:201], v225 offset:8192\n"
"v_mfma_f32_16x16x32_bf16 v[64:67], a[208:211], a[112:115], v[64:67]\n"
"buffer_load_dwordx4 v253, s[92:95], s90 offen offset:3072 lds\n"
"v_mfma_f32_16x16x32_bf16 v[64:67], a[212:215], a[136:139], v[64:67]\n"
"ds_read_b64_tr_b16 a[202:203], v225 offset:10240\n"
"v_mfma_f32_16x16x32_bf16 v[64:67], a[216:219], a[160:163], v[64:67]\n"
"s_add_u32 s90, s87, s42\n"
"ds_read_b64_tr_b16 a[204:205], v225 offset:12288\n"
"v_mfma_f32_16x16x32_bf16 v[64:67], a[220:223], a[184:187], v[64:67]\n"
"s_add_u32 m0, s91, 0x0\n"
"ds_read_b64_tr_b16 a[206:207], v225 offset:14336\n"
"v_mfma_f32_16x16x32_bf16 v[68:71], a[220:223], a[188:191], v[68:71]\n"
"buffer_load_dwordx4 v245, s[92:95], s90 offen lds\n"
"v_mfma_f32_16x16x32_bf16 v[68:71], a[216:219], a[164:167], v[68:71]\n"
"ds_read_b128 a[76:79], v228 offset:19456\n"
"v_mfma_f32_16x16x32_bf16 v[68:71], a[212:215], a[140:143], v[68:71]\n"
"s_waitcnt lgkmcnt(5)\n"
"s_mul_i32 s55, %[kq], 81920\n"
"v_mfma_f32_16x16x32_bf16 v[68:71], a[208:211], a[116:119], v[68:71]\n"
"s_add_u32 s55, s55, 0x200\n"
"v_mfma_f32_16x16x32_bf16 v[72:75], a[192:195], a[96:99], v[72:75]\n"
"s_add_u32 m0, s91, 0x0\n"
"s_nop 0\n"
"buffer_load_dwordx4 v251, s[92:95], s90 offen offset:1024 lds\n"
"s_waitcnt lgkmcnt(3)\n"
"v_mfma_f32_16x16x32_bf16 v[72:75], a[196:199], a[120:123], v[72:75]\n"
"s_waitcnt lgkmcnt(1)\n"
"ds_read_b128 a[8:11], v228 offset:2048\n"
"v_mfma_f32_16x16x32_bf16 v[72:75], a[200:203], a[144:147], v[72:75]\n"
"ds_read_b128 a[32:35], v228 offset:8192\n"
"v_mfma_f32_16x16x32_bf16 v[72:75], a[204:207], a[168:171], v[72:75]\n"
"ds_read_b128 a[56:59], v228 offset:14336\n"
"v_mfma_f32_16x16x32_bf16 v[76:79], a[204:207], a[172:175], v[76:79]\n"
"buffer_load_dwordx4 v252, s[92:95], s90 offen offset:2048 lds\n"
"v_mfma_f32_16x16x32_bf16 v[76:79], a[200:203], a[148:151], v[76:79]\n"
"ds_read_b128 a[80:83], v228 offset:20480\n"
"v_mfma_f32_16x16x32_bf16 v[76:79], a[196:199], a[124:127], v[76:79]\n"
"ds_read_b128 a[12:15], v228 offset:3072\n"
"v_mfma_f32_16x16x32_bf16 v[76:79], a[192:195], a[100:103], v[76:79]\n"
"ds_read_b128 a[36:39], v228 offset:9216\n"
"v_mfma_f32_16x16x32_bf16 v[80:83], a[192:195], a[104:107], v[80:83]\n"
"buffer_load_dwordx4 v253, s[92:95], s90 offen offset:3072 lds\n"
"v_mfma_f32_16x16x32_bf16 v[80:83], a[196:199], a[128:131], v[80:83]\n"
"ds_read_b128 a[60:63], v228 offset:15360\n"
"v_mfma_f32_16x16x32_bf16 v[80:83], a[200:203], a[152:155], v[80:83]\n"
"s_waitcnt vmcnt(18)\n"
"s_barrier\n"
"ds_read_b64_tr_b16 a[208:209], v224 offset:16384\n"
"v_mfma_f32_16x16x32_bf16 v[80:83], a[204:207], a[176:179], v[80:83]\n"
"s_add_u32 s90, s87, s43\n"
"s_add_u32 m0, s91, 0x10000\n"
"ds_read_b64_tr_b16 a[210:211], v224 offset:18432\n"
"v_mfma_f32_16x16x32_bf16 v[84:87], a[204:207], a[180:183], v[84:87]\n"
"buffer_load_dwordx4 v245, s[92:95], s90 offen lds\n"
"v_mfma_f32_16x16x32_bf16 v[84:87], a[200:203], a[156:159], v[84:87]\n"
"ds_read_b64_tr_b16 a[212:213], v224 offset:20480\n"
"v_mfma_f32_16x16x32_bf16 v[84:87], a[196:199], a[132:135], v[84:87]\n"
"ds_read_b64_tr_b16 a[214:215], v224 offset:22528\n"
"v_mfma_f32_16x16x32_bf16 v[84:87], a[192:195], a[108:111], v[84:87]\n"
"ds_read_b64_tr_b16 a[216:217], v224 offset:24576\n"
"v_mfma_f32_16x16x32_bf16 v[88:91], a[192:195], a[112:115], v[88:91]\n"
"buffer_load_dwordx4 v251, s[92:95], s90 offen offset:1024 lds\n"
"v_mfma_f32_16x16x32_bf16 v[88:91], a[196:199], a[136:139], v[88:91]\n"
"ds_read_b64_tr_b16 a[218:219], v224 offset:26624\n"
"v_mfma_f32_16x16x32_bf16 v[88:91], a[200:203], a[160:163], v[88:91]\n"
"ds_read_b64_tr_b16 a[220:221], v224 offset:28672\n"
"v_mfma_f32_16x16x32_bf16 v[88:91], a[204:207], a[184:187], v[88:91]\n"
"ds_read_b64_tr_b16 a[222:223], v224 offset:30720\n"
"v_mfma_f32_16x16x32_bf16 v[92:95], a[204:207], a[188:191], v[92:95]\n"
"buffer_load_dwordx4 v252, s[92:95], s90 offen offset:2048 lds\n"
"v_mfma_f32_16x16x32_bf16 v[92:95], a[200:203], a[164:167], v[92:95]\n"
"ds_read_b128 a[84:87], v228 offset:21504\n"
"v_mfma_f32_16x16x32_bf16 v[92:95], a[196:199], a[140:143], v[92:95]\n"
"s_waitcnt lgkmcnt(5)\n"
"v_mfma_f32_16x16x32_bf16 v[92:95], a[192:195], a[116:119], v[92:95]\n"
"v_mfma_f32_16x16x32_bf16 v[96:99], a[208:211], a[96:99], v[96:99]\n"
"s_add_u32 s48, s48, 0x1000\n"
"s_and_b32 s48, s48, 0x7fff\n"
"s_add_u32 s49, s48, s39\n"
"s_add_u32 m0, s50, 0xa000\n"
"s_nop 0\n"
"buffer_load_dwordx4 v230, s[56:59], s49 offen sc1 lds\n"
"s_waitcnt lgkmcnt(3)\n"
"v_mfma_f32_16x16x32_bf16 v[96:99], a[212:215], a[120:123], v[96:99]\n"
"s_waitcnt lgkmcnt(1)\n"
"ds_read_b128 a[16:19], v228 offset:4096\n"
"v_mfma_f32_16x16x32_bf16 v[96:99], a[216:219], a[144:147], v[96:99]\n"
"ds_read_b128 a[40:43], v228 offset:10240\n"
"v_mfma_f32_16x16x32_bf16 v[96:99], a[220:223], a[168:171], v[96:99]\n"
"ds_read_b128 a[64:67], v228 offset:16384\n"
"v_mfma_f32_16x16x32_bf16 v[100:103], a[220:223], a[172:175], v[100:103]\n"
"buffer_load_dwordx4 v231, s[56:59], s49 offen offset:1024 sc1 lds\n"
"v_mfma_f32_16x16x32_bf16 v[100:103], a[216:219], a[148:151], v[100:103]\n"
"ds_read_b128 a[88:91], v228 offset:22528\n"
"v_mfma_f32_16x16x32_bf16 v[100:103], a[212:215], a[124:127], v[100:103]\n"
"ds_read_b128 a[20:23], v228 offset:5120\n"
"v_mfma_f32_16x16x32_bf16 v[100:103], a[208:211], a[100:103], v[100:103]\n"
"s_add_u32 m0, s50, 0xa000\n"
"ds_read_b128 a[44:47], v228 offset:11264\n"
"v_mfma_f32_16x16x32_bf16 v[104:107], a[208:211], a[104:107], v[104:107]\n"
"buffer_load_dwordx4 v232, s[56:59], s49 offen offset:2048 sc1 lds\n"
"v_mfma_f32_16x16x32_bf16 v[104:107], a[212:215], a[128:131], v[104:107]\n"
"ds_read_b128 a[68:71], v228 offset:17408\n"
"v_mfma_f32_16x16x32_bf16 v[104:107], a[216:219], a[152:155], v[104:107]\n"
"s_waitcnt vmcnt(14)\n"
"s_barrier\n"
"ds_read_b64_tr_b16 a[192:193], v226 offset:0\n"
"v_mfma_f32_16x16x32_bf16 v[104:107], a[220:223], a[176:179], v[104:107]\n"
"ds_read_b64_tr_b16 a[194:195], v226 offset:2048\n"
"v_mfma_f32_16x16x32_bf16 v[108:111], a[220:223], a[180:183], v[108:111]\n"
"buffer_load_dwordx4 v233, s[56:59], s49 offen offset:3072 sc1 lds\n"
"v_mfma_f32_16x16x32_bf16 v[108:111], a[216:219], a[156:159], v[108:111]\n"
"ds_read_b64_tr_b16 a[196:197], v226 offset:4096\n"
"v_mfma_f32_16x16x32_bf16 v[108:111], a[212:215], a[132:135], v[108:111]\n"
"s_add_u32 s49, s49, 0x80000\n"
"ds_read_b64_tr_b16 a[198:199], v226 offset:6144\n"
"v_mfma_f32_16x16x32_bf16 v[108:111], a[208:211], a[108:111], v[108:111]\n"
"s_add_u32 m0, s50, 0xb000\n"
"ds_read_b64_tr_b16 a[200:201], v226 offset:8192\n"
"v_mfma_f32_16x16x32_bf16 v[112:115], a[208:211], a[112:115], v[112:115]\n"
"buffer_load_dwordx4 v230, s[56:59], s49 offen sc1 lds\n"
"v_mfma_f32_16x16x32_bf16 v[112:115], a[212:215], a[136:139], v[112:115]\n"
"ds_read_b64_tr_b16 a[202:203], v226 offset:10240\n"
"v_mfma_f32_16x16x32_bf16 v[112:115], a[216:219], a[160:163], v[112:115]\n"
"ds_read_b64_tr_b16 a[204:205], v226 offset:12288\n"
"v_mfma_f32_16x16x32_bf16 v[112:115], a[220:223], a[184:187], v[112:115]\n"
"ds_read_b64_tr_b16 a[206:207], v226 offset:14336\n"
"v_mfma_f32_16x16x32_bf16 v[116:119], a[220:223], a[188:191], v[116:119]\n"
"buffer_load_dwordx4 v231, s[56:59], s49 offen offset:1024 sc1 lds\n"
"v_mfma_f32_16x16x32_bf16 v[116:119], a[216:219], a[164:167], v[116:119]\n"
"ds_read_b128 a[92:95], v228 offset:23552\n"
"s_lshr_b32 s72, s43, 7\n"
"s_sub_u32 s72, s72, %[kq]\n"
"s_lshl_b32 s72, s72, 10\n"
"s_add_u32 s68, s72, s64\n"
"s_mov_b32 s52, 1\n"
"v_mfma_f32_16x16x32_bf16 v[116:119], a[212:215], a[140:143], v[116:119]\n"
"s_waitcnt lgkmcnt(5)\n"
"v_mfma_f32_16x16x32_bf16 v[116:119], a[208:211], a[116:119], v[116:119]\n"
"buffer_load_dwordx2 v[248:249], v238, s[56:59], s68 offen sc1\n"
"v_mfma_f32_16x16x32_bf16 v[0:3], a[192:195], a[0:3], v[0:3]\n"
"s_add_u32 m0, s91, 0x10000\n"
"s_nop 0\n"
"buffer_load_dwordx4 v253, s[92:95], s90 offen offset:3072 lds\n"
"s_waitcnt lgkmcnt(3)\n"
"v_mfma_f32_16x16x32_bf16 v[0:3], a[196:199], a[24:27], v[0:3]\n"
"s_waitcnt lgkmcnt(1)\n"
"ds_read_b128 a[96:99], v227 offset:40960\n"
"v_mfma_f32_16x16x32_bf16 v[0:3], a[200:203], a[48:51], v[0:3]\n"
"s_add_u32 s90, s87, s65\n"
"ds_read_b128 a[120:123], v227 offset:47104\n"
"v_mfma_f32_16x16x32_bf16 v[0:3], a[204:207], a[72:75], v[0:3]\n"
"s_add_u32 m0, s91, 0x4000\n"
"ds_read_b128 a[144:147], v227 offset:53248\n"
"v_mfma_f32_16x16x32_bf16 v[4:7], a[204:207], a[76:79], v[4:7]\n"
"buffer_load_dwordx4 v245, s[92:95], s90 offen lds\n"
"v_mfma_f32_16x16x32_bf16 v[4:7], a[200:203], a[52:55], v[4:7]\n"
"ds_read_b128 a[168:171], v227 offset:59392\n"
"v_mfma_f32_16x16x32_bf16 v[4:7], a[196:199], a[28:31], v[4:7]\n"
"ds_read_b128 a[100:103], v227 offset:41984\n"
"v_mfma_f32_16x16x32_bf16 v[4:7], a[192:195], a[4:7], v[4:7]\n"
"ds_read_b128 a[124:127], v227 offset:48128\n"
"v_mfma_f32_16x16x32_bf16 v[8:11], a[192:195], a[8:11], v[8:11]\n"
"buffer_load_dwordx4 v251, s[92:95], s90 offen offset:1024 lds\n"
"v_mfma_f32_16x16x32_bf16 v[8:11], a[196:199], a[32:35], v[8:11]\n"
"ds_read_b128 a[148:151], v227 offset:54272\n"
"v_mfma_f32_16x16x32_bf16 v[8:11], a[200:203], a[56:59], v[8:11]\n"
"s_waitcnt vmcnt(17)\n"
"s_barrier\n"
"ds_read_b64_tr_b16 a[208:209], v226 offset:16384\n"
"v_mfma_f32_16x16x32_bf16 v[8:11], a[204:207], a[80:83], v[8:11]\n"
"ds_read_b64_tr_b16 a[210:211], v226 offset:18432\n"
"v_mfma_f32_16x16x32_bf16 v[12:15], a[204:207], a[84:87], v[12:15]\n"
"buffer_load_dwordx4 v252, s[92:95], s90 offen offset:2048 lds\n"
"v_mfma_f32_16x16x32_bf16 v[12:15], a[200:203], a[60:63], v[12:15]\n"
"ds_read_b64_tr_b16 a[212:213], v226 offset:20480\n"
"v_mfma_f32_16x16x32_bf16 v[12:15], a[196:199], a[36:39], v[12:15]\n"
"ds_read_b64_tr_b16 a[214:215], v226 offset:22528\n"
"v_mfma_f32_16x16x32_bf16 v[12:15], a[192:195], a[12:15], v[12:15]\n"
"ds_read_b64_tr_b16 a[216:217], v226 offset:24576\n"
"v_mfma_f32_16x16x32_bf16 v[16:19], a[192:195], a[16:19], v[16:19]\n"
"buffer_load_dwordx4 v253, s[92:95], s90 offen offset:3072 lds\n"
"v_mfma_f32_16x16x32_bf16 v[16:19], a[196:199], a[40:43], v[16:19]\n"
"s_add_u32 s87, s87, 0x28000\n"
"s_cmp_eq_u32 s87, 0x140000\n"
"s_cselect_b32 s87, 0, s87\n"
"s_add_u32 s90, s87, 0x200\n"
"ds_read_b64_tr_b16 a[218:219], v226 offset:26624\n"
"v_mfma_f32_16x16x32_bf16 v[16:19], a[200:203], a[64:67], v[16:19]\n"
"s_waitcnt lgkmcnt(13)\n"
"ds_read_b64_tr_b16 a[220:221], v226 offset:28672\n"
"v_mfma_f32_16x16x32_bf16 v[16:19], a[204:207], a[88:91], v[16:19]\n"
"s_add_u32 m0, s91, 0x20000\n"
"ds_read_b64_tr_b16 a[222:223], v226 offset:30720\n"
"v_mfma_f32_16x16x32_bf16 v[20:23], a[204:207], a[92:95], v[20:23]\n"
"buffer_load_dwordx4 v245, s[92:95], s90 offen lds\n"
"v_mfma_f32_16x16x32_bf16 v[20:23], a[200:203], a[68:71], v[20:23]\n"
"ds_read_b128 a[172:175], v227 offset:60416\n"
"v_mfma_f32_16x16x32_bf16 v[20:23], a[196:199], a[44:47], v[20:23]\n"
"s_waitcnt lgkmcnt(5)\n"
"ds_read_b128 a[104:107], v227 offset:43008\n"
"v_mfma_f32_16x16x32_bf16 v[20:23], a[192:195], a[20:23], v[20:23]\n"
"ds_read_b128 a[128:131], v227 offset:49152\n"
"v_mfma_f32_16x16x32_bf16 v[24:27], a[208:211], a[0:3], v[24:27]\n"
"s_add_u32 m0, s91, 0x20000\n"
"s_nop 0\n"
"buffer_load_dwordx4 v251, s[92:95], s90 offen offset:1024 lds\n"
"s_waitcnt lgkmcnt(5)\n"
"v_mfma_f32_16x16x32_bf16 v[24:27], a[212:215], a[24:27], v[24:27]\n"
"s_waitcnt lgkmcnt(3)\n"
"ds_read_b128 a[152:155], v227 offset:55296\n"
"v_mfma_f32_16x16x32_bf16 v[24:27], a[216:219], a[48:51], v[24:27]\n"
"ds_read_b128 a[176:179], v227 offset:61440\n"
"v_mfma_f32_16x16x32_bf16 v[24:27], a[220:223], a[72:75], v[24:27]\n"
"ds_read_b128 a[108:111], v227 offset:44032\n"
"v_mfma_f32_16x16x32_bf16 v[28:31], a[220:223], a[76:79], v[28:31]\n"
"buffer_load_dwordx4 v252, s[92:95], s90 offen offset:2048 lds\n"
"v_mfma_f32_16x16x32_bf16 v[28:31], a[216:219], a[52:55], v[28:31]\n"
"ds_read_b128 a[132:135], v227 offset:50176\n"
"v_mfma_f32_16x16x32_bf16 v[28:31], a[212:215], a[28:31], v[28:31]\n"
"ds_read_b128 a[156:159], v227 offset:56320\n"
"v_mfma_f32_16x16x32_bf16 v[28:31], a[208:211], a[4:7], v[28:31]\n"
"ds_read_b128 a[180:183], v227 offset:62464\n"
"v_mfma_f32_16x16x32_bf16 v[32:35], a[208:211], a[8:11], v[32:35]\n"
"buffer_load_dwordx4 v253, s[92:95], s90 offen offset:3072 lds\n"
"v_mfma_f32_16x16x32_bf16 v[32:35], a[212:215], a[32:35], v[32:35]\n"
"ds_read_b128 a[112:115], v227 offset:45056\n"
"v_mfma_f32_16x16x32_bf16 v[32:35], a[216:219], a[56:59], v[32:35]\n"
"s_waitcnt vmcnt(19)\n"
"s_barrier\n"
"ds_read_b64_tr_b16 a[192:193], v224 offset:0\n"
"v_mfma_f32_16x16x32_bf16 v[32:35], a[220:223], a[80:83], v[32:35]\n"
"s_add_u32 s51, s87, 0x28000\n"
"s_cmp_eq_u32 s51, 0x140000\n"
"s_cselect_b32 s51, 0, s51\n"
"s_add_u32 s90, s51, 0x200\n"
"s_add_u32 m0, s91, 0x24000\n"
"ds_read_b64_tr_b16 a[194:195], v224 offset:2048\n"
"v_mfma_f32_16x16x32_bf16 v[36:39], a[220:223], a[84:87], v[36:39]\n"
"buffer_load_dwordx4 v245, s[92:95], s90 offen lds\n"
"v_mfma_f32_16x16x32_bf16 v[36:39], a[216:219], a[60:63], v[36:39]\n"
"ds_read_b64_tr_b16 a[196:197], v224 offset:4096\n"
"v_mfma_f32_16x16x32_bf16 v[36:39], a[212:215], a[36:39], v[36:39]\n"
"ds_read_b64_tr_b16 a[198:199], v224 offset:6144\n"
"v_mfma_f32_16x16x32_bf16 v[36:39], a[208:211], a[12:15], v[36:39]\n"
"ds_read_b64_tr_b16 a[200:201], v224 offset:8192\n"
"v_mfma_f32_16x16x32_bf16 v[40:43], a[208:211], a[16:19], v[40:43]\n"
"buffer_load_dwordx4 v251, s[92:95], s90 offen offset:1024 lds\n"
"v_mfma_f32_16x16x32_bf16 v[40:43], a[212:215], a[40:43], v[40:43]\n"
"ds_read_b64_tr_b16 a[202:203], v224 offset:10240\n"
"v_mfma_f32_16x16x32_bf16 v[40:43], a[216:219], a[64:67], v[40:43]\n"
"ds_read_b64_tr_b16 a[204:205], v224 offset:12288\n"
"v_mfma_f32_16x16x32_bf16 v[40:43], a[220:223], a[88:91], v[40:43]\n"
"ds_read_b64_tr_b16 a[206:207], v224 offset:14336\n"
"v_mfma_f32_16x16x32_bf16 v[44:47], a[220:223], a[92:95], v[44:47]\n"
"buffer_load_dwordx4 v252, s[92:95], s90 offen offset:2048 lds\n"
"v_mfma_f32_16x16x32_bf16 v[44:47], a[216:219], a[68:71], v[44:47]\n"
"ds_read_b128 a[136:139], v227 offset:51200\n"
"v_mfma_f32_16x16x32_bf16 v[44:47], a[212:215], a[44:47], v[44:47]\n"
"s_waitcnt lgkmcnt(5)\n"
"ds_read_b128 a[160:163], v227 offset:57344\n"
"v_mfma_f32_16x16x32_bf16 v[44:47], a[208:211], a[20:23], v[44:47]\n"
"ds_read_b128 a[184:187], v227 offset:63488\n"
"s_waitcnt vmcnt(12)\n"
"s_mov_b32 s62, 0\n"
"LA22%=:\n"
"v_cmp_ne_u64 vcc, s[60:61], v[248:249]\n"
"s_and_b64 vcc, vcc, s[52:53]\n"
"s_cbranch_scc0 LB22%=\n"
"s_sleep 0\n"
"buffer_load_dwordx2 v[248:249], v238, s[56:59], s68 offen sc1\n"
"s_add_u32 s62, s62, 1\n"
"s_cmp_lt_u32 s62, 0x100000\n"
"s_waitcnt vmcnt(0)\n"
"s_cbranch_scc1 LA22%=\n"
"LB22%=:\n"
"v_mfma_f32_16x16x32_bf16 v[48:51], a[192:195], a[0:3], v[48:51]\n"
"s_add_u32 s48, s48, 0x1000\n"
"s_and_b32 s48, s48, 0x7fff\n"
"s_add_u32 s49, s48, s39\n"
"s_add_u32 m0, s50, 0x1a000\n"
"s_nop 0\n"
"buffer_load_dwordx4 v230, s[56:59], s49 offen sc1 lds\n"
"s_waitcnt lgkmcnt(5)\n"
"v_mfma_f32_16x16x32_bf16 v[48:51], a[196:199], a[24:27], v[48:51]\n"
"s_waitcnt lgkmcnt(3)\n"
"ds_read_b128 a[116:119], v227 offset:46080\n"
"v_mfma_f32_16x16x32_bf16 v[48:51], a[200:203], a[48:51], v[48:51]\n"
"ds_read_b128 a[140:143], v227 offset:52224\n"
"v_mfma_f32_16x16x32_bf16 v[48:51], a[204:207], a[72:75], v[48:51]\n"
"ds_read_b128 a[164:167], v227 offset:58368\n"
"v_mfma_f32_16x16x32_bf16 v[52:55], a[204:207], a[76:79], v[52:55]\n"
"buffer_load_dwordx4 v231, s[56:59], s49 offen offset:1024 sc1 lds\n"
"v_mfma_f32_16x16x32_bf16 v[52:55], a[200:203], a[52:55], v[52:55]\n"
"ds_read_b128 a[188:191], v227 offset:64512\n"
"v_mfma_f32_16x16x32_bf16 v[52:55], a[196:199], a[28:31], v[52:55]\n"
"v_mfma_f32_16x16x32_bf16 v[52:55], a[192:195], a[4:7], v[52:55]\n"
"s_add_u32 m0, s50, 0x1a000\n"
"v_mfma_f32_16x16x32_bf16 v[56:59], a[192:195], a[8:11], v[56:59]\n"
"buffer_load_dwordx4 v232, s[56:59], s49 offen offset:2048 sc1 lds\n"
"v_mfma_f32_16x16x32_bf16 v[56:59], a[196:199], a[32:35], v[56:59]\n"
"v_mfma_f32_16x16x32_bf16 v[56:59], a[200:203], a[56:59], v[56:59]\n"
"s_waitcnt vmcnt(14)\n"
"s_barrier\n"
"ds_read_b64_tr_b16 a[208:209], v225 offset:0\n"
"v_mfma_f32_16x16x32_bf16 v[56:59], a[204:207], a[80:83], v[56:59]\n"
"ds_read_b64_tr_b16 a[210:211], v225 offset:2048\n"
"v_mfma_f32_16x16x32_bf16 v[60:63], a[204:207], a[84:87], v[60:63]\n"
"buffer_load_dwordx4 v233, s[56:59], s49 offen offset:3072 sc1 lds\n"
"v_mfma_f32_16x16x32_bf16 v[60:63], a[200:203], a[60:63], v[60:63]\n"
"ds_read_b64_tr_b16 a[212:213], v225 offset:4096\n"
"v_mfma_f32_16x16x32_bf16 v[60:63], a[196:199], a[36:39], v[60:63]\n"
"s_add_u32 s49, s49, 0x80000\n"
"ds_read_b64_tr_b16 a[214:215], v225 offset:6144\n"
"v_mfma_f32_16x16x32_bf16 v[60:63], a[192:195], a[12:15], v[60:63]\n"
"s_add_u32 m0, s50, 0x1b000\n"
"ds_read_b64_tr_b16 a[216:217], v225 offset:8192\n"
"v_mfma_f32_16x16x32_bf16 v[64:67], a[192:195], a[16:19], v[64:67]\n"
"buffer_load_dwordx4 v230, s[56:59], s49 offen sc1 lds\n"
"v_mfma_f32_16x16x32_bf16 v[64:67], a[196:199], a[40:43], v[64:67]\n"
"ds_read_b64_tr_b16 a[218:219], v225 offset:10240\n"
"v_mfma_f32_16x16x32_bf16 v[64:67], a[200:203], a[64:67], v[64:67]\n"
"ds_read_b64_tr_b16 a[220:221], v225 offset:12288\n"
"v_mfma_f32_16x16x32_bf16 v[64:67], a[204:207], a[88:91], v[64:67]\n"
"ds_read_b64_tr_b16 a[222:223], v225 offset:14336\n"
"v_mfma_f32_16x16x32_bf16 v[68:71], a[204:207], a[92:95], v[68:71]\n"
"buffer_load_dwordx4 v231, s[56:59], s49 offen offset:1024 sc1 lds\n"
"v_mfma_f32_16x16x32_bf16 v[68:71], a[200:203], a[68:71], v[68:71]\n"
"ds_read_b128 v[144:147], v227 offset:0\n"
"v_mfma_f32_16x16x32_bf16 v[68:71], a[196:199], a[44:47], v[68:71]\n"
"s_waitcnt lgkmcnt(5)\n"
"ds_read_b128 v[168:171], v227 offset:6144\n"
"v_mfma_f32_16x16x32_bf16 v[68:71], a[192:195], a[20:23], v[68:71]\n"
"ds_read_b128 a[224:227], v227 offset:12288\n"
"v_mfma_f32_16x16x32_bf16 v[72:75], a[208:211], a[0:3], v[72:75]\n"
"s_add_u32 m0, s91, 0x24000\n"
"s_nop 0\n"
"buffer_load_dwordx4 v253, s[92:95], s90 offen offset:3072 lds\n"
"s_waitcnt lgkmcnt(5)\n"
"v_mfma_f32_16x16x32_bf16 v[72:75], a[212:215], a[24:27], v[72:75]\n"
"s_add_u32 s54, s51, 0x28000\n"
"s_cmp_eq_u32 s54, 0x140000\n"
"s_cselect_b32 s54, 0, s54\n"
"s_waitcnt lgkmcnt(3)\n"
"ds_read_b128 a[248:251], v227 offset:18432\n"
"v_mfma_f32_16x16x32_bf16 v[72:75], a[216:219], a[48:51], v[72:75]\n"
"s_add_u32 s90, s54, 0x200\n"
"ds_read_b128 v[148:151], v227 offset:1024\n"
"v_mfma_f32_16x16x32_bf16 v[72:75], a[220:223], a[72:75], v[72:75]\n"
"s_add_u32 m0, s91, 0x0\n"
"ds_read_b128 v[172:175], v227 offset:7168\n"
"v_mfma_f32_16x16x32_bf16 v[76:79], a[220:223], a[76:79], v[76:79]\n"
"buffer_load_dwordx4 v245, s[92:95], s90 offen lds\n"
"v_mfma_f32_16x16x32_bf16 v[76:79], a[216:219], a[52:55], v[76:79]\n"
"ds_read_b128 a[228:231], v227 offset:13312\n"
"v_mfma_f32_16x16x32_bf16 v[76:79], a[212:215], a[28:31], v[76:79]\n"
"ds_read_b128 a[252:255], v227 offset:19456\n"
"v_mfma_f32_16x16x32_bf16 v[76:79], a[208:211], a[4:7], v[76:79]\n"
"ds_read_b128 v[152:155], v227 offset:2048\n"
"v_mfma_f32_16x16x32_bf16 v[80:83], a[208:211], a[8:11], v[80:83]\n"
"buffer_load_dwordx4 v251, s[92:95], s90 offen offset:1024 lds\n"
"v_mfma_f32_16x16x32_bf16 v[80:83], a[212:215], a[32:35], v[80:83]\n"
"ds_read_b128 v[176:179], v227 offset:8192\n"
"v_mfma_f32_16x16x32_bf16 v[80:83], a[216:219], a[56:59], v[80:83]\n"
"s_waitcnt vmcnt(16)\n"
"s_barrier\n"
"ds_read_b64_tr_b16 a[192:193], v224 offset:16384\n"
"v_mfma_f32_16x16x32_bf16 v[80:83], a[220:223], a[80:83], v[80:83]\n"
"ds_read_b64_tr_b16 a[194:195], v224 offset:18432\n"
"v_mfma_f32_16x16x32_bf16 v[84:87], a[220:223], a[84:87], v[84:87]\n"
"buffer_load_dwordx4 v252, s[92:95], s90 offen offset:2048 lds\n"
"v_mfma_f32_16x16x32_bf16 v[84:87], a[216:219], a[60:63], v[84:87]\n"
"ds_read_b64_tr_b16 a[196:197], v224 offset:20480\n"
"v_mfma_f32_16x16x32_bf16 v[84:87], a[212:215], a[36:39], v[84:87]\n"
"ds_read_b64_tr_b16 a[198:199], v224 offset:22528\n"
"v_mfma_f32_16x16x32_bf16 v[84:87], a[208:211], a[12:15], v[84:87]\n"
"ds_read_b64_tr_b16 a[200:201], v224 offset:24576\n"
"v_mfma_f32_16x16x32_bf16 v[88:91], a[208:211], a[16:19], v[88:91]\n"
"buffer_load_dwordx4 v253, s[92:95], s90 offen offset:3072 lds\n"
"v_mfma_f32_16x16x32_bf16 v[88:91], a[212:215], a[40:43], v[88:91]\n"
"ds_read_b64_tr_b16 a[202:203], v224 offset:26624\n"
"v_mfma_f32_16x16x32_bf16 v[88:91], a[216:219], a[64:67], v[88:91]\n"
"s_add_u32 s90, s87, s41\n"
"ds_read_b64_tr_b16 a[204:205], v224 offset:28672\n"
"v_mfma_f32_16x16x32_bf16 v[88:91], a[220:223], a[88:91], v[88:91]\n"
"s_add_u32 m0, s91, 0x10000\n"
"ds_read_b64_tr_b16 a[206:207], v224 offset:30720\n"
"v_mfma_f32_16x16x32_bf16 v[92:95], a[220:223], a[92:95], v[92:95]\n"
"buffer_load_dwordx4 v245, s[92:95], s90 offen lds\n"
"v_mfma_f32_16x16x32_bf16 v[92:95], a[216:219], a[68:71], v[92:95]\n"
"ds_read_b128 a[232:235], v227 offset:14336\n"
"v_mfma_f32_16x16x32_bf16 v[92:95], a[212:215], a[44:47], v[92:95]\n"
"s_waitcnt lgkmcnt(5)\n"
"ds_read_b128 v[208:211], v227 offset:20480\n"
"v_and_b32 v238, 3, v244\n"
"v_mfma_f32_16x16x32_bf16 v[92:95], a[208:211], a[20:23], v[92:95]\n"
"ds_read_b128 v[156:159], v227 offset:3072\n"
"v_lshlrev_b32 v238, 9, v238\n"
"v_mfma_f32_16x16x32_bf16 v[96:99], a[192:195], a[0:3], v[96:99]\n"
"s_add_u32 m0, s91, 0x10000\n"
"s_nop 0\n"
"buffer_load_dwordx4 v251, s[92:95], s90 offen offset:1024 lds\n"
"s_waitcnt lgkmcnt(5)\n"
"v_mfma_f32_16x16x32_bf16 v[96:99], a[196:199], a[24:27], v[96:99]\n"
"s_waitcnt lgkmcnt(3)\n"
"ds_read_b128 v[180:183], v227 offset:9216\n"
"v_mfma_f32_16x16x32_bf16 v[96:99], a[200:203], a[48:51], v[96:99]\n"
"ds_read_b128 a[236:239], v227 offset:15360\n"
"v_mfma_f32_16x16x32_bf16 v[96:99], a[204:207], a[72:75], v[96:99]\n"
"ds_read_b128 v[212:215], v227 offset:21504\n"
"v_mfma_f32_16x16x32_bf16 v[100:103], a[204:207], a[76:79], v[100:103]\n"
"buffer_load_dwordx4 v252, s[92:95], s90 offen offset:2048 lds\n"
"v_mfma_f32_16x16x32_bf16 v[100:103], a[200:203], a[52:55], v[100:103]\n"
"ds_read_b128 v[160:163], v227 offset:4096\n"
"v_mfma_f32_16x16x32_bf16 v[100:103], a[196:199], a[28:31], v[100:103]\n"
"ds_read_b128 v[184:187], v227 offset:10240\n"
"v_mfma_f32_16x16x32_bf16 v[100:103], a[192:195], a[4:7], v[100:103]\n"
"ds_read_b128 a[240:243], v227 offset:16384\n"
"v_mfma_f32_16x16x32_bf16 v[104:107], a[192:195], a[8:11], v[104:107]\n"
"buffer_load_dwordx4 v253, s[92:95], s90 offen offset:3072 lds\n"
"v_mfma_f32_16x16x32_bf16 v[104:107], a[196:199], a[32:35], v[104:107]\n"
"ds_read_b128 v[216:219], v227 offset:22528\n"
"v_mfma_f32_16x16x32_bf16 v[104:107], a[200:203], a[56:59], v[104:107]\n"
"s_waitcnt vmcnt(9)\n"
"s_barrier\n"
"ds_read_b64_tr_b16 a[208:209], v226 offset:0\n"
"v_mfma_f32_16x16x32_bf16 v[104:107], a[204:207], a[80:83], v[104:107]\n"
"s_add_u32 s90, s51, s41\n"
"s_add_u32 m0, s91, 0x4000\n"
"ds_read_b64_tr_b16 a[210:211], v226 offset:2048\n"
"v_mfma_f32_16x16x32_bf16 v[108:111], a[204:207], a[84:87], v[108:111]\n"
"buffer_load_dwordx4 v245, s[92:95], s90 offen lds\n"
"v_mfma_f32_16x16x32_bf16 v[108:111], a[200:203], a[60:63], v[108:111]\n"
"ds_read_b64_tr_b16 a[212:213], v226 offset:4096\n"
"v_mfma_f32_16x16x32_bf16 v[108:111], a[196:199], a[36:39], v[108:111]\n"
"ds_read_b64_tr_b16 a[214:215], v226 offset:6144\n"
"v_mfma_f32_16x16x32_bf16 v[108:111], a[192:195], a[12:15], v[108:111]\n"
"ds_read_b64_tr_b16 a[216:217], v226 offset:8192\n"
"v_mfma_f32_16x16x32_bf16 v[112:115], a[192:195], a[16:19], v[112:115]\n"
"buffer_load_dwordx4 v251, s[92:95], s90 offen offset:1024 lds\n"
"v_mfma_f32_16x16x32_bf16 v[112:115], a[196:199], a[40:43], v[112:115]\n"
"ds_read_b64_tr_b16 a[218:219], v226 offset:10240\n"
"v_mfma_f32_16x16x32_bf16 v[112:115], a[200:203], a[64:67], v[112:115]\n"
"ds_read_b64_tr_b16 a[220:221], v226 offset:12288\n"
"v_mfma_f32_16x16x32_bf16 v[112:115], a[204:207], a[88:91], v[112:115]\n"
"ds_read_b64_tr_b16 a[222:223], v226 offset:14336\n"
"v_mfma_f32_16x16x32_bf16 v[116:119], a[204:207], a[92:95], v[116:119]\n"
"buffer_load_dwordx4 v252, s[92:95], s90 offen offset:2048 lds\n"
"v_mfma_f32_16x16x32_bf16 v[116:119], a[200:203], a[68:71], v[116:119]\n"
"ds_read_b128 v[164:167], v227 offset:5120\n"
"v_mfma_f32_16x16x32_bf16 v[116:119], a[196:199], a[44:47], v[116:119]\n"
"s_waitcnt lgkmcnt(5)\n"
"ds_read_b128 v[188:191], v227 offset:11264\n"
"v_mfma_f32_16x16x32_bf16 v[116:119], a[192:195], a[20:23], v[116:119]\n"
"ds_read_b128 a[244:247], v227 offset:17408\n"
"v_mfma_f32_16x16x32_bf16 v[0:3], a[208:211], a[96:99], v[0:3]\n"
"s_add_u32 m0, s91, 0x4000\n"
"s_nop 0\n"
"buffer_load_dwordx4 v253, s[92:95], s90 offen offset:3072 lds\n"
"s_waitcnt lgkmcnt(5)\n"
"v_mfma_f32_16x16x32_bf16 v[0:3], a[212:215], a[120:123], v[0:3]\n"
"s_waitcnt lgkmcnt(3)\n"
"ds_read_b128 a[0:3], v228 offset:0\n"
"v_mfma_f32_16x16x32_bf16 v[0:3], a[216:219], a[144:147], v[0:3]\n"
"ds_read_b128 a[24:27], v228 offset:6144\n"
"v_mfma_f32_16x16x32_bf16 v[0:3], a[220:223], a[168:171], v[0:3]\n"
"ds_read_b128 a[48:51], v228 offset:12288\n"
"v_mfma_f32_16x16x32_bf16 v[4:7], a[220:223], a[172:175], v[4:7]\n"
"ds_read_b128 v[220:223], v227 offset:23552\n"
"v_mfma_f32_16x16x32_bf16 v[4:7], a[216:219], a[148:151], v[4:7]\n"
"ds_read_b128 a[72:75], v228 offset:18432\n"
"v_mfma_f32_16x16x32_bf16 v[4:7], a[212:215], a[124:127], v[4:7]\n"
"ds_read_b128 a[4:7], v228 offset:1024\n"
"v_mfma_f32_16x16x32_bf16 v[4:7], a[208:211], a[100:103], v[4:7]\n"
"ds_read_b128 a[28:31], v228 offset:7168\n"
"v_mfma_f32_16x16x32_bf16 v[8:11], a[208:211], a[104:107], v[8:11]\n"
"v_mfma_f32_16x16x32_bf16 v[8:11], a[212:215], a[128:131], v[8:11]\n"
"ds_read_b128 a[52:55], v228 offset:13312\n"
"v_mfma_f32_16x16x32_bf16 v[8:11], a[216:219], a[152:155], v[8:11]\n"
"s_waitcnt vmcnt(12)\n"
"s_barrier\n"
"ds_read_b64_tr_b16 a[192:193], v226 offset:16384\n"
"v_mfma_f32_16x16x32_bf16 v[8:11], a[220:223], a[176:179], v[8:11]\n"
"s_add_u32 s90, s54, s41\n"
"s_add_u32 m0, s91, 0x20000\n"
"ds_read_b64_tr_b16 a[194:195], v226 offset:18432\n"
"v_mfma_f32_16x16x32_bf16 v[12:15], a[220:223], a[180:183], v[12:15]\n"
"buffer_load_dwordx4 v245, s[92:95], s90 offen lds\n"
"v_mfma_f32_16x16x32_bf16 v[12:15], a[216:219], a[156:159], v[12:15]\n"
"ds_read_b64_tr_b16 a[196:197], v226 offset:20480\n"
"v_mfma_f32_16x16x32_bf16 v[12:15], a[212:215], a[132:135], v[12:15]\n"
"ds_read_b64_tr_b16 a[198:199], v226 offset:22528\n"
"v_mfma_f32_16x16x32_bf16 v[12:15], a[208:211], a[108:111], v[12:15]\n"
"ds_read_b64_tr_b16 a[200:201], v226 offset:24576\n"
"v_mfma_f32_16x16x32_bf16 v[16:19], a[208:211], a[112:115], v[16:19]\n"
"buffer_load_dwordx4 v251, s[92:95], s90 offen offset:1024 lds\n"
"v_mfma_f32_16x16x32_bf16 v[16:19], a[212:215], a[136:139], v[16:19]\n"
"ds_read_b64_tr_b16 a[202:203], v226 offset:26624\n"
"v_mfma_f32_16x16x32_bf16 v[16:19], a[216:219], a[160:163], v[16:19]\n"
"ds_read_b64_tr_b16 a[204:205], v226 offset:28672\n"
"v_mfma_f32_16x16x32_bf16 v[16:19], a[220:223], a[184:187], v[16:19]\n"
"ds_read_b64_tr_b16 a[206:207], v226 offset:30720\n"
"v_mfma_f32_16x16x32_bf16 v[20:23], a[220:223], a[188:191], v[20:23]\n"
"buffer_load_dwordx4 v252, s[92:95], s90 offen offset:2048 lds\n"
"v_mfma_f32_16x16x32_bf16 v[20:23], a[216:219], a[164:167], v[20:23]\n"
"ds_read_b128 a[76:79], v228 offset:19456\n"
"v_mfma_f32_16x16x32_bf16 v[20:23], a[212:215], a[140:143], v[20:23]\n"
"s_waitcnt lgkmcnt(5)\n"
"ds_read_b128 a[8:11], v228 offset:2048\n"
"v_mfma_f32_16x16x32_bf16 v[20:23], a[208:211], a[116:119], v[20:23]\n"
"ds_read_b128 a[32:35], v228 offset:8192\n"
"v_mfma_f32_16x16x32_bf16 v[0:3], a[192:195], v[144:147], v[0:3]\n"
"s_add_u32 m0, s91, 0x20000\n"
"s_nop 0\n"
"buffer_load_dwordx4 v253, s[92:95], s90 offen offset:3072 lds\n"
"s_waitcnt lgkmcnt(5)\n"
"v_mfma_f32_16x16x32_bf16 v[0:3], a[196:199], v[168:171], v[0:3]\n"
"s_waitcnt lgkmcnt(3)\n"
"ds_read_b128 a[56:59], v228 offset:14336\n"
"v_mfma_f32_16x16x32_bf16 v[0:3], a[200:203], a[224:227], v[0:3]\n"
"ds_read_b128 a[80:83], v228 offset:20480\n"
"v_mfma_f32_16x16x32_bf16 v[0:3], a[204:207], a[248:251], v[0:3]\n"
"ds_read_b128 a[12:15], v228 offset:3072\n"
"v_mfma_f32_16x16x32_bf16 v[4:7], a[204:207], a[252:255], v[4:7]\n"
"v_mfma_f32_16x16x32_bf16 v[4:7], a[200:203], a[228:231], v[4:7]\n"
"ds_read_b128 a[36:39], v228 offset:9216\n"
"v_mfma_f32_16x16x32_bf16 v[4:7], a[196:199], v[172:175], v[4:7]\n"
"ds_read_b128 a[60:63], v228 offset:15360\n"
"v_mfma_f32_16x16x32_bf16 v[4:7], a[192:195], v[148:151], v[4:7]\n"
"ds_read_b128 a[84:87], v228 offset:21504\n"
"v_mfma_f32_16x16x32_bf16 v[8:11], a[192:195], v[152:155], v[8:11]\n"
"v_mfma_f32_16x16x32_bf16 v[8:11], a[196:199], v[176:179], v[8:11]\n"
"ds_read_b128 a[16:19], v228 offset:4096\n"
"v_mfma_f32_16x16x32_bf16 v[8:11], a[200:203], a[232:235], v[8:11]\n"
"s_waitcnt vmcnt(12)\n"
"s_barrier\n"
"ds_read_b64_tr_b16 a[208:209], v224 offset:0\n"
"v_mfma_f32_16x16x32_bf16 v[8:11], a[204:207], v[208:211], v[8:11]\n"
"s_add_u32 s90, s87, s42\n"
"s_add_u32 m0, s91, 0x24000\n"
"ds_read_b64_tr_b16 a[210:211], v224 offset:2048\n"
"v_mfma_f32_16x16x32_bf16 v[12:15], a[204:207], v[212:215], v[12:15]\n"
"buffer_load_dwordx4 v245, s[92:95], s90 offen lds\n"
"v_mfma_f32_16x16x32_bf16 v[12:15], a[200:203], a[236:239], v[12:15]\n"
"ds_read_b64_tr_b16 a[212:213], v224 offset:4096\n"
"v_mfma_f32_16x16x32_bf16 v[12:15], a[196:199], v[180:183], v[12:15]\n"
"ds_read_b64_tr_b16 a[214:215], v224 offset:6144\n"
"v_mfma_f32_16x16x32_bf16 v[12:15], a[192:195], v[156:159], v[12:15]\n"
"ds_read_b64_tr_b16 a[216:217], v224 offset:8192\n"
"v_mfma_f32_16x16x32_bf16 v[16:19], a[192:195], v[160:163], v[16:19]\n"
"buffer_load_dwordx4 v251, s[92:95], s90 offen offset:1024 lds\n"
"v_mfma_f32_16x16x32_bf16 v[16:19], a[196:199], v[184:187], v[16:19]\n"
"ds_read_b64_tr_b16 a[218:219], v224 offset:10240\n"
"v_mfma_f32_16x16x32_bf16 v[16:19], a[200:203], a[240:243], v[16:19]\n"
"ds_read_b64_tr_b16 a[220:221], v224 offset:12288\n"
"v_mfma_f32_16x16x32_bf16 v[16:19], a[204:207], v[216:219], v[16:19]\n"
"ds_read_b64_tr_b16 a[222:223], v224 offset:14336\n"
"v_mfma_f32_16x16x32_bf16 v[20:23], a[204:207], v[220:223], v[20:23]\n"
"buffer_load_dwordx4 v252, s[92:95], s90 offen offset:2048 lds\n"
"v_mfma_f32_16x16x32_bf16 v[20:23], a[200:203], a[244:247], v[20:23]\n"
"ds_read_b128 a[40:43], v228 offset:10240\n"
"v_mfma_f32_16x16x32_bf16 v[20:23], a[196:199], v[188:191], v[20:23]\n"
"s_waitcnt lgkmcnt(5)\n"
"ds_read_b128 a[64:67], v228 offset:16384\n"
"v_mfma_f32_16x16x32_bf16 v[20:23], a[192:195], v[164:167], v[20:23]\n"
"ds_read_b128 a[88:91], v228 offset:22528\n"
"v_mfma_f32_16x16x32_bf16 v[0:3], a[208:211], a[0:3], v[0:3]\n"
"s_add_u32 m0, s91, 0x24000\n"
"s_nop 0\n"
"buffer_load_dwordx4 v253, s[92:95], s90 offen offset:3072 lds\n"
"s_waitcnt lgkmcnt(5)\n"
"v_mfma_f32_16x16x32_bf16 v[0:3], a[212:215], a[24:27], v[0:3]\n"
"s_waitcnt lgkmcnt(3)\n"
"ds_read_b128 a[20:23], v228 offset:5120\n"
"v_mfma_f32_16x16x32_bf16 v[0:3], a[216:219], a[48:51], v[0:3]\n"
"ds_read_b128 a[44:47], v228 offset:11264\n"
"v_mfma_f32_16x16x32_bf16 v[0:3], a[220:223], a[72:75], v[0:3]\n"
"ds_read_b128 a[68:71], v228 offset:17408\n"
"v_mfma_f32_16x16x32_bf16 v[4:7], a[220:223], a[76:79], v[4:7]\n"
"v_mfma_f32_16x16x32_bf16 v[4:7], a[216:219], a[52:55], v[4:7]\n"
"ds_read_b128 a[92:95], v228 offset:23552\n"
"v_mfma_f32_16x16x32_bf16 v[4:7], a[212:215], a[28:31], v[4:7]\n"
"v_mfma_f32_16x16x32_bf16 v[4:7], a[208:211], a[4:7], v[4:7]\n"
"v_mfma_f32_16x16x32_bf16 v[8:11], a[208:211], a[8:11], v[8:11]\n"
"v_mfma_f32_16x16x32_bf16 v[8:11], a[212:215], a[32:35], v[8:11]\n"
"v_mfma_f32_16x16x32_bf16 v[8:11], a[216:219], a[56:59], v[8:11]\n"
"s_waitcnt vmcnt(12)\n"
"s_barrier\n"
"ds_read_b64_tr_b16 a[192:193], v225 offset:0\n"
"v_mfma_f32_16x16x32_bf16 v[8:11], a[220:223], a[80:83], v[8:11]\n"
"s_add_u32 s90, s51, s42\n"
"s_add_u32 m0, s91, 0x0\n"
"ds_read_b64_tr_b16 a[194:195], v225 offset:2048\n"
"v_mfma_f32_16x16x32_bf16 v[12:15], a[220:223], a[84:87], v[12:15]\n"
"buffer_load_dwordx4 v245, s[92:95], s90 offen lds\n"
"v_mfma_f32_16x16x32_bf16 v[12:15], a[216:219], a[60:63], v[12:15]\n"
"ds_read_b64_tr_b16 a[196:197], v225 offset:4096\n"
"v_mfma_f32_16x16x32_bf16 v[12:15], a[212:215], a[36:39], v[12:15]\n"
"s_waitcnt lgkmcnt(7)\n"
"ds_read_b64_tr_b16 a[198:199], v225 offset:6144\n"
"v_mfma_f32_16x16x32_bf16 v[12:15], a[208:211], a[12:15], v[12:15]\n"
"ds_read_b64_tr_b16 a[200:201], v225 offset:8192\n"
"v_mfma_f32_16x16x32_bf16 v[16:19], a[208:211], a[16:19], v[16:19]\n"
"buffer_load_dwordx4 v251, s[92:95], s90 offen offset:1024 lds\n"
"v_mfma_f32_16x16x32_bf16 v[16:19], a[212:215], a[40:43], v[16:19]\n"
"ds_read_b64_tr_b16 a[202:203], v225 offset:10240\n"
"v_mfma_f32_16x16x32_bf16 v[16:19], a[216:219], a[64:67], v[16:19]\n"
"s_waitcnt lgkmcnt(6)\n"
"ds_read_b64_tr_b16 a[204:205], v225 offset:12288\n"
"v_mfma_f32_16x16x32_bf16 v[16:19], a[220:223], a[88:91], v[16:19]\n"
"ds_read_b64_tr_b16 a[206:207], v225 offset:14336\n"
"v_mfma_f32_16x16x32_bf16 v[20:23], a[220:223], a[92:95], v[20:23]\n"
"buffer_load_dwordx4 v252, s[92:95], s90 offen offset:2048 lds\n"
"v_mfma_f32_16x16x32_bf16 v[20:23], a[216:219], a[68:71], v[20:23]\n"
"v_mfma_f32_16x16x32_bf16 v[20:23], a[212:215], a[44:47], v[20:23]\n"
"s_waitcnt lgkmcnt(4)\n"
"v_mfma_f32_16x16x32_bf16 v[20:23], a[208:211], a[20:23], v[20:23]\n"
"v_mfma_f32_16x16x32_bf16 v[24:27], a[192:195], a[96:99], v[24:27]\n"
"s_add_u32 m0, s91, 0x0\n"
"s_nop 0\n"
"buffer_load_dwordx4 v253, s[92:95], s90 offen offset:3072 lds\n"
"s_waitcnt lgkmcnt(2)\n"
"v_mfma_f32_16x16x32_bf16 v[24:27], a[196:199], a[120:123], v[24:27]\n"
"s_waitcnt lgkmcnt(0)\n"
"v_mfma_f32_16x16x32_bf16 v[24:27], a[200:203], a[144:147], v[24:27]\n"
"v_cvt_pk_bf16_f32 v120, v0, v1\n"
"v_cvt_pk_bf16_f32 v121, v2, v3\n"
"v_mfma_f32_16x16x32_bf16 v[24:27], a[204:207], a[168:171], v[24:27]\n"
"v_cvt_pk_bf16_f32 v122, v4, v5\n"
"v_cvt_pk_bf16_f32 v123, v6, v7\n"
"v_mfma_f32_16x16x32_bf16 v[28:31], a[204:207], a[172:175], v[28:31]\n"
"v_mfma_f32_16x16x32_bf16 v[28:31], a[200:203], a[148:151], v[28:31]\n"
"v_cvt_pk_bf16_f32 v124, v8, v9\n"
"v_cvt_pk_bf16_f32 v125, v10, v11\n"
"v_mfma_f32_16x16x32_bf16 v[28:31], a[196:199], a[124:127], v[28:31]\n"
"v_cvt_pk_bf16_f32 v126, v12, v13\n"
"v_cvt_pk_bf16_f32 v127, v14, v15\n"
"v_mfma_f32_16x16x32_bf16 v[28:31], a[192:195], a[100:103], v[28:31]\n"
"v_cvt_pk_bf16_f32 v128, v16, v17\n"
"v_cvt_pk_bf16_f32 v129, v18, v19\n"
"v_mfma_f32_16x16x32_bf16 v[32:35], a[192:195], a[104:107], v[32:35]\n"
"s_add_u32 s49, %[pw1], 0x0\n"
"buffer_store_dwordx4 v[120:123], v243, s[56:59], s49 offen offset:3072 sc1\n"
"v_mfma_f32_16x16x32_bf16 v[32:35], a[196:199], a[128:131], v[32:35]\n"
"v_cvt_pk_bf16_f32 v130, v20, v21\n"
"v_cvt_pk_bf16_f32 v131, v22, v23\n"
"v_mfma_f32_16x16x32_bf16 v[32:35], a[200:203], a[152:155], v[32:35]\n"
"s_waitcnt vmcnt(13)\n"
"s_barrier\n"
"ds_read_b64_tr_b16 a[208:209], v224 offset:16384\n"
"v_mfma_f32_16x16x32_bf16 v[32:35], a[204:207], a[176:179], v[32:35]\n"
"ds_read_b64_tr_b16 a[210:211], v224 offset:18432\n"
"v_mfma_f32_16x16x32_bf16 v[36:39], a[204:207], a[180:183], v[36:39]\n"
"s_add_u32 s49, %[pw1], 0x10000\n"
"buffer_store_dwordx4 v[124:127], v243, s[56:59], s49 offen offset:3072 sc1\n"
"v_mfma_f32_16x16x32_bf16 v[36:39], a[200:203], a[156:159], v[36:39]\n"
"ds_read_b64_tr_b16 a[212:213], v224 offset:20480\n"
"v_mfma_f32_16x16x32_bf16 v[36:39], a[196:199], a[132:135], v[36:39]\n"
"s_add_u32 s90, s54, s42\n"
"ds_read_b64_tr_b16 a[214:215], v224 offset:22528\n"
"v_mfma_f32_16x16x32_bf16 v[36:39], a[192:195], a[108:111], v[36:39]\n"
"s_add_u32 m0, s91, 0x10000\n"
"ds_read_b64_tr_b16 a[216:217], v224 offset:24576\n"
"v_mfma_f32_16x16x32_bf16 v[40:43], a[192:195], a[112:115], v[40:43]\n"
"buffer_load_dwordx4 v245, s[92:95], s90 offen lds\n"
"v_mfma_f32_16x16x32_bf16 v[40:43], a[196:199], a[136:139], v[40:43]\n"
"ds_read_b64_tr_b16 a[218:219], v224 offset:26624\n"
"v_mfma_f32_16x16x32_bf16 v[40:43], a[200:203], a[160:163], v[40:43]\n"
"ds_read_b64_tr_b16 a[220:221], v224 offset:28672\n"
"v_mfma_f32_16x16x32_bf16 v[40:43], a[204:207], a[184:187], v[40:43]\n"
"ds_read_b64_tr_b16 a[222:223], v224 offset:30720\n"
"v_mfma_f32_16x16x32_bf16 v[44:47], a[204:207], a[188:191], v[44:47]\n"
"buffer_load_dwordx4 v251, s[92:95], s90 offen offset:1024 lds\n"
"v_mfma_f32_16x16x32_bf16 v[44:47], a[200:203], a[164:167], v[44:47]\n"
"v_mfma_f32_16x16x32_bf16 v[44:47], a[196:199], a[140:143], v[44:47]\n"
"s_waitcnt lgkmcnt(4)\n"
"v_mfma_f32_16x16x32_bf16 v[44:47], a[192:195], a[116:119], v[44:47]\n"
"v_mfma_f32_16x16x32_bf16 v[24:27], a[208:211], v[144:147], v[24:27]\n"
"s_add_u32 s49, %[pw1], 0x20000\n"
"buffer_store_dwordx4 v[128:131], v243, s[56:59], s49 offen offset:3072 sc1\n"
"s_waitcnt lgkmcnt(2)\n"
"v_mfma_f32_16x16x32_bf16 v[24:27], a[212:215], v[168:171], v[24:27]\n"
"s_waitcnt lgkmcnt(0)\n"
"v_mfma_f32_16x16x32_bf16 v[24:27], a[216:219], a[224:227], v[24:27]\n"
"v_mfma_f32_16x16x32_bf16 v[24:27], a[220:223], a[248:251], v[24:27]\n"
"s_add_u32 m0, s91, 0x10000\n"
"v_mfma_f32_16x16x32_bf16 v[28:31], a[220:223], a[252:255], v[28:31]\n"
"buffer_load_dwordx4 v252, s[92:95], s90 offen offset:2048 lds\n"
"v_mfma_f32_16x16x32_bf16 v[28:31], a[216:219], a[228:231], v[28:31]\n"
"v_mfma_f32_16x16x32_bf16 v[28:31], a[212:215], v[172:175], v[28:31]\n"
"v_mfma_f32_16x16x32_bf16 v[28:31], a[208:211], v[148:151], v[28:31]\n"
"v_mfma_f32_16x16x32_bf16 v[32:35], a[208:211], v[152:155], v[32:35]\n"
"buffer_load_dwordx4 v253, s[92:95], s90 offen offset:3072 lds\n"
"v_mfma_f32_16x16x32_bf16 v[32:35], a[212:215], v[176:179], v[32:35]\n"
"v_mfma_f32_16x16x32_bf16 v[32:35], a[216:219], a[232:235], v[32:35]\n"
"s_waitcnt vmcnt(15)\n"
"s_barrier\n"
"ds_read_b64_tr_b16 a[192:193], v226 offset:0\n"
"v_mfma_f32_16x16x32_bf16 v[32:35], a[220:223], v[208:211], v[32:35]\n"
"s_add_u32 s90, s87, s43\n"
"s_add_u32 m0, s91, 0x4000\n"
"ds_read_b64_tr_b16 a[194:195], v226 offset:2048\n"
"v_mfma_f32_16x16x32_bf16 v[36:39], a[220:223], v[212:215], v[36:39]\n"
"buffer_load_dwordx4 v245, s[92:95], s90 offen lds\n"
"v_mfma_f32_16x16x32_bf16 v[36:39], a[216:219], a[236:239], v[36:39]\n"
"ds_read_b64_tr_b16 a[196:197], v226 offset:4096\n"
"v_mfma_f32_16x16x32_bf16 v[36:39], a[212:215], v[180:183], v[36:39]\n"
"ds_read_b64_tr_b16 a[198:199], v226 offset:6144\n"
"v_mfma_f32_16x16x32_bf16 v[36:39], a[208:211], v[156:159], v[36:39]\n"
"ds_read_b64_tr_b16 a[200:201], v226 offset:8192\n"
"v_mfma_f32_16x16x32_bf16 v[40:43], a[208:211], v[160:163], v[40:43]\n"
"buffer_load_dwordx4 v251, s[92:95], s90 offen offset:1024 lds\n"
"v_mfma_f32_16x16x32_bf16 v[40:43], a[212:215], v[184:187], v[40:43]\n"
"ds_read_b64_tr_b16 a[202:203], v226 offset:10240\n"
"v_mfma_f32_16x16x32_bf16 v[40:43], a[216:219], a[240:243], v[40:43]\n"
"ds_read_b64_tr_b16 a[204:205], v226 offset:12288\n"
"v_mfma_f32_16x16x32_bf16 v[40:43], a[220:223], v[216:219], v[40:43]\n"
"ds_read_b64_tr_b16 a[206:207], v226 offset:14336\n"
"v_mfma_f32_16x16x32_bf16 v[44:47], a[220:223], v[220:223], v[44:47]\n"
"buffer_load_dwordx4 v252, s[92:95], s90 offen offset:2048 lds\n"
"v_mfma_f32_16x16x32_bf16 v[44:47], a[216:219], a[244:247], v[44:47]\n"
"v_mfma_f32_16x16x32_bf16 v[44:47], a[212:215], v[188:191], v[44:47]\n"
"s_waitcnt lgkmcnt(4)\n"
"v_mfma_f32_16x16x32_bf16 v[44:47], a[208:211], v[164:167], v[44:47]\n"
"v_mfma_f32_16x16x32_bf16 v[24:27], a[192:195], a[0:3], v[24:27]\n"
"s_add_u32 m0, s91, 0x4000\n"
"s_nop 0\n"
"buffer_load_dwordx4 v253, s[92:95], s90 offen offset:3072 lds\n"
"s_waitcnt lgkmcnt(2)\n"
"v_mfma_f32_16x16x32_bf16 v[24:27], a[196:199], a[24:27], v[24:27]\n"
"s_waitcnt lgkmcnt(0)\n"
"v_mfma_f32_16x16x32_bf16 v[24:27], a[200:203], a[48:51], v[24:27]\n"
"v_mfma_f32_16x16x32_bf16 v[24:27], a[204:207], a[72:75], v[24:27]\n"
"v_mfma_f32_16x16x32_bf16 v[28:31], a[204:207], a[76:79], v[28:31]\n"
"v_mfma_f32_16x16x32_bf16 v[28:31], a[200:203], a[52:55], v[28:31]\n"
"v_mfma_f32_16x16x32_bf16 v[28:31], a[196:199], a[28:31], v[28:31]\n"
"v_mfma_f32_16x16x32_bf16 v[28:31], a[192:195], a[4:7], v[28:31]\n"
"v_mfma_f32_16x16x32_bf16 v[32:35], a[192:195], a[8:11], v[32:35]\n"
"v_mfma_f32_16x16x32_bf16 v[32:35], a[196:199], a[32:35], v[32:35]\n"
"v_mfma_f32_16x16x32_bf16 v[32:35], a[200:203], a[56:59], v[32:35]\n"
"s_waitcnt vmcnt(15)\n"
"s_barrier\n"
"ds_read_b64_tr_b16 a[208:209], v226 offset:16384\n"
"v_mfma_f32_16x16x32_bf16 v[32:35], a[204:207], a[80:83], v[32:35]\n"
"s_add_u32 s90, s51, s43\n"
"s_add_u32 m0, s91, 0x20000\n"
"ds_read_b64_tr_b16 a[210:211], v226 offset:18432\n"
"v_mfma_f32_16x16x32_bf16 v[36:39], a[204:207], a[84:87], v[36:39]\n"
"buffer_load_dwordx4 v245, s[92:95], s90 offen lds\n"
"v_mfma_f32_16x16x32_bf16 v[36:39], a[200:203], a[60:63], v[36:39]\n"
"ds_read_b64_tr_b16 a[212:213], v226 offset:20480\n"
"v_mfma_f32_16x16x32_bf16 v[36:39], a[196:199], a[36:39], v[36:39]\n"
"ds_read_b64_tr_b16 a[214:215], v226 offset:22528\n"
"v_mfma_f32_16x16x32_bf16 v[36:39], a[192:195], a[12:15], v[36:39]\n"
"ds_read_b64_tr_b16 a[216:217], v226 offset:24576\n"
"v_mfma_f32_16x16x32_bf16 v[40:43], a[192:195], a[16:19], v[40:43]\n"
"buffer_load_dwordx4 v251, s[92:95], s90 offen offset:1024 lds\n"
"v_mfma_f32_16x16x32_bf16 v[40:43], a[196:199], a[40:43], v[40:43]\n"
"ds_read_b64_tr_b16 a[218:219], v226 offset:26624\n"
"v_mfma_f32_16x16x32_bf16 v[40:43], a[200:203], a[64:67], v[40:43]\n"
"ds_read_b64_tr_b16 a[220:221], v226 offset:28672\n"
"v_mfma_f32_16x16x32_bf16 v[40:43], a[204:207], a[88:91], v[40:43]\n"
"ds_read_b64_tr_b16 a[222:223], v226 offset:30720\n"
"v_mfma_f32_16x16x32_bf16 v[44:47], a[204:207], a[92:95], v[44:47]\n"
"buffer_load_dwordx4 v252, s[92:95], s90 offen offset:2048 lds\n"
"v_mfma_f32_16x16x32_bf16 v[44:47], a[200:203], a[68:71], v[44:47]\n"
"v_mfma_f32_16x16x32_bf16 v[44:47], a[196:199], a[44:47], v[44:47]\n"
"s_waitcnt lgkmcnt(4)\n"
"v_mfma_f32_16x16x32_bf16 v[44:47], a[192:195], a[20:23], v[44:47]\n"
"v_mfma_f32_16x16x32_bf16 v[48:51], a[208:211], a[96:99], v[48:51]\n"
"s_add_u32 m0, s91, 0x20000\n"
"s_nop 0\n"
"buffer_load_dwordx4 v253, s[92:95], s90 offen offset:3072 lds\n"
"s_waitcnt lgkmcnt(2)\n"
"v_mfma_f32_16x16x32_bf16 v[48:51], a[212:215], a[120:123], v[48:51]\n"
"s_waitcnt lgkmcnt(0)\n"
"v_mfma_f32_16x16x32_bf16 v[48:51], a[216:219], a[144:147], v[48:51]\n"
"v_cvt_pk_bf16_f32 v132, v24, v25\n"
"v_cvt_pk_bf16_f32 v133, v26, v27\n"
"v_mfma_f32_16x16x32_bf16 v[48:51], a[220:223], a[168:171], v[48:51]\n"
"v_cvt_pk_bf16_f32 v134, v28, v29\n"
"v_cvt_pk_bf16_f32 v135, v30, v31\n"
"v_mfma_f32_16x16x32_bf16 v[52:55], a[220:223], a[172:175], v[52:55]\n"
"v_mfma_f32_16x16x32_bf16 v[52:55], a[216:219], a[148:151], v[52:55]\n"
"v_cvt_pk_bf16_f32 v136, v32, v33\n"
"v_cvt_pk_bf16_f32 v137, v34, v35\n"
"v_mfma_f32_16x16x32_bf16 v[52:55], a[212:215], a[124:127], v[52:55]\n"
"v_cvt_pk_bf16_f32 v138, v36, v37\n"
"v_cvt_pk_bf16_f32 v139, v38, v39\n"
"v_mfma_f32_16x16x32_bf16 v[52:55], a[208:211], a[100:103], v[52:55]\n"
"v_cvt_pk_bf16_f32 v140, v40, v41\n"
"v_cvt_pk_bf16_f32 v141, v42, v43\n"
"v_mfma_f32_16x16x32_bf16 v[56:59], a[208:211], a[104:107], v[56:59]\n"
"s_lshl_b32 s72, s41, 9\n"
"s_add_u32 s49, s72, %[pw1]\n"
"buffer_store_dwordx4 v[132:135], v243, s[56:59], s49 offen sc1\n"
"v_mfma_f32_16x16x32_bf16 v[56:59], a[212:215], a[128:131], v[56:59]\n"
"v_cvt_pk_bf16_f32 v142, v44, v45\n"
"v_cvt_pk_bf16_f32 v143, v46, v47\n"
"v_mfma_f32_16x16x32_bf16 v[56:59], a[216:219], a[152:155], v[56:59]\n"
"s_waitcnt vmcnt(16)\n"
"s_barrier\n"
"ds_read_b64_tr_b16 a[192:193], v224 offset:0\n"
"v_mfma_f32_16x16x32_bf16 v[56:59], a[220:223], a[176:179], v[56:59]\n"
"ds_read_b64_tr_b16 a[194:195], v224 offset:2048\n"
"v_mfma_f32_16x16x32_bf16 v[60:63], a[220:223], a[180:183], v[60:63]\n"
"s_lshl_b32 s72, s41, 9\n"
"s_add_u32 s49, s72, %[pw1]\n"
"buffer_store_dwordx4 v[136:139], v243, s[56:59], s49 offen offset:1024 sc1\n"
"v_mfma_f32_16x16x32_bf16 v[60:63], a[216:219], a[156:159], v[60:63]\n"
"ds_read_b64_tr_b16 a[196:197], v224 offset:4096\n"
"v_mfma_f32_16x16x32_bf16 v[60:63], a[212:215], a[132:135], v[60:63]\n"
"s_add_u32 s90, s54, s43\n"
"ds_read_b64_tr_b16 a[198:199], v224 offset:6144\n"
"v_mfma_f32_16x16x32_bf16 v[60:63], a[208:211], a[108:111], v[60:63]\n"
"s_add_u32 m0, s91, 0x24000\n"
"ds_read_b64_tr_b16 a[200:201], v224 offset:8192\n"
"v_mfma_f32_16x16x32_bf16 v[64:67], a[208:211], a[112:115], v[64:67]\n"
"buffer_load_dwordx4 v245, s[92:95], s90 offen lds\n"
"v_mfma_f32_16x16x32_bf16 v[64:67], a[212:215], a[136:139], v[64:67]\n"
"ds_read_b64_tr_b16 a[202:203], v224 offset:10240\n"
"v_mfma_f32_16x16x32_bf16 v[64:67], a[216:219], a[160:163], v[64:67]\n"
"ds_read_b64_tr_b16 a[204:205], v224 offset:12288\n"
"v_mfma_f32_16x16x32_bf16 v[64:67], a[220:223], a[184:187], v[64:67]\n"
"ds_read_b64_tr_b16 a[206:207], v224 offset:14336\n"
"v_mfma_f32_16x16x32_bf16 v[68:71], a[220:223], a[188:191], v[68:71]\n"
"buffer_load_dwordx4 v251, s[92:95], s90 offen offset:1024 lds\n"
"v_mfma_f32_16x16x32_bf16 v[68:71], a[216:219], a[164:167], v[68:71]\n"
"v_mfma_f32_16x16x32_bf16 v[68:71], a[212:215], a[140:143], v[68:71]\n"
"s_waitcnt lgkmcnt(4)\n"
"v_mfma_f32_16x16x32_bf16 v[68:71], a[208:211], a[116:119], v[68:71]\n"
"v_mfma_f32_16x16x32_bf16 v[48:51], a[192:195], v[144:147], v[48:51]\n"
"s_lshl_b32 s72, s41, 9\n"
"s_add_u32 s49, s72, %[pw1]\n"
"buffer_store_dwordx4 v[140:143], v243, s[56:59], s49 offen offset:2048 sc1\n"
"s_waitcnt lgkmcnt(2)\n"
"v_mfma_f32_16x16x32_bf16 v[48:51], a[196:199], v[168:171], v[48:51]\n"
"s_waitcnt lgkmcnt(0)\n"
"v_mfma_f32_16x16x32_bf16 v[48:51], a[200:203], a[224:227], v[48:51]\n"
"v_mfma_f32_16x16x32_bf16 v[48:51], a[204:207], a[248:251], v[48:51]\n"
"s_add_u32 m0, s91, 0x24000\n"
"v_mfma_f32_16x16x32_bf16 v[52:55], a[204:207], a[252:255], v[52:55]\n"
"buffer_load_dwordx4 v252, s[92:95], s90 offen offset:2048 lds\n"
"v_mfma_f32_16x16x32_bf16 v[52:55], a[200:203], a[228:231], v[52:55]\n"
"v_mfma_f32_16x16x32_bf16 v[52:55], a[196:199], v[172:175], v[52:55]\n"
"v_mfma_f32_16x16x32_bf16 v[52:55], a[192:195], v[148:151], v[52:55]\n"
"v_mfma_f32_16x16x32_bf16 v[56:59], a[192:195], v[152:155], v[56:59]\n"
"buffer_load_dwordx4 v253, s[92:95], s90 offen offset:3072 lds\n"
"v_mfma_f32_16x16x32_bf16 v[56:59], a[196:199], v[176:179], v[56:59]\n"
"v_mfma_f32_16x16x32_bf16 v[56:59], a[200:203], a[232:235], v[56:59]\n"
"s_waitcnt vmcnt(15)\n"
"s_barrier\n"
"ds_read_b64_tr_b16 a[208:209], v225 offset:0\n"
"v_mfma_f32_16x16x32_bf16 v[56:59], a[204:207], v[208:211], v[56:59]\n"
"s_add_u32 s90, s87, s65\n"
"s_add_u32 m0, s91, 0x0\n"
"ds_read_b64_tr_b16 a[210:211], v225 offset:2048\n"
"v_mfma_f32_16x16x32_bf16 v[60:63], a[204:207], v[212:215], v[60:63]\n"
"buffer_load_dwordx4 v245, s[92:95], s90 offen lds\n"
"v_mfma_f32_16x16x32_bf16 v[60:63], a[200:203], a[236:239], v[60:63]\n"
"ds_read_b64_tr_b16 a[212:213], v225 offset:4096\n"
"v_mfma_f32_16x16x32_bf16 v[60:63], a[196:199], v[180:183], v[60:63]\n"
"ds_read_b64_tr_b16 a[214:215], v225 offset:6144\n"
"v_mfma_f32_16x16x32_bf16 v[60:63], a[192:195], v[156:159], v[60:63]\n"
"ds_read_b64_tr_b16 a[216:217], v225 offset:8192\n"
"v_mfma_f32_16x16x32_bf16 v[64:67], a[192:195], v[160:163], v[64:67]\n"
"buffer_load_dwordx4 v251, s[92:95], s90 offen offset:1024 lds\n"
"v_mfma_f32_16x16x32_bf16 v[64:67], a[196:199], v[184:187], v[64:67]\n"
"ds_read_b64_tr_b16 a[218:219], v225 offset:10240\n"
"v_mfma_f32_16x16x32_bf16 v[64:67], a[200:203], a[240:243], v[64:67]\n"
"ds_read_b64_tr_b16 a[220:221], v225 offset:12288\n"
"v_mfma_f32_16x16x32_bf16 v[64:67], a[204:207], v[216:219], v[64:67]\n"
"ds_read_b64_tr_b16 a[222:223], v225 offset:14336\n"
"v_mfma_f32_16x16x32_bf16 v[68:71], a[204:207], v[220:223], v[68:71]\n"
"buffer_load_dwordx4 v252, s[92:95], s90 offen offset:2048 lds\n"
"v_mfma_f32_16x16x32_bf16 v[68:71], a[200:203], a[244:247], v[68:71]\n"
"v_mfma_f32_16x16x32_bf16 v[68:71], a[196:199], v[188:191], v[68:71]\n"
"s_waitcnt lgkmcnt(4)\n"
"v_mfma_f32_16x16x32_bf16 v[68:71], a[192:195], v[164:167], v[68:71]\n"
"v_mfma_f32_16x16x32_bf16 v[48:51], a[208:211], a[0:3], v[48:51]\n"
"s_add_u32 m0, s91, 0x0\n"
"s_nop 0\n"
"buffer_load_dwordx4 v253, s[92:95], s90 offen offset:3072 lds\n"
"s_waitcnt lgkmcnt(2)\n"
"v_mfma_f32_16x16x32_bf16 v[48:51], a[212:215], a[24:27], v[48:51]\n"
"s_waitcnt lgkmcnt(0)\n"
"v_mfma_f32_16x16x32_bf16 v[48:51], a[216:219], a[48:51], v[48:51]\n"
"v_mfma_f32_16x16x32_bf16 v[48:51], a[220:223], a[72:75], v[48:51]\n"
"v_mfma_f32_16x16x32_bf16 v[52:55], a[220:223], a[76:79], v[52:55]\n"
"v_mfma_f32_16x16x32_bf16 v[52:55], a[216:219], a[52:55], v[52:55]\n"
"v_mfma_f32_16x16x32_bf16 v[52:55], a[212:215], a[28:31], v[52:55]\n"
"v_mfma_f32_16x16x32_bf16 v[52:55], a[208:211], a[4:7], v[52:55]\n"
"v_mfma_f32_16x16x32_bf16 v[56:59], a[208:211], a[8:11], v[56:59]\n"
"v_mfma_f32_16x16x32_bf16 v[56:59], a[212:215], a[32:35], v[56:59]\n"
"v_mfma_f32_16x16x32_bf16 v[56:59], a[216:219], a[56:59], v[56:59]\n"
"s_waitcnt vmcnt(15)\n"
"s_barrier\n"
"ds_read_b64_tr_b16 a[192:193], v224 offset:16384\n"
"v_mfma_f32_16x16x32_bf16 v[56:59], a[220:223], a[80:83], v[56:59]\n"
"s_add_u32 s90, s51, s65\n"
"s_add_u32 m0, s91, 0x10000\n"
"ds_read_b64_tr_b16 a[194:195], v224 offset:18432\n"
"v_mfma_f32_16x16x32_bf16 v[60:63], a[220:223], a[84:87], v[60:63]\n"
"buffer_load_dwordx4 v245, s[92:95], s90 offen lds\n"
"v_mfma_f32_16x16x32_bf16 v[60:63], a[216:219], a[60:63], v[60:63]\n"
"ds_read_b64_tr_b16 a[196:197], v224 offset:20480\n"
"v_mfma_f32_16x16x32_bf16 v[60:63], a[212:215], a[36:39], v[60:63]\n"
"ds_read_b64_tr_b16 a[198:199], v224 offset:22528\n"
"v_mfma_f32_16x16x32_bf16 v[60:63], a[208:211], a[12:15], v[60:63]\n"
"ds_read_b64_tr_b16 a[200:201], v224 offset:24576\n"
"v_mfma_f32_16x16x32_bf16 v[64:67], a[208:211], a[16:19], v[64:67]\n"
"buffer_load_dwordx4 v251, s[92:95], s90 offen offset:1024 lds\n"
"v_mfma_f32_16x16x32_bf16 v[64:67], a[212:215], a[40:43], v[64:67]\n"
"ds_read_b64_tr_b16 a[202:203], v224 offset:26624\n"
"v_mfma_f32_16x16x32_bf16 v[64:67], a[216:219], a[64:67], v[64:67]\n"
"ds_read_b64_tr_b16 a[204:205], v224 offset:28672\n"
"v_mfma_f32_16x16x32_bf16 v[64:67], a[220:223], a[88:91], v[64:67]\n"
"ds_read_b64_tr_b16 a[206:207], v224 offset:30720\n"
"v_mfma_f32_16x16x32_bf16 v[68:71], a[220:223], a[92:95], v[68:71]\n"
"buffer_load_dwordx4 v252, s[92:95], s90 offen offset:2048 lds\n"
"v_mfma_f32_16x16x32_bf16 v[68:71], a[216:219], a[68:71], v[68:71]\n"
"v_mfma_f32_16x16x32_bf16 v[68:71], a[212:215], a[44:47], v[68:71]\n"
"s_waitcnt lgkmcnt(4)\n"
"v_mfma_f32_16x16x32_bf16 v[68:71], a[208:211], a[20:23], v[68:71]\n"
"v_mfma_f32_16x16x32_bf16 v[72:75], a[192:195], a[96:99], v[72:75]\n"
"s_add_u32 m0, s91, 0x10000\n"
"s_nop 0\n"
"buffer_load_dwordx4 v253, s[92:95], s90 offen offset:3072 lds\n"
"s_waitcnt lgkmcnt(2)\n"
"v_mfma_f32_16x16x32_bf16 v[72:75], a[196:199], a[120:123], v[72:75]\n"
"s_waitcnt lgkmcnt(0)\n"
"v_mfma_f32_16x16x32_bf16 v[72:75], a[200:203], a[144:147], v[72:75]\n"
"v_cvt_pk_bf16_f32 v120, v48, v49\n"
"v_cvt_pk_bf16_f32 v121, v50, v51\n"
"v_mfma_f32_16x16x32_bf16 v[72:75], a[204:207], a[168:171], v[72:75]\n"
"v_cvt_pk_bf16_f32 v122, v52, v53\n"
"v_cvt_pk_bf16_f32 v123, v54, v55\n"
"v_mfma_f32_16x16x32_bf16 v[76:79], a[204:207], a[172:175], v[76:79]\n"
"v_mfma_f32_16x16x32_bf16 v[76:79], a[200:203], a[148:151], v[76:79]\n"
"v_cvt_pk_bf16_f32 v124, v56, v57\n"
"v_cvt_pk_bf16_f32 v125, v58, v59\n"
"v_mfma_f32_16x16x32_bf16 v[76:79], a[196:199], a[124:127], v[76:79]\n"
"v_cvt_pk_bf16_f32 v126, v60, v61\n"
"v_cvt_pk_bf16_f32 v127, v62, v63\n"
"v_mfma_f32_16x16x32_bf16 v[76:79], a[192:195], a[100:103], v[76:79]\n"
"v_cvt_pk_bf16_f32 v128, v64, v65\n"
"v_cvt_pk_bf16_f32 v129, v66, v67\n"
"v_mfma_f32_16x16x32_bf16 v[80:83], a[192:195], a[104:107], v[80:83]\n"
"s_lshl_b32 s72, s42, 9\n"
"s_add_u32 s49, s72, %[pw1]\n"
"buffer_store_dwordx4 v[120:123], v243, s[56:59], s49 offen sc1\n"
"v_mfma_f32_16x16x32_bf16 v[80:83], a[196:199], a[128:131], v[80:83]\n"
"v_cvt_pk_bf16_f32 v130, v68, v69\n"
"v_cvt_pk_bf16_f32 v131, v70, v71\n"
"v_mfma_f32_16x16x32_bf16 v[80:83], a[200:203], a[152:155], v[80:83]\n"
"s_waitcnt vmcnt(16)\n"
"s_barrier\n"
"ds_read_b64_tr_b16 a[208:209], v226 offset:0\n"
"v_mfma_f32_16x16x32_bf16 v[80:83], a[204:207], a[176:179], v[80:83]\n"
"ds_read_b64_tr_b16 a[210:211], v226 offset:2048\n"
"v_mfma_f32_16x16x32_bf16 v[84:87], a[204:207], a[180:183], v[84:87]\n"
"s_lshl_b32 s72, s42, 9\n"
"s_add_u32 s49, s72, %[pw1]\n"
"buffer_store_dwordx4 v[124:127], v243, s[56:59], s49 offen offset:1024 sc1\n"
"v_mfma_f32_16x16x32_bf16 v[84:87], a[200:203], a[156:159], v[84:87]\n"
"ds_read_b64_tr_b16 a[212:213], v226 offset:4096\n"
"v_mfma_f32_16x16x32_bf16 v[84:87], a[196:199], a[132:135], v[84:87]\n"
"s_add_u32 s90, s54, s65\n"
"ds_read_b64_tr_b16 a[214:215], v226 offset:6144\n"
"v_mfma_f32_16x16x32_bf16 v[84:87], a[192:195], a[108:111], v[84:87]\n"
"s_add_u32 m0, s91, 0x4000\n"
"ds_read_b64_tr_b16 a[216:217], v226 offset:8192\n"
"v_mfma_f32_16x16x32_bf16 v[88:91], a[192:195], a[112:115], v[88:91]\n"
"buffer_load_dwordx4 v245, s[92:95], s90 offen lds\n"
"v_mfma_f32_16x16x32_bf16 v[88:91], a[196:199], a[136:139], v[88:91]\n"
"ds_read_b64_tr_b16 a[218:219], v226 offset:10240\n"
"v_mfma_f32_16x16x32_bf16 v[88:91], a[200:203], a[160:163], v[88:91]\n"
"ds_read_b64_tr_b16 a[220:221], v226 offset:12288\n"
"v_mfma_f32_16x16x32_bf16 v[88:91], a[204:207], a[184:187], v[88:91]\n"
"ds_read_b64_tr_b16 a[222:223], v226 offset:14336\n"
"v_mfma_f32_16x16x32_bf16 v[92:95], a[204:207], a[188:191], v[92:95]\n"
"buffer_load_dwordx4 v251, s[92:95], s90 offen offset:1024 lds\n"
"v_mfma_f32_16x16x32_bf16 v[92:95], a[200:203], a[164:167], v[92:95]\n"
"v_mfma_f32_16x16x32_bf16 v[92:95], a[196:199], a[140:143], v[92:95]\n"
"s_waitcnt lgkmcnt(4)\n"
"v_mfma_f32_16x16x32_bf16 v[92:95], a[192:195], a[116:119], v[92:95]\n"
"v_mfma_f32_16x16x32_bf16 v[72:75], a[208:211], v[144:147], v[72:75]\n"
"s_add_u32 m0, s91, 0x4000\n"
"s_nop 0\n"
"buffer_load_dwordx4 v252, s[92:95], s90 offen offset:2048 lds\n"
"s_waitcnt lgkmcnt(2)\n"
"v_mfma_f32_16x16x32_bf16 v[72:75], a[212:215], v[168:171], v[72:75]\n"
"s_waitcnt lgkmcnt(0)\n"
"v_mfma_f32_16x16x32_bf16 v[72:75], a[216:219], a[224:227], v[72:75]\n"
"v_mfma_f32_16x16x32_bf16 v[72:75], a[220:223], a[248:251], v[72:75]\n"
"v_mfma_f32_16x16x32_bf16 v[76:79], a[220:223], a[252:255], v[76:79]\n"
"buffer_load_dwordx4 v253, s[92:95], s90 offen offset:3072 lds\n"
"v_mfma_f32_16x16x32_bf16 v[76:79], a[216:219], a[228:231], v[76:79]\n"
"v_mfma_f32_16x16x32_bf16 v[76:79], a[212:215], v[172:175], v[76:79]\n"
"v_mfma_f32_16x16x32_bf16 v[76:79], a[208:211], v[148:151], v[76:79]\n"
"v_mfma_f32_16x16x32_bf16 v[80:83], a[208:211], v[152:155], v[80:83]\n"
"s_lshl_b32 s72, s42, 9\n"
"s_add_u32 s49, s72, %[pw1]\n"
"buffer_store_dwordx4 v[128:131], v243, s[56:59], s49 offen offset:2048 sc1\n"
"v_mfma_f32_16x16x32_bf16 v[80:83], a[212:215], v[176:179], v[80:83]\n"
"v_mfma_f32_16x16x32_bf16 v[80:83], a[216:219], a[232:235], v[80:83]\n"
"s_waitcnt vmcnt(15)\n"
"s_barrier\n"
"ds_read_b64_tr_b16 a[192:193], v226 offset:16384\n"
"v_mfma_f32_16x16x32_bf16 v[80:83], a[220:223], v[208:211], v[80:83]\n"
"ds_read_b64_tr_b16 a[194:195], v226 offset:18432\n"
"v_mfma_f32_16x16x32_bf16 v[84:87], a[220:223], v[212:215], v[84:87]\n"
"v_mfma_f32_16x16x32_bf16 v[84:87], a[216:219], a[236:239], v[84:87]\n"
"ds_read_b64_tr_b16 a[196:197], v226 offset:20480\n"
"v_mfma_f32_16x16x32_bf16 v[84:87], a[212:215], v[180:183], v[84:87]\n"
"ds_read_b64_tr_b16 a[198:199], v226 offset:22528\n"
"v_mfma_f32_16x16x32_bf16 v[84:87], a[208:211], v[156:159], v[84:87]\n"
"ds_read_b64_tr_b16 a[200:201], v226 offset:24576\n"
"v_mfma_f32_16x16x32_bf16 v[88:91], a[208:211], v[160:163], v[88:91]\n"
"v_mfma_f32_16x16x32_bf16 v[88:91], a[212:215], v[184:187], v[88:91]\n"
"ds_read_b64_tr_b16 a[202:203], v226 offset:26624\n"
"v_mfma_f32_16x16x32_bf16 v[88:91], a[216:219], a[240:243], v[88:91]\n"
"ds_read_b64_tr_b16 a[204:205], v226 offset:28672\n"
"v_mfma_f32_16x16x32_bf16 v[88:91], a[220:223], v[216:219], v[88:91]\n"
"ds_read_b64_tr_b16 a[206:207], v226 offset:30720\n"
"v_mfma_f32_16x16x32_bf16 v[92:95], a[220:223], v[220:223], v[92:95]\n"
"v_mfma_f32_16x16x32_bf16 v[92:95], a[216:219], a[244:247], v[92:95]\n"
"v_mfma_f32_16x16x32_bf16 v[92:95], a[212:215], v[188:191], v[92:95]\n"
"s_waitcnt lgkmcnt(4)\n"
"v_mfma_f32_16x16x32_bf16 v[92:95], a[208:211], v[164:167], v[92:95]\n"
"v_mfma_f32_16x16x32_bf16 v[72:75], a[192:195], a[0:3], v[72:75]\n"
"s_waitcnt lgkmcnt(2)\n"
"v_mfma_f32_16x16x32_bf16 v[72:75], a[196:199], a[24:27], v[72:75]\n"
"s_waitcnt lgkmcnt(0)\n"
"v_mfma_f32_16x16x32_bf16 v[72:75], a[200:203], a[48:51], v[72:75]\n"
"v_mfma_f32_16x16x32_bf16 v[72:75], a[204:207], a[72:75], v[72:75]\n"
"v_mfma_f32_16x16x32_bf16 v[76:79], a[204:207], a[76:79], v[76:79]\n"
"v_mfma_f32_16x16x32_bf16 v[76:79], a[200:203], a[52:55], v[76:79]\n"
"v_mfma_f32_16x16x32_bf16 v[76:79], a[196:199], a[28:31], v[76:79]\n"
"v_mfma_f32_16x16x32_bf16 v[76:79], a[192:195], a[4:7], v[76:79]\n"
"v_mfma_f32_16x16x32_bf16 v[80:83], a[192:195], a[8:11], v[80:83]\n"
"v_mfma_f32_16x16x32_bf16 v[80:83], a[196:199], a[32:35], v[80:83]\n"
"v_mfma_f32_16x16x32_bf16 v[80:83], a[200:203], a[56:59], v[80:83]\n"
"s_waitcnt vmcnt(11)\n"
"s_barrier\n"
"ds_read_b64_tr_b16 a[208:209], v224 offset:0\n"
"v_mfma_f32_16x16x32_bf16 v[80:83], a[204:207], a[80:83], v[80:83]\n"
"ds_read_b64_tr_b16 a[210:211], v224 offset:2048\n"
"v_mfma_f32_16x16x32_bf16 v[84:87], a[204:207], a[84:87], v[84:87]\n"
"v_mfma_f32_16x16x32_bf16 v[84:87], a[200:203], a[60:63], v[84:87]\n"
"ds_read_b64_tr_b16 a[212:213], v224 offset:4096\n"
"v_mfma_f32_16x16x32_bf16 v[84:87], a[196:199], a[36:39], v[84:87]\n"
"ds_read_b64_tr_b16 a[214:215], v224 offset:6144\n"
"v_mfma_f32_16x16x32_bf16 v[84:87], a[192:195], a[12:15], v[84:87]\n"
"ds_read_b64_tr_b16 a[216:217], v224 offset:8192\n"
"v_mfma_f32_16x16x32_bf16 v[88:91], a[192:195], a[16:19], v[88:91]\n"
"v_mfma_f32_16x16x32_bf16 v[88:91], a[196:199], a[40:43], v[88:91]\n"
"ds_read_b64_tr_b16 a[218:219], v224 offset:10240\n"
"v_mfma_f32_16x16x32_bf16 v[88:91], a[200:203], a[64:67], v[88:91]\n"
"ds_read_b64_tr_b16 a[220:221], v224 offset:12288\n"
"v_mfma_f32_16x16x32_bf16 v[88:91], a[204:207], a[88:91], v[88:91]\n"
"ds_read_b64_tr_b16 a[222:223], v224 offset:14336\n"
"v_mfma_f32_16x16x32_bf16 v[92:95], a[204:207], a[92:95], v[92:95]\n"
"v_mfma_f32_16x16x32_bf16 v[92:95], a[200:203], a[68:71], v[92:95]\n"
"v_mfma_f32_16x16x32_bf16 v[92:95], a[196:199], a[44:47], v[92:95]\n"
"s_waitcnt lgkmcnt(4)\n"
"s_lshr_b32 s72, s41, 7\n"
"s_lshl_b32 s80, 1, s72\n"
"s_mov_b32 s81, 0\n"
"v_mov_b32 v246, s60\n"
"v_mov_b32 v247, s61\n"
"s_mov_b64 exec, s[80:81]\n"
"buffer_store_dwordx2 v[246:247], v239, s[56:59], s67 offen sc1\n"
"s_mov_b64 exec, -1\n"
"v_mfma_f32_16x16x32_bf16 v[92:95], a[192:195], a[20:23], v[92:95]\n"
"v_mfma_f32_16x16x32_bf16 v[96:99], a[208:211], a[96:99], v[96:99]\n"
"s_waitcnt lgkmcnt(2)\n"
"v_mfma_f32_16x16x32_bf16 v[96:99], a[212:215], a[120:123], v[96:99]\n"
"s_waitcnt lgkmcnt(0)\n"
"v_mfma_f32_16x16x32_bf16 v[96:99], a[216:219], a[144:147], v[96:99]\n"
"v_cvt_pk_bf16_f32 v132, v72, v73\n"
"v_cvt_pk_bf16_f32 v133, v74, v75\n"
"v_mfma_f32_16x16x32_bf16 v[96:99], a[220:223], a[168:171], v[96:99]\n"
"v_cvt_pk_bf16_f32 v134, v76, v77\n"
"v_cvt_pk_bf16_f32 v135, v78, v79\n"
"v_mfma_f32_16x16x32_bf16 v[100:103], a[220:223], a[172:175], v[100:103]\n"
"v_mfma_f32_16x16x32_bf16 v[100:103], a[216:219], a[148:151], v[100:103]\n"
"v_cvt_pk_bf16_f32 v136, v80, v81\n"
"v_cvt_pk_bf16_f32 v137, v82, v83\n"
"v_mfma_f32_16x16x32_bf16 v[100:103], a[212:215], a[124:127], v[100:103]\n"
"v_cvt_pk_bf16_f32 v138, v84, v85\n"
"v_cvt_pk_bf16_f32 v139, v86, v87\n"
"v_mfma_f32_16x16x32_bf16 v[100:103], a[208:211], a[100:103], v[100:103]\n"
"v_cvt_pk_bf16_f32 v140, v88, v89\n"
"v_cvt_pk_bf16_f32 v141, v90, v91\n"
"v_mfma_f32_16x16x32_bf16 v[104:107], a[208:211], a[104:107], v[104:107]\n"
"s_lshl_b32 s72, s43, 9\n"
"s_add_u32 s49, s72, %[pw1]\n"
"buffer_store_dwordx4 v[132:135], v243, s[56:59], s49 offen sc1\n"
"v_mfma_f32_16x16x32_bf16 v[104:107], a[212:215], a[128:131], v[104:107]\n"
"v_cvt_pk_bf16_f32 v142, v92, v93\n"
"v_cvt_pk_bf16_f32 v143, v94, v95\n"
"v_mfma_f32_16x16x32_bf16 v[104:107], a[216:219], a[152:155], v[104:107]\n"
"s_waitcnt vmcnt(9)\n"
"s_barrier\n"
"ds_read_b64_tr_b16 a[192:193], v225 offset:0\n"
"v_mfma_f32_16x16x32_bf16 v[104:107], a[220:223], a[176:179], v[104:107]\n"
"ds_read_b64_tr_b16 a[194:195], v225 offset:2048\n"
"v_mfma_f32_16x16x32_bf16 v[108:111], a[220:223], a[180:183], v[108:111]\n"
"s_lshl_b32 s72, s43, 9\n"
"s_add_u32 s49, s72, %[pw1]\n"
"buffer_store_dwordx4 v[136:139], v243, s[56:59], s49 offen offset:1024 sc1\n"
"v_mfma_f32_16x16x32_bf16 v[108:111], a[216:219], a[156:159], v[108:111]\n"
"ds_read_b64_tr_b16 a[196:197], v225 offset:4096\n"
"v_mfma_f32_16x16x32_bf16 v[108:111], a[212:215], a[132:135], v[108:111]\n"
"ds_read_b64_tr_b16 a[198:199], v225 offset:6144\n"
"v_mfma_f32_16x16x32_bf16 v[108:111], a[208:211], a[108:111], v[108:111]\n"
"ds_read_b64_tr_b16 a[200:201], v225 offset:8192\n"
"v_mfma_f32_16x16x32_bf16 v[112:115], a[208:211], a[112:115], v[112:115]\n"
"s_lshl_b32 s72, s43, 9\n"
"s_add_u32 s49, s72, %[pw1]\n"
"buffer_store_dwordx4 v[140:143], v243, s[56:59], s49 offen offset:2048 sc1\n"
"v_mfma_f32_16x16x32_bf16 v[112:115], a[212:215], a[136:139], v[112:115]\n"
"ds_read_b64_tr_b16 a[202:203], v225 offset:10240\n"
"v_mfma_f32_16x16x32_bf16 v[112:115], a[216:219], a[160:163], v[112:115]\n"
"ds_read_b64_tr_b16 a[204:205], v225 offset:12288\n"
"v_mfma_f32_16x16x32_bf16 v[112:115], a[220:223], a[184:187], v[112:115]\n"
"ds_read_b64_tr_b16 a[206:207], v225 offset:14336\n"
"v_mfma_f32_16x16x32_bf16 v[116:119], a[220:223], a[188:191], v[116:119]\n"
"v_mfma_f32_16x16x32_bf16 v[116:119], a[216:219], a[164:167], v[116:119]\n"
"v_mfma_f32_16x16x32_bf16 v[116:119], a[212:215], a[140:143], v[116:119]\n"
"s_waitcnt lgkmcnt(4)\n"
"v_mfma_f32_16x16x32_bf16 v[116:119], a[208:211], a[116:119], v[116:119]\n"
"v_mfma_f32_16x16x32_bf16 v[96:99], a[192:195], v[144:147], v[96:99]\n"
"s_waitcnt lgkmcnt(2)\n"
"v_mfma_f32_16x16x32_bf16 v[96:99], a[196:199], v[168:171], v[96:99]\n"
"s_waitcnt lgkmcnt(0)\n"
"v_mfma_f32_16x16x32_bf16 v[96:99], a[200:203], a[224:227], v[96:99]\n"
"v_mfma_f32_16x16x32_bf16 v[96:99], a[204:207], a[248:251], v[96:99]\n"
"v_mfma_f32_16x16x32_bf16 v[100:103], a[204:207], a[252:255], v[100:103]\n"
"v_mfma_f32_16x16x32_bf16 v[100:103], a[200:203], a[228:231], v[100:103]\n"
"v_mfma_f32_16x16x32_bf16 v[100:103], a[196:199], v[172:175], v[100:103]\n"
"v_mfma_f32_16x16x32_bf16 v[100:103], a[192:195], v[148:151], v[100:103]\n"
"v_mfma_f32_16x16x32_bf16 v[104:107], a[192:195], v[152:155], v[104:107]\n"
"v_mfma_f32_16x16x32_bf16 v[104:107], a[196:199], v[176:179], v[104:107]\n"
"v_mfma_f32_16x16x32_bf16 v[104:107], a[200:203], a[232:235], v[104:107]\n"
"s_waitcnt vmcnt(5)\n"
"s_barrier\n"
"ds_read_b64_tr_b16 a[208:209], v224 offset:16384\n"
"v_mfma_f32_16x16x32_bf16 v[104:107], a[204:207], v[208:211], v[104:107]\n"
"ds_read_b64_tr_b16 a[210:211], v224 offset:18432\n"
"v_mfma_f32_16x16x32_bf16 v[108:111], a[204:207], v[212:215], v[108:111]\n"
"v_mfma_f32_16x16x32_bf16 v[108:111], a[200:203], a[236:239], v[108:111]\n"
"ds_read_b64_tr_b16 a[212:213], v224 offset:20480\n"
"v_mfma_f32_16x16x32_bf16 v[108:111], a[196:199], v[180:183], v[108:111]\n"
"ds_read_b64_tr_b16 a[214:215], v224 offset:22528\n"
"v_mfma_f32_16x16x32_bf16 v[108:111], a[192:195], v[156:159], v[108:111]\n"
"ds_read_b64_tr_b16 a[216:217], v224 offset:24576\n"
"v_mfma_f32_16x16x32_bf16 v[112:115], a[192:195], v[160:163], v[112:115]\n"
"v_mfma_f32_16x16x32_bf16 v[112:115], a[196:199], v[184:187], v[112:115]\n"
"ds_read_b64_tr_b16 a[218:219], v224 offset:26624\n"
"v_mfma_f32_16x16x32_bf16 v[112:115], a[200:203], a[240:243], v[112:115]\n"
"ds_read_b64_tr_b16 a[220:221], v224 offset:28672\n"
"v_mfma_f32_16x16x32_bf16 v[112:115], a[204:207], v[216:219], v[112:115]\n"
"ds_read_b64_tr_b16 a[222:223], v224 offset:30720\n"
"v_mfma_f32_16x16x32_bf16 v[116:119], a[204:207], v[220:223], v[116:119]\n"
"v_mfma_f32_16x16x32_bf16 v[116:119], a[200:203], a[244:247], v[116:119]\n"
"v_mfma_f32_16x16x32_bf16 v[116:119], a[196:199], v[188:191], v[116:119]\n"
"s_waitcnt lgkmcnt(4)\n"
"v_mfma_f32_16x16x32_bf16 v[116:119], a[192:195], v[164:167], v[116:119]\n"
"v_mfma_f32_16x16x32_bf16 v[96:99], a[208:211], a[0:3], v[96:99]\n"
"s_waitcnt lgkmcnt(2)\n"
"v_mfma_f32_16x16x32_bf16 v[96:99], a[212:215], a[24:27], v[96:99]\n"
"s_waitcnt lgkmcnt(0)\n"
"v_mfma_f32_16x16x32_bf16 v[96:99], a[216:219], a[48:51], v[96:99]\n"
"v_mfma_f32_16x16x32_bf16 v[96:99], a[220:223], a[72:75], v[96:99]\n"
"v_mfma_f32_16x16x32_bf16 v[100:103], a[220:223], a[76:79], v[100:103]\n"
"v_mfma_f32_16x16x32_bf16 v[100:103], a[216:219], a[52:55], v[100:103]\n"
"v_mfma_f32_16x16x32_bf16 v[100:103], a[212:215], a[28:31], v[100:103]\n"
"v_mfma_f32_16x16x32_bf16 v[100:103], a[208:211], a[4:7], v[100:103]\n"
"v_mfma_f32_16x16x32_bf16 v[104:107], a[208:211], a[8:11], v[104:107]\n"
"buffer_load_dwordx2 v[248:249], v238, s[56:59], %[fl2] offen sc1\n"
"v_mfma_f32_16x16x32_bf16 v[104:107], a[212:215], a[32:35], v[104:107]\n"
"v_mfma_f32_16x16x32_bf16 v[104:107], a[216:219], a[56:59], v[104:107]\n"
"v_mfma_f32_16x16x32_bf16 v[104:107], a[220:223], a[80:83], v[104:107]\n"
"v_mfma_f32_16x16x32_bf16 v[108:111], a[220:223], a[84:87], v[108:111]\n"
"v_mfma_f32_16x16x32_bf16 v[108:111], a[216:219], a[60:63], v[108:111]\n"
"v_mfma_f32_16x16x32_bf16 v[108:111], a[212:215], a[36:39], v[108:111]\n"
"v_mfma_f32_16x16x32_bf16 v[108:111], a[208:211], a[12:15], v[108:111]\n"
"v_mfma_f32_16x16x32_bf16 v[112:115], a[208:211], a[16:19], v[112:115]\n"
"v_mfma_f32_16x16x32_bf16 v[112:115], a[212:215], a[40:43], v[112:115]\n"
"v_mfma_f32_16x16x32_bf16 v[112:115], a[216:219], a[64:67], v[112:115]\n"
"v_mfma_f32_16x16x32_bf16 v[112:115], a[220:223], a[88:91], v[112:115]\n"
"v_mfma_f32_16x16x32_bf16 v[116:119], a[220:223], a[92:95], v[116:119]\n"
"v_mfma_f32_16x16x32_bf16 v[116:119], a[216:219], a[68:71], v[116:119]\n"
"v_mfma_f32_16x16x32_bf16 v[116:119], a[212:215], a[44:47], v[116:119]\n"
"s_lshr_b32 s72, s42, 7\n"
"s_lshl_b32 s80, 1, s72\n"
"s_mov_b32 s81, 0\n"
"s_waitcnt vmcnt(5)\n"
"v_mov_b32 v246, s60\n"
"v_mov_b32 v247, s61\n"
"s_mov_b64 exec, s[80:81]\n"
"buffer_store_dwordx2 v[246:247], v239, s[56:59], s67 offen sc1\n"
"s_mov_b64 exec, -1\n"
"v_mfma_f32_16x16x32_bf16 v[116:119], a[208:211], a[20:23], v[116:119]\n"
"s_cmp_lg_u32 %[fin], 15\n"
"s_cbranch_scc1 LFX%=\n"
"s_mov_b32 s62, 0\n"
"v_lshlrev_b32 v143, 7, v244\n"
"LFA%=:\n"
"s_mov_b32 s72, 0x2000000\n"
"buffer_load_dwordx2 v[144:145], v143, s[56:59], s72 offen sc1\n"
"s_mov_b32 s72, 0x2002000\n"
"buffer_load_dwordx2 v[146:147], v143, s[56:59], s72 offen sc1\n"
"s_mov_b32 s72, 0x2004000\n"
"buffer_load_dwordx2 v[148:149], v143, s[56:59], s72 offen sc1\n"
"s_mov_b32 s72, 0x2006000\n"
"buffer_load_dwordx2 v[150:151], v143, s[56:59], s72 offen sc1\n"
"s_mov_b32 s72, 0x2008000\n"
"buffer_load_dwordx2 v[152:153], v143, s[56:59], s72 offen sc1\n"
"s_mov_b32 s72, 0x200a000\n"
"buffer_load_dwordx2 v[154:155], v143, s[56:59], s72 offen sc1\n"
"s_mov_b32 s72, 0x200c000\n"
"buffer_load_dwordx2 v[156:157], v143, s[56:59], s72 offen sc1\n"
"s_mov_b32 s72, 0x200e000\n"
"buffer_load_dwordx2 v[158:159], v143, s[56:59], s72 offen sc1\n"
"s_mov_b32 s72, 0x2010000\n"
"buffer_load_dwordx2 v[160:161], v143, s[56:59], s72 offen sc1\n"
"s_mov_b32 s72, 0x2012000\n"
"buffer_load_dwordx2 v[162:163], v143, s[56:59], s72 offen sc1\n"
"s_mov_b32 s72, 0x2014000\n"
"buffer_load_dwordx2 v[164:165], v143, s[56:59], s72 offen sc1\n"
"s_mov_b32 s72, 0x2016000\n"
"buffer_load_dwordx2 v[166:167], v143, s[56:59], s72 offen sc1\n"
"s_mov_b32 s72, 0x2018000\n"
"buffer_load_dwordx2 v[168:169], v143, s[56:59], s72 offen sc1\n"
"s_mov_b32 s72, 0x201a000\n"
"buffer_load_dwordx2 v[170:171], v143, s[56:59], s72 offen sc1\n"
"s_mov_b32 s72, 0x201c000\n"
"buffer_load_dwordx2 v[172:173], v143, s[56:59], s72 offen sc1\n"
"s_mov_b32 s72, 0x201e000\n"
"buffer_load_dwordx2 v[174:175], v143, s[56:59], s72 offen sc1\n"
"s_waitcnt vmcnt(0)\n"
"s_mov_b64 s[74:75], 0\n"
"v_cmp_ne_u64 vcc, s[60:61], v[144:145]\n"
"s_or_b64 s[74:75], s[74:75], vcc\n"
"v_cmp_ne_u64 vcc, s[60:61], v[146:147]\n"
"s_or_b64 s[74:75], s[74:75], vcc\n"
"v_cmp_ne_u64 vcc, s[60:61], v[148:149]\n"
"s_or_b64 s[74:75], s[74:75], vcc\n"
"v_cmp_ne_u64 vcc, s[60:61], v[150:151]\n"
"s_or_b64 s[74:75], s[74:75], vcc\n"
"v_cmp_ne_u64 vcc, s[60:61], v[152:153]\n"
"s_or_b64 s[74:75], s[74:75], vcc\n"
"v_cmp_ne_u64 vcc, s[60:61], v[154:155]\n"
"s_or_b64 s[74:75], s[74:75], vcc\n"
"v_cmp_ne_u64 vcc, s[60:61], v[156:157]\n"
"s_or_b64 s[74:75], s[74:75], vcc\n"
"v_cmp_ne_u64 vcc, s[60:61], v[158:159]\n"
"s_or_b64 s[74:75], s[74:75], vcc\n"
"v_cmp_ne_u64 vcc, s[60:61], v[160:161]\n"
"s_or_b64 s[74:75], s[74:75], vcc\n"
"v_cmp_ne_u64 vcc, s[60:61], v[162:163]\n"
"s_or_b64 s[74:75], s[74:75], vcc\n"
"v_cmp_ne_u64 vcc, s[60:61], v[164:165]\n"
"s_or_b64 s[74:75], s[74:75], vcc\n"
"v_cmp_ne_u64 vcc, s[60:61], v[166:167]\n"
"s_or_b64 s[74:75], s[74:75], vcc\n"
"v_cmp_ne_u64 vcc, s[60:61], v[168:169]\n"
"s_or_b64 s[74:75], s[74:75], vcc\n"
"v_cmp_ne_u64 vcc, s[60:61], v[170:171]\n"
"s_or_b64 s[74:75], s[74:75], vcc\n"
"v_cmp_ne_u64 vcc, s[60:61], v[172:173]\n"
"s_or_b64 s[74:75], s[74:75], vcc\n"
"v_cmp_ne_u64 vcc, s[60:61], v[174:175]\n"
"s_or_b64 s[74:75], s[74:75], vcc\n"
"s_cbranch_scc0 LFB%=\n"
"s_sleep 8\n"
"s_add_u32 s62, s62, 1\n"
"s_cmp_lt_u32 s62, 0x100000\n"
"s_cbranch_scc1 LFA%=\n"
"LFB%=:\n"
"s_mov_b32 s72, 0x20c0000\n"
"v_mov_b32 v246, s60\n"
"v_mov_b32 v247, s61\n"
"s_mov_b64 exec, 1\n"
"buffer_store_dwordx2 v[246:247], v239, s[56:59], s72 offen sc1\n"
"s_mov_b64 exec, -1\n"
"LFX%=:\n"
"s_sub_u32 s72, %[kq], 1\n"
"s_and_b32 s72, s72, 3\n"
"s_lshl_b32 s52, 1, s72\n"
"s_waitcnt vmcnt(1)\n"
"s_mov_b32 s62, 0\n"
"LAG%=:\n"
"v_cmp_ne_u64 vcc, s[60:61], v[248:249]\n"
"s_and_b64 vcc, vcc, s[52:53]\n"
"s_cbranch_scc0 LBG%=\n"
"s_sleep 0\n"
"buffer_load_dwordx2 v[248:249], v238, s[56:59], %[fl2] offen sc1\n"
"s_add_u32 s62, s62, 1\n"
"s_cmp_lt_u32 s62, 0x100000\n"
"s_waitcnt vmcnt(0)\n"
"s_cbranch_scc1 LAG%=\n"
"LBG%=:\n"
"s_sub_u32 s72, %[kq], 1\n"
"s_and_b32 s72, s72, 3\n"
"s_lshl_b32 s72, s72, 14\n"
"s_add_u32 s49, s72, %[pr1]\n"
"buffer_load_dwordx4 v[0:3], v243, s[56:59], s49 offen sc1\n"
"buffer_load_dwordx4 v[4:7], v243, s[56:59], s49 offen offset:1024 sc1\n"
"buffer_load_dwordx4 v[8:11], v243, s[56:59], s49 offen offset:2048 sc1\n"
"buffer_load_dwordx4 v[12:15], v243, s[56:59], s49 offen offset:3072 sc1\n"
"s_lshl_b32 s72, %[kq], 14\n"
"s_add_u32 s49, s72, %[pr1]\n"
"buffer_load_dwordx4 v[16:19], v243, s[56:59], s49 offen offset:3072 sc1\n"
"s_sub_u32 s72, %[kq], 2\n"
"s_and_b32 s72, s72, 3\n"
"s_lshl_b32 s52, 1, s72\n"
"s_mov_b32 s62, 0\n"
"LAH%=:\n"
"v_cmp_ne_u64 vcc, s[60:61], v[248:249]\n"
"s_and_b64 vcc, vcc, s[52:53]\n"
"s_cbranch_scc0 LBH%=\n"
"s_sleep 0\n"
"buffer_load_dwordx2 v[248:249], v238, s[56:59], %[fl2] offen sc1\n"
"s_add_u32 s62, s62, 1\n"
"s_cmp_lt_u32 s62, 0x100000\n"
"s_waitcnt vmcnt(0)\n"
"s_cbranch_scc1 LAH%=\n"
"LBH%=:\n"
"s_sub_u32 s72, %[kq], 2\n"
"s_and_b32 s72, s72, 3\n"
"s_lshl_b32 s72, s72, 14\n"
"s_add_u32 s49, s72, %[pr1]\n"
"buffer_load_dwordx4 v[24:27], v243, s[56:59], s49 offen sc1\n"
"buffer_load_dwordx4 v[28:31], v243, s[56:59], s49 offen offset:1024 sc1\n"
"buffer_load_dwordx4 v[32:35], v243, s[56:59], s49 offen offset:2048 sc1\n"
"buffer_load_dwordx4 v[36:39], v243, s[56:59], s49 offen offset:3072 sc1\n"
"s_sub_u32 s72, %[kq], 3\n"
"s_and_b32 s72, s72, 3\n"
"s_lshl_b32 s72, s72, 14\n"
"s_add_u32 s49, s72, %[pr1]\n"
"buffer_load_dwordx4 v[48:51], v243, s[56:59], s49 offen sc1\n"
"buffer_load_dwordx4 v[52:55], v243, s[56:59], s49 offen offset:1024 sc1\n"
"buffer_load_dwordx4 v[56:59], v243, s[56:59], s49 offen offset:2048 sc1\n"
"buffer_load_dwordx4 v[60:63], v243, s[56:59], s49 offen offset:3072 sc1\n"
"s_waitcnt vmcnt(12)\n"
"v_mfma_f32_16x16x32_bf16 v[96:99], v[192:195], v[0:3], v[96:99]\n"
"v_mfma_f32_16x16x32_bf16 v[100:103], v[196:199], v[0:3], v[100:103]\n"
"s_waitcnt vmcnt(11)\n"
"v_mfma_f32_16x16x32_bf16 v[104:107], v[192:195], v[4:7], v[104:107]\n"
"v_mfma_f32_16x16x32_bf16 v[108:111], v[196:199], v[4:7], v[108:111]\n"
"s_waitcnt vmcnt(10)\n"
"v_mfma_f32_16x16x32_bf16 v[112:115], v[192:195], v[8:11], v[112:115]\n"
"v_mfma_f32_16x16x32_bf16 v[116:119], v[196:199], v[8:11], v[116:119]\n"
"s_waitcnt vmcnt(9)\n"
"v_mfma_f32_16x16x32_bf16 v[64:67], v[192:195], v[12:15], 0\n"
"v_mfma_f32_16x16x32_bf16 v[68:71], v[196:199], v[12:15], 0\n"
"s_waitcnt vmcnt(8)\n"
"v_mfma_f32_16x16x32_bf16 v[64:67], v[192:195], v[16:19], v[64:67]\n"
"v_mfma_f32_16x16x32_bf16 v[68:71], v[196:199], v[16:19], v[68:71]\n"
"s_waitcnt vmcnt(7)\n"
"v_mfma_f32_16x16x32_bf16 v[96:99], v[192:195], v[24:27], v[96:99]\n"
"v_mfma_f32_16x16x32_bf16 v[100:103], v[196:199], v[24:27], v[100:103]\n"
"s_waitcnt vmcnt(6)\n"
"v_mfma_f32_16x16x32_bf16 v[104:107], v[192:195], v[28:31], v[104:107]\n"
"v_mfma_f32_16x16x32_bf16 v[108:111], v[196:199], v[28:31], v[108:111]\n"
"s_waitcnt vmcnt(5)\n"
"v_mfma_f32_16x16x32_bf16 v[112:115], v[192:195], v[32:35], v[112:115]\n"
"v_mfma_f32_16x16x32_bf16 v[116:119], v[196:199], v[32:35], v[116:119]\n"
"s_waitcnt vmcnt(4)\n"
"v_mfma_f32_16x16x32_bf16 v[64:67], v[192:195], v[36:39], v[64:67]\n"
"v_mfma_f32_16x16x32_bf16 v[68:71], v[196:199], v[36:39], v[68:71]\n"
"s_waitcnt vmcnt(0)\n"
"s_mov_b32 s62, 0\n"
"LS3%=:\n"
"v_max3_u32 v246, v48, v49, v50\n"
"v_max3_u32 v246, v246, v51, v52\n"
"v_max3_u32 v246, v246, v53, v54\n"
"v_max3_u32 v246, v246, v55, v56\n"
"v_max3_u32 v246, v246, v57, v58\n"
"v_max3_u32 v246, v246, v59, v60\n"
"v_max3_u32 v246, v246, v61, v62\n"
"v_max_u32 v246, v246, v63\n"
"v_cmp_eq_u32 vcc, -1, v246\n"
"s_cmp_eq_u64 vcc, 0\n"
"s_cbranch_scc1 LS4%=\n"
"buffer_load_dwordx4 v[48:51], v243, s[56:59], s49 offen sc1\n"
"buffer_load_dwordx4 v[52:55], v243, s[56:59], s49 offen offset:1024 sc1\n"
"buffer_load_dwordx4 v[56:59], v243, s[56:59], s49 offen offset:2048 sc1\n"
"buffer_load_dwordx4 v[60:63], v243, s[56:59], s49 offen offset:3072 sc1\n"
"s_add_u32 s62, s62, 1\n"
"s_cmp_lt_u32 s62, 0x100000\n"
"s_waitcnt vmcnt(0)\n"
"s_cbranch_scc1 LS3%=\n"
"LS4%=:\n"
"v_mfma_f32_16x16x32_bf16 v[96:99], v[192:195], v[48:51], v[96:99]\n"
"v_mfma_f32_16x16x32_bf16 v[100:103], v[196:199], v[48:51], v[100:103]\n"
"v_mfma_f32_16x16x32_bf16 v[104:107], v[192:195], v[52:55], v[104:107]\n"
"v_mfma_f32_16x16x32_bf16 v[108:111], v[196:199], v[52:55], v[108:111]\n"
"v_mfma_f32_16x16x32_bf16 v[112:115], v[192:195], v[56:59], v[112:115]\n"
"v_mfma_f32_16x16x32_bf16 v[116:119], v[196:199], v[56:59], v[116:119]\n"
"v_mfma_f32_16x16x32_bf16 v[64:67], v[192:195], v[60:63], v[64:67]\n"
"v_mfma_f32_16x16x32_bf16 v[68:71], v[196:199], v[60:63], v[68:71]\n"
"s_nop 7\n"
"v_cvt_pk_bf16_f32 v72, v96, v97\n"
"v_cvt_pk_bf16_f32 v73, v98, v99\n"
"ds_write_b64 v234, v[72:73] offset:0\n"
"v_cvt_pk_bf16_f32 v74, v100, v101\n"
"v_cvt_pk_bf16_f32 v75, v102, v103\n"
"ds_write_b64 v234, v[74:75] offset:2048\n"
"v_cvt_pk_bf16_f32 v76, v104, v105\n"
"v_cvt_pk_bf16_f32 v77, v106, v107\n"
"ds_write_b64 v234, v[76:77] offset:4096\n"
"v_cvt_pk_bf16_f32 v78, v108, v109\n"
"v_cvt_pk_bf16_f32 v79, v110, v111\n"
"ds_write_b64 v234, v[78:79] offset:6144\n"
"v_cvt_pk_bf16_f32 v80, v112, v113\n"
"v_cvt_pk_bf16_f32 v81, v114, v115\n"
"ds_write_b64 v234, v[80:81] offset:8192\n"
"v_cvt_pk_bf16_f32 v82, v116, v117\n"
"v_cvt_pk_bf16_f32 v83, v118, v119\n"
"ds_write_b64 v234, v[82:83] offset:10240\n"
"v_cvt_pk_bf16_f32 v84, v64, v65\n"
"v_cvt_pk_bf16_f32 v85, v66, v67\n"
"ds_write_b64 v234, v[84:85] offset:12288\n"
"v_cvt_pk_bf16_f32 v86, v68, v69\n"
"v_cvt_pk_bf16_f32 v87, v70, v71\n"
"ds_write_b64 v234, v[86:87] offset:14336\n"
"s_waitcnt lgkmcnt(0)\n"
"s_barrier\n"
"ds_read_b128 v[0:3], v235 offset:0\n"
"ds_read_b128 v[4:7], v235 offset:4096\n"
"ds_read_b128 v[8:11], v235 offset:8192\n"
"ds_read_b128 v[12:15], v235 offset:12288\n"
"s_waitcnt lgkmcnt(3)\n"
"buffer_store_dwordx4 v[0:3], v240, s[44:47], s40 offen\n"
"s_add_u32 s40, s40, 0x14000\n"
"s_waitcnt lgkmcnt(2)\n"
"buffer_store_dwordx4 v[4:7], v240, s[44:47], s40 offen\n"
"s_add_u32 s40, s40, 0x14000\n"
"s_waitcnt lgkmcnt(1)\n"
"buffer_store_dwordx4 v[8:11], v240, s[44:47], s40 offen\n"
"s_cmp_eq_u32 %[kq], 3\n"
"s_cbranch_scc1 LDONE%=\n"
"s_waitcnt lgkmcnt(0)\n"
"buffer_store_dwordx4 v[12:15], v240, s[44:47], s55 offen\n"
"LDONE%=:\n"
"s_endpgm\n"
"LREENT%=:\n"
"s_mov_b32 %[flag], 0\n"
"s_and_b32 s71, s77, 1\n"
"s_lshr_b32 s70, s77, 1\n"
"v_mbcnt_lo_u32_b32 v244, -1, 0\n"
"v_mbcnt_hi_u32_b32 v244, -1, v244\n"
"v_lshlrev_b32 v243, 4, v244\n"
"s_waitcnt vmcnt(0) lgkmcnt(0)\n"
"s_and_b32 s57, s57, 0xffff\n"
"s_mov_b32 s58, 0x4000000\n"
"s_mov_b32 s59, 0x20000\n"
"s_and_b32 s93, s93, 0xffff\n"
"s_mov_b32 s95, 0x20000\n"
"s_mul_i32 s87, %[hq0], 163840\n"
"s_lshl_b32 s72, s77, 12\n"
"s_add_u32 s91, s72, s76\n"
"v_mov_b32 v207, s77\n"
"v_mul_u32_u24 v207, 0xa000, v207\n"
"v_and_b32 v206, 1, v244\n"
"v_lshlrev_b32 v206, 4, v206\n"
"v_lshrrev_b32 v204, 4, v244\n"
"v_and_b32 v204, 3, v204\n"
"v_and_b32 v205, 7, v244\n"
"v_lshrrev_b32 v205, 1, v205\n"
"v_xor_b32 v205, v204, v205\n"
"v_lshlrev_b32 v205, 5, v205\n"
"v_lshrrev_b32 v245, 3, v244\n"
"v_mul_u32_u24 v245, 0x500, v245\n"
"v_add_u32 v245, v205, v245\n"
"v_add_u32 v245, v206, v245\n"
"v_add_u32 v245, v207, v245\n"
"v_add_u32 v251, 0x2400, v245\n"
"v_add_u32 v252, 0x4800, v245\n"
"v_add_u32 v253, 0x6c00, v245\n"
"s_add_u32 s41, %[kq], 1\n"
"s_and_b32 s41, s41, 3\n"
"s_lshl_b32 s41, s41, 7\n"
"s_add_u32 s42, %[kq], 2\n"
"s_and_b32 s42, s42, 3\n"
"s_lshl_b32 s42, s42, 7\n"
"s_add_u32 s43, %[kq], 3\n"
"s_and_b32 s43, s43, 3\n"
"s_lshl_b32 s43, s43, 7\n"
"s_add_u32 s65, %[kq], 4\n"
"s_and_b32 s65, s65, 3\n"
"s_lshl_b32 s65, s65, 7\n"
"v_and_b32 v206, 3, v244\n"
"v_lshlrev_b32 v206, 3, v206\n"
"v_lshrrev_b32 v202, 2, v244\n"
"v_and_b32 v202, 3, v202\n"
"v_lshrrev_b32 v202, 1, v202\n"
"v_lshrrev_b32 v205, 4, v244\n"
"v_lshlrev_b32 v205, 1, v205\n"
"v_add_u32 v205, v202, v205\n"
"v_and_b32 v205, 3, v205\n"
"v_xor_b32 v205, s77, v205\n"
"v_lshlrev_b32 v205, 5, v205\n"
"v_lshrrev_b32 v204, 2, v244\n"
"v_and_b32 v204, 3, v204\n"
"v_lshrrev_b32 v224, 4, v244\n"
"v_lshlrev_b32 v224, 2, v224\n"
"v_add_u32 v224, v204, v224\n"
"v_lshlrev_b32 v224, 7, v224\n"
"v_add_u32 v224, v205, v224\n"
"v_add_u32 v224, v206, v224\n"
"v_add_u32 v224, s76, v224\n"
"v_add_u32 v225, 0x10000, v224\n"
"v_add_u32 v226, 0x20000, v224\n"
"s_add_u32 s73, s76, 0xa000\n"
"v_add_u32 v227, s73, v243\n"
"v_add_u32 v228, 0x10000, v227\n"
"s_lshl_b32 s73, s70, 16\n"
"v_add_u32 v229, s73, v227\n"
"s_lshl_b32 s73, s77, 10\n"
"v_add_u32 v230, s73, v243\n"
"v_add_u32 v231, 0x1fc00, v230\n"
"v_add_u32 v232, 0x3f800, v230\n"
"v_add_u32 v233, 0x5f400, v230\n"
"s_mul_i32 s73, s77, 6144\n"
"s_add_u32 s50, s73, s76\n"
"v_and_b32 v238, 7, v244\n"
"v_lshlrev_b32 v238, 7, v238\n"
"v_lshlrev_b32 v239, 11, v244\n"
"s_add_u32 s60, s36, 1\n"
"s_addc_u32 s61, s37, 0\n"
"s_lshr_b32 s63, %[hq0], 2\n"
"s_xor_b32 s63, s63, s71\n"
"s_lshl_b32 s48, %[hq0], 12\n"
"s_add_u32 s48, s48, 0x1000\n"
"s_cmp_lg_u32 s63, 0\n"
"s_cbranch_scc1 LRE2_%=\n"
"s_lshl_b32 s72, s70, 16\n"
"s_add_u32 s72, s72, s76\n"
"s_add_u32 m0, s72, 0xa000\n"
"s_add_u32 s68, s38, 0x0\n"
"buffer_load_dwordx4 v243, s[56:59], s68 offen sc1 lds\n"
"s_add_u32 m0, s72, 0xb800\n"
"s_add_u32 s68, s38, 0x400\n"
"buffer_load_dwordx4 v243, s[56:59], s68 offen sc1 lds\n"
"s_add_u32 m0, s72, 0xd000\n"
"s_add_u32 s68, s38, 0x800\n"
"buffer_load_dwordx4 v243, s[56:59], s68 offen sc1 lds\n"
"s_add_u32 m0, s72, 0xe800\n"
"s_add_u32 s68, s38, 0xc00\n"
"buffer_load_dwordx4 v243, s[56:59], s68 offen sc1 lds\n"
"s_add_u32 m0, s72, 0xa400\n"
"s_add_u32 s68, s38, 0x20000\n"
"buffer_load_dwordx4 v243, s[56:59], s68 offen sc1 lds\n"
"s_add_u32 m0, s72, 0xbc00\n"
"s_add_u32 s68, s38, 0x20400\n"
"buffer_load_dwordx4 v243, s[56:59], s68 offen sc1 lds\n"
"s_add_u32 m0, s72, 0xd400\n"
"s_add_u32 s68, s38, 0x20800\n"
"buffer_load_dwordx4 v243, s[56:59], s68 offen sc1 lds\n"
"s_add_u32 m0, s72, 0xec00\n"
"s_add_u32 s68, s38, 0x20c00\n"
"buffer_load_dwordx4 v243, s[56:59], s68 offen sc1 lds\n"
"s_add_u32 m0, s72, 0xa800\n"
"s_add_u32 s68, s38, 0x40000\n"
"buffer_load_dwordx4 v243, s[56:59], s68 offen sc1 lds\n"
"s_add_u32 m0, s72, 0xc000\n"
"s_add_u32 s68, s38, 0x40400\n"
"buffer_load_dwordx4 v243, s[56:59], s68 offen sc1 lds\n"
"s_add_u32 m0, s72, 0xd800\n"
"s_add_u32 s68, s38, 0x40800\n"
"buffer_load_dwordx4 v243, s[56:59], s68 offen sc1 lds\n"
"s_add_u32 m0, s72, 0xf000\n"
"s_add_u32 s68, s38, 0x40c00\n"
"buffer_load_dwordx4 v243, s[56:59], s68 offen sc1 lds\n"
"s_add_u32 m0, s72, 0xac00\n"
"s_add_u32 s68, s38, 0x60000\n"
"buffer_load_dwordx4 v243, s[56:59], s68 offen sc1 lds\n"
"s_add_u32 m0, s72, 0xc400\n"
"s_add_u32 s68, s38, 0x60400\n"
"buffer_load_dwordx4 v243, s[56:59], s68 offen sc1 lds\n"
"s_add_u32 m0, s72, 0xdc00\n"
"s_add_u32 s68, s38, 0x60800\n"
"buffer_load_dwordx4 v243, s[56:59], s68 offen sc1 lds\n"
"s_add_u32 m0, s72, 0xf400\n"
"s_add_u32 s68, s38, 0x60c00\n"
"buffer_load_dwordx4 v243, s[56:59], s68 offen sc1 lds\n"
"s_add_u32 m0, s72, 0xb000\n"
"s_add_u32 s68, s38, 0x80000\n"
"buffer_load_dwordx4 v243, s[56:59], s68 offen sc1 lds\n"
"s_add_u32 m0, s72, 0xc800\n"
"s_add_u32 s68, s38, 0x80400\n"
"buffer_load_dwordx4 v243, s[56:59], s68 offen sc1 lds\n"
"s_add_u32 m0, s72, 0xe000\n"
"s_add_u32 s68, s38, 0x80800\n"
"buffer_load_dwordx4 v243, s[56:59], s68 offen sc1 lds\n"
"s_add_u32 m0, s72, 0xf800\n"
"s_add_u32 s68, s38, 0x80c00\n"
"buffer_load_dwordx4 v243, s[56:59], s68 offen sc1 lds\n"
"s_add_u32 m0, s72, 0xb400\n"
"s_add_u32 s68, s38, 0xa0000\n"
"buffer_load_dwordx4 v243, s[56:59], s68 offen sc1 lds\n"
"s_add_u32 m0, s72, 0xcc00\n"
"s_add_u32 s68, s38, 0xa0400\n"
"buffer_load_dwordx4 v243, s[56:59], s68 offen sc1 lds\n"
"s_add_u32 m0, s72, 0xe400\n"
"s_add_u32 s68, s38, 0xa0800\n"
"buffer_load_dwordx4 v243, s[56:59], s68 offen sc1 lds\n"
"s_add_u32 m0, s72, 0xfc00\n"
"s_add_u32 s68, s38, 0xa0c00\n"
"buffer_load_dwordx4 v243, s[56:59], s68 offen sc1 lds\n"
"LRE2_%=:\n"
"ds_read_b64_tr_b16 a[192:193], v226 offset:0\n"
"ds_read_b64_tr_b16 a[194:195], v226 offset:2048\n"
"ds_read_b64_tr_b16 a[196:197], v226 offset:4096\n"
"ds_read_b64_tr_b16 a[198:199], v226 offset:6144\n"
"ds_read_b64_tr_b16 a[200:201], v226 offset:8192\n"
"ds_read_b64_tr_b16 a[202:203], v226 offset:10240\n"
"ds_read_b64_tr_b16 a[204:205], v226 offset:12288\n"
"ds_read_b64_tr_b16 a[206:207], v226 offset:14336\n"
"s_waitcnt vmcnt(0) lgkmcnt(0)\n"
"s_branch LCOMMON%=\n"
"LBIG%=:\n"
"s_movk_i32 s72, 0x207\n"
"v_accvgpr_read_b32 v0, a0\n"
"v_accvgpr_read_b32 v1, a1\n"
"v_accvgpr_read_b32 v2, a2\n"
"v_accvgpr_read_b32 v3, a3\n"
"v_cmp_class_f32_e64 vcc, v0, s72\n"
"s_or_b64 s[74:75], s[74:75], vcc\n"
"v_cmp_class_f32_e64 vcc, v1, s72\n"
"s_or_b64 s[74:75], s[74:75], vcc\n"
"v_cmp_class_f32_e64 vcc, v2, s72\n"
"s_or_b64 s[74:75], s[74:75], vcc\n"
"v_cmp_class_f32_e64 vcc, v3, s72\n"
"s_or_b64 s[74:75], s[74:75], vcc\n"
"v_accvgpr_read_b32 v0, a4\n"
"v_accvgpr_read_b32 v1, a5\n"
"v_accvgpr_read_b32 v2, a6\n"
"v_accvgpr_read_b32 v3, a7\n"
"v_cmp_class_f32_e64 vcc, v0, s72\n"
"s_or_b64 s[74:75], s[74:75], vcc\n"
"v_cmp_class_f32_e64 vcc, v1, s72\n"
"s_or_b64 s[74:75], s[74:75], vcc\n"
"v_cmp_class_f32_e64 vcc, v2, s72\n"
"s_or_b64 s[74:75], s[74:75], vcc\n"
"v_cmp_class_f32_e64 vcc, v3, s72\n"
"s_or_b64 s[74:75], s[74:75], vcc\n"
"v_accvgpr_read_b32 v0, a8\n"
"v_accvgpr_read_b32 v1, a9\n"
"v_accvgpr_read_b32 v2, a10\n"
"v_accvgpr_read_b32 v3, a11\n"
"v_cmp_class_f32_e64 vcc, v0, s72\n"
"s_or_b64 s[74:75], s[74:75], vcc\n"
"v_cmp_class_f32_e64 vcc, v1, s72\n"
"s_or_b64 s[74:75], s[74:75], vcc\n"
"v_cmp_class_f32_e64 vcc, v2, s72\n"
"s_or_b64 s[74:75], s[74:75], vcc\n"
"v_cmp_class_f32_e64 vcc, v3, s72\n"
"s_or_b64 s[74:75], s[74:75], vcc\n"
"v_accvgpr_read_b32 v0, a12\n"
"v_accvgpr_read_b32 v1, a13\n"
"v_accvgpr_read_b32 v2, a14\n"
"v_accvgpr_read_b32 v3, a15\n"
"v_cmp_class_f32_e64 vcc, v0, s72\n"
"s_or_b64 s[74:75], s[74:75], vcc\n"
"v_cmp_class_f32_e64 vcc, v1, s72\n"
"s_or_b64 s[74:75], s[74:75], vcc\n"
"v_cmp_class_f32_e64 vcc, v2, s72\n"
"s_or_b64 s[74:75], s[74:75], vcc\n"
"v_cmp_class_f32_e64 vcc, v3, s72\n"
"s_or_b64 s[74:75], s[74:75], vcc\n"
"v_accvgpr_read_b32 v0, a16\n"
"v_accvgpr_read_b32 v1, a17\n"
"v_accvgpr_read_b32 v2, a18\n"
"v_accvgpr_read_b32 v3, a19\n"
"v_cmp_class_f32_e64 vcc, v0, s72\n"
"s_or_b64 s[74:75], s[74:75], vcc\n"
"v_cmp_class_f32_e64 vcc, v1, s72\n"
"s_or_b64 s[74:75], s[74:75], vcc\n"
"v_cmp_class_f32_e64 vcc, v2, s72\n"
"s_or_b64 s[74:75], s[74:75], vcc\n"
"v_cmp_class_f32_e64 vcc, v3, s72\n"
"s_or_b64 s[74:75], s[74:75], vcc\n"
"v_accvgpr_read_b32 v0, a20\n"
"v_accvgpr_read_b32 v1, a21\n"
"v_accvgpr_read_b32 v2, a22\n"
"v_accvgpr_read_b32 v3, a23\n"
"v_cmp_class_f32_e64 vcc, v0, s72\n"
"s_or_b64 s[74:75], s[74:75], vcc\n"
"v_cmp_class_f32_e64 vcc, v1, s72\n"
"s_or_b64 s[74:75], s[74:75], vcc\n"
"v_cmp_class_f32_e64 vcc, v2, s72\n"
"s_or_b64 s[74:75], s[74:75], vcc\n"
"v_cmp_class_f32_e64 vcc, v3, s72\n"
"s_or_b64 s[74:75], s[74:75], vcc\n"
"v_accvgpr_read_b32 v0, a24\n"
"v_accvgpr_read_b32 v1, a25\n"
"v_accvgpr_read_b32 v2, a26\n"
"v_accvgpr_read_b32 v3, a27\n"
"v_cmp_class_f32_e64 vcc, v0, s72\n"
"s_or_b64 s[74:75], s[74:75], vcc\n"
"v_cmp_class_f32_e64 vcc, v1, s72\n"
"s_or_b64 s[74:75], s[74:75], vcc\n"
"v_cmp_class_f32_e64 vcc, v2, s72\n"
"s_or_b64 s[74:75], s[74:75], vcc\n"
"v_cmp_class_f32_e64 vcc, v3, s72\n"
"s_or_b64 s[74:75], s[74:75], vcc\n"
"v_accvgpr_read_b32 v0, a28\n"
"v_accvgpr_read_b32 v1, a29\n"
"v_accvgpr_read_b32 v2, a30\n"
"v_accvgpr_read_b32 v3, a31\n"
"v_cmp_class_f32_e64 vcc, v0, s72\n"
"s_or_b64 s[74:75], s[74:75], vcc\n"
"v_cmp_class_f32_e64 vcc, v1, s72\n"
"s_or_b64 s[74:75], s[74:75], vcc\n"
"v_cmp_class_f32_e64 vcc, v2, s72\n"
"s_or_b64 s[74:75], s[74:75], vcc\n"
"v_cmp_class_f32_e64 vcc, v3, s72\n"
"s_or_b64 s[74:75], s[74:75], vcc\n"
"v_accvgpr_read_b32 v0, a32\n"
"v_accvgpr_read_b32 v1, a33\n"
"v_accvgpr_read_b32 v2, a34\n"
"v_accvgpr_read_b32 v3, a35\n"
"v_cmp_class_f32_e64 vcc, v0, s72\n"
"s_or_b64 s[74:75], s[74:75], vcc\n"
"v_cmp_class_f32_e64 vcc, v1, s72\n"
"s_or_b64 s[74:75], s[74:75], vcc\n"
"v_cmp_class_f32_e64 vcc, v2, s72\n"
"s_or_b64 s[74:75], s[74:75], vcc\n"
"v_cmp_class_f32_e64 vcc, v3, s72\n"
"s_or_b64 s[74:75], s[74:75], vcc\n"
"v_accvgpr_read_b32 v0, a36\n"
"v_accvgpr_read_b32 v1, a37\n"
"v_accvgpr_read_b32 v2, a38\n"
"v_accvgpr_read_b32 v3, a39\n"
"v_cmp_class_f32_e64 vcc, v0, s72\n"
"s_or_b64 s[74:75], s[74:75], vcc\n"
"v_cmp_class_f32_e64 vcc, v1, s72\n"
"s_or_b64 s[74:75], s[74:75], vcc\n"
"v_cmp_class_f32_e64 vcc, v2, s72\n"
"s_or_b64 s[74:75], s[74:75], vcc\n"
"v_cmp_class_f32_e64 vcc, v3, s72\n"
"s_or_b64 s[74:75], s[74:75], vcc\n"
"v_accvgpr_read_b32 v0, a40\n"
"v_accvgpr_read_b32 v1, a41\n"
"v_accvgpr_read_b32 v2, a42\n"
"v_accvgpr_read_b32 v3, a43\n"
"v_cmp_class_f32_e64 vcc, v0, s72\n"
"s_or_b64 s[74:75], s[74:75], vcc\n"
"v_cmp_class_f32_e64 vcc, v1, s72\n"
"s_or_b64 s[74:75], s[74:75], vcc\n"
"v_cmp_class_f32_e64 vcc, v2, s72\n"
"s_or_b64 s[74:75], s[74:75], vcc\n"
"v_cmp_class_f32_e64 vcc, v3, s72\n"
"s_or_b64 s[74:75], s[74:75], vcc\n"
"v_accvgpr_read_b32 v0, a44\n"
"v_accvgpr_read_b32 v1, a45\n"
"v_accvgpr_read_b32 v2, a46\n"
"v_accvgpr_read_b32 v3, a47\n"
"v_cmp_class_f32_e64 vcc, v0, s72\n"
"s_or_b64 s[74:75], s[74:75], vcc\n"
"v_cmp_class_f32_e64 vcc, v1, s72\n"
"s_or_b64 s[74:75], s[74:75], vcc\n"
"v_cmp_class_f32_e64 vcc, v2, s72\n"
"s_or_b64 s[74:75], s[74:75], vcc\n"
"v_cmp_class_f32_e64 vcc, v3, s72\n"
"s_or_b64 s[74:75], s[74:75], vcc\n"
"v_accvgpr_read_b32 v0, a48\n"
"v_accvgpr_read_b32 v1, a49\n"
"v_accvgpr_read_b32 v2, a50\n"
"v_accvgpr_read_b32 v3, a51\n"
"v_cmp_class_f32_e64 vcc, v0, s72\n"
"s_or_b64 s[74:75], s[74:75], vcc\n"
"v_cmp_class_f32_e64 vcc, v1, s72\n"
"s_or_b64 s[74:75], s[74:75], vcc\n"
"v_cmp_class_f32_e64 vcc, v2, s72\n"
"s_or_b64 s[74:75], s[74:75], vcc\n"
"v_cmp_class_f32_e64 vcc, v3, s72\n"
"s_or_b64 s[74:75], s[74:75], vcc\n"
"v_accvgpr_read_b32 v0, a52\n"
"v_accvgpr_read_b32 v1, a53\n"
"v_accvgpr_read_b32 v2, a54\n"
"v_accvgpr_read_b32 v3, a55\n"
"v_cmp_class_f32_e64 vcc, v0, s72\n"
"s_or_b64 s[74:75], s[74:75], vcc\n"
"v_cmp_class_f32_e64 vcc, v1, s72\n"
"s_or_b64 s[74:75], s[74:75], vcc\n"
"v_cmp_class_f32_e64 vcc, v2, s72\n"
"s_or_b64 s[74:75], s[74:75], vcc\n"
"v_cmp_class_f32_e64 vcc, v3, s72\n"
"s_or_b64 s[74:75], s[74:75], vcc\n"
"v_accvgpr_read_b32 v0, a56\n"
"v_accvgpr_read_b32 v1, a57\n"
"v_accvgpr_read_b32 v2, a58\n"
"v_accvgpr_read_b32 v3, a59\n"
"v_cmp_class_f32_e64 vcc, v0, s72\n"
"s_or_b64 s[74:75], s[74:75], vcc\n"
"v_cmp_class_f32_e64 vcc, v1, s72\n"
"s_or_b64 s[74:75], s[74:75], vcc\n"
"v_cmp_class_f32_e64 vcc, v2, s72\n"
"s_or_b64 s[74:75], s[74:75], vcc\n"
"v_cmp_class_f32_e64 vcc, v3, s72\n"
"s_or_b64 s[74:75], s[74:75], vcc\n"
"v_accvgpr_read_b32 v0, a60\n"
"v_accvgpr_read_b32 v1, a61\n"
"v_accvgpr_read_b32 v2, a62\n"
"v_accvgpr_read_b32 v3, a63\n"
"v_cmp_class_f32_e64 vcc, v0, s72\n"
"s_or_b64 s[74:75], s[74:75], vcc\n"
"v_cmp_class_f32_e64 vcc, v1, s72\n"
"s_or_b64 s[74:75], s[74:75], vcc\n"
"v_cmp_class_f32_e64 vcc, v2, s72\n"
"s_or_b64 s[74:75], s[74:75], vcc\n"
"v_cmp_class_f32_e64 vcc, v3, s72\n"
"s_or_b64 s[74:75], s[74:75], vcc\n"
"v_accvgpr_read_b32 v0, a64\n"
"v_accvgpr_read_b32 v1, a65\n"
"v_accvgpr_read_b32 v2, a66\n"
"v_accvgpr_read_b32 v3, a67\n"
"v_cmp_class_f32_e64 vcc, v0, s72\n"
"s_or_b64 s[74:75], s[74:75], vcc\n"
"v_cmp_class_f32_e64 vcc, v1, s72\n"
"s_or_b64 s[74:75], s[74:75], vcc\n"
"v_cmp_class_f32_e64 vcc, v2, s72\n"
"s_or_b64 s[74:75], s[74:75], vcc\n"
"v_cmp_class_f32_e64 vcc, v3, s72\n"
"s_or_b64 s[74:75], s[74:75], vcc\n"
"v_accvgpr_read_b32 v0, a68\n"
"v_accvgpr_read_b32 v1, a69\n"
"v_accvgpr_read_b32 v2, a70\n"
"v_accvgpr_read_b32 v3, a71\n"
"v_cmp_class_f32_e64 vcc, v0, s72\n"
"s_or_b64 s[74:75], s[74:75], vcc\n"
"v_cmp_class_f32_e64 vcc, v1, s72\n"
"s_or_b64 s[74:75], s[74:75], vcc\n"
"v_cmp_class_f32_e64 vcc, v2, s72\n"
"s_or_b64 s[74:75], s[74:75], vcc\n"
"v_cmp_class_f32_e64 vcc, v3, s72\n"
"s_or_b64 s[74:75], s[74:75], vcc\n"
"v_accvgpr_read_b32 v0, a72\n"
"v_accvgpr_read_b32 v1, a73\n"
"v_accvgpr_read_b32 v2, a74\n"
"v_accvgpr_read_b32 v3, a75\n"
"v_cmp_class_f32_e64 vcc, v0, s72\n"
"s_or_b64 s[74:75], s[74:75], vcc\n"
"v_cmp_class_f32_e64 vcc, v1, s72\n"
"s_or_b64 s[74:75], s[74:75], vcc\n"
"v_cmp_class_f32_e64 vcc, v2, s72\n"
"s_or_b64 s[74:75], s[74:75], vcc\n"
"v_cmp_class_f32_e64 vcc, v3, s72\n"
"s_or_b64 s[74:75], s[74:75], vcc\n"
"v_accvgpr_read_b32 v0, a76\n"
"v_accvgpr_read_b32 v1, a77\n"
"v_accvgpr_read_b32 v2, a78\n"
"v_accvgpr_read_b32 v3, a79\n"
"v_cmp_class_f32_e64 vcc, v0, s72\n"
"s_or_b64 s[74:75], s[74:75], vcc\n"
"v_cmp_class_f32_e64 vcc, v1, s72\n"
"s_or_b64 s[74:75], s[74:75], vcc\n"
"v_cmp_class_f32_e64 vcc, v2, s72\n"
"s_or_b64 s[74:75], s[74:75], vcc\n"
"v_cmp_class_f32_e64 vcc, v3, s72\n"
"s_or_b64 s[74:75], s[74:75], vcc\n"
"v_accvgpr_read_b32 v0, a80\n"
"v_accvgpr_read_b32 v1, a81\n"
"v_accvgpr_read_b32 v2, a82\n"
"v_accvgpr_read_b32 v3, a83\n"
"v_cmp_class_f32_e64 vcc, v0, s72\n"
"s_or_b64 s[74:75], s[74:75], vcc\n"
"v_cmp_class_f32_e64 vcc, v1, s72\n"
"s_or_b64 s[74:75], s[74:75], vcc\n"
"v_cmp_class_f32_e64 vcc, v2, s72\n"
"s_or_b64 s[74:75], s[74:75], vcc\n"
"v_cmp_class_f32_e64 vcc, v3, s72\n"
"s_or_b64 s[74:75], s[74:75], vcc\n"
"v_accvgpr_read_b32 v0, a84\n"
"v_accvgpr_read_b32 v1, a85\n"
"v_accvgpr_read_b32 v2, a86\n"
"v_accvgpr_read_b32 v3, a87\n"
"v_cmp_class_f32_e64 vcc, v0, s72\n"
"s_or_b64 s[74:75], s[74:75], vcc\n"
"v_cmp_class_f32_e64 vcc, v1, s72\n"
"s_or_b64 s[74:75], s[74:75], vcc\n"
"v_cmp_class_f32_e64 vcc, v2, s72\n"
"s_or_b64 s[74:75], s[74:75], vcc\n"
"v_cmp_class_f32_e64 vcc, v3, s72\n"
"s_or_b64 s[74:75], s[74:75], vcc\n"
"v_accvgpr_read_b32 v0, a88\n"
"v_accvgpr_read_b32 v1, a89\n"
"v_accvgpr_read_b32 v2, a90\n"
"v_accvgpr_read_b32 v3, a91\n"
"v_cmp_class_f32_e64 vcc, v0, s72\n"
"s_or_b64 s[74:75], s[74:75], vcc\n"
"v_cmp_class_f32_e64 vcc, v1, s72\n"
"s_or_b64 s[74:75], s[74:75], vcc\n"
"v_cmp_class_f32_e64 vcc, v2, s72\n"
"s_or_b64 s[74:75], s[74:75], vcc\n"
"v_cmp_class_f32_e64 vcc, v3, s72\n"
"s_or_b64 s[74:75], s[74:75], vcc\n"
"v_accvgpr_read_b32 v0, a92\n"
"v_accvgpr_read_b32 v1, a93\n"
"v_accvgpr_read_b32 v2, a94\n"
"v_accvgpr_read_b32 v3, a95\n"
"v_cmp_class_f32_e64 vcc, v0, s72\n"
"s_or_b64 s[74:75], s[74:75], vcc\n"
"v_cmp_class_f32_e64 vcc, v1, s72\n"
"s_or_b64 s[74:75], s[74:75], vcc\n"
"v_cmp_class_f32_e64 vcc, v2, s72\n"
"s_or_b64 s[74:75], s[74:75], vcc\n"
"v_cmp_class_f32_e64 vcc, v3, s72\n"
"s_or_b64 s[74:75], s[74:75], vcc\n"
"v_accvgpr_read_b32 v0, a96\n"
"v_accvgpr_read_b32 v1, a97\n"
"v_accvgpr_read_b32 v2, a98\n"
"v_accvgpr_read_b32 v3, a99\n"
"v_cmp_class_f32_e64 vcc, v0, s72\n"
"s_or_b64 s[74:75], s[74:75], vcc\n"
"v_cmp_class_f32_e64 vcc, v1, s72\n"
"s_or_b64 s[74:75], s[74:75], vcc\n"
"v_cmp_class_f32_e64 vcc, v2, s72\n"
"s_or_b64 s[74:75], s[74:75], vcc\n"
"v_cmp_class_f32_e64 vcc, v3, s72\n"
"s_or_b64 s[74:75], s[74:75], vcc\n"
"v_accvgpr_read_b32 v0, a100\n"
"v_accvgpr_read_b32 v1, a101\n"
"v_accvgpr_read_b32 v2, a102\n"
"v_accvgpr_read_b32 v3, a103\n"
"v_cmp_class_f32_e64 vcc, v0, s72\n"
"s_or_b64 s[74:75], s[74:75], vcc\n"
"v_cmp_class_f32_e64 vcc, v1, s72\n"
"s_or_b64 s[74:75], s[74:75], vcc\n"
"v_cmp_class_f32_e64 vcc, v2, s72\n"
"s_or_b64 s[74:75], s[74:75], vcc\n"
"v_cmp_class_f32_e64 vcc, v3, s72\n"
"s_or_b64 s[74:75], s[74:75], vcc\n"
"v_accvgpr_read_b32 v0, a104\n"
"v_accvgpr_read_b32 v1, a105\n"
"v_accvgpr_read_b32 v2, a106\n"
"v_accvgpr_read_b32 v3, a107\n"
"v_cmp_class_f32_e64 vcc, v0, s72\n"
"s_or_b64 s[74:75], s[74:75], vcc\n"
"v_cmp_class_f32_e64 vcc, v1, s72\n"
"s_or_b64 s[74:75], s[74:75], vcc\n"
"v_cmp_class_f32_e64 vcc, v2, s72\n"
"s_or_b64 s[74:75], s[74:75], vcc\n"
"v_cmp_class_f32_e64 vcc, v3, s72\n"
"s_or_b64 s[74:75], s[74:75], vcc\n"
"v_accvgpr_read_b32 v0, a108\n"
"v_accvgpr_read_b32 v1, a109\n"
"v_accvgpr_read_b32 v2, a110\n"
"v_accvgpr_read_b32 v3, a111\n"
"v_cmp_class_f32_e64 vcc, v0, s72\n"
"s_or_b64 s[74:75], s[74:75], vcc\n"
"v_cmp_class_f32_e64 vcc, v1, s72\n"
"s_or_b64 s[74:75], s[74:75], vcc\n"
"v_cmp_class_f32_e64 vcc, v2, s72\n"
"s_or_b64 s[74:75], s[74:75], vcc\n"
"v_cmp_class_f32_e64 vcc, v3, s72\n"
"s_or_b64 s[74:75], s[74:75], vcc\n"
"v_accvgpr_read_b32 v0, a112\n"
"v_accvgpr_read_b32 v1, a113\n"
"v_accvgpr_read_b32 v2, a114\n"
"v_accvgpr_read_b32 v3, a115\n"
"v_cmp_class_f32_e64 vcc, v0, s72\n"
"s_or_b64 s[74:75], s[74:75], vcc\n"
"v_cmp_class_f32_e64 vcc, v1, s72\n"
"s_or_b64 s[74:75], s[74:75], vcc\n"
"v_cmp_class_f32_e64 vcc, v2, s72\n"
"s_or_b64 s[74:75], s[74:75], vcc\n"
"v_cmp_class_f32_e64 vcc, v3, s72\n"
"s_or_b64 s[74:75], s[74:75], vcc\n"
"v_accvgpr_read_b32 v0, a116\n"
"v_accvgpr_read_b32 v1, a117\n"
"v_accvgpr_read_b32 v2, a118\n"
"v_accvgpr_read_b32 v3, a119\n"
"v_cmp_class_f32_e64 vcc, v0, s72\n"
"s_or_b64 s[74:75], s[74:75], vcc\n"
"v_cmp_class_f32_e64 vcc, v1, s72\n"
"s_or_b64 s[74:75], s[74:75], vcc\n"
"v_cmp_class_f32_e64 vcc, v2, s72\n"
"s_or_b64 s[74:75], s[74:75], vcc\n"
"v_cmp_class_f32_e64 vcc, v3, s72\n"
"s_or_b64 s[74:75], s[74:75], vcc\n"
"v_accvgpr_read_b32 v0, a120\n"
"v_accvgpr_read_b32 v1, a121\n"
"v_accvgpr_read_b32 v2, a122\n"
"v_accvgpr_read_b32 v3, a123\n"
"v_cmp_class_f32_e64 vcc, v0, s72\n"
"s_or_b64 s[74:75], s[74:75], vcc\n"
"v_cmp_class_f32_e64 vcc, v1, s72\n"
"s_or_b64 s[74:75], s[74:75], vcc\n"
"v_cmp_class_f32_e64 vcc, v2, s72\n"
"s_or_b64 s[74:75], s[74:75], vcc\n"
"v_cmp_class_f32_e64 vcc, v3, s72\n"
"s_or_b64 s[74:75], s[74:75], vcc\n"
"v_accvgpr_read_b32 v0, a124\n"
"v_accvgpr_read_b32 v1, a125\n"
"v_accvgpr_read_b32 v2, a126\n"
"v_accvgpr_read_b32 v3, a127\n"
"v_cmp_class_f32_e64 vcc, v0, s72\n"
"s_or_b64 s[74:75], s[74:75], vcc\n"
"v_cmp_class_f32_e64 vcc, v1, s72\n"
"s_or_b64 s[74:75], s[74:75], vcc\n"
"v_cmp_class_f32_e64 vcc, v2, s72\n"
"s_or_b64 s[74:75], s[74:75], vcc\n"
"v_cmp_class_f32_e64 vcc, v3, s72\n"
"s_or_b64 s[74:75], s[74:75], vcc\n"
"v_accvgpr_read_b32 v0, a128\n"
"v_accvgpr_read_b32 v1, a129\n"
"v_accvgpr_read_b32 v2, a130\n"
"v_accvgpr_read_b32 v3, a131\n"
"v_cmp_class_f32_e64 vcc, v0, s72\n"
"s_or_b64 s[74:75], s[74:75], vcc\n"
"v_cmp_class_f32_e64 vcc, v1, s72\n"
"s_or_b64 s[74:75], s[74:75], vcc\n"
"v_cmp_class_f32_e64 vcc, v2, s72\n"
"s_or_b64 s[74:75], s[74:75], vcc\n"
"v_cmp_class_f32_e64 vcc, v3, s72\n"
"s_or_b64 s[74:75], s[74:75], vcc\n"
"v_accvgpr_read_b32 v0, a132\n"
"v_accvgpr_read_b32 v1, a133\n"
"v_accvgpr_read_b32 v2, a134\n"
"v_accvgpr_read_b32 v3, a135\n"
"v_cmp_class_f32_e64 vcc, v0, s72\n"
"s_or_b64 s[74:75], s[74:75], vcc\n"
"v_cmp_class_f32_e64 vcc, v1, s72\n"
"s_or_b64 s[74:75], s[74:75], vcc\n"
"v_cmp_class_f32_e64 vcc, v2, s72\n"
"s_or_b64 s[74:75], s[74:75], vcc\n"
"v_cmp_class_f32_e64 vcc, v3, s72\n"
"s_or_b64 s[74:75], s[74:75], vcc\n"
"v_accvgpr_read_b32 v0, a136\n"
"v_accvgpr_read_b32 v1, a137\n"
"v_accvgpr_read_b32 v2, a138\n"
"v_accvgpr_read_b32 v3, a139\n"
"v_cmp_class_f32_e64 vcc, v0, s72\n"
"s_or_b64 s[74:75], s[74:75], vcc\n"
"v_cmp_class_f32_e64 vcc, v1, s72\n"
"s_or_b64 s[74:75], s[74:75], vcc\n"
"v_cmp_class_f32_e64 vcc, v2, s72\n"
"s_or_b64 s[74:75], s[74:75], vcc\n"
"v_cmp_class_f32_e64 vcc, v3, s72\n"
"s_or_b64 s[74:75], s[74:75], vcc\n"
"v_accvgpr_read_b32 v0, a140\n"
"v_accvgpr_read_b32 v1, a141\n"
"v_accvgpr_read_b32 v2, a142\n"
"v_accvgpr_read_b32 v3, a143\n"
"v_cmp_class_f32_e64 vcc, v0, s72\n"
"s_or_b64 s[74:75], s[74:75], vcc\n"
"v_cmp_class_f32_e64 vcc, v1, s72\n"
"s_or_b64 s[74:75], s[74:75], vcc\n"
"v_cmp_class_f32_e64 vcc, v2, s72\n"
"s_or_b64 s[74:75], s[74:75], vcc\n"
"v_cmp_class_f32_e64 vcc, v3, s72\n"
"s_or_b64 s[74:75], s[74:75], vcc\n"
"v_accvgpr_read_b32 v0, a144\n"
"v_accvgpr_read_b32 v1, a145\n"
"v_accvgpr_read_b32 v2, a146\n"
"v_accvgpr_read_b32 v3, a147\n"
"v_cmp_class_f32_e64 vcc, v0, s72\n"
"s_or_b64 s[74:75], s[74:75], vcc\n"
"v_cmp_class_f32_e64 vcc, v1, s72\n"
"s_or_b64 s[74:75], s[74:75], vcc\n"
"v_cmp_class_f32_e64 vcc, v2, s72\n"
"s_or_b64 s[74:75], s[74:75], vcc\n"
"v_cmp_class_f32_e64 vcc, v3, s72\n"
"s_or_b64 s[74:75], s[74:75], vcc\n"
"v_accvgpr_read_b32 v0, a148\n"
"v_accvgpr_read_b32 v1, a149\n"
"v_accvgpr_read_b32 v2, a150\n"
"v_accvgpr_read_b32 v3, a151\n"
"v_cmp_class_f32_e64 vcc, v0, s72\n"
"s_or_b64 s[74:75], s[74:75], vcc\n"
"v_cmp_class_f32_e64 vcc, v1, s72\n"
"s_or_b64 s[74:75], s[74:75], vcc\n"
"v_cmp_class_f32_e64 vcc, v2, s72\n"
"s_or_b64 s[74:75], s[74:75], vcc\n"
"v_cmp_class_f32_e64 vcc, v3, s72\n"
"s_or_b64 s[74:75], s[74:75], vcc\n"
"v_accvgpr_read_b32 v0, a152\n"
"v_accvgpr_read_b32 v1, a153\n"
"v_accvgpr_read_b32 v2, a154\n"
"v_accvgpr_read_b32 v3, a155\n"
"v_cmp_class_f32_e64 vcc, v0, s72\n"
"s_or_b64 s[74:75], s[74:75], vcc\n"
"v_cmp_class_f32_e64 vcc, v1, s72\n"
"s_or_b64 s[74:75], s[74:75], vcc\n"
"v_cmp_class_f32_e64 vcc, v2, s72\n"
"s_or_b64 s[74:75], s[74:75], vcc\n"
"v_cmp_class_f32_e64 vcc, v3, s72\n"
"s_or_b64 s[74:75], s[74:75], vcc\n"
"v_accvgpr_read_b32 v0, a156\n"
"v_accvgpr_read_b32 v1, a157\n"
"v_accvgpr_read_b32 v2, a158\n"
"v_accvgpr_read_b32 v3, a159\n"
"v_cmp_class_f32_e64 vcc, v0, s72\n"
"s_or_b64 s[74:75], s[74:75], vcc\n"
"v_cmp_class_f32_e64 vcc, v1, s72\n"
"s_or_b64 s[74:75], s[74:75], vcc\n"
"v_cmp_class_f32_e64 vcc, v2, s72\n"
"s_or_b64 s[74:75], s[74:75], vcc\n"
"v_cmp_class_f32_e64 vcc, v3, s72\n"
"s_or_b64 s[74:75], s[74:75], vcc\n"
"v_accvgpr_read_b32 v0, a160\n"
"v_accvgpr_read_b32 v1, a161\n"
"v_accvgpr_read_b32 v2, a162\n"
"v_accvgpr_read_b32 v3, a163\n"
"v_cmp_class_f32_e64 vcc, v0, s72\n"
"s_or_b64 s[74:75], s[74:75], vcc\n"
"v_cmp_class_f32_e64 vcc, v1, s72\n"
"s_or_b64 s[74:75], s[74:75], vcc\n"
"v_cmp_class_f32_e64 vcc, v2, s72\n"
"s_or_b64 s[74:75], s[74:75], vcc\n"
"v_cmp_class_f32_e64 vcc, v3, s72\n"
"s_or_b64 s[74:75], s[74:75], vcc\n"
"v_accvgpr_read_b32 v0, a164\n"
"v_accvgpr_read_b32 v1, a165\n"
"v_accvgpr_read_b32 v2, a166\n"
"v_accvgpr_read_b32 v3, a167\n"
"v_cmp_class_f32_e64 vcc, v0, s72\n"
"s_or_b64 s[74:75], s[74:75], vcc\n"
"v_cmp_class_f32_e64 vcc, v1, s72\n"
"s_or_b64 s[74:75], s[74:75], vcc\n"
"v_cmp_class_f32_e64 vcc, v2, s72\n"
"s_or_b64 s[74:75], s[74:75], vcc\n"
"v_cmp_class_f32_e64 vcc, v3, s72\n"
"s_or_b64 s[74:75], s[74:75], vcc\n"
"v_accvgpr_read_b32 v0, a168\n"
"v_accvgpr_read_b32 v1, a169\n"
"v_accvgpr_read_b32 v2, a170\n"
"v_accvgpr_read_b32 v3, a171\n"
"v_cmp_class_f32_e64 vcc, v0, s72\n"
"s_or_b64 s[74:75], s[74:75], vcc\n"
"v_cmp_class_f32_e64 vcc, v1, s72\n"
"s_or_b64 s[74:75], s[74:75], vcc\n"
"v_cmp_class_f32_e64 vcc, v2, s72\n"
"s_or_b64 s[74:75], s[74:75], vcc\n"
"v_cmp_class_f32_e64 vcc, v3, s72\n"
"s_or_b64 s[74:75], s[74:75], vcc\n"
"v_accvgpr_read_b32 v0, a172\n"
"v_accvgpr_read_b32 v1, a173\n"
"v_accvgpr_read_b32 v2, a174\n"
"v_accvgpr_read_b32 v3, a175\n"
"v_cmp_class_f32_e64 vcc, v0, s72\n"
"s_or_b64 s[74:75], s[74:75], vcc\n"
"v_cmp_class_f32_e64 vcc, v1, s72\n"
"s_or_b64 s[74:75], s[74:75], vcc\n"
"v_cmp_class_f32_e64 vcc, v2, s72\n"
"s_or_b64 s[74:75], s[74:75], vcc\n"
"v_cmp_class_f32_e64 vcc, v3, s72\n"
"s_or_b64 s[74:75], s[74:75], vcc\n"
"v_accvgpr_read_b32 v0, a176\n"
"v_accvgpr_read_b32 v1, a177\n"
"v_accvgpr_read_b32 v2, a178\n"
"v_accvgpr_read_b32 v3, a179\n"
"v_cmp_class_f32_e64 vcc, v0, s72\n"
"s_or_b64 s[74:75], s[74:75], vcc\n"
"v_cmp_class_f32_e64 vcc, v1, s72\n"
"s_or_b64 s[74:75], s[74:75], vcc\n"
"v_cmp_class_f32_e64 vcc, v2, s72\n"
"s_or_b64 s[74:75], s[74:75], vcc\n"
"v_cmp_class_f32_e64 vcc, v3, s72\n"
"s_or_b64 s[74:75], s[74:75], vcc\n"
"v_accvgpr_read_b32 v0, a180\n"
"v_accvgpr_read_b32 v1, a181\n"
"v_accvgpr_read_b32 v2, a182\n"
"v_accvgpr_read_b32 v3, a183\n"
"v_cmp_class_f32_e64 vcc, v0, s72\n"
"s_or_b64 s[74:75], s[74:75], vcc\n"
"v_cmp_class_f32_e64 vcc, v1, s72\n"
"s_or_b64 s[74:75], s[74:75], vcc\n"
"v_cmp_class_f32_e64 vcc, v2, s72\n"
"s_or_b64 s[74:75], s[74:75], vcc\n"
"v_cmp_class_f32_e64 vcc, v3, s72\n"
"s_or_b64 s[74:75], s[74:75], vcc\n"
"v_accvgpr_read_b32 v0, a184\n"
"v_accvgpr_read_b32 v1, a185\n"
"v_accvgpr_read_b32 v2, a186\n"
"v_accvgpr_read_b32 v3, a187\n"
"v_cmp_class_f32_e64 vcc, v0, s72\n"
"s_or_b64 s[74:75], s[74:75], vcc\n"
"v_cmp_class_f32_e64 vcc, v1, s72\n"
"s_or_b64 s[74:75], s[74:75], vcc\n"
"v_cmp_class_f32_e64 vcc, v2, s72\n"
"s_or_b64 s[74:75], s[74:75], vcc\n"
"v_cmp_class_f32_e64 vcc, v3, s72\n"
"s_or_b64 s[74:75], s[74:75], vcc\n"
"v_accvgpr_read_b32 v0, a188\n"
"v_accvgpr_read_b32 v1, a189\n"
"v_accvgpr_read_b32 v2, a190\n"
"v_accvgpr_read_b32 v3, a191\n"
"v_cmp_class_f32_e64 vcc, v0, s72\n"
"s_or_b64 s[74:75], s[74:75], vcc\n"
"v_cmp_class_f32_e64 vcc, v1, s72\n"
"s_or_b64 s[74:75], s[74:75], vcc\n"
"v_cmp_class_f32_e64 vcc, v2, s72\n"
"s_or_b64 s[74:75], s[74:75], vcc\n"
"v_cmp_class_f32_e64 vcc, v3, s72\n"
"s_or_b64 s[74:75], s[74:75], vcc\n"
"s_or_b32 %[flag], s74, s75\n"
"s_cbranch_scc0 LBIGR%=\n"
"LEND%=:\n"
      : [flag] "=&s"(flag), "+{s[40:41]}"(px), "+{s[44:45]}"(pw), "+{s46}"(wsz), "+{s[48:49]}"(pk), "+{s[52:53]}"(pv), "+{s[56:57]}"(pws), "+{s[92:93]}"(pwo), "+{s94}"(wosz), "+{s77}"(wvi), "+{s76}"(ldsi), "+{s38}"(o1), "+{s39}"(o2), "+{s64}"(my1), "+{s66}"(fl1), "+{s67}"(my2)
      : [pwm] "s"(pwm), [pout] "s"(pout), [fl2] "s"(fl2), [pw1] "s"(pw1), [pr1] "s"(pr1),
        [idx] "s"(idx), [rot] "s"(rot), [krot] "s"(krot), [hq0] "s"(hq0), [mode] "s"(mode), [fin] "s"(wg * 4 + wv), [kq] "s"(kq)
      : "memory", "vcc", "scc", "v0", "v1", "v2", "v3", "v4", "v5", "v6", "v7", "v8", "v9", "v10", "v11", "v12", "v13", "v14", "v15", "v16", "v17", "v18", "v19", "v20", "v21", "v22", "v23", "v24", "v25", "v26", "v27", "v28", "v29", "v30", "v31", "v32", "v33", "v34", "v35", "v36", "v37", "v38", "v39", "v40", "v41", "v42", "v43", "v44", "v45", "v46", "v47", "v48", "v49", "v50", "v51", "v52", "v53", "v54", "v55", "v56", "v57", "v58", "v59", "v60", "v61", "v62", "v63", "v64", "v65", "v66", "v67", "v68", "v69", "v70", "v71", "v72", "v73", "v74", "v75", "v76", "v77", "v78", "v79", "v80", "v81", "v82", "v83", "v84", "v85", "v86", "v87", "v88", "v89", "v90", "v91", "v92", "v93", "v94", "v95", "v96", "v97", "v98", "v99", "v100", "v101", "v102", "v103", "v104", "v105", "v106", "v107", "v108", "v109", "v110", "v111", "v112", "v113", "v114", "v115", "v116", "v117", "v118", "v119", "v120", "v121", "v122", "v123", "v124", "v125", "v126", "v127", "v128", "v129", "v130", "v131", "v132", "v133", "v134", "v135", "v136", "v137", "v138", "v139", "v140", "v141", "v142", "v143", "v144", "v145", "v146", "v147", "v148", "v149", "v150", "v151", "v152", "v153", "v154", "v155", "v156", "v157", "v158", "v159", "v160", "v161", "v162", "v163", "v164", "v165", "v166", "v167", "v168", "v169", "v170", "v171", "v172", "v173", "v174", "v175", "v176", "v177", "v178", "v179", "v180", "v181", "v182", "v183", "v184", "v185", "v186", "v187", "v188", "v189", "v190", "v191", "v192", "v193", "v194", "v195", "v196", "v197", "v198", "v199", "v200", "v201", "v202", "v203", "v204", "v205", "v206", "v207", "v208", "v209", "v210", "v211", "v212", "v213", "v214", "v215", "v216", "v217", "v218", "v219", "v220", "v221", "v222", "v223", "v224", "v225", "v226", "v227", "v228", "v229", "v230", "v231", "v232", "v233", "v234", "v235", "v236", "v237", "v238", "v239", "v240", "v241", "v242", "v243", "v244", "v245", "v246", "v247", "v248", "v249", "v250", "v251", "v252", "v253", "v254", "v255", "a0", "a1", "a2", "a3", "a4", "a5", "a6", "a7", "a8", "a9", "a10", "a11", "a12", "a13", "a14", "a15", "a16", "a17", "a18", "a19", "a20", "a21", "a22", "a23", "a24", "a25", "a26", "a27", "a28", "a29", "a30", "a31", "a32", "a33", "a34", "a35", "a36", "a37", "a38", "a39", "a40", "a41", "a42", "a43", "a44", "a45", "a46", "a47", "a48", "a49", "a50", "a51", "a52", "a53", "a54", "a55", "a56", "a57", "a58", "a59", "a60", "a61", "a62", "a63", "a64", "a65", "a66", "a67", "a68", "a69", "a70", "a71", "a72", "a73", "a74", "a75", "a76", "a77", "a78", "a79", "a80", "a81", "a82", "a83", "a84", "a85", "a86", "a87", "a88", "a89", "a90", "a91", "a92", "a93", "a94", "a95", "a96", "a97", "a98", "a99", "a100", "a101", "a102", "a103", "a104", "a105", "a106", "a107", "a108", "a109", "a110", "a111", "a112", "a113", "a114", "a115", "a116", "a117", "a118", "a119", "a120", "a121", "a122", "a123", "a124", "a125", "a126", "a127", "a128", "a129", "a130", "a131", "a132", "a133", "a134", "a135", "a136", "a137", "a138", "a139", "a140", "a141", "a142", "a143", "a144", "a145", "a146", "a147", "a148", "a149", "a150", "a151", "a152", "a153", "a154", "a155", "a156", "a157", "a158", "a159", "a160", "a161", "a162", "a163", "a164", "a165", "a166", "a167", "a168", "a169", "a170", "a171", "a172", "a173", "a174", "a175", "a176", "a177", "a178", "a179", "a180", "a181", "a182", "a183", "a184", "a185", "a186", "a187", "a188", "a189", "a190", "a191", "a192", "a193", "a194", "a195", "a196", "a197", "a198", "a199", "a200", "a201", "a202", "a203", "a204", "a205", "a206", "a207", "a208", "a209", "a210", "a211", "a212", "a213", "a214", "a215", "a216", "a217", "a218", "a219", "a220", "a221", "a222", "a223", "a224", "a225", "a226", "a227", "a228", "a229", "a230", "a231", "a232", "a233", "a234", "a235", "a236", "a237", "a238", "a239", "a240", "a241", "a242", "a243", "a244", "a245", "a246", "a247", "a248", "a249", "a250", "a251", "a252", "a253", "a254", "a255", "s36", "s37", "s42", "s43", "s47", "s50", "s51", "s54", "s55", "s58", "s59", "s60", "s61", "s62", "s63", "s65", "s68", "s69", "s70", "s71", "s72", "s73", "s74", "s75", "s78", "s79", "s80", "s81", "s82", "s83", "s84", "s85", "s86", "s87", "s88", "s89", "s90", "s91", "s95");
    asm volatile("" : "+s"(wg), "+s"(wv), "+s"(a));
    if (!(flag)) break;
    const bool bb = wg & 4;
    x = bb ? a->xB : a->xA; k = bb ? a->kB : a->kA; v = bb ? a->vB : a->vA; wq = bb ? a->wqB : a->wqA; wo = bb ? a->woB : a->woA; ws = a->ws; out = a->out;
    const unsigned b2 = wg >> 5, h2 = 8 * (wg & 3) + 2 * ((wg >> 3) & 3) + (wv >> 1), r2 = wv & 1;
    asm volatile("s_waitcnt vmcnt(0)" ::: "memory");
    attn_true_max(x + (size_t)(b2 * 192 + r2 * 96) * 640, wq + h2 * 128, k + (size_t)(b2 * 32 + h2) * 65536, v + (size_t)(b2 * 32 + h2) * 65536,
                  ws + ((size_t)(((wg >> 2 & 1) * 8 + b2) * 12 + 6 * r2) * 128 + h2 * 4) * 1024);
    asm volatile("s_waitcnt vmcnt(0) lgkmcnt(0)" ::: "memory");
    mode = 1;
  }
}

extern "C" void kernel_launch(void* const* d_in, const int* in_sizes, int n_in, void* d_out, int out_size, void* d_ws, size_t ws_size, hipStream_t stream) {
  Args a;
  a.xA = (const u16*)d_in[0]; a.kA = (const u16*)d_in[1]; a.vA = (const u16*)d_in[2]; a.wqA = (const u16*)d_in[3]; a.woA = (const u16*)d_in[4];
  a.xB = (const u16*)d_in[5]; a.kB = (const u16*)d_in[6]; a.vB = (const u16*)d_in[7]; a.wqB = (const u16*)d_in[8]; a.woB = (const u16*)d_in[9];
  a.ws = (char*)d_ws; a.out = (u16*)d_out;
  k_block<<<256, 256, 0, stream>>>(a);
}
```
